```python
import torch

def _softmax(x, axis=-1):
    return torch.softmax(x, dim=axis)
def _gelu(x):
    return torch.nn.functional.gelu(x, approximate="tanh")
def _silu(x):
    return torch.nn.functional.silu(x)
def _sigmoid(x):
    return torch.sigmoid(x)
def _layernorm(x, axis=-1, eps=1e-5):
    m = x.mean(dim=axis, keepdim=True)
    v = x.var(dim=axis, keepdim=True, unbiased=False)
    return (x - m) / torch.sqrt(v + eps)
def _scatter_add(tgt, idx, src, axis=0):
    return tgt.clone().index_add_(axis, idx, src)
def _segment_sum(src, seg, n):
    out = torch.zeros((n,) + src.shape[1:], dtype=src.dtype)
    return out.index_add_(0, seg, src)
def _pad(x, pw):
    flat = tuple(v for pair in reversed(pw) for v in pair)
    return torch.nn.functional.pad(x, flat)

kernel_name = 'concat_heads__ssm_conv_s4d__compressed_kv_cla__prompt'

def setup_inputs():

    _w = (lambda a: (lambda t: t.to(torch.bfloat16) if t.is_floating_point() else t)(torch.as_tensor(a)))
    def _A_setup():
        Bsz, L, D, N = 1, 2048, 512, 128
        return {
            "u": _w((lambda *s: torch.randn(*s, dtype=torch.bfloat16) * 0.05)(Bsz, L, D)),
            "log_A_real": _w((lambda *s: torch.randn(*s, dtype=torch.bfloat16) * 6.4)(D, N) * 0.5),
            "A_imag": _w((lambda *s: torch.randn(*s, dtype=torch.bfloat16) * 0.05)(D, N) * 3.14),
            "C_real": _w((lambda *s: torch.randn(*s, dtype=torch.bfloat16) * 0.05)(D, N) / N ** 0.5),
            "C_imag": _w((lambda *s: torch.randn(*s, dtype=torch.bfloat16) * 0.05)(D, N) / N ** 0.5),
            "log_dt": _w((lambda *s: torch.randn(*s, dtype=torch.bfloat16) * 0.05)(D) * 0.5 - 4.0),
            "D_skip": _w((lambda *s: torch.randn(*s, dtype=torch.bfloat16) * 0.05)(D)),
        }
    _A_in = _A_setup()

    def _B_setup():
        B, H, Sq, Sk, D, M = 1, 32, 2048, 2048, 128, 512
        Dc = 4 * D
        return {
            "x": _w((lambda *s: torch.randn(*s, dtype=torch.bfloat16) * 0.8)(B, Sq, M)),
            "c_kv": _w((lambda *s: torch.randn(*s, dtype=torch.bfloat16) * 0.2)(B, Sk, Dc)),
            "w_q": _w((lambda *s: torch.randn(*s, dtype=torch.bfloat16) * 3.2)(M, H, D) * (M ** -0.5)),
            "w_uk": _w((lambda *s: torch.randn(*s, dtype=torch.bfloat16) * 3.2)(Dc, H, D) * (Dc ** -0.5)),
            "w_uv": _w((lambda *s: torch.randn(*s, dtype=torch.bfloat16) * 0.2)(Dc, H, D) * (Dc ** -0.5)),
            "w_o": _w((lambda *s: torch.randn(*s, dtype=torch.bfloat16) * 0.2)(H, D, M) * ((H * D) ** -0.5)),
        }
    _B_in = _B_setup()

    return {
        "A_u": _A_in["u"],
        "A_log_A_real": _A_in["log_A_real"],
        "A_A_imag": _A_in["A_imag"],
        "A_C_real": _A_in["C_real"],
        "A_C_imag": _A_in["C_imag"],
        "A_log_dt": _A_in["log_dt"],
        "A_D_skip": _A_in["D_skip"],
        "B_x": _B_in["x"],
        "B_c_kv": _B_in["c_kv"],
        "B_w_q": _B_in["w_q"],
        "B_w_uk": _B_in["w_uk"],
        "B_w_uv": _B_in["w_uv"],
        "B_w_o": _B_in["w_o"],
    }

def reference(A_u, A_log_A_real, A_A_imag, A_C_real, A_C_imag, A_log_dt, A_D_skip, B_x, B_c_kv, B_w_q, B_w_uk, B_w_uv, B_w_o):
    def _A_ref(u, log_A_real, A_imag, C_real, C_imag, log_dt, D_skip):
        L = u.shape[1]
        dt = torch.exp(log_dt)
        A = -torch.exp(log_A_real) + 1j * A_imag
        C = (C_real + 1j * C_imag) * dt[:, None]
        pos = (lambda x, dt: x.to(dt))(torch.arange(L), log_dt.dtype)
        P = torch.exp((A * dt[:, None])[:, :, None] * pos[None, None, :])
        K = 2.0 * torch.einsum("dn,dnl->dl", C, P).real
        L2 = 2 * L
        u_t = (lambda x, *d: x.permute(*d))(u, 0, 2, 1)
        Kf = (lambda x, *a, **k: torch.fft.rfft(x.float(), *a, **k))(K, L2)
        Uf = (lambda x, *a, **k: torch.fft.rfft(x.float(), *a, **k))(u_t, L2)
        y = (lambda x, *a, **k: torch.fft.irfft(x, *a, **k).to(torch.bfloat16))(Uf * Kf[None, :, :], L2)[:, :, :L]
        y = (lambda x, *d: x.permute(*d))(y, 0, 2, 1)
        return y + u * D_skip[None, None, :]

    def _B_ref(x, c_kv, w_q, w_uk, w_uv, w_o):
        B, Sq, M = x.shape[0], x.shape[1], x.shape[2]
        Sk = c_kv.shape[1]
        H, D = w_q.shape[1], w_q.shape[2]
        q = torch.einsum("bqm,mhd->bhqd", x, w_q)
        k = torch.einsum("bkc,chd->bhkd", c_kv, w_uk)
        v = torch.einsum("bkc,chd->bhkd", c_kv, w_uv)
        scores = torch.einsum("bhqd,bhkd->bhqk", q, k) * (D ** -0.5)
        off = Sk - Sq
        qi = torch.arange(Sq)[:, None]
        kj = torch.arange(Sk)[None, :]
        cmask = kj <= (qi + off)
        scores = torch.where(cmask[None, None], scores, -1e9)
        p = _softmax(scores, axis=-1)
        ctx = torch.einsum("bhqk,bhkd->bhqd", p, v)
        return torch.einsum("bhqd,hdm->bqm", ctx, w_o)

    a = _A_ref(A_u, A_log_A_real, A_A_imag, A_C_real, A_C_imag, A_log_dt, A_D_skip)
    b = _B_ref(B_x, B_c_kv, B_w_q, B_w_uk, B_w_uv, B_w_o)
    return torch.concatenate([a, b], axis=-1)
```

```cpp
#include <hip/hip_runtime.h>
#include <hip/hip_bf16.h>
#include <stdint.h>

typedef unsigned short u16;
typedef short bf16x8 __attribute__((ext_vector_type(8)));
typedef _Float16 f16x8 __attribute__((ext_vector_type(8)));
typedef _Float16 f16x2 __attribute__((ext_vector_type(2)));
typedef __bf16 bf16x2_t __attribute__((ext_vector_type(2)));
typedef float f32x2 __attribute__((ext_vector_type(2)));
typedef float f32x4 __attribute__((ext_vector_type(4)));
typedef float f32x16 __attribute__((ext_vector_type(16)));
typedef unsigned u32x4 __attribute__((ext_vector_type(4)));
typedef unsigned u32x2 __attribute__((ext_vector_type(2)));
#define DI __device__ __forceinline__

DI float bf2f(unsigned b) { return __uint_as_float(b << 16); }
DI unsigned pk_bf16(float lo, float hi) { f32x2 v = {lo, hi}; bf16x2_t b = __builtin_convertvector(v, bf16x2_t); return __builtin_bit_cast(unsigned, b); }
DI unsigned pk_f16(float lo, float hi) { f32x2 v = {lo, hi}; f16x2 b = __builtin_convertvector(v, f16x2); return __builtin_bit_cast(unsigned, b); }
DI float rbf(float x) { return bf2f(pk_bf16(x, 0.f) & 0xffffu); }

constexpr int SEQ = 2048, NH = 32, HD = 128, DM = 512;
constexpr size_t WS_K = 0, WS_V = 16u << 20, WS_Q = 32u << 20, WS_C = 48u << 20;

constexpr int TC_STRIDE = 4160;
constexpr int A_TC = 0, A_KD = 2 * 8 * TC_STRIDE;
constexpr int A_RED = A_KD + 2 * 4608;
constexpr int A_LDS = A_RED + 64;
__global__ __launch_bounds__(256) void ssm_kernel(const u16* __restrict__ U, const u16* __restrict__ LAR, const u16* __restrict__ AIM,
                                                  const u16* __restrict__ CR, const u16* __restrict__ CI, const u16* __restrict__ LDT,
                                                  const u16* __restrict__ DSK, u16* __restrict__ OUT) {
    extern __shared__ __attribute__((aligned(16))) char lds[];
    const int tid = threadIdx.x, lane = tid & 63, wid = tid >> 6, ch = wid >> 1, hw = wid & 1;
    const int b_ = blockIdx.x, d0 = 2 * ((b_ & 7) * 32 + (b_ >> 3)), d = d0 + ch;
    const int r16 = lane & 15, quad = lane >> 4;
    unsigned uu[8];
#pragma unroll
    for (int i = 0; i < 8; ++i) uu[i] = *(const unsigned*)(U + (size_t)(tid + 256 * i) * DM + d0);
    const float dt = rbf(expf(bf2f(LDT[d])));
    unsigned cm; { unsigned a = *(const unsigned*)(CR + d * 128 + 2 * lane), c = *(const unsigned*)(CI + d * 128 + 2 * lane);
        unsigned m0 = max(a & 0x7fffu, (a >> 16) & 0x7fffu), m1 = max(c & 0x7fffu, (c >> 16) & 0x7fffu); cm = max(m0, m1);
#pragma unroll
        for (int o = 32; o >= 1; o >>= 1) cm = max(cm, (unsigned)__shfl_xor((int)cm, o)); }
    const int eC = (int)(cm >> 7) - 126;
    float ph[3];
    ph[0] = (float)((r16 & 7) + 128 * (r16 >> 3));
    ph[1] = r16 < 8 ? (float)(256 + 2 * (r16 & 3) + 128 * (r16 >> 2)) : (float)(512 + 4 * (r16 & 1) + 128 * ((r16 - 8) >> 1));
    ph[2] = r16 < 8 ? (float)(1024 + 128 * r16) : (r16 == 8 ? 2048.f : 0.f);
    const float plo = (float)(8 * r16);
    f32x4 kacc[3] = {{0,0,0,0},{0,0,0,0},{0,0,0,0}};
#pragma unroll
    for (int ks = 0; ks < 4; ++ks) {
        const int n0 = d * 128 + 64 * hw + 16 * ks + 4 * quad;
        const u32x2 vl = *(const u32x2*)(LAR + n0), vi = *(const u32x2*)(AIM + n0), vr = *(const u32x2*)(CR + n0), vc = *(const u32x2*)(CI + n0);
        u32x4 af[3], bfr;
#pragma unroll
        for (int j = 0; j < 4; ++j) {
            const unsigned sh = (j & 1) * 16;
            const float lar = bf2f((vl[j >> 1] >> sh) & 0xffffu), aim = bf2f((vi[j >> 1] >> sh) & 0xffffu);
            const float cr = ldexpf(bf2f((vr[j >> 1] >> sh) & 0xffffu), -eC), ci = ldexpf(bf2f((vc[j >> 1] >> sh) & 0xffffu), -eC);
            const float zr = -rbf(expf(lar)) * dt * 1.4426950408889634f, zi = aim * dt * 0.15915494309189535f;
#pragma unroll
            for (int t = 0; t < 3; ++t) {
                const float mag = __builtin_amdgcn_exp2f(zr * ph[t]); const float fr = __builtin_amdgcn_fractf(zi * ph[t]);
                const float er = mag * __builtin_amdgcn_cosf(fr), ei = mag * __builtin_amdgcn_sinf(fr);
                af[t][j] = pk_f16(cr * er - ci * ei, -(cr * ei + ci * er));
            }
            { const float mag = __builtin_amdgcn_exp2f(zr * plo); const float fr = __builtin_amdgcn_fractf(zi * plo);
              bfr[j] = pk_f16(mag * __builtin_amdgcn_cosf(fr), mag * __builtin_amdgcn_sinf(fr)); }
        }
#pragma unroll
        for (int t = 0; t < 3; ++t) kacc[t] = __builtin_amdgcn_mfma_f32_16x16x32_f16(__builtin_bit_cast(f16x8, af[t]), __builtin_bit_cast(f16x8, bfr), kacc[t], 0, 0, 0);
    }
    float* KP = (float*)(lds + A_TC);
#pragma unroll
    for (int t = 0; t < 3; ++t)
#pragma unroll
        for (int r = 0; r < 4; ++r) KP[(ch * 2 + hw) * 768 + (16 * t + 4 * quad + r) * 16 + r16] = kacc[t][r];
    unsigned um0 = 0, um1 = 0;
#pragma unroll
    for (int i = 0; i < 8; ++i) { um0 = max(um0, uu[i] & 0x7fffu); um1 = max(um1, (uu[i] >> 16) & 0x7fffu); }
    unsigned um = um0 | (um1 << 16);
#pragma unroll
    for (int o = 32; o >= 1; o >>= 1) { unsigned x = (unsigned)__shfl_xor((int)um, o); um = max(um & 0xffffu, x & 0xffffu) | (max(um >> 16, x >> 16) << 16); }
    unsigned* RED = (unsigned*)(lds + A_RED);
    if (lane == 0) RED[wid] = um;
    __syncthreads();
    { unsigned a = RED[0], b = RED[1], c = RED[2], e = RED[3];
      um0 = max(max(a & 0xffffu, b & 0xffffu), max(c & 0xffffu, e & 0xffffu)); um1 = max(max(a >> 16, b >> 16), max(c >> 16, e >> 16)); }
    const int sh0 = min(141 - (int)(um0 >> 7), 126), sh1 = min(141 - (int)(um1 >> 7), 126);
    {
        u32x4 w0, w1;
#pragma unroll
        for (int j = 0; j < 4; ++j) { float v[2][2];
#pragma unroll
            for (int e = 0; e < 2; ++e) { const int l = tid * 8 + 2 * j + e;
                unsigned fb = __float_as_uint((float)l); fb = (fb + 0x7fffu + ((fb >> 16) & 1u)) & 0xffff0000u; const int p = (int)__uint_as_float(fb);
                const int col = (p >> 3) & 15, a = p >> 7, c = p & 7;
                const int R = p < 256 ? c + 8 * a : (p < 512 ? 16 + (c >> 1) + 4 * (a - 2) : (p < 1024 ? 24 + (c >> 2) + 2 * (a - 4) : 32 + (a - 8)));
                const int idx = R * 16 + col;
                v[0][e] = KP[idx] + KP[768 + idx]; v[1][e] = KP[1536 + idx] + KP[2304 + idx]; }
            w0[j] = pk_f16(v[0][0], v[0][1]); w1[j] = pk_f16(v[1][0], v[1][1]); }
        *(u32x4*)(lds + A_KD + 512 + tid * 16) = w0; *(u32x4*)(lds + A_KD + 4608 + 512 + tid * 16) = w1;
        if (tid < 64) { const u32x4 z = {0, 0, 0, 0}; *(u32x4*)(lds + A_KD + (tid >> 5) * 4608 + (tid & 31) * 16) = z; }
    }
    __syncthreads();
#pragma unroll
    for (int i = 0; i < 8; ++i) { const int row = tid + 256 * i;
        const unsigned w = pk_f16(ldexpf(bf2f(uu[i] & 0xffffu), sh0), ldexpf(bf2f(uu[i] >> 16), sh1));
        *(u16*)(lds + A_TC + 2 * (2047 - row)) = (u16)(w & 0xffffu); *(u16*)(lds + A_TC + 8 * TC_STRIDE + 2 * (2047 - row)) = (u16)(w >> 16); }
    if (tid < 8) { const u32x4 z = {0, 0, 0, 0}; *(u32x4*)(lds + A_TC + (tid >> 2) * 8 * TC_STRIDE + 4096 + (tid & 3) * 16) = z; }
    __syncthreads();
    for (int task = tid; task < 2 * 258; task += 256) { const int c2 = task >= 258, k = task - 258 * c2;
        char* base = lds + A_TC + c2 * 8 * TC_STRIDE;
        const u32x4 x = *(const u32x4*)(base + 16 * k), y = *(const u32x4*)(base + 16 * k + 16);
        unsigned w[8] = {x[0], x[1], x[2], x[3], y[0], y[1], y[2], y[3]}, o[7];
#pragma unroll
        for (int q = 0; q < 7; ++q) o[q] = __builtin_amdgcn_alignbit(w[q + 1], w[q], 16);
#pragma unroll
        for (int c = 1; c < 8; ++c) { u32x4 v;
#pragma unroll
            for (int q = 0; q < 4; ++q) v[q] = (c & 1) ? o[(c >> 1) + q] : w[(c >> 1) + q];
            *(u32x4*)(base + c * TC_STRIDE + 16 * k) = v; } }
    __syncthreads();
    f32x4 acc[8];
#pragma unroll
    for (int i = 0; i < 8; ++i) acc[i] = (f32x4){0, 0, 0, 0};
    const int s_ = quad >> 1, q1 = quad & 1, cc = (7 - r16) & 7;
    const char* abase = lds + A_TC + ch * 8 * TC_STRIDE + cc * TC_STRIDE + 2 * (31 - cc - r16 - 16 * s_ + 8 * q1);
    const char* bbase = lds + A_KD + ch * 4608 + 2 * (256 + 16 * (r16 - s_) + 8 * q1);
#pragma unroll
    for (int e4 = 0; e4 < 4; ++e4) {
        const int eps = 2 * (4 * hw + e4);
        f16x8 af[8], bf[8];
#pragma unroll
        for (int g = 0; g < 8; ++g) { af[g] = *(const f16x8*)(abase + 2 * (256 * (7 - g) + 16 * (14 - eps))); bf[g] = *(const f16x8*)(bbase + 2 * (256 * g - 16 * eps)); }
#pragma unroll
        for (int cb = 0; cb < 8; ++cb)
#pragma unroll
            for (int g = 0; g <= cb; ++g) acc[cb] = __builtin_amdgcn_mfma_f32_16x16x32_f16(af[g], bf[cb - g], acc[cb], 0, 0, 0);
    }
    __syncthreads();
    float* YP = (float*)(lds + A_TC);
#pragma unroll
    for (int cb = 0; cb < 8; ++cb) *(f32x4*)(YP + (ch * 2 + hw) * 2048 + 256 * cb + 16 * r16 + 4 * quad) = acc[cb];
    if (lane == 0 && hw == 0) { ((float*)RED)[8 + 2 * ch] = 2.f * dt; ((int*)RED)[9 + 2 * ch] = eC; }
    __syncthreads();
    const float sc0 = ((float*)RED)[8], sc1 = ((float*)RED)[10]; const int ex0 = ((int*)RED)[9] - sh0, ex1 = ((int*)RED)[11] - sh1;
    const float dk0 = bf2f(DSK[d0]), dk1 = bf2f(DSK[d0 + 1]);
#pragma unroll
    for (int i = 0; i < 8; ++i) { const int row = tid + 256 * i;
        const float y0 = ldexpf((YP[row] + YP[2048 + row]) * sc0, ex0), y1 = ldexpf((YP[4096 + row] + YP[6144 + row]) * sc1, ex1);
        const float o0 = rbf(y0) + rbf(bf2f(uu[i] & 0xffffu) * dk0), o1 = rbf(y1) + rbf(bf2f(uu[i] >> 16) * dk1);
        *(unsigned*)(OUT + (size_t)row * 1024 + d0) = pk_bf16(o0, o1); }
}

#define MFMA32(a, b, c) __builtin_amdgcn_mfma_f32_32x32x16_bf16(a, b, c, 0, 0, 0)
DI u32x4 pack8(const f32x16& a, int o, float s) { u32x4 w; w[0] = pk_bf16(a[o] * s, a[o + 1] * s); w[1] = pk_bf16(a[o + 2] * s, a[o + 3] * s); w[2] = pk_bf16(a[o + 4] * s, a[o + 5] * s); w[3] = pk_bf16(a[o + 6] * s, a[o + 7] * s); return w; }
__global__ __launch_bounds__(256) void proj_simple(const u16* __restrict__ X, const u16* __restrict__ CKV, const u16* __restrict__ WQ, const u16* __restrict__ WK, const u16* __restrict__ WV, char* __restrict__ ws) {
    const int lane = threadIdx.x & 63, wid = threadIdx.x >> 6, r = lane & 31, hh = lane >> 5;
    const int gw = blockIdx.x * 4 + wid, rb = gw & 63, h = (gw >> 6) & 31, mat = blockIdx.x >> 9;
    const u16* A = (mat == 0 ? X : CKV) + (size_t)(rb * 32 + r) * DM; const u16* W = (mat == 0 ? WQ : mat == 1 ? WK : WV) + h * HD + r;
    f32x16 acc[4] = {};
    for (int ks = 0; ks < 32; ++ks) {
        const bf16x8 a = *(const bf16x8*)(A + ks * 16 + 8 * hh);
#pragma unroll
        for (int db = 0; db < 4; ++db) { bf16x8 w;
#pragma unroll
            for (int j = 0; j < 8; ++j) w[j] = (short)W[(size_t)(ks * 16 + 8 * hh + j) * 4096 + db * 32];
            if (mat == 2) acc[db] = MFMA32(a, w, acc[db]); else acc[db] = MFMA32(w, a, acc[db]); }
    }
    const float sc = mat == 0 ? 0.12751743082f : 1.f;
    if (mat < 2) { char* o = ws + (mat == 0 ? WS_Q : WS_K) + ((size_t)h * 64 + rb) * 8192 + lane * 16;
#pragma unroll
        for (int db = 0; db < 4; ++db) { *(u32x4*)(o + (2 * db) * 1024) = pack8(acc[db], 0, sc); *(u32x4*)(o + (2 * db + 1) * 1024) = pack8(acc[db], 8, sc); } }
    else { char* o = ws + WS_V + ((size_t)h * 32 + (rb >> 1)) * 16384 + lane * 16;
#pragma unroll
        for (int db = 0; db < 4; ++db) { *(u32x4*)(o + ((2 * (rb & 1)) * 4 + db) * 1024) = pack8(acc[db], 0, 1.f); *(u32x4*)(o + ((2 * (rb & 1) + 1) * 4 + db) * 1024) = pack8(acc[db], 8, 1.f); } }
}
__global__ __launch_bounds__(256) void attn_simple(char* __restrict__ ws) {
    const int lane = threadIdx.x & 63, wid = threadIdx.x >> 6, r = lane & 31, hh = lane >> 5;
    const int h = blockIdx.x & 31, qb = (15 - (blockIdx.x >> 5)) * 4 + wid;
    const char* Qp = ws + WS_Q + ((size_t)h * 64 + qb) * 8192 + lane * 16;
    bf16x8 q[8];
#pragma unroll
    for (int t = 0; t < 8; ++t) q[t] = *(const bf16x8*)(Qp + t * 1024);
    f32x16 o[4] = {}; float m = -1e30f, l = 0.f;
    const int nt = (qb * 32 + 31) / 64 + 1, qrow = qb * 32 + r;
    for (int t = 0; t < nt; ++t) {
        const char* Kp = ws + WS_K + ((size_t)h * 64 + 2 * t) * 8192 + lane * 16; const char* Vp = ws + WS_V + ((size_t)h * 32 + t) * 16384 + lane * 16;
        f32x16 s[2] = {};
#pragma unroll
        for (int kb = 0; kb < 2; ++kb)
#pragma unroll
            for (int d = 0; d < 8; ++d) s[kb] = MFMA32(*(const bf16x8*)(Kp + kb * 8192 + d * 1024), q[d], s[kb]);
        float mx = -1e30f;
#pragma unroll
        for (int kb = 0; kb < 2; ++kb)
#pragma unroll
            for (int e = 0; e < 16; ++e) { const int key = t * 64 + kb * 32 + (e & 3) + 8 * (e >> 2) + 4 * hh; if (key > qrow) s[kb][e] = -INFINITY; mx = fmaxf(mx, s[kb][e]); }
        mx = fmaxf(mx, __shfl_xor(mx, 32));
        const float mn = fmaxf(m, mx), al = __builtin_amdgcn_exp2f(m - mn); m = mn; float ps = 0.f;
#pragma unroll
        for (int kb = 0; kb < 2; ++kb)
#pragma unroll
            for (int e = 0; e < 16; ++e) { s[kb][e] = __builtin_amdgcn_exp2f(s[kb][e] - mn); ps += s[kb][e]; }
        l = l * al + ps;
#pragma unroll
        for (int db = 0; db < 4; ++db) o[db] *= al;
#pragma unroll
        for (int ks = 0; ks < 4; ++ks) { const bf16x8 p = __builtin_bit_cast(bf16x8, pack8(s[ks >> 1], 8 * (ks & 1), 1.f));
#pragma unroll
            for (int db = 0; db < 4; ++db) o[db] = MFMA32(*(const bf16x8*)(Vp + (ks * 4 + db) * 1024), p, o[db]); }
    }
    l += __shfl_xor(l, 32); const float il = 1.f / l;
    char* Cp = ws + WS_C + ((size_t)qb * 256 + h * 8) * 1024 + lane * 16;
#pragma unroll
    for (int db = 0; db < 4; ++db) { *(u32x4*)(Cp + (2 * db) * 1024) = pack8(o[db], 0, il); *(u32x4*)(Cp + (2 * db + 1) * 1024) = pack8(o[db], 8, il); }
}
__global__ __launch_bounds__(256) void out_simple(const char* __restrict__ ws, const u16* __restrict__ WO, u16* __restrict__ OUT) {
    const int lane = threadIdx.x & 63, wid = threadIdx.x >> 6, r = lane & 31, hh = lane >> 5;
    const int gw = blockIdx.x * 4 + wid, mb = gw & 15, qb = gw >> 4;
    const char* Cp = ws + WS_C + (size_t)qb * 256 * 1024 + lane * 16; const u16* W = WO + mb * 32 + r;
    f32x16 acc = {};
    for (int t = 0; t < 256; ++t) { bf16x8 w;
#pragma unroll
        for (int j = 0; j < 8; ++j) w[j] = (short)W[(size_t)(16 * t + 8 * (j >> 2) + 4 * hh + (j & 3)) * 512];
        acc = MFMA32(*(const bf16x8*)(Cp + t * 1024), w, acc); }
#pragma unroll
    for (int e = 0; e < 16; ++e) OUT[(size_t)(qb * 32 + (e & 3) + 8 * (e >> 2) + 4 * hh) * 1024 + 512 + mb * 32 + r] = (u16)(pk_bf16(acc[e], 0.f) & 0xffffu);
}

extern "C" void kernel_launch(void* const* d_in, const int* in_sizes, int n_in, void* d_out, int out_size, void* d_ws, size_t ws_size, hipStream_t stream) {
    const u16* const* in = (const u16* const*)d_in; u16* out = (u16*)d_out; char* ws = (char*)d_ws;
    hipFuncSetAttribute((const void*)ssm_kernel, hipFuncAttributeMaxDynamicSharedMemorySize, A_LDS);
    ssm_kernel<<<256, 256, A_LDS, stream>>>(in[0], in[1], in[2], in[3], in[4], in[5], in[6], out);
    proj_simple<<<3 * 32 * 64 / 4, 256, 0, stream>>>(in[7], in[8], in[9], in[10], in[11], ws);
    attn_simple<<<512, 256, 0, stream>>>(ws);
    out_simple<<<64 * 16 / 4, 256, 0, stream>>>(ws, in[12], out);
}
```

```cpp
#include <hip/hip_runtime.h>
#include <hip/hip_bf16.h>
#include <stdint.h>

typedef unsigned short u16;
typedef short bf16x8 __attribute__((ext_vector_type(8)));
typedef _Float16 f16x8 __attribute__((ext_vector_type(8)));
typedef _Float16 f16x2 __attribute__((ext_vector_type(2)));
typedef __bf16 bf16x2_t __attribute__((ext_vector_type(2)));
typedef float f32x2 __attribute__((ext_vector_type(2)));
typedef float f32x4 __attribute__((ext_vector_type(4)));
typedef float f32x16 __attribute__((ext_vector_type(16)));
typedef unsigned u32x4 __attribute__((ext_vector_type(4)));
typedef unsigned u32x2 __attribute__((ext_vector_type(2)));
#define DI __device__ __forceinline__

DI float bf2f(unsigned b) { return __uint_as_float(b << 16); }
DI unsigned pk_bf16(float lo, float hi) { f32x2 v = {lo, hi}; bf16x2_t b = __builtin_convertvector(v, bf16x2_t); return __builtin_bit_cast(unsigned, b); }
DI unsigned pk_f16(float lo, float hi) { f32x2 v = {lo, hi}; f16x2 b = __builtin_convertvector(v, f16x2); return __builtin_bit_cast(unsigned, b); }
DI float rbf(float x) { return bf2f(pk_bf16(x, 0.f) & 0xffffu); }

constexpr int SEQ = 2048, NH = 32, HD = 128, DM = 512;
constexpr size_t WS_K = 0, WS_V = 16u << 20, WS_Q = 32u << 20, WS_C = 48u << 20, WS_F = 96u << 20;

DI int lane_id() { int l; asm volatile("v_mbcnt_lo_u32_b32 %0, -1, 0\n\tv_mbcnt_hi_u32_b32 %0, -1, %0" : "=v"(l)); return l & 63; }

__device__ __forceinline__ void attn_slow(char* ws, const u16* X, const u16* WQ, int h, int blk, int wid) {
    const int lane = lane_id(), r = lane & 15, qd = lane >> 4;
    for (int qi = 0; qi < 4; ++qi) {
        const int qb = blk * 8 + 4 * (qi >> 1) + (wid ^ 2), g = qi & 1;
        bf16x8 q[4];
#pragma unroll
        for (int t = 0; t < 4; ++t) {
            f32x4 a[2] = {};
            for (int ks = 0; ks < 16; ++ks) {
                const bf16x8 xb = *(const bf16x8*)(X + (size_t)(qb * 16 + r) * DM + ks * 32 + 8 * qd);
#pragma unroll
                for (int i = 0; i < 2; ++i) { bf16x8 w;
#pragma unroll
                    for (int j = 0; j < 8; ++j) w[j] = (short)WQ[(size_t)(ks * 32 + 8 * qd + j) * 4096 + h * 128 + t * 32 + i * 16 + r];
                    a[i] = __builtin_amdgcn_mfma_f32_16x16x32_bf16(w, xb, a[i], 0, 0, 0); }
            }
            u32x4 w; w[0] = pk_bf16(a[0][0], a[0][1]); w[1] = pk_bf16(a[0][2], a[0][3]); w[2] = pk_bf16(a[1][0], a[1][1]); w[3] = pk_bf16(a[1][2], a[1][3]);
            q[t] = __builtin_bit_cast(bf16x8, w);
        }
        f32x4 o[4] = {}; float m = -3.0e38f, l = 0.f;
        const int nt = (qb >> 1) + 1, qrow = qb * 16 + r;
        for (int t = 0; t < nt; ++t) {
            const int tp = (t & ~3) | (t & 1) << 1 | (t >> 1 & 1);
            const char* Kp = ws + WS_K + ((size_t)h * 128 + 2 * tp) * 4096 + lane * 16; const char* Vp = ws + WS_V + ((size_t)h * 64 + tp) * 8192 + g * 4096 + lane * 16;
            f32x4 s[2] = {};
#pragma unroll
            for (int kb = 0; kb < 2; ++kb)
#pragma unroll
                for (int d = 0; d < 4; ++d) s[kb] = __builtin_amdgcn_mfma_f32_16x16x32_bf16(*(const bf16x8*)(Kp + kb * 4096 + d * 1024), q[d], s[kb], 0, 0, 0);
            float mx = m;
#pragma unroll
            for (int kb = 0; kb < 2; ++kb)
#pragma unroll
                for (int e = 0; e < 4; ++e) { const int key = t * 32 + kb * 16 + 4 * qd + e; if (key > qrow) s[kb][e] = -INFINITY; mx = fmaxf(mx, s[kb][e]); }
            mx = fmaxf(mx, __shfl_xor(mx, 32)); mx = fmaxf(mx, __shfl_xor(mx, 16));
            const float al = __builtin_amdgcn_exp2f(m - mx); m = mx; float ps = 0.f;
#pragma unroll
            for (int kb = 0; kb < 2; ++kb)
#pragma unroll
                for (int e = 0; e < 4; ++e) { s[kb][e] = __builtin_amdgcn_exp2f(s[kb][e] - mx); ps += s[kb][e]; }
            l = l * al + ps;
            u32x4 pw; pw[0] = pk_bf16(s[0][0], s[0][1]); pw[1] = pk_bf16(s[0][2], s[0][3]); pw[2] = pk_bf16(s[1][0], s[1][1]); pw[3] = pk_bf16(s[1][2], s[1][3]);
            const bf16x8 p = __builtin_bit_cast(bf16x8, pw);
#pragma unroll
            for (int db = 0; db < 4; ++db) { o[db] *= al; o[db] = __builtin_amdgcn_mfma_f32_16x16x32_bf16(*(const bf16x8*)(Vp + db * 1024), p, o[db], 0, 0, 0); }
        }
        l += __shfl_xor(l, 32); l += __shfl_xor(l, 16); const float il = 1.f / l;
        char* Cp = ws + WS_C + ((size_t)qb * 128 + h * 4 + 2 * g) * 1024 + lane * 16;
#pragma unroll
        for (int i = 0; i < 2; ++i) { u32x4 w; w[0] = pk_bf16(o[2*i][0] * il, o[2*i][1] * il); w[1] = pk_bf16(o[2*i][2] * il, o[2*i][3] * il); w[2] = pk_bf16(o[2*i+1][0] * il, o[2*i+1][1] * il); w[3] = pk_bf16(o[2*i+1][2] * il, o[2*i+1][3] * il);
            asm volatile("global_store_dwordx4 %0, %1, off sc1\n\ts_nop 1" :: "v"(Cp + i * 1024), "v"(w) : "memory"); }
    }
}

typedef const u16* cp16;
typedef const __attribute__((address_space(1))) u16* gp16;
struct KArgs { gp16 in[13]; gp16 out; gp16 ws; };
typedef const __attribute__((address_space(4))) KArgs* kap;
__global__ __launch_bounds__(256, 1) __attribute__((aligned(4096))) void fused_kernel(cp16 a0, cp16 a1, cp16 a2, cp16 a3, cp16 a4, cp16 a5, cp16 a6, cp16 a7, cp16 a8, cp16 a9, cp16 a10, cp16 a11, cp16 a12, u16* a13, char* a14) {
    unsigned wid = __builtin_amdgcn_readfirstlane(threadIdx.x >> 6), bid = blockIdx.x, fail;
    kap ka = (kap)__builtin_amdgcn_kernarg_segment_ptr();

    asm volatile(
    "s_load_dwordx8 s[24:31], %1, 0x40\n\t"
    "s_load_dwordx8 s[8:15], %1, 0x0\n\t"
    "s_load_dwordx4 s[16:19], %1, 0x20\n\t"
    "s_load_dwordx2 s[22:23], %1, 0x38\n\t"
    "s_load_dwordx2 s[20:21], %1, 0x70\n\t"
    "s_load_dwordx2 s[82:83], %1, 0x60\n\t"
    "s_load_dwordx2 s[50:51], %1, 0x68\n\t"
    "s_load_dwordx2 s[96:97], %1, 0x30\n\t"
    "v_mbcnt_lo_u32_b32 v246, -1, 0\n\t"
    "s_mov_b32 s58, %3\n\t"
    "v_mbcnt_hi_u32_b32 v246, -1, v246\n\t"
    "s_getpc_b64 s[88:89]\n\t"
    "Lpf_%=:\n\t"
    "s_mov_b32 s90, Lkend_fused-Lpf_%=\n\t"
    "s_mov_b32 s91, 0x00020000\n\t"
    "s_lshr_b32 s56, %2, 3\n\t"
    "s_lshl_b32 s56, s56, 2\n\t"
    "s_add_u32 s56, s56, s58\n\t"
    "s_lshl_b32 s56, s56, 10\n\t"
    "v_lshlrev_b32 v249, 4, v246\n\t"
    "buffer_load_dwordx4 a[248:251], v249, s[88:91], s56 offen\n\t"
    "s_bfe_u32 s60, %2, 0x30003\n\t"
    "s_and_b32 s59, %2, 7\n\t"
    "s_lshl_b32 s59, s59, 2\n\t"
    "s_lshr_b32 s56, %2, 6\n\t"
    "s_add_u32 s59, s59, s56\n\t"
    "s_lshr_b32 s61, s58, 1\n\t"
    "s_and_b32 s98, s58, 1\n\t"
    "v_lshrrev_b32 v247, 3, v246\n\t"
    "v_and_b32 v248, 7, v246\n\t"
    "s_mov_b32 s48, 0\n\t"
    "s_add_u32 s48, s48, s60\n\t"
    "s_add_u32 s48, s48, %2\n\t"
    "s_and_b32 s48, s48, 7\n\t"
    "s_lshl_b32 s48, s48, 7\n\t"
    "v_lshrrev_b32 v249, 4, v246\n\t"
    "v_xor_b32 v249, v249, v248\n\t"
    "v_lshlrev_b32 v249, 4, v249\n\t"
    "v_lshl_add_u32 v224, v247, 10, v249\n\t"
    "v_and_b32 v249, 2, v247\n\t"
    "v_xor_b32 v249, v249, v248\n\t"
    "v_lshlrev_b32 v249, 4, v249\n\t"
    "v_lshl_add_u32 v232, v247, 13, v249\n\t"
    "s_mov_b32 s38, 0x10000\n\t"
    "s_mov_b32 s39, 0x00020000\n\t"
    "s_mov_b32 s43, 0x00020000\n\t"
    "s_mov_b32 s47, 0x00020000\n\t"
    "s_mov_b32 s46, 0x200000\n\t"
    "s_lshl_b32 s75, s58, 13\n\t"
    "s_add_u32 s75, s75, 0x10000\n\t"
    "s_lshl_b32 s52, s58, 12\n\t"
    "s_lshl_b32 s74, s59, 8\n\t"
    "s_lshl_b32 s56, s61, 7\n\t"
    "s_add_u32 s74, s74, s56\n\t"
    "s_lshl_b32 s56, s98, 18\n\t"
    "s_add_u32 s74, s74, s56\n\t"
    "s_sub_u32 s42, 0x400000, s74\n\t"
    "s_lshl_b32 s49, s48, 12\n\t"
    "s_lshl_b32 s99, s60, 5\n\t"
    "s_add_u32 s99, s99, s59\n\t"
    "s_lshl_b32 s54, s99, 2\n\t"
    "s_lshl_b32 s56, s99, 1\n\t"
    "s_add_u32 s56, s56, s61\n\t"
    "s_lshl_b32 s56, s56, 8\n\t"
    "v_lshl_add_u32 v250, v246, 2, s56\n\t"
    "s_sub_u32 s70, 15, s60\n\t"
    "s_cmp_eq_u32 s98, 1\n\t"
    "s_cselect_b32 s56, s60, s70\n\t"
    "s_lshl_b32 s56, s56, 17\n\t"
    "s_lshl_b32 s57, s61, 16\n\t"
    "s_add_u32 s62, s56, s57\n\t"
    "s_add_u32 m0, s75, 0x0\n\t"
    "s_waitcnt lgkmcnt(0)\n\t"
    "s_add_u32 s36, s24, s62\n\t"
    "s_addc_u32 s37, s25, 0\n\t"
    "buffer_load_dwordx4 v224, s[36:39], s48 offen offset:0 lds\n\t"
    "s_add_u32 s40, s28, s74\n\t"
    "s_addc_u32 s41, s29, 0\n\t"
    "s_add_u32 m0, s52, 0x8000\n\t"
    "s_nop 0\n\t"
    "buffer_load_dwordx4 v232, s[40:43], s49 offen offset:0 lds\n\t"
    "global_load_dword v128, v250, s[10:11]\n\t"
    "global_load_dword v129, v250, s[12:13]\n\t"
    "global_load_dword v130, v250, s[14:15]\n\t"
    "global_load_dword v131, v250, s[16:17]\n\t"
    "s_load_dword s34, s[18:19], s54\n\t"
    "s_load_dword s71, s[96:97], s54\n\t"
    "v_xor_b32 v250, 64, v232\n\t"
    "v_add_u32 v233, 0xfc00, v250\n\t"
    "buffer_load_dwordx4 v233, s[40:43], s49 offen offset:1024 lds\n\t"
    "v_add_u32 v234, 0x1f800, v232\n\t"
    "buffer_load_dwordx4 v234, s[40:43], s49 offen offset:2048 lds\n\t"
    "v_add_u32 v235, 0x2f400, v250\n\t"
    "buffer_load_dwordx4 v235, s[40:43], s49 offen offset:3072 lds\n\t"
    "v_xor_b32 v250, 64, v224\n\t"
    "s_add_u32 m0, s75, 0x0\n\t"
    "v_add_u32 v225, 0x1c00, v250\n\t"
    "buffer_load_dwordx4 v225, s[36:39], s48 offen offset:1024 lds\n\t"
    "v_add_u32 v226, 0x3800, v224\n\t"
    "buffer_load_dwordx4 v226, s[36:39], s48 offen offset:2048 lds\n\t"
    "v_add_u32 v227, 0x5400, v250\n\t"
    "buffer_load_dwordx4 v227, s[36:39], s48 offen offset:3072 lds\n\t"
    "v_add_u32 v228, 0x8000, v224\n\t"
    "s_add_u32 m0, s75, 0x1000\n\t"
    "s_nop 0\n\t"
    "buffer_load_dwordx4 v228, s[36:39], s48 offen offset:0 lds\n\t"
    "v_add_u32 v229, 0x9c00, v250\n\t"
    "buffer_load_dwordx4 v229, s[36:39], s48 offen offset:1024 lds\n\t"
    "v_add_u32 v230, 0xb800, v224\n\t"
    "buffer_load_dwordx4 v230, s[36:39], s48 offen offset:2048 lds\n\t"
    "v_add_u32 v231, 0xd400, v250\n\t"
    "buffer_load_dwordx4 v231, s[36:39], s48 offen offset:3072 lds\n\t"
    "v_lshlrev_b32 v218, 4, v248\n\t"
    "v_lshl_add_u32 v218, v247, 10, v218\n\t"
    "s_add_u32 s84, s20, 0x6000000\n\t"
    "s_addc_u32 s85, s21, 0\n\t"
    "s_lshl_b32 s56, %2, 7\n\t"
    "s_xor_b32 s56, s56, 0x1400\n\t"
    "v_mov_b32 v245, s56\n\t"
    "v_mov_b32 v252, 1\n\t"
    "s_mov_b64 s[86:87], 0\n\t"
    "s_mov_b64 s[94:95], 0\n\t"
    "s_cmp_eq_u32 %2, 40\n\t"
    "s_cbranch_scc1 L_lead_%=\n\t"
    "s_cmp_lg_u32 s58, 0\n\t"
    "s_cbranch_scc1 L_sy_%=\n\t"
    "s_mov_b64 s[86:87], 1\n\t"
    "s_mov_b64 exec, 1\n\t"
    "global_atomic_add v251, v245, v252, s[84:85] sc0 sc1\n\t"
    "s_mov_b64 exec, -1\n\t"
    "L_sy_%=:\n\t"
    "s_add_u32 s48, s48, 128\n\t"
    "s_and_b32 s48, s48, 1023\n\t"
    "s_lshl_b32 s49, s48, 12\n\t"
    "s_add_u32 m0, s52, 0x0\n\t"
    "s_nop 0\n\t"
    "buffer_load_dwordx4 v232, s[40:43], s49 offen offset:0 lds\n\t"
    "s_mov_b64 exec, 0\n\t"
    "s_cmp_eq_u32 s58, 0\n\t"
    "s_cbranch_scc1 Lis1_%=\n\t"
    "s_cmp_eq_u32 s58, 1\n\t"
    "s_cbranch_scc1 Lis2_%=\n\t"
    "s_cmp_eq_u32 s58, 2\n\t"
    "s_cbranch_scc1 Lis3_%=\n\t"
    "s_branch Lis4_%=\n\t"
    "Lis99_%=:\n\t"
    "s_mov_b64 exec, -1\n\t"
    "v_lshlrev_b32 v132, 5, v246\n\t"
    "v_and_b32 v138, 4, v246\n\t"
    "v_lshl_add_u32 v132, v138, 2, v132\n\t"
    "s_lshl_b32 s56, s58, 12\n\t"
    "s_add_u32 s56, s56, 0xc000\n\t"
    "v_add_u32 v132, s56, v132\n\t"
    "v_xor_b32 v133, 16, v132\n\t"
    "v_lshlrev_b32 v134, 3, v246\n\t"
    "v_and_b32 v138, 16, v246\n\t"
    "v_xor_b32 v134, v134, v138\n\t"
    "v_add_u32 v134, s56, v134\n\t"
    "buffer_load_dwordx4 v233, s[40:43], s49 offen offset:1024 lds\n\t"
    "s_cmp_eq_u32 s98, 0\n\t"
    "s_cselect_b32 s53, 0x01000c0c, s39\n\t"
    "s_cmp_eq_u32 s98, 1\n\t"
    "s_cselect_b32 s53, 0x03020c0c, s53\n\t"
    "v_and_b32 v247, 15, v246\n\t"
    "v_lshrrev_b32 v248, 4, v246\n\t"
    "v_lshrrev_b32 v249, 1, v247\n\t"
    "v_xor_b32 v249, v249, v248\n\t"
    "v_lshlrev_b32 v249, 4, v249\n\t"
    "v_lshl_add_u32 v236, v247, 7, v249\n\t"
    "s_lshl_b32 s56, s98, 14\n\t"
    "s_add_u32 s56, s56, 0x10000\n\t"
    "buffer_load_dwordx4 v234, s[40:43], s49 offen offset:2048 lds\n\t"
    "v_add_u32 v236, s56, v236\n\t"
    "v_xor_b32 v237, 64, v236\n\t"
    "v_add_u32 v238, 0x10000, v236\n\t"
    "v_add_u32 v239, 0x10000, v237\n\t"
    "v_lshrrev_b32 v249, 2, v247\n\t"
    "v_lshl_add_u32 v250, v248, 3, v249\n\t"
    "v_lshlrev_b32 v240, 7, v250\n\t"
    "s_lshl_b32 s56, s58, 5\n\t"
    "s_add_u32 s56, s56, 0xe000\n\t"
    "v_add_u32 v216, s56, v240\n\t"
    "buffer_load_dwordx4 v235, s[40:43], s49 offen offset:3072 lds\n\t"
    "v_and_b32 v249, 2, v249\n\t"
    "v_and_b32 v250, 1, v248\n\t"
    "v_lshl_add_u32 v249, v250, 2, v249\n\t"
    "v_lshl_add_u32 v240, v249, 4, v240\n\t"
    "v_and_b32 v249, 3, v246\n\t"
    "v_lshl_add_u32 v240, v249, 3, v240\n\t"
    "v_lshl_add_u32 v216, v249, 3, v216\n\t"
    "v_and_b32 v249, 2, v246\n\t"
    "v_mul_u32_u24 v249, 2040, v249\n\t"
    "v_add_u32 v216, v216, v249\n\t"
    "s_add_u32 m0, s75, 0x8000\n\t"
    "s_nop 0\n\t"
    "buffer_load_dwordx4 v224, s[36:39], s48 offen offset:0 lds\n\t"
    "s_lshl_b32 s56, s61, 13\n\t"
    "v_add_u32 v240, s56, v240\n\t"
    "v_xor_b32 v241, 32, v240\n\t"
    "v_xor_b32 v242, 64, v240\n\t"
    "v_xor_b32 v243, 96, v240\n\t"
    "v_lshlrev_b32 v244, 4, v246\n\t"
    "v_and_b32 v249, 7, v247\n\t"
    "v_lshlrev_b32 v217, 12, v249\n\t"
    "v_and_b32 v249, 8, v247\n\t"
    "v_lshl_add_u32 v217, v249, 3, v217\n\t"
    "v_lshl_add_u32 v217, v248, 4, v217\n\t"
    "buffer_load_dwordx4 v225, s[36:39], s48 offen offset:1024 lds\n\t"
    "s_lshl_b32 s56, s60, 18\n\t"
    "s_lshl_b32 s57, s58, 16\n\t"
    "s_add_u32 s56, s56, s57\n\t"
    "s_lshl_b32 s57, s59, 7\n\t"
    "s_add_u32 s56, s56, s57\n\t"
    "s_add_u32 s56, s56, 0x400000\n\t"
    "v_add_u32 v217, s56, v217\n\t"
    "v_add_u32 v221, 0x8000, v217\n\t"
    "s_add_u32 s64, s26, s74\n\t"
    "s_addc_u32 s65, s27, 0\n\t"
    "s_add_u32 s72, s30, s74\n\t"
    "s_addc_u32 s73, s31, 0\n\t"
    "buffer_load_dwordx4 v226, s[36:39], s48 offen offset:2048 lds\n\t"
    "s_mov_b32 s97, 0\n\t"
    "s_add_u32 s97, s97, s60\n\t"
    "s_add_u32 s97, s97, %2\n\t"
    "s_and_b32 s97, s97, 7\n\t"
    "s_lshl_b32 s97, s97, 7\n\t"
    "s_lshl_b32 s57, s70, 15\n\t"
    "s_lshl_b32 s56, s59, 19\n\t"
    "s_add_u32 s57, s56, s57\n\t"
    "s_lshl_b32 s56, s98, 13\n\t"
    "s_add_u32 s57, s56, s57\n\t"
    "buffer_load_dwordx4 v227, s[36:39], s48 offen offset:3072 lds\n\t"
    "s_add_u32 s66, s20, s57\n\t"
    "s_addc_u32 s67, s21, 0\n\t"
    "s_lshl_b32 s57, s61, 12\n\t"
    "s_add_u32 s57, s57, 0x1000000\n\t"
    "s_add_u32 s68, s66, s57\n\t"
    "s_addc_u32 s69, s67, 0\n\t"
    "s_lshl_b32 s57, s61, 11\n\t"
    "s_add_u32 s57, s57, 0x1000\n\t"
    "s_add_u32 s66, s66, s57\n\t"
    "s_addc_u32 s67, s67, 0\n\t"
    "s_lshl_b32 s70, s60, 16\n\t"
    "s_sub_u32 s70, 0x7c000, s70\n\t"
    "s_add_u32 s62, s22, s62\n\t"
    "s_addc_u32 s63, s23, 0\n\t"
    "s_add_u32 m0, s75, 0x9000\n\t"
    "s_nop 0\n\t"
    "buffer_load_dwordx4 v228, s[36:39], s48 offen offset:0 lds\n\t"
    "v_lshlrev_b32 v220, 4, v247\n\t"
    "v_lshl_add_u32 v220, v248, 13, v220\n\t"
    "s_lshl_b32 s56, s60, 19\n\t"
    "s_lshl_b32 s57, s58, 17\n\t"
    "s_add_u32 s56, s56, s57\n\t"
    "s_and_b32 s57, s74, 0x1f00\n\t"
    "s_add_u32 s56, s56, s57\n\t"
    "v_add_u32 v220, s56, v220\n\t"
    "s_lshl_b32 s56, %2, 2\n\t"
    "s_add_u32 s56, s56, s58\n\t"
    "buffer_load_dwordx4 v229, s[36:39], s48 offen offset:1024 lds\n\t"
    "s_lshl_b32 s57, s56, 11\n\t"
    "s_add_u32 s22, s22, s57\n\t"
    "s_addc_u32 s23, s23, 0\n\t"
    "v_mov_b32 v250, 1\n\t"
    "s_lshl_b32 s56, s59, 7\n\t"
    "s_add_u32 s56, s56, 0x8000\n\t"
    "v_mov_b32 v249, s56\n\t"
    "s_mov_b32 s55, 0x3e0293ee\n\t"
    "s_lshl_b32 s56, s60, 7\n\t"
    "s_add_u32 s56, s56, 0xa000\n\t"
    "v_writelane_b32 v249, s56, 1\n\t"
    "buffer_load_dwordx4 v230, s[36:39], s48 offen offset:2048 lds\n\t"
    "buffer_load_dwordx4 v231, s[36:39], s48 offen offset:3072 lds\n\t"
    "s_mov_b64 s[44:45], s[8:9]\n\t"
    "s_lshl_b32 s56, s59, 16\n\t"
    "s_lshl_b32 s57, s58, 14\n\t"
    "s_add_u32 s56, s56, s57\n\t"
    "s_lshl_b32 s57, s60, 7\n\t"
    "s_add_u32 s56, s56, s57\n\t"
    "s_lshl_b32 s57, s58, 11\n\t"
    "s_add_u32 m0, s57, 0xe000\n\t"
    "s_add_u32 s57, s56, 0x1c00\n\t"
    "buffer_load_dwordx4 v218, s[44:47], s56 offen lds\n\t"
    "buffer_load_dwordx4 v218, s[44:47], s57 offen offset:1024 lds\n\t"
    "s_waitcnt lgkmcnt(0)\n\t"
    "s_cmp_lg_u32 s94, -2\n\t"
    "s_cbranch_scc1 L_nz_%=\n\t"
    "s_waitcnt vmcnt(0)\n\t"
    "L_nz_%=:\n\t"
    "s_waitcnt vmcnt(14) lgkmcnt(0)\n\t"
    "s_barrier\n\t"
    "s_cmp_eq_u64 s[94:95], 0\n\t"
    "s_cbranch_scc1 L_nl_%=\n\t"
    "s_mov_b64 exec, s[94:95]\n\t"
    "global_atomic_add v251, v245, v250, s[84:85] sc0 sc1\n\t"
    "s_mov_b64 exec, -1\n\t"
    "L_nl_%=:\n\t"
    "ds_read_b128 a[64:67], v236 offset:0\n\t"
    "ds_read_b128 a[68:71], v237 offset:0\n\t"
    "ds_read_b64_tr_b16 a[0:1], v240 offset:32768\n\t"
    "ds_read_b64_tr_b16 a[2:3], v240 offset:33280\n\t"
    "ds_read_b64_tr_b16 a[4:5], v240 offset:36864\n\t"
    "ds_read_b64_tr_b16 a[6:7], v240 offset:37376\n\t"
    "ds_read_b64_tr_b16 a[8:9], v241 offset:32768\n\t"
    "ds_read_b64_tr_b16 a[10:11], v241 offset:33280\n\t"
    "ds_read_b64_tr_b16 a[12:13], v241 offset:36864\n\t"
    "ds_read_b64_tr_b16 a[14:15], v241 offset:37376\n\t"
    "ds_read_b64_tr_b16 a[16:17], v242 offset:32768\n\t"
    "ds_read_b64_tr_b16 a[18:19], v242 offset:33280\n\t"
    "ds_read_b64_tr_b16 a[20:21], v242 offset:36864\n\t"
    "ds_read_b64_tr_b16 a[22:23], v242 offset:37376\n\t"
    "ds_read_b64_tr_b16 a[24:25], v243 offset:32768\n\t"
    "ds_read_b64_tr_b16 a[26:27], v243 offset:33280\n\t"
    "ds_read_b64_tr_b16 a[28:29], v243 offset:36864\n\t"
    "ds_read_b64_tr_b16 a[30:31], v243 offset:37376\n\t"
    "ds_read_b128 a[72:75], v236 offset:2048\n\t"
    "ds_read_b128 a[76:79], v237 offset:2048\n\t"
    "ds_read_b128 a[80:83], v236 offset:4096\n\t"
    "ds_read_b128 a[84:87], v237 offset:4096\n\t"
    "ds_read_b128 a[88:91], v236 offset:6144\n\t"
    "ds_read_b128 a[92:95], v237 offset:6144\n\t"
    "ds_read_b128 a[96:99], v236 offset:8192\n\t"
    "ds_read_b128 a[100:103], v237 offset:8192\n\t"
    "ds_read_b128 a[104:107], v236 offset:10240\n\t"
    "ds_read_b128 a[108:111], v237 offset:10240\n\t"
    "ds_read_b128 a[112:115], v236 offset:12288\n\t"
    "ds_read_b128 a[116:119], v237 offset:12288\n\t"
    "ds_read_b128 a[120:123], v236 offset:14336\n\t"
    "ds_read_b128 a[124:127], v237 offset:14336\n\t"
    "s_waitcnt lgkmcnt(15)\n\t"
    "v_mfma_f32_16x16x32_bf16 v[0:3], a[0:3], a[64:67], 0\n\t"
    "s_add_u32 s48, s48, 128\n\t"
    "s_lshl_b32 s56, s61, 4\n\t"
    "v_mfma_f32_16x16x32_bf16 v[0:3], a[4:7], a[68:71], v[0:3]\n\t"
    "s_and_b32 s48, s48, 1023\n\t"
    "s_lshr_b32 s34, s34, s56\n\t"
    "v_mfma_f32_16x16x32_bf16 v[4:7], a[12:15], a[68:71], 0\n\t"
    "s_lshl_b32 s49, s48, 12\n\t"
    "s_add_u32 m0, s52, 0x4000\n\t"
    "v_mfma_f32_16x16x32_bf16 v[4:7], a[8:11], a[64:67], v[4:7]\n\t"
    "buffer_load_dwordx4 v232, s[40:43], s49 offen offset:0 lds\n\t"
    "v_mfma_f32_16x16x32_bf16 v[8:11], a[16:19], a[64:67], 0\n\t"
    "buffer_load_dwordx4 v233, s[40:43], s49 offen offset:1024 lds\n\t"
    "v_mfma_f32_16x16x32_bf16 v[8:11], a[20:23], a[68:71], v[8:11]\n\t"
    "s_lshl_b32 s34, s34, 16\n\t"
    "v_mov_b32 v140, s34\n\t"
    "s_waitcnt lgkmcnt(8)\n\t"
    "v_mfma_f32_16x16x32_bf16 v[12:15], a[28:31], a[68:71], 0\n\t"
    "buffer_load_dwordx4 v234, s[40:43], s49 offen offset:2048 lds\n\t"
    "v_mfma_f32_16x16x32_bf16 v[12:15], a[24:27], a[64:67], v[12:15]\n\t"
    "s_lshr_b32 s71, s71, s56\n\t"
    "s_lshl_b32 s71, s71, 16\n\t"
    "v_mfma_f32_16x16x32_bf16 v[28:31], a[24:27], a[72:75], 0\n\t"
    "v_mul_f32 v222, 0x3fb8aa00, v140\n\t"
    "v_mul_f32 v141, 0x36a407db, v140\n\t"
    "v_mfma_f32_16x16x32_bf16 v[28:31], a[28:31], a[76:79], v[28:31]\n\t"
    "buffer_load_dwordx4 v235, s[40:43], s49 offen offset:3072 lds\n\t"
    "v_mfma_f32_16x16x32_bf16 v[24:27], a[20:23], a[76:79], 0\n\t"
    "v_exp_f32 v222, v222\n\t"
    "v_mfma_f32_16x16x32_bf16 v[24:27], a[16:19], a[72:75], v[24:27]\n\t"
    "v_fmac_f32 v222, v222, v141\n\t"
    "v_cvt_pk_bf16_f32 v222, v222, v222\n\t"
    "v_mfma_f32_16x16x32_bf16 v[20:23], a[8:11], a[72:75], 0\n\t"
    "s_add_u32 m0, s75, 0x10000\n\t"
    "v_and_b32 v222, 0xffff0000, v222\n\t"
    "v_mfma_f32_16x16x32_bf16 v[20:23], a[12:15], a[76:79], v[20:23]\n\t"
    "buffer_load_dwordx4 v224, s[36:39], s48 offen offset:0 lds\n\t"
    "v_mfma_f32_16x16x32_bf16 v[16:19], a[4:7], a[76:79], 0\n\t"
    "v_and_b32 v137, 0x7fff7fff, v130\n\t"
    "v_and_b32 v138, 0x7fff7fff, v131\n\t"
    "v_mfma_f32_16x16x32_bf16 v[16:19], a[0:3], a[72:75], v[16:19]\n\t"
    "v_pk_max_u16 v137, v137, v138\n\t"
    "v_max_u32_sdwa v137, v137, v137 dst_sel:DWORD dst_unused:UNUSED_PAD src0_sel:WORD_0 src1_sel:WORD_1\n\t"
    "v_mfma_f32_16x16x32_bf16 v[32:35], a[0:3], a[80:83], 0\n\t"
    "v_perm_b32 v128, v128, v128, s53\n\t"
    "v_mfma_f32_16x16x32_bf16 v[32:35], a[4:7], a[84:87], v[32:35]\n\t"
    "buffer_load_dwordx4 v225, s[36:39], s48 offen offset:1024 lds\n\t"
    "v_mfma_f32_16x16x32_bf16 v[36:39], a[12:15], a[84:87], 0\n\t"
    "v_perm_b32 v129, v129, v129, s53\n\t"
    "v_mfma_f32_16x16x32_bf16 v[36:39], a[8:11], a[80:83], v[36:39]\n\t"
    "buffer_load_dwordx4 v226, s[36:39], s48 offen offset:2048 lds\n\t"
    "v_mfma_f32_16x16x32_bf16 v[40:43], a[16:19], a[80:83], 0\n\t"
    "v_max_u32_dpp v137, v137, v137 row_shr:1 row_mask:0xf bank_mask:0xf\n\t"
    "v_perm_b32 v130, v130, v130, s53\n\t"
    "v_mfma_f32_16x16x32_bf16 v[40:43], a[20:23], a[84:87], v[40:43]\n\t"
    "buffer_load_dwordx4 v227, s[36:39], s48 offen offset:3072 lds\n\t"
    "v_mfma_f32_16x16x32_bf16 v[44:47], a[28:31], a[84:87], 0\n\t"
    "v_perm_b32 v131, v131, v131, s53\n\t"
    "v_max_u32_dpp v137, v137, v137 row_shr:2 row_mask:0xf bank_mask:0xf\n\t"
    "v_mfma_f32_16x16x32_bf16 v[44:47], a[24:27], a[80:83], v[44:47]\n\t"
    "v_mul_f32 v135, 0x3fb8aa00, v128\n\t"
    "v_mul_f32 v141, 0x36a407db, v128\n\t"
    "v_mfma_f32_16x16x32_bf16 v[60:63], a[24:27], a[88:91], 0\n\t"
    "s_add_u32 m0, s75, 0x11000\n\t"
    "v_max_u32_dpp v137, v137, v137 row_shr:4 row_mask:0xf bank_mask:0xf\n\t"
    "v_mfma_f32_16x16x32_bf16 v[60:63], a[28:31], a[92:95], v[60:63]\n\t"
    "buffer_load_dwordx4 v228, s[36:39], s48 offen offset:0 lds\n\t"
    "v_mfma_f32_16x16x32_bf16 v[56:59], a[20:23], a[92:95], 0\n\t"
    "buffer_load_dwordx4 v229, s[36:39], s48 offen offset:1024 lds\n\t"
    "v_mfma_f32_16x16x32_bf16 v[56:59], a[16:19], a[88:91], v[56:59]\n\t"
    "v_exp_f32 v135, v135\n\t"
    "v_mfma_f32_16x16x32_bf16 v[52:55], a[8:11], a[88:91], 0\n\t"
    "v_fmac_f32 v135, v135, v141\n\t"
    "v_max_u32_dpp v137, v137, v137 row_shr:8 row_mask:0xf bank_mask:0xf\n\t"
    "v_mfma_f32_16x16x32_bf16 v[52:55], a[12:15], a[92:95], v[52:55]\n\t"
    "v_cvt_pk_bf16_f32 v135, v135, v135\n\t"
    "v_and_b32 v135, 0xffff0000, v135\n\t"
    "v_mfma_f32_16x16x32_bf16 v[48:51], a[4:7], a[92:95], 0\n\t"
    "v_max_u32_dpp v137, v137, v137 row_bcast:15 row_mask:0xa bank_mask:0xf\n\t"
    "v_mfma_f32_16x16x32_bf16 v[48:51], a[0:3], a[88:91], v[48:51]\n\t"
    "buffer_load_dwordx4 v230, s[36:39], s48 offen offset:2048 lds\n\t"
    "s_waitcnt lgkmcnt(0)\n\t"
    "v_mfma_f32_16x16x32_bf16 v[64:67], a[0:3], a[96:99], 0\n\t"
    "v_mul_f32 v136, v129, v222\n\t"
    "v_mul_f32 v135, v135, v222\n\t"
    "v_mfma_f32_16x16x32_bf16 v[64:67], a[4:7], a[100:103], v[64:67]\n\t"
    "v_max_u32_dpp v137, v137, v137 row_bcast:31 row_mask:0xc bank_mask:0xf\n\t"
    "v_mul_f32 v136, 0x3e22f983, v136\n\t"
    "v_mfma_f32_16x16x32_bf16 v[68:71], a[12:15], a[100:103], 0\n\t"
    "buffer_load_dwordx4 v231, s[36:39], s48 offen offset:3072 lds\n\t"
    "v_mfma_f32_16x16x32_bf16 v[68:71], a[8:11], a[96:99], v[68:71]\n\t"
    "s_add_u32 s48, s48, 128\n\t"
    "s_and_b32 s48, s48, 1023\n\t"
    "v_mfma_f32_16x16x32_bf16 v[72:75], a[16:19], a[96:99], 0\n\t"
    "v_mul_f32 v135, 0xbfb8aa3b, v135\n\t"
    "v_readlane_b32 s35, v137, 63\n\t"
    "v_mfma_f32_16x16x32_bf16 v[72:75], a[20:23], a[100:103], v[72:75]\n\t"
    "v_mul_f32 v138, 0x41000000, v135\n\t"
    "v_mul_f32 v139, 0x41000000, v136\n\t"
    "v_mfma_f32_16x16x32_bf16 v[76:79], a[28:31], a[100:103], 0\n\t"
    "s_lshl_b32 s49, s48, 12\n\t"
    "v_fract_f32 v139, v139\n\t"
    "v_mfma_f32_16x16x32_bf16 v[76:79], a[24:27], a[96:99], v[76:79]\n\t"
    "v_exp_f32 v138, v138\n\t"
    "v_mfma_f32_16x16x32_bf16 v[92:95], a[24:27], a[104:107], 0\n\t"
    "v_cos_f32 v142, v139\n\t"
    "v_mfma_f32_16x16x32_bf16 v[92:95], a[28:31], a[108:111], v[92:95]\n\t"
    "v_sin_f32 v143, v139\n\t"
    "v_mfma_f32_16x16x32_bf16 v[88:91], a[20:23], a[108:111], 0\n\t"
    "s_add_u32 m0, s52, 0x8000\n\t"
    "v_mul_f32 v142, v142, v138\n\t"
    "v_mul_f32 v143, v143, v138\n\t"
    "v_mfma_f32_16x16x32_bf16 v[88:91], a[16:19], a[104:107], v[88:91]\n\t"
    "s_lshr_b32 s35, s35, 7\n\t"
    "s_sub_i32 s56, 126, s35\n\t"
    "s_sub_i32 s35, s35, 126\n\t"
    "v_mfma_f32_16x16x32_bf16 v[84:87], a[8:11], a[104:107], 0\n\t"
    "v_ldexp_f32 v172, v130, s56\n\t"
    "s_waitcnt vmcnt(14) lgkmcnt(0)\n\t"
    "s_barrier\n\t"
    "v_mfma_f32_16x16x32_bf16 v[84:87], a[12:15], a[108:111], v[84:87]\n\t"
    "ds_read_b128 a[68:71], v237 offset:32768\n\t"
    "ds_read_b128 a[92:95], v237 offset:38912\n\t"
    "ds_read_b64_tr_b16 a[56:57], v243 offset:0\n\t"
    "v_mfma_f32_16x16x32_bf16 v[80:83], a[4:7], a[108:111], 0\n\t"
    "buffer_load_dwordx4 v232, s[40:43], s49 offen offset:0 lds\n\t"
    "v_mfma_f32_16x16x32_bf16 v[80:83], a[0:3], a[104:107], v[80:83]\n\t"
    "ds_read_b128 a[64:67], v236 offset:32768\n\t"
    "ds_read_b128 a[76:79], v237 offset:34816\n\t"
    "ds_read_b64_tr_b16 a[36:37], v240 offset:4096\n\t"
    "v_mfma_f32_16x16x32_bf16 v[96:99], a[0:3], a[112:115], 0\n\t"
    "ds_read_b128 a[72:75], v236 offset:34816\n\t"
    "ds_read_b128 a[84:87], v237 offset:36864\n\t"
    "ds_read_b64_tr_b16 a[44:45], v241 offset:4096\n\t"
    "v_mfma_f32_16x16x32_bf16 v[96:99], a[4:7], a[116:119], v[96:99]\n\t"
    "buffer_load_dwordx4 v233, s[40:43], s49 offen offset:1024 lds\n\t"
    "v_mfma_f32_16x16x32_bf16 v[100:103], a[12:15], a[116:119], 0\n\t"
    "ds_read_b128 a[96:99], v236 offset:40960\n\t"
    "ds_read_b64_tr_b16 a[32:33], v240 offset:0\n\t"
    "ds_read_b64_tr_b16 a[38:39], v240 offset:4608\n\t"
    "v_mfma_f32_16x16x32_bf16 v[100:103], a[8:11], a[112:115], v[100:103]\n\t"
    "buffer_load_dwordx4 v234, s[40:43], s49 offen offset:2048 lds\n\t"
    "v_mfma_f32_16x16x32_bf16 v[104:107], a[16:19], a[112:115], 0\n\t"
    "ds_read_b128 a[80:83], v236 offset:36864\n\t"
    "ds_read_b64_tr_b16 a[52:53], v242 offset:4096\n\t"
    "ds_read_b64_tr_b16 a[58:59], v243 offset:512\n\t"
    "v_mfma_f32_16x16x32_bf16 v[104:107], a[20:23], a[116:119], v[104:107]\n\t"
    "buffer_load_dwordx4 v235, s[40:43], s49 offen offset:3072 lds\n\t"
    "v_mfma_f32_16x16x32_bf16 v[108:111], a[28:31], a[116:119], 0\n\t"
    "ds_read_b64_tr_b16 a[40:41], v241 offset:0\n\t"
    "ds_read_b64_tr_b16 a[42:43], v241 offset:512\n\t"
    "ds_read_b64_tr_b16 a[54:55], v242 offset:4608\n\t"
    "v_mfma_f32_16x16x32_bf16 v[108:111], a[24:27], a[112:115], v[108:111]\n\t"
    "ds_read_b64_tr_b16 a[46:47], v241 offset:4608\n\t"
    "ds_read_b64_tr_b16 a[62:63], v243 offset:4608\n\t"
    "s_add_u32 m0, s75, 0x0\n\t"
    "v_mfma_f32_16x16x32_bf16 v[124:127], a[24:27], a[120:123], 0\n\t"
    "ds_read_b128 a[100:103], v237 offset:40960\n\t"
    "ds_read_b64_tr_b16 a[34:35], v240 offset:512\n\t"
    "ds_read_b64_tr_b16 a[50:51], v242 offset:512\n\t"
    "v_mfma_f32_16x16x32_bf16 v[124:127], a[28:31], a[124:127], v[124:127]\n\t"
    "ds_read_b128 a[88:91], v236 offset:38912\n\t"
    "ds_read_b128 a[108:111], v237 offset:43008\n\t"
    "ds_read_b64_tr_b16 a[60:61], v243 offset:4096\n\t"
    "v_mfma_f32_16x16x32_bf16 v[120:123], a[20:23], a[124:127], 0\n\t"
    "ds_read_b128 a[104:107], v236 offset:43008\n\t"
    "ds_read_b128 a[112:115], v236 offset:45056\n\t"
    "ds_read_b64_tr_b16 a[48:49], v242 offset:0\n\t"
    "v_mfma_f32_16x16x32_bf16 v[120:123], a[16:19], a[120:123], v[120:123]\n\t"
    "buffer_load_dwordx4 v224, s[36:39], s48 offen offset:0 lds\n\t"
    "v_mfma_f32_16x16x32_bf16 v[116:119], a[8:11], a[120:123], 0\n\t"
    "buffer_load_dwordx4 v225, s[36:39], s48 offen offset:1024 lds\n\t"
    "v_mfma_f32_16x16x32_bf16 v[116:119], a[12:15], a[124:127], v[116:119]\n\t"
    "buffer_load_dwordx4 v226, s[36:39], s48 offen offset:2048 lds\n\t"
    "v_mfma_f32_16x16x32_bf16 v[112:115], a[4:7], a[124:127], 0\n\t"
    "v_ldexp_f32 v173, v131, s56\n\t"
    "v_mul_f32 v144, v142, v142\n\t"
    "v_mul_f32 v145, v142, v143\n\t"
    "v_mfma_f32_16x16x32_bf16 v[112:115], a[0:3], a[120:123], v[112:115]\n\t"
    "ds_read_b128 a[116:119], v237 offset:45056\n\t"
    "ds_read_b128 a[120:123], v236 offset:47104\n\t"
    "ds_read_b128 a[124:127], v237 offset:47104\n\t"
    "s_waitcnt lgkmcnt(10)\n\t"
    "v_mfma_f32_16x16x32_bf16 v[0:3], a[32:35], a[64:67], v[0:3]\n\t"
    "v_fma_f32 v144, -v143, v143, v144\n\t"
    "v_fmac_f32 v145, v143, v142\n\t"
    "v_mul_f32 v174, v172, v142\n\t"
    "v_mfma_f32_16x16x32_bf16 v[0:3], a[36:39], a[68:71], v[0:3]\n\t"
    "buffer_load_dwordx4 v227, s[36:39], s48 offen offset:3072 lds\n\t"
    "v_mfma_f32_16x16x32_bf16 v[4:7], a[44:47], a[68:71], v[4:7]\n\t"
    "v_mul_f32 v175, v172, v143\n\t"
    "v_fma_f32 v174, -v173, v143, v174\n\t"
    "v_fmac_f32 v175, v173, v142\n\t"
    "v_mfma_f32_16x16x32_bf16 v[4:7], a[40:43], a[64:67], v[4:7]\n\t"
    "s_add_u32 m0, s75, 0x1000\n\t"
    "v_mul_f32 v146, v144, v144\n\t"
    "v_mul_f32 v147, v144, v145\n\t"
    "s_waitcnt lgkmcnt(3)\n\t"
    "v_mfma_f32_16x16x32_bf16 v[8:11], a[48:51], a[64:67], v[8:11]\n\t"
    "buffer_load_dwordx4 v228, s[36:39], s48 offen offset:0 lds\n\t"
    "v_mfma_f32_16x16x32_bf16 v[8:11], a[52:55], a[68:71], v[8:11]\n\t"
    "v_fma_f32 v146, -v145, v145, v146\n\t"
    "v_fmac_f32 v147, v145, v144\n\t"
    "v_mul_f32 v176, v172, v144\n\t"
    "v_mfma_f32_16x16x32_bf16 v[12:15], a[60:63], a[68:71], v[12:15]\n\t"
    "buffer_load_dwordx4 v229, s[36:39], s48 offen offset:1024 lds\n\t"
    "v_mfma_f32_16x16x32_bf16 v[12:15], a[56:59], a[64:67], v[12:15]\n\t"
    "v_mul_f32 v177, v172, v145\n\t"
    "v_fma_f32 v176, -v173, v145, v176\n\t"
    "v_fmac_f32 v177, v173, v144\n\t"
    "v_mfma_f32_16x16x32_bf16 v[28:31], a[56:59], a[72:75], v[28:31]\n\t"
    "buffer_load_dwordx4 v230, s[36:39], s48 offen offset:2048 lds\n\t"
    "v_mfma_f32_16x16x32_bf16 v[28:31], a[60:63], a[76:79], v[28:31]\n\t"
    "v_mul_f32 v178, v174, v144\n\t"
    "v_mul_f32 v179, v174, v145\n\t"
    "v_mfma_f32_16x16x32_bf16 v[24:27], a[52:55], a[76:79], v[24:27]\n\t"
    "v_fma_f32 v178, -v175, v145, v178\n\t"
    "v_fmac_f32 v179, v175, v144\n\t"
    "v_mul_f32 v180, v172, v146\n\t"
    "v_mfma_f32_16x16x32_bf16 v[24:27], a[48:51], a[72:75], v[24:27]\n\t"
    "buffer_load_dwordx4 v231, s[36:39], s48 offen offset:3072 lds\n\t"
    "v_mfma_f32_16x16x32_bf16 v[20:23], a[40:43], a[72:75], v[20:23]\n\t"
    "s_add_u32 s48, s48, 128\n\t"
    "s_and_b32 s48, s48, 1023\n\t"
    "v_mul_f32 v181, v172, v147\n\t"
    "v_mfma_f32_16x16x32_bf16 v[20:23], a[44:47], a[76:79], v[20:23]\n\t"
    "v_fma_f32 v180, -v173, v147, v180\n\t"
    "v_fmac_f32 v181, v173, v146\n\t"
    "v_mfma_f32_16x16x32_bf16 v[16:19], a[36:39], a[76:79], v[16:19]\n\t"
    "v_mul_f32 v182, v174, v146\n\t"
    "v_mul_f32 v183, v174, v147\n\t"
    "v_fma_f32 v182, -v175, v147, v182\n\t"
    "v_mfma_f32_16x16x32_bf16 v[16:19], a[32:35], a[72:75], v[16:19]\n\t"
    "s_lshl_b32 s49, s48, 12\n\t"
    "v_fmac_f32 v183, v175, v146\n\t"
    "v_mul_f32 v184, v176, v146\n\t"
    "v_mfma_f32_16x16x32_bf16 v[32:35], a[32:35], a[80:83], v[32:35]\n\t"
    "s_waitcnt vmcnt(12) lgkmcnt(0)\n\t"
    "s_barrier\n\t"
    "v_mfma_f32_16x16x32_bf16 v[32:35], a[36:39], a[84:87], v[32:35]\n\t"
    "ds_read_b64_tr_b16 a[16:17], v242 offset:16384\n\t"
    "s_add_u32 m0, s52, 0x0\n\t"
    "v_mul_f32 v185, v176, v147\n\t"
    "v_mfma_f32_16x16x32_bf16 v[36:39], a[44:47], a[84:87], v[36:39]\n\t"
    "v_fma_f32 v184, -v177, v147, v184\n\t"
    "v_fmac_f32 v185, v177, v146\n\t"
    "v_mul_f32 v186, v178, v146\n\t"
    "v_mfma_f32_16x16x32_bf16 v[36:39], a[40:43], a[80:83], v[36:39]\n\t"
    "buffer_load_dwordx4 v232, s[40:43], s49 offen offset:0 lds\n\t"
    "v_mfma_f32_16x16x32_bf16 v[40:43], a[48:51], a[80:83], v[40:43]\n\t"
    "ds_read_b128 a[68:71], v239 offset:0\n\t"
    "ds_read_b64_tr_b16 a[20:21], v242 offset:20480\n\t"
    "v_mfma_f32_16x16x32_bf16 v[40:43], a[52:55], a[84:87], v[40:43]\n\t"
    "buffer_load_dwordx4 v233, s[40:43], s49 offen offset:1024 lds\n\t"
    "v_mfma_f32_16x16x32_bf16 v[44:47], a[60:63], a[84:87], v[44:47]\n\t"
    "v_mul_f32 v187, v178, v147\n\t"
    "v_fma_f32 v186, -v179, v147, v186\n\t"
    "v_mfma_f32_16x16x32_bf16 v[44:47], a[56:59], a[80:83], v[44:47]\n\t"
    "v_fmac_f32 v187, v179, v146\n\t"
    "v_mul_f32 v188, v180, v146\n\t"
    "v_mfma_f32_16x16x32_bf16 v[60:63], a[56:59], a[88:91], v[60:63]\n\t"
    "v_mul_f32 v189, v180, v147\n\t"
    "v_fma_f32 v188, -v181, v147, v188\n\t"
    "v_mfma_f32_16x16x32_bf16 v[60:63], a[60:63], a[92:95], v[60:63]\n\t"
    "buffer_load_dwordx4 v234, s[40:43], s49 offen offset:2048 lds\n\t"
    "v_mfma_f32_16x16x32_bf16 v[56:59], a[52:55], a[92:95], v[56:59]\n\t"
    "ds_read_b64_tr_b16 a[14:15], v241 offset:20992\n\t"
    "ds_read_b64_tr_b16 a[244:245], v216 offset:16\n\t"
    "v_mfma_f32_16x16x32_bf16 v[56:59], a[48:51], a[88:91], v[56:59]\n\t"
    "ds_read_b128 a[64:67], v238 offset:0\n\t"
    "ds_read_b128 a[76:79], v239 offset:2048\n\t"
    "ds_read_b128 a[80:83], v238 offset:4096\n\t"
    "v_mfma_f32_16x16x32_bf16 v[52:55], a[40:43], a[88:91], v[52:55]\n\t"
    "ds_read_b128 a[72:75], v238 offset:2048\n\t"
    "v_fmac_f32 v189, v181, v146\n\t"
    "v_mfma_f32_16x16x32_bf16 v[52:55], a[44:47], a[92:95], v[52:55]\n\t"
    "buffer_load_dwordx4 v235, s[40:43], s49 offen offset:3072 lds\n\t"
    "v_mfma_f32_16x16x32_bf16 v[48:51], a[36:39], a[92:95], v[48:51]\n\t"
    "s_add_u32 m0, s75, 0x8000\n\t"
    "v_mul_f32 v190, v182, v146\n\t"
    "v_mul_f32 v191, v182, v147\n\t"
    "v_mfma_f32_16x16x32_bf16 v[48:51], a[32:35], a[88:91], v[48:51]\n\t"
    "ds_read_b64_tr_b16 a[242:243], v216 offset:512\n\t"
    "v_fma_f32 v190, -v183, v147, v190\n\t"
    "v_fmac_f32 v191, v183, v146\n\t"
    "v_mfma_f32_16x16x32_bf16 v[64:67], a[32:35], a[96:99], v[64:67]\n\t"
    "ds_read_b128 a[84:87], v239 offset:4096\n\t"
    "ds_read_b64_tr_b16 a[246:247], v216 offset:528\n\t"
    "v_mul_f32 v192, v184, v146\n\t"
    "v_mfma_f32_16x16x32_bf16 v[64:67], a[36:39], a[100:103], v[64:67]\n\t"
    "ds_read_b128 a[92:95], v239 offset:6144\n\t"
    "ds_read_b64_tr_b16 a[240:241], v216 offset:0\n\t"
    "v_mfma_f32_16x16x32_bf16 v[68:71], a[44:47], a[100:103], v[68:71]\n\t"
    "v_mul_f32 v193, v184, v147\n\t"
    "v_fma_f32 v192, -v185, v147, v192\n\t"
    "v_mfma_f32_16x16x32_bf16 v[68:71], a[40:43], a[96:99], v[68:71]\n\t"
    "buffer_load_dwordx4 v224, s[36:39], s48 offen offset:0 lds\n\t"
    "v_mfma_f32_16x16x32_bf16 v[72:75], a[48:51], a[96:99], v[72:75]\n\t"
    "buffer_load_dwordx4 v225, s[36:39], s48 offen offset:1024 lds\n\t"
    "v_mfma_f32_16x16x32_bf16 v[72:75], a[52:55], a[100:103], v[72:75]\n\t"
    "ds_read_b64_tr_b16 a[0:1], v240 offset:16384\n\t"
    "v_fmac_f32 v193, v185, v146\n\t"
    "v_mfma_f32_16x16x32_bf16 v[76:79], a[60:63], a[100:103], v[76:79]\n\t"
    "v_mul_f32 v194, v186, v146\n\t"
    "v_mul_f32 v195, v186, v147\n\t"
    "v_mfma_f32_16x16x32_bf16 v[76:79], a[56:59], a[96:99], v[76:79]\n\t"
    "v_fma_f32 v194, -v187, v147, v194\n\t"
    "v_fmac_f32 v195, v187, v146\n\t"
    "v_mfma_f32_16x16x32_bf16 v[92:95], a[56:59], a[104:107], v[92:95]\n\t"
    "ds_read_b128 a[100:103], v239 offset:8192\n\t"
    "v_mul_f32 v196, v188, v146\n\t"
    "v_mfma_f32_16x16x32_bf16 v[92:95], a[60:63], a[108:111], v[92:95]\n\t"
    "buffer_load_dwordx4 v226, s[36:39], s48 offen offset:2048 lds\n\t"
    "v_mfma_f32_16x16x32_bf16 v[88:91], a[52:55], a[108:111], v[88:91]\n\t"
    "ds_read_b128 a[88:91], v238 offset:6144\n\t"
    "Lis1_%=:\n\t"
    "s_cbranch_execz Lis5_%=\n\t"
    "v_mul_f32 v197, v188, v147\n\t"
    "v_mfma_f32_16x16x32_bf16 v[88:91], a[48:51], a[104:107], v[88:91]\n\t"
    "ds_read_b128 a[96:99], v238 offset:8192\n\t"
    "ds_read_b64_tr_b16 a[10:11], v241 offset:16896\n\t"
    "v_mfma_f32_16x16x32_bf16 v[84:87], a[40:43], a[104:107], v[84:87]\n\t"
    "buffer_load_dwordx4 v227, s[36:39], s48 offen offset:3072 lds\n\t"
    "v_mfma_f32_16x16x32_bf16 v[84:87], a[44:47], a[108:111], v[84:87]\n\t"
    "ds_read_b64_tr_b16 a[26:27], v243 offset:16896\n\t"
    "v_fma_f32 v196, -v189, v147, v196\n\t"
    "v_mfma_f32_16x16x32_bf16 v[80:83], a[36:39], a[108:111], v[80:83]\n\t"
    "v_fmac_f32 v197, v189, v146\n\t"
    "v_mul_f32 v198, v190, v146\n\t"
    "v_mfma_f32_16x16x32_bf16 v[80:83], a[32:35], a[104:107], v[80:83]\n\t"
    "ds_read_b64_tr_b16 a[24:25], v243 offset:16384\n\t"
    "v_mul_f32 v199, v190, v147\n\t"
    "v_mfma_f32_16x16x32_bf16 v[96:99], a[32:35], a[112:115], v[96:99]\n\t"
    "ds_read_b128 a[108:111], v239 offset:10240\n\t"
    "s_add_u32 m0, s75, 0x9000\n\t"
    "v_mfma_f32_16x16x32_bf16 v[96:99], a[36:39], a[116:119], v[96:99]\n\t"
    "buffer_load_dwordx4 v228, s[36:39], s48 offen offset:0 lds\n\t"
    "v_mfma_f32_16x16x32_bf16 v[100:103], a[44:47], a[116:119], v[100:103]\n\t"
    "ds_read_b64_tr_b16 a[6:7], v240 offset:20992\n\t"
    "v_fma_f32 v198, -v191, v147, v198\n\t"
    "v_mfma_f32_16x16x32_bf16 v[100:103], a[40:43], a[112:115], v[100:103]\n\t"
    "ds_read_b64_tr_b16 a[8:9], v241 offset:16384\n\t"
    "ds_read_b64_tr_b16 a[28:29], v243 offset:20480\n\t"
    "v_mfma_f32_16x16x32_bf16 v[104:107], a[48:51], a[112:115], v[104:107]\n\t"
    "ds_read_b64_tr_b16 a[22:23], v242 offset:20992\n\t"
    "v_fmac_f32 v199, v191, v146\n\t"
    "v_mfma_f32_16x16x32_bf16 v[104:107], a[52:55], a[116:119], v[104:107]\n\t"
    "buffer_load_dwordx4 v229, s[36:39], s48 offen offset:1024 lds\n\t"
    "v_mfma_f32_16x16x32_bf16 v[108:111], a[60:63], a[116:119], v[108:111]\n\t"
    "v_mul_f32 v200, v192, v146\n\t"
    "v_mul_f32 v201, v192, v147\n\t"
    "v_fma_f32 v200, -v193, v147, v200\n\t"
    "v_mfma_f32_16x16x32_bf16 v[108:111], a[56:59], a[112:115], v[108:111]\n\t"
    "ds_read_b64_tr_b16 a[12:13], v241 offset:20480\n\t"
    "ds_read_b64_tr_b16 a[18:19], v242 offset:16896\n\t"
    "v_mfma_f32_16x16x32_bf16 v[124:127], a[56:59], a[120:123], v[124:127]\n\t"
    "ds_read_b64_tr_b16 a[4:5], v240 offset:20480\n\t"
    "v_fmac_f32 v201, v193, v146\n\t"
    "v_mfma_f32_16x16x32_bf16 v[124:127], a[60:63], a[124:127], v[124:127]\n\t"
    "buffer_load_dwordx4 v230, s[36:39], s48 offen offset:2048 lds\n\t"
    "v_mfma_f32_16x16x32_bf16 v[120:123], a[52:55], a[124:127], v[120:123]\n\t"
    "ds_read_b64_tr_b16 a[2:3], v240 offset:16896\n\t"
    "ds_read_b64_tr_b16 a[30:31], v243 offset:20992\n\t"
    "v_mfma_f32_16x16x32_bf16 v[120:123], a[48:51], a[120:123], v[120:123]\n\t"
    "ds_read_b128 a[104:107], v238 offset:10240\n\t"
    "ds_read_b128 a[116:119], v239 offset:12288\n\t"
    "v_mfma_f32_16x16x32_bf16 v[116:119], a[40:43], a[120:123], v[116:119]\n\t"
    "ds_read_b128 a[112:115], v238 offset:12288\n\t"
    "v_mul_f32 v202, v194, v146\n\t"
    "v_mfma_f32_16x16x32_bf16 v[116:119], a[44:47], a[124:127], v[116:119]\n\t"
    "buffer_load_dwordx4 v231, s[36:39], s48 offen offset:3072 lds\n\t"
    "v_mfma_f32_16x16x32_bf16 v[112:115], a[36:39], a[124:127], v[112:115]\n\t"
    "v_mul_f32 v203, v194, v147\n\t"
    "v_fma_f32 v202, -v195, v147, v202\n\t"
    "v_mfma_f32_16x16x32_bf16 v[112:115], a[32:35], a[120:123], v[112:115]\n\t"
    "ds_read_b128 a[120:123], v238 offset:14336\n\t"
    "ds_read_b128 a[124:127], v239 offset:14336\n\t"
    "s_waitcnt lgkmcnt(6)\n\t"
    "v_mfma_f32_16x16x32_bf16 v[0:3], a[0:3], a[64:67], v[0:3]\n\t"
    "global_store_dwordx4 v217, a[240:243], s[84:85] sc1\n\t"
    "v_mfma_f32_16x16x32_bf16 v[0:3], a[4:7], a[68:71], v[0:3]\n\t"
    "global_store_dwordx4 v221, a[244:247], s[84:85] sc1\n\t"
    "v_mfma_f32_16x16x32_bf16 v[4:7], a[12:15], a[68:71], v[4:7]\n\t"
    "global_load_dword v252, v245, s[84:85] sc1\n\t"
    "v_mfma_f32_16x16x32_bf16 v[4:7], a[8:11], a[64:67], v[4:7]\n\t"
    "s_add_u32 s48, s48, 128\n\t"
    "v_fmac_f32 v203, v195, v146\n\t"
    "v_mfma_f32_16x16x32_bf16 v[8:11], a[16:19], a[64:67], v[8:11]\n\t"
    "s_and_b32 s48, s48, 1023\n\t"
    "s_lshl_b32 s49, s48, 12\n\t"
    "v_mfma_f32_16x16x32_bf16 v[8:11], a[20:23], a[68:71], v[8:11]\n\t"
    "s_waitcnt vmcnt(15) lgkmcnt(0)\n\t"
    "s_barrier\n\t"
    "v_mfma_f32_16x16x32_bf16 v[12:15], a[28:31], a[68:71], v[12:15]\n\t"
    "s_add_u32 m0, s52, 0x4000\n\t"
    "v_cvt_pk_f16_f32 v164, v172, v174\n\t"
    "v_mfma_f32_16x16x32_bf16 v[12:15], a[24:27], a[64:67], v[12:15]\n\t"
    "v_cvt_pk_f16_f32 v165, v176, v178\n\t"
    "v_cvt_pk_f16_f32 v166, v180, v182\n\t"
    "v_mfma_f32_16x16x32_bf16 v[28:31], a[24:27], a[72:75], v[28:31]\n\t"
    "ds_read_b128 a[68:71], v237 offset:0\n\t"
    "v_cvt_pk_f16_f32 v167, v184, v186\n\t"
    "v_mfma_f32_16x16x32_bf16 v[28:31], a[28:31], a[76:79], v[28:31]\n\t"
    "ds_write_b128 v132, v[164:167] offset:0\n\t"
    "v_mfma_f32_16x16x32_bf16 v[24:27], a[20:23], a[76:79], v[24:27]\n\t"
    "buffer_load_dwordx4 v232, s[40:43], s49 offen offset:0 lds\n\t"
    "v_mfma_f32_16x16x32_bf16 v[24:27], a[16:19], a[72:75], v[24:27]\n\t"
    "v_cvt_pk_f16_f32 v168, v188, v190\n\t"
    "v_cvt_pk_f16_f32 v169, v192, v194\n\t"
    "v_mfma_f32_16x16x32_bf16 v[20:23], a[8:11], a[72:75], v[20:23]\n\t"
    "buffer_load_dwordx4 v233, s[40:43], s49 offen offset:1024 lds\n\t"
    "v_mfma_f32_16x16x32_bf16 v[20:23], a[12:15], a[76:79], v[20:23]\n\t"
    "ds_read_b128 a[64:67], v236 offset:0\n\t"
    "v_cvt_pk_f16_f32 v170, v196, v198\n\t"
    "v_cvt_pk_f16_f32 v171, v200, v202\n\t"
    "v_mfma_f32_16x16x32_bf16 v[16:19], a[4:7], a[76:79], v[16:19]\n\t"
    "ds_write_b128 v133, v[168:171] offset:0\n\t"
    "v_mfma_f32_16x16x32_bf16 v[16:19], a[0:3], a[72:75], v[16:19]\n\t"
    "ds_read_b128 a[76:79], v237 offset:2048\n\t"
    "v_cvt_pk_f16_f32 v164, -v173, -v175\n\t"
    "v_mfma_f32_16x16x32_bf16 v[32:35], a[0:3], a[80:83], v[32:35]\n\t"
    "v_cvt_pk_f16_f32 v165, -v177, -v179\n\t"
    "v_cvt_pk_f16_f32 v166, -v181, -v183\n\t"
    "v_mfma_f32_16x16x32_bf16 v[32:35], a[4:7], a[84:87], v[32:35]\n\t"
    "buffer_load_dwordx4 v234, s[40:43], s49 offen offset:2048 lds\n\t"
    "v_mfma_f32_16x16x32_bf16 v[36:39], a[12:15], a[84:87], v[36:39]\n\t"
    "ds_read_b128 a[72:75], v236 offset:2048\n\t"
    "v_cvt_pk_f16_f32 v167, -v185, -v187\n\t"
    "v_mfma_f32_16x16x32_bf16 v[36:39], a[8:11], a[80:83], v[36:39]\n\t"
    "ds_write_b128 v132, v[164:167] offset:2048\n\t"
    "v_mfma_f32_16x16x32_bf16 v[40:43], a[16:19], a[80:83], v[40:43]\n\t"
    "ds_read_b64_tr_b16 a[54:55], v242 offset:37376\n\t"
    "v_cvt_pk_f16_f32 v168, -v189, -v191\n\t"
    "v_mfma_f32_16x16x32_bf16 v[40:43], a[20:23], a[84:87], v[40:43]\n\t"
    "ds_read_b64_tr_b16 a[46:47], v241 offset:37376\n\t"
    "v_cvt_pk_f16_f32 v169, -v193, -v195\n\t"
    "v_mfma_f32_16x16x32_bf16 v[44:47], a[28:31], a[84:87], v[44:47]\n\t"
    "buffer_load_dwordx4 v235, s[40:43], s49 offen offset:3072 lds\n\t"
    "v_mfma_f32_16x16x32_bf16 v[44:47], a[24:27], a[80:83], v[44:47]\n\t"
    "ds_read_b64_tr_b16 a[42:43], v241 offset:33280\n\t"
    "s_add_u32 m0, s75, 0x10000\n\t"
    "v_mfma_f32_16x16x32_bf16 v[60:63], a[24:27], a[88:91], v[60:63]\n\t"
    "v_cvt_pk_f16_f32 v170, -v197, -v199\n\t"
    "v_cvt_pk_f16_f32 v171, -v201, -v203\n\t"
    "v_mfma_f32_16x16x32_bf16 v[60:63], a[28:31], a[92:95], v[60:63]\n\t"
    "buffer_load_dwordx4 v224, s[36:39], s48 offen offset:0 lds\n\t"
    "v_mfma_f32_16x16x32_bf16 v[56:59], a[20:23], a[92:95], v[56:59]\n\t"
    "ds_write_b128 v133, v[168:171] offset:2048\n\t"
    "v_mfma_f32_16x16x32_bf16 v[56:59], a[16:19], a[88:91], v[56:59]\n\t"
    "ds_read_b128 a[84:87], v237 offset:4096\n\t"
    "ds_read_b64_tr_b16 a[128:129], v134 offset:0\n\t"
    "v_mfma_f32_16x16x32_bf16 v[52:55], a[8:11], a[88:91], v[52:55]\n\t"
    "ds_read_b64_tr_b16 a[130:131], v134 offset:512\n\t"
    "ds_read_b64_tr_b16 a[132:133], v134 offset:1024\n\t"
    "v_mfma_f32_16x16x32_bf16 v[52:55], a[12:15], a[92:95], v[52:55]\n\t"
    "ds_read_b64_tr_b16 a[48:49], v242 offset:32768\n\t"
    "ds_read_b64_tr_b16 a[134:135], v134 offset:1536\n\t"
    "v_mfma_f32_16x16x32_bf16 v[48:51], a[4:7], a[92:95], v[48:51]\n\t"
    "buffer_load_dwordx4 v225, s[36:39], s48 offen offset:1024 lds\n\t"
    "v_mfma_f32_16x16x32_bf16 v[48:51], a[0:3], a[88:91], v[48:51]\n\t"
    "ds_read_b64_tr_b16 a[36:37], v240 offset:36864\n\t"
    "ds_read_b64_tr_b16 a[136:137], v134 offset:2048\n\t"
    "v_mfma_f32_16x16x32_bf16 v[64:67], a[0:3], a[96:99], v[64:67]\n\t"
    "ds_read_b128 a[80:83], v236 offset:4096\n\t"
    "ds_read_b64_tr_b16 a[138:139], v134 offset:2560\n\t"
    "v_mfma_f32_16x16x32_bf16 v[64:67], a[4:7], a[100:103], v[64:67]\n\t"
    "ds_read_b128 a[88:91], v236 offset:6144\n\t"
    "ds_read_b64_tr_b16 a[140:141], v134 offset:3072\n\t"
    "v_mfma_f32_16x16x32_bf16 v[68:71], a[12:15], a[100:103], v[68:71]\n\t"
    "ds_read_b64_tr_b16 a[40:41], v241 offset:32768\n\t"
    "ds_read_b64_tr_b16 a[50:51], v242 offset:33280\n\t"
    "v_mfma_f32_16x16x32_bf16 v[68:71], a[8:11], a[96:99], v[68:71]\n\t"
    "buffer_load_dwordx4 v226, s[36:39], s48 offen offset:2048 lds\n\t"
    "v_mfma_f32_16x16x32_bf16 v[72:75], a[16:19], a[96:99], v[72:75]\n\t"
    "ds_read_b64_tr_b16 a[142:143], v134 offset:3584\n\t"
    "v_mul_f32 v138, 0x3f800000, v135\n\t"
    "v_mfma_f32_16x16x32_bf16 v[72:75], a[20:23], a[100:103], v[72:75]\n\t"
    "buffer_load_dwordx4 v227, s[36:39], s48 offen offset:3072 lds\n\t"
    "v_mfma_f32_16x16x32_bf16 v[76:79], a[28:31], a[100:103], v[76:79]\n\t"
    "v_mul_f32 v139, 0x3f800000, v136\n\t"
    "v_fract_f32 v139, v139\n\t"
    "v_mfma_f32_16x16x32_bf16 v[76:79], a[24:27], a[96:99], v[76:79]\n\t"
    "ds_read_b64_tr_b16 a[34:35], v240 offset:33280\n\t"
    "v_exp_f32 v138, v138\n\t"
    "v_mfma_f32_16x16x32_bf16 v[92:95], a[24:27], a[104:107], v[92:95]\n\t"
    "ds_read_b128 a[100:103], v237 offset:8192\n\t"
    "ds_read_b64_tr_b16 a[62:63], v243 offset:37376\n\t"
    "v_mfma_f32_16x16x32_bf16 v[92:95], a[28:31], a[108:111], v[92:95]\n\t"
    "v_cos_f32 v150, v139\n\t"
    "v_mfma_f32_16x16x32_bf16 v[88:91], a[20:23], a[108:111], v[88:91]\n\t"
    "ds_read_b128 a[92:95], v237 offset:6144\n\t"
    "s_add_u32 m0, s75, 0x11000\n\t"
    "v_mfma_f32_16x16x32_bf16 v[88:91], a[16:19], a[104:107], v[88:91]\n\t"
    "v_sin_f32 v151, v139\n\t"
    "v_mfma_f32_16x16x32_bf16 v[84:87], a[8:11], a[104:107], v[84:87]\n\t"
    "v_mul_f32 v150, v150, v138\n\t"
    "v_mul_f32 v151, v151, v138\n\t"
    "v_mfma_f32_16x16x32_bf16 v[84:87], a[12:15], a[108:111], v[84:87]\n\t"
    "buffer_load_dwordx4 v228, s[36:39], s48 offen offset:0 lds\n\t"
    "v_mfma_f32_16x16x32_bf16 v[80:83], a[4:7], a[108:111], v[80:83]\n\t"
    "ds_read_b128 a[96:99], v236 offset:8192\n\t"
    "ds_read_b128 a[108:111], v237 offset:10240\n\t"
    "v_mfma_f32_16x16x32_bf16 v[80:83], a[0:3], a[104:107], v[80:83]\n\t"
    "v_mul_f32 v138, 0x43000000, v135\n\t"
    "v_mul_f32 v139, 0x43000000, v136\n\t"
    "v_mfma_f32_16x16x32_bf16 v[96:99], a[0:3], a[112:115], v[96:99]\n\t"
    "ds_read_b64_tr_b16 a[32:33], v240 offset:32768\n\t"
    "v_fract_f32 v139, v139\n\t"
    "v_mfma_f32_16x16x32_bf16 v[96:99], a[4:7], a[116:119], v[96:99]\n\t"
    "buffer_load_dwordx4 v229, s[36:39], s48 offen offset:1024 lds\n\t"
    "v_mfma_f32_16x16x32_bf16 v[100:103], a[12:15], a[116:119], v[100:103]\n\t"
    "v_exp_f32 v138, v138\n\t"
    "v_mfma_f32_16x16x32_bf16 v[100:103], a[8:11], a[112:115], v[100:103]\n\t"
    "v_cos_f32 v204, v139\n\t"
    "v_mfma_f32_16x16x32_bf16 v[104:107], a[16:19], a[112:115], v[104:107]\n\t"
    "buffer_load_dwordx4 v230, s[36:39], s48 offen offset:2048 lds\n\t"
    "v_mfma_f32_16x16x32_bf16 v[104:107], a[20:23], a[116:119], v[104:107]\n\t"
    "v_sin_f32 v205, v139\n\t"
    "v_mfma_f32_16x16x32_bf16 v[108:111], a[28:31], a[116:119], v[108:111]\n\t"
    "ds_read_b64_tr_b16 a[60:61], v243 offset:36864\n\t"
    "v_mul_f32 v204, v204, v138\n\t"
    "v_mfma_f32_16x16x32_bf16 v[108:111], a[24:27], a[112:115], v[108:111]\n\t"
    "ds_read_b128 a[104:107], v236 offset:10240\n\t"
    "v_mul_f32 v205, v205, v138\n\t"
    "v_mfma_f32_16x16x32_bf16 v[124:127], a[24:27], a[120:123], v[124:127]\n\t"
    "ds_read_b64_tr_b16 a[44:45], v241 offset:36864\n\t"
    "ds_read_b64_tr_b16 a[52:53], v242 offset:36864\n\t"
    "v_mfma_f32_16x16x32_bf16 v[124:127], a[28:31], a[124:127], v[124:127]\n\t"
    "ds_read_b64_tr_b16 a[56:57], v243 offset:32768\n\t"
    "v_mul_f32 v138, 0x00000000, v135\n\t"
    "v_mfma_f32_16x16x32_bf16 v[120:123], a[20:23], a[124:127], v[120:123]\n\t"
    "ds_read_b64_tr_b16 a[38:39], v240 offset:37376\n\t"
    "ds_read_b64_tr_b16 a[58:59], v243 offset:33280\n\t"
    "v_mfma_f32_16x16x32_bf16 v[120:123], a[16:19], a[120:123], v[120:123]\n\t"
    "buffer_load_dwordx4 v231, s[36:39], s48 offen offset:3072 lds\n\t"
    "v_mfma_f32_16x16x32_bf16 v[116:119], a[8:11], a[120:123], v[116:119]\n\t"
    "s_add_u32 s48, s48, 128\n\t"
    "s_and_b32 s48, s48, 1023\n\t"
    "v_mfma_f32_16x16x32_bf16 v[116:119], a[12:15], a[124:127], v[116:119]\n\t"
    "ds_read_b128 a[112:115], v236 offset:12288\n\t"
    "ds_read_b128 a[116:119], v237 offset:12288\n\t"
    "v_mfma_f32_16x16x32_bf16 v[112:115], a[4:7], a[124:127], v[112:115]\n\t"
    "v_mul_f32 v139, 0x00000000, v136\n\t"
    "v_fract_f32 v139, v139\n\t"
    "v_mfma_f32_16x16x32_bf16 v[112:115], a[0:3], a[120:123], v[112:115]\n\t"
    "ds_read_b128 a[120:123], v236 offset:14336\n\t"
    "ds_read_b128 a[124:127], v237 offset:14336\n\t"
    "s_waitcnt lgkmcnt(9)\n\t"
    "v_mfma_f32_16x16x32_bf16 v[0:3], a[32:35], a[64:67], v[0:3]\n\t"
    "v_exp_f32 v138, v138\n\t"
    "s_waitcnt lgkmcnt(5)\n\t"
    "v_mfma_f32_16x16x32_bf16 v[0:3], a[36:39], a[68:71], v[0:3]\n\t"
    "v_cos_f32 v148, v139\n\t"
    "v_mfma_f32_16x16x32_bf16 v[4:7], a[44:47], a[68:71], v[4:7]\n\t"
    "v_sin_f32 v149, v139\n\t"
    "v_mfma_f32_16x16x32_bf16 v[4:7], a[40:43], a[64:67], v[4:7]\n\t"
    "s_lshl_b32 s49, s48, 12\n\t"
    "v_mul_f32 v148, v148, v138\n\t"
    "v_mfma_f32_16x16x32_bf16 v[8:11], a[48:51], a[64:67], v[8:11]\n\t"
    "s_add_u32 m0, s52, 0x8000\n\t"
    "v_mul_f32 v149, v149, v138\n\t"
    "v_mfma_f32_16x16x32_bf16 v[8:11], a[52:55], a[68:71], v[8:11]\n\t"
    "s_waitcnt vmcnt(15) lgkmcnt(0)\n\t"
    "s_barrier\n\t"
    "v_mfma_f32_16x16x32_bf16 v[12:15], a[60:63], a[68:71], v[12:15]\n\t"
    "v_mul_f32 v152, v150, v150\n\t"
    "v_mul_f32 v153, v150, v151\n\t"
    "v_fma_f32 v152, -v151, v151, v152\n\t"
    "v_mfma_f32_16x16x32_bf16 v[12:15], a[56:59], a[64:67], v[12:15]\n\t"
    "v_fmac_f32 v153, v151, v150\n\t"
    "v_mul_f32 v206, v204, v204\n\t"
    "v_mfma_f32_16x16x32_bf16 v[28:31], a[56:59], a[72:75], v[28:31]\n\t"
    "buffer_load_dwordx4 v232, s[40:43], s49 offen offset:0 lds\n\t"
    "v_mfma_f32_16x16x32_bf16 v[28:31], a[60:63], a[76:79], v[28:31]\n\t"
    "v_mul_f32 v207, v204, v205\n\t"
    "v_fma_f32 v206, -v205, v205, v206\n\t"
    "v_mfma_f32_16x16x32_bf16 v[24:27], a[52:55], a[76:79], v[24:27]\n\t"
    "ds_read_b128 a[68:71], v237 offset:32768\n\t"
    "v_fmac_f32 v207, v205, v204\n\t"
    "v_mfma_f32_16x16x32_bf16 v[24:27], a[48:51], a[72:75], v[24:27]\n\t"
    "buffer_load_dwordx4 v233, s[40:43], s49 offen offset:1024 lds\n\t"
    "v_mfma_f32_16x16x32_bf16 v[20:23], a[40:43], a[72:75], v[20:23]\n\t"
    "v_mul_f32 v154, v152, v150\n\t"
    "v_mul_f32 v155, v152, v151\n\t"
    "v_mfma_f32_16x16x32_bf16 v[20:23], a[44:47], a[76:79], v[20:23]\n\t"
    "ds_read_b64_tr_b16 a[18:19], v242 offset:512\n\t"
    "v_fma_f32 v154, -v153, v151, v154\n\t"
    "v_mfma_f32_16x16x32_bf16 v[16:19], a[36:39], a[76:79], v[16:19]\n\t"
    "v_fmac_f32 v155, v153, v150\n\t"
    "v_mul_f32 v156, v152, v152\n\t"
    "v_mfma_f32_16x16x32_bf16 v[16:19], a[32:35], a[72:75], v[16:19]\n\t"
    "ds_read_b64_tr_b16 a[2:3], v240 offset:512\n\t"
    "v_mul_f32 v157, v152, v153\n\t"
    "v_mfma_f32_16x16x32_bf16 v[32:35], a[32:35], a[80:83], v[32:35]\n\t"
    "ds_read_b128 a[64:67], v236 offset:32768\n\t"
    "v_fma_f32 v156, -v153, v153, v156\n\t"
    "v_mfma_f32_16x16x32_bf16 v[32:35], a[36:39], a[84:87], v[32:35]\n\t"
    "v_fmac_f32 v157, v153, v152\n\t"
    "v_mul_f32 v158, v156, v150\n\t"
    "v_mfma_f32_16x16x32_bf16 v[36:39], a[44:47], a[84:87], v[36:39]\n\t"
    "buffer_load_dwordx4 v234, s[40:43], s49 offen offset:2048 lds\n\t"
    "v_mfma_f32_16x16x32_bf16 v[36:39], a[40:43], a[80:83], v[36:39]\n\t"
    "v_mul_f32 v159, v156, v151\n\t"
    "v_fma_f32 v158, -v157, v151, v158\n\t"
    "v_mfma_f32_16x16x32_bf16 v[40:43], a[48:51], a[80:83], v[40:43]\n\t"
    "buffer_load_dwordx4 v235, s[40:43], s49 offen offset:3072 lds\n\t"
    "v_mfma_f32_16x16x32_bf16 v[40:43], a[52:55], a[84:87], v[40:43]\n\t"
    "v_fmac_f32 v159, v157, v150\n\t"
    "v_mul_f32 v160, v156, v152\n\t"
    "v_mfma_f32_16x16x32_bf16 v[44:47], a[60:63], a[84:87], v[44:47]\n\t"
    "ds_read_b128 a[76:79], v237 offset:34816\n\t"
    "v_mul_f32 v161, v156, v153\n\t"
    "v_mfma_f32_16x16x32_bf16 v[44:47], a[56:59], a[80:83], v[44:47]\n\t"
    "v_fma_f32 v160, -v157, v153, v160\n\t"
    "v_fmac_f32 v161, v157, v152\n\t"
    "v_mfma_f32_16x16x32_bf16 v[60:63], a[56:59], a[88:91], v[60:63]\n\t"
    "ds_read_b128 a[72:75], v236 offset:34816\n\t"
    "ds_read_b64_tr_b16 a[20:21], v242 offset:4096\n\t"
    "v_mfma_f32_16x16x32_bf16 v[60:63], a[60:63], a[92:95], v[60:63]\n\t"
    "s_add_u32 m0, s75, 0x0\n\t"
    "v_mul_f32 v162, v156, v154\n\t"
    "v_mfma_f32_16x16x32_bf16 v[56:59], a[52:55], a[92:95], v[56:59]\n\t"
    "buffer_load_dwordx4 v224, s[36:39], s48 offen offset:0 lds\n\t"
    "v_mfma_f32_16x16x32_bf16 v[56:59], a[48:51], a[88:91], v[56:59]\n\t"
    "ds_read_b64_tr_b16 a[10:11], v241 offset:512\n\t"
    "ds_read_b64_tr_b16 a[30:31], v243 offset:4608\n\t"
    "v_mfma_f32_16x16x32_bf16 v[52:55], a[40:43], a[88:91], v[52:55]\n\t"
    "ds_read_b64_tr_b16 a[16:17], v242 offset:0\n\t"
    "v_mul_f32 v163, v156, v155\n\t"
    "v_mfma_f32_16x16x32_bf16 v[52:55], a[44:47], a[92:95], v[52:55]\n\t"
    "ds_read_b128 a[80:83], v236 offset:36864\n\t"
    "ds_read_b128 a[84:87], v237 offset:36864\n\t"
    "v_mfma_f32_16x16x32_bf16 v[48:51], a[36:39], a[92:95], v[48:51]\n\t"
    "buffer_load_dwordx4 v225, s[36:39], s48 offen offset:1024 lds\n\t"
    "v_mfma_f32_16x16x32_bf16 v[48:51], a[32:35], a[88:91], v[48:51]\n\t"
    "ds_read_b128 a[88:91], v236 offset:38912\n\t"
    "v_fma_f32 v162, -v157, v155, v162\n\t"
    "v_mfma_f32_16x16x32_bf16 v[64:67], a[32:35], a[96:99], v[64:67]\n\t"
    "v_fmac_f32 v163, v157, v154\n\t"
    "v_mul_f32 v174, v150, v204\n\t"
    "v_mfma_f32_16x16x32_bf16 v[64:67], a[36:39], a[100:103], v[64:67]\n\t"
    "buffer_load_dwordx4 v226, s[36:39], s48 offen offset:2048 lds\n\t"
    "v_mfma_f32_16x16x32_bf16 v[68:71], a[44:47], a[100:103], v[68:71]\n\t"
    "v_mul_f32 v175, v150, v205\n\t"
    "v_mfma_f32_16x16x32_bf16 v[68:71], a[40:43], a[96:99], v[68:71]\n\t"
    "ds_read_b64_tr_b16 a[24:25], v243 offset:0\n\t"
    "v_fma_f32 v174, -v151, v205, v174\n\t"
    "v_mfma_f32_16x16x32_bf16 v[72:75], a[48:51], a[96:99], v[72:75]\n\t"
    "v_fmac_f32 v175, v151, v204\n\t"
    "v_mul_f32 v176, v152, v204\n\t"
    "v_mfma_f32_16x16x32_bf16 v[72:75], a[52:55], a[100:103], v[72:75]\n\t"
    "v_mul_f32 v177, v152, v205\n\t"
    "v_mfma_f32_16x16x32_bf16 v[76:79], a[60:63], a[100:103], v[76:79]\n\t"
    "buffer_load_dwordx4 v227, s[36:39], s48 offen offset:3072 lds\n\t"
    "v_mfma_f32_16x16x32_bf16 v[76:79], a[56:59], a[96:99], v[76:79]\n\t"
    "v_fma_f32 v176, -v153, v205, v176\n\t"
    "v_fmac_f32 v177, v153, v204\n\t"
    "v_mfma_f32_16x16x32_bf16 v[92:95], a[56:59], a[104:107], v[92:95]\n\t"
    "ds_read_b64_tr_b16 a[12:13], v241 offset:4096\n\t"
    "v_mul_f32 v178, v154, v204\n\t"
    "v_mfma_f32_16x16x32_bf16 v[92:95], a[60:63], a[108:111], v[92:95]\n\t"
    "ds_read_b64_tr_b16 a[0:1], v240 offset:0\n\t"
    "s_add_u32 m0, s75, 0x1000\n\t"
    "v_mfma_f32_16x16x32_bf16 v[88:91], a[52:55], a[108:111], v[88:91]\n\t"
    "ds_read_b128 a[92:95], v237 offset:38912\n\t"
    "v_mul_f32 v179, v154, v205\n\t"
    "v_mfma_f32_16x16x32_bf16 v[88:91], a[48:51], a[104:107], v[88:91]\n\t"
    "v_fma_f32 v178, -v155, v205, v178\n\t"
    "v_fmac_f32 v179, v155, v204\n\t"
    "v_mfma_f32_16x16x32_bf16 v[84:87], a[40:43], a[104:107], v[84:87]\n\t"
    "v_mul_f32 v180, v156, v204\n\t"
    "v_mul_f32 v181, v156, v205\n\t"
    "v_mfma_f32_16x16x32_bf16 v[84:87], a[44:47], a[108:111], v[84:87]\n\t"
    "buffer_load_dwordx4 v228, s[36:39], s48 offen offset:0 lds\n\t"
    "v_mfma_f32_16x16x32_bf16 v[80:83], a[36:39], a[108:111], v[80:83]\n\t"
    "v_fma_f32 v180, -v157, v205, v180\n\t"
    "v_fmac_f32 v181, v157, v204\n\t"
    "v_mfma_f32_16x16x32_bf16 v[80:83], a[32:35], a[104:107], v[80:83]\n\t"
    "ds_read_b128 a[96:99], v236 offset:40960\n\t"
    "ds_read_b64_tr_b16 a[14:15], v241 offset:4608\n\t"
    "v_mfma_f32_16x16x32_bf16 v[96:99], a[32:35], a[112:115], v[96:99]\n\t"
    "ds_read_b64_tr_b16 a[8:9], v241 offset:0\n\t"
    "v_mul_f32 v182, v158, v204\n\t"
    "v_mfma_f32_16x16x32_bf16 v[96:99], a[36:39], a[116:119], v[96:99]\n\t"
    "buffer_load_dwordx4 v229, s[36:39], s48 offen offset:1024 lds\n\t"
    "v_mfma_f32_16x16x32_bf16 v[100:103], a[44:47], a[116:119], v[100:103]\n\t"
    "ds_read_b128 a[100:103], v237 offset:40960\n\t"
    "ds_read_b128 a[104:107], v236 offset:43008\n\t"
    "v_mfma_f32_16x16x32_bf16 v[100:103], a[40:43], a[112:115], v[100:103]\n\t"
    "ds_read_b64_tr_b16 a[22:23], v242 offset:4608\n\t"
    "ds_read_b64_tr_b16 a[26:27], v243 offset:512\n\t"
    "v_mfma_f32_16x16x32_bf16 v[104:107], a[48:51], a[112:115], v[104:107]\n\t"
    "ds_read_b64_tr_b16 a[4:5], v240 offset:4096\n\t"
    "ds_read_b64_tr_b16 a[6:7], v240 offset:4608\n\t"
    "v_mfma_f32_16x16x32_bf16 v[104:107], a[52:55], a[116:119], v[104:107]\n\t"
    "buffer_load_dwordx4 v230, s[36:39], s48 offen offset:2048 lds\n\t"
    "v_mfma_f32_16x16x32_bf16 v[108:111], a[60:63], a[116:119], v[108:111]\n\t"
    "ds_read_b128 a[108:111], v237 offset:43008\n\t"
    "v_mfma_f32_16x16x32_bf16 v[108:111], a[56:59], a[112:115], v[108:111]\n\t"
    "ds_read_b128 a[112:115], v236 offset:45056\n\t"
    "v_mul_f32 v183, v158, v205\n\t"
    "v_mfma_f32_16x16x32_bf16 v[124:127], a[56:59], a[120:123], v[124:127]\n\t"
    "ds_read_b64_tr_b16 a[28:29], v243 offset:4096\n\t"
    "v_fma_f32 v182, -v159, v205, v182\n\t"
    "v_mfma_f32_16x16x32_bf16 v[124:127], a[60:63], a[124:127], v[124:127]\n\t"
    "v_fmac_f32 v183, v159, v204\n\t"
    "v_mul_f32 v184, v160, v204\n\t"
    "v_mfma_f32_16x16x32_bf16 v[120:123], a[52:55], a[124:127], v[120:123]\n\t"
    "buffer_load_dwordx4 v231, s[36:39], s48 offen offset:3072 lds\n\t"
    "v_mfma_f32_16x16x32_bf16 v[120:123], a[48:51], a[120:123], v[120:123]\n\t"
    "ds_read_b128 a[116:119], v237 offset:45056\n\t"
    "v_mul_f32 v185, v160, v205\n\t"
    "v_mfma_f32_16x16x32_bf16 v[116:119], a[40:43], a[120:123], v[116:119]\n\t"
    "s_add_u32 s48, s48, 128\n\t"
    "v_fma_f32 v184, -v161, v205, v184\n\t"
    "v_mfma_f32_16x16x32_bf16 v[116:119], a[44:47], a[124:127], v[116:119]\n\t"
    "v_fmac_f32 v185, v161, v204\n\t"
    "v_mul_f32 v186, v162, v204\n\t"
    "v_mfma_f32_16x16x32_bf16 v[112:115], a[36:39], a[124:127], v[112:115]\n\t"
    "ds_read_b128 a[124:127], v237 offset:47104\n\t"
    "v_mfma_f32_16x16x32_bf16 v[112:115], a[32:35], a[120:123], v[112:115]\n\t"
    "ds_read_b128 a[120:123], v236 offset:47104\n\t"
    "v_mul_f32 v187, v162, v205\n\t"
    "s_waitcnt lgkmcnt(5)\n\t"
    "v_mfma_f32_16x16x32_bf16 v[0:3], a[0:3], a[64:67], v[0:3]\n\t"
    "v_fma_f32 v186, -v163, v205, v186\n\t"
    "v_mfma_f32_16x16x32_bf16 v[0:3], a[4:7], a[68:71], v[0:3]\n\t"
    "v_fmac_f32 v187, v163, v204\n\t"
    "v_cvt_pk_f16_f32 v164, v148, v150\n\t"
    "v_mfma_f32_16x16x32_bf16 v[4:7], a[12:15], a[68:71], v[4:7]\n\t"
    "s_and_b32 s48, s48, 1023\n\t"
    "s_lshl_b32 s49, s48, 12\n\t"
    "v_mfma_f32_16x16x32_bf16 v[4:7], a[8:11], a[64:67], v[4:7]\n\t"
    "s_add_u32 m0, s52, 0x0\n\t"
    "v_cvt_pk_f16_f32 v165, v152, v154\n\t"
    "v_mfma_f32_16x16x32_bf16 v[8:11], a[16:19], a[64:67], v[8:11]\n\t"
    "v_cvt_pk_f16_f32 v166, v156, v158\n\t"
    "v_cvt_pk_f16_f32 v167, v160, v162\n\t"
    "v_mfma_f32_16x16x32_bf16 v[8:11], a[20:23], a[68:71], v[8:11]\n\t"
    "s_waitcnt vmcnt(12) lgkmcnt(0)\n\t"
    "s_barrier\n\t"
    "s_cmp_lg_u64 s[86:87], 0\n\t"
    "s_cbranch_scc1 L_hsM_%=\n\t"
    "global_load_dword v252, v245, s[84:85]\n\t"
    "L_hrM_%=:\n\t"
    "v_mfma_f32_16x16x32_bf16 v[12:15], a[28:31], a[68:71], v[12:15]\n\t"
    "buffer_load_dwordx4 v232, s[40:43], s49 offen offset:0 lds\n\t"
    "v_mfma_f32_16x16x32_bf16 v[12:15], a[24:27], a[64:67], v[12:15]\n\t"
    "ds_write_b128 v132, v[164:167] offset:0\n\t"
    "v_mfma_f32_16x16x32_bf16 v[28:31], a[24:27], a[72:75], v[28:31]\n\t"
    "v_cvt_pk_f16_f32 v168, v204, v174\n\t"
    "v_cvt_pk_f16_f32 v169, v176, v178\n\t"
    "v_mfma_f32_16x16x32_bf16 v[28:31], a[28:31], a[76:79], v[28:31]\n\t"
    "ds_read_b128 a[68:71], v239 offset:0\n\t"
    "v_cvt_pk_f16_f32 v170, v180, v182\n\t"
    "v_mfma_f32_16x16x32_bf16 v[24:27], a[20:23], a[76:79], v[24:27]\n\t"
    "ds_read_b64_tr_b16 a[42:43], v241 offset:16896\n\t"
    "v_cvt_pk_f16_f32 v171, v184, v186\n\t"
    "v_mfma_f32_16x16x32_bf16 v[24:27], a[16:19], a[72:75], v[24:27]\n\t"
    "buffer_load_dwordx4 v233, s[40:43], s49 offen offset:1024 lds\n\t"
    "v_mfma_f32_16x16x32_bf16 v[20:23], a[8:11], a[72:75], v[20:23]\n\t"
    "ds_write_b128 v133, v[168:171] offset:0\n\t"
    "v_mfma_f32_16x16x32_bf16 v[20:23], a[12:15], a[76:79], v[20:23]\n\t"
    "ds_read_b128 a[64:67], v238 offset:0\n\t"
    "v_mfma_f32_16x16x32_bf16 v[16:19], a[4:7], a[76:79], v[16:19]\n\t"
    "ds_read_b64_tr_b16 a[34:35], v240 offset:16896\n\t"
    "v_mfma_f32_16x16x32_bf16 v[16:19], a[0:3], a[72:75], v[16:19]\n\t"
    "v_cvt_pk_f16_f32 v164, v149, v151\n\t"
    "v_cvt_pk_f16_f32 v165, v153, v155\n\t"
    "v_mfma_f32_16x16x32_bf16 v[32:35], a[0:3], a[80:83], v[32:35]\n\t"
    "ds_read_b64_tr_b16 a[38:39], v240 offset:20992\n\t"
    "v_cvt_pk_f16_f32 v166, v157, v159\n\t"
    "v_mfma_f32_16x16x32_bf16 v[32:35], a[4:7], a[84:87], v[32:35]\n\t"
    "buffer_load_dwordx4 v234, s[40:43], s49 offen offset:2048 lds\n\t"
    "v_mfma_f32_16x16x32_bf16 v[36:39], a[12:15], a[84:87], v[36:39]\n\t"
    "ds_read_b128 a[76:79], v239 offset:2048\n\t"
    "v_cvt_pk_f16_f32 v167, v161, v163\n\t"
    "v_mfma_f32_16x16x32_bf16 v[36:39], a[8:11], a[80:83], v[36:39]\n\t"
    "ds_write_b128 v132, v[164:167] offset:2048\n\t"
    "v_mfma_f32_16x16x32_bf16 v[40:43], a[16:19], a[80:83], v[40:43]\n\t"
    "ds_read_b128 a[72:75], v238 offset:2048\n\t"
    "v_cvt_pk_f16_f32 v168, v205, v175\n\t"
    "v_mfma_f32_16x16x32_bf16 v[40:43], a[20:23], a[84:87], v[40:43]\n\t"
    "buffer_load_dwordx4 v235, s[40:43], s49 offen offset:3072 lds\n\t"
    "v_mfma_f32_16x16x32_bf16 v[44:47], a[28:31], a[84:87], v[44:47]\n\t"
    "v_cvt_pk_f16_f32 v169, v177, v179\n\t"
    "v_mfma_f32_16x16x32_bf16 v[44:47], a[24:27], a[80:83], v[44:47]\n\t"
    "v_cvt_pk_f16_f32 v170, v181, v183\n\t"
    "v_cvt_pk_f16_f32 v171, v185, v187\n\t"
    "v_mfma_f32_16x16x32_bf16 v[60:63], a[24:27], a[88:91], v[60:63]\n\t"
    "ds_write_b128 v133, v[168:171] offset:2048\n\t"
    "v_mfma_f32_16x16x32_bf16 v[60:63], a[28:31], a[92:95], v[60:63]\n\t"
    "ds_read_b64_tr_b16 a[144:145], v134 offset:0\n\t"
    "ds_read_b64_tr_b16 a[146:147], v134 offset:512\n\t"
    "v_mfma_f32_16x16x32_bf16 v[56:59], a[20:23], a[92:95], v[56:59]\n\t"
    "ds_read_b64_tr_b16 a[148:149], v134 offset:1024\n\t"
    "ds_read_b64_tr_b16 a[150:151], v134 offset:1536\n\t"
    "v_mfma_f32_16x16x32_bf16 v[56:59], a[16:19], a[88:91], v[56:59]\n\t"
    "s_add_u32 m0, s75, 0x8000\n\t"
    "v_mfma_f32_16x16x32_bf16 v[52:55], a[8:11], a[88:91], v[52:55]\n\t"
    "buffer_load_dwordx4 v224, s[36:39], s48 offen offset:0 lds\n\t"
    "v_mfma_f32_16x16x32_bf16 v[52:55], a[12:15], a[92:95], v[52:55]\n\t"
    "buffer_load_dwordx4 v225, s[36:39], s48 offen offset:1024 lds\n\t"
    "v_mfma_f32_16x16x32_bf16 v[48:51], a[4:7], a[92:95], v[48:51]\n\t"
    "ds_read_b128 a[92:95], v239 offset:6144\n\t"
    "ds_read_b64_tr_b16 a[62:63], v243 offset:20992\n\t"
    "v_mfma_f32_16x16x32_bf16 v[48:51], a[0:3], a[88:91], v[48:51]\n\t"
    "ds_read_b64_tr_b16 a[56:57], v243 offset:16384\n\t"
    "ds_read_b64_tr_b16 a[152:153], v134 offset:2048\n\t"
    "v_mfma_f32_16x16x32_bf16 v[64:67], a[0:3], a[96:99], v[64:67]\n\t"
    "ds_read_b128 a[84:87], v239 offset:4096\n\t"
    "ds_read_b64_tr_b16 a[54:55], v242 offset:20992\n\t"
    "v_mfma_f32_16x16x32_bf16 v[64:67], a[4:7], a[100:103], v[64:67]\n\t"
    "ds_read_b64_tr_b16 a[46:47], v241 offset:20992\n\t"
    "ds_read_b64_tr_b16 a[154:155], v134 offset:2560\n\t"
    "v_mfma_f32_16x16x32_bf16 v[68:71], a[12:15], a[100:103], v[68:71]\n\t"
    "buffer_load_dwordx4 v226, s[36:39], s48 offen offset:2048 lds\n\t"
    "v_mfma_f32_16x16x32_bf16 v[68:71], a[8:11], a[96:99], v[68:71]\n\t"
    "ds_read_b128 a[80:83], v238 offset:4096\n\t"
    "v_mfma_f32_16x16x32_bf16 v[72:75], a[16:19], a[96:99], v[72:75]\n\t"
    "ds_read_b64_tr_b16 a[156:157], v134 offset:3072\n\t"
    "ds_read_b64_tr_b16 a[158:159], v134 offset:3584\n\t"
    "v_mfma_f32_16x16x32_bf16 v[72:75], a[20:23], a[100:103], v[72:75]\n\t"
    "ds_read_b64_tr_b16 a[44:45], v241 offset:20480\n\t"
    "ds_read_b64_tr_b16 a[58:59], v243 offset:16896\n\t"
    "v_mfma_f32_16x16x32_bf16 v[76:79], a[28:31], a[100:103], v[76:79]\n\t"
    "buffer_load_dwordx4 v227, s[36:39], s48 offen offset:3072 lds\n\t"
    "v_mfma_f32_16x16x32_bf16 v[76:79], a[24:27], a[96:99], v[76:79]\n\t"
    "ds_read_b128 a[88:91], v238 offset:6144\n\t"
    "v_mul_f32 v208, v206, v204\n\t"
    "v_mfma_f32_16x16x32_bf16 v[92:95], a[24:27], a[104:107], v[92:95]\n\t"
    "v_mul_f32 v209, v206, v205\n\t"
    "v_fma_f32 v208, -v207, v205, v208\n\t"
    "v_mfma_f32_16x16x32_bf16 v[92:95], a[28:31], a[108:111], v[92:95]\n\t"
    "ds_read_b64_tr_b16 a[40:41], v241 offset:16384\n\t"
    "ds_read_b64_tr_b16 a[60:61], v243 offset:20480\n\t"
    "v_mfma_f32_16x16x32_bf16 v[88:91], a[20:23], a[108:111], v[88:91]\n\t"
    "ds_read_b128 a[100:103], v239 offset:8192\n\t"
    "ds_read_b64_tr_b16 a[36:37], v240 offset:20480\n\t"
    "v_mfma_f32_16x16x32_bf16 v[88:91], a[16:19], a[104:107], v[88:91]\n\t"
    "s_add_u32 m0, s75, 0x9000\n\t"
    "v_fmac_f32 v209, v207, v204\n\t"
    "v_mfma_f32_16x16x32_bf16 v[84:87], a[8:11], a[104:107], v[84:87]\n\t"
    "buffer_load_dwordx4 v228, s[36:39], s48 offen offset:0 lds\n\t"
    "v_mfma_f32_16x16x32_bf16 v[84:87], a[12:15], a[108:111], v[84:87]\n\t"
    "v_mul_f32 v210, v206, v206\n\t"
    "v_mul_f32 v211, v206, v207\n\t"
    "v_mfma_f32_16x16x32_bf16 v[80:83], a[4:7], a[108:111], v[80:83]\n\t"
    "buffer_load_dwordx4 v229, s[36:39], s48 offen offset:1024 lds\n\t"
    "v_mfma_f32_16x16x32_bf16 v[80:83], a[0:3], a[104:107], v[80:83]\n\t"
    "v_fma_f32 v210, -v207, v207, v210\n\t"
    "v_fmac_f32 v211, v207, v206\n\t"
    "v_mfma_f32_16x16x32_bf16 v[96:99], a[0:3], a[112:115], v[96:99]\n\t"
    "v_mul_f32 v212, v204, v210\n\t"
    "v_mul_f32 v213, v204, v211\n\t"
    "v_mfma_f32_16x16x32_bf16 v[96:99], a[4:7], a[116:119], v[96:99]\n\t"
    "ds_read_b64_tr_b16 a[48:49], v242 offset:16384\n\t"
    "v_fma_f32 v212, -v205, v211, v212\n\t"
    "v_mfma_f32_16x16x32_bf16 v[100:103], a[12:15], a[116:119], v[100:103]\n\t"
    "ds_read_b128 a[108:111], v239 offset:10240\n\t"
    "v_fmac_f32 v213, v205, v210\n\t"
    "v_mfma_f32_16x16x32_bf16 v[100:103], a[8:11], a[112:115], v[100:103]\n\t"
    "ds_read_b128 a[96:99], v238 offset:8192\n\t"
    "v_mul_f32 v214, v206, v210\n\t"
    "v_mfma_f32_16x16x32_bf16 v[104:107], a[16:19], a[112:115], v[104:107]\n\t"
    "ds_read_b64_tr_b16 a[52:53], v242 offset:20480\n\t"
    "v_mul_f32 v215, v206, v211\n\t"
    "v_mfma_f32_16x16x32_bf16 v[104:107], a[20:23], a[116:119], v[104:107]\n\t"
    "ds_read_b64_tr_b16 a[50:51], v242 offset:16896\n\t"
    "v_mfma_f32_16x16x32_bf16 v[108:111], a[28:31], a[116:119], v[108:111]\n\t"
    "buffer_load_dwordx4 v230, s[36:39], s48 offen offset:2048 lds\n\t"
    "v_mfma_f32_16x16x32_bf16 v[108:111], a[24:27], a[112:115], v[108:111]\n\t"
    "buffer_load_dwordx4 v231, s[36:39], s48 offen offset:3072 lds\n\t"
    "v_mfma_f32_16x16x32_bf16 v[124:127], a[24:27], a[120:123], v[124:127]\n\t"
    "ds_read_b128 a[104:107], v238 offset:10240\n\t"
    "ds_read_b64_tr_b16 a[32:33], v240 offset:16384\n\t"
    "v_mfma_f32_16x16x32_bf16 v[124:127], a[28:31], a[124:127], v[124:127]\n\t"
    "v_fma_f32 v214, -v207, v211, v214\n\t"
    "v_fmac_f32 v215, v207, v210\n\t"
    "v_mfma_f32_16x16x32_bf16 v[120:123], a[20:23], a[124:127], v[120:123]\n\t"
    "v_mul_f32 v216, v208, v210\n\t"
    "v_mul_f32 v217, v208, v211\n\t"
    "v_mfma_f32_16x16x32_bf16 v[120:123], a[16:19], a[120:123], v[120:123]\n\t"
    "Lis2_%=:\n\t"
    "s_cbranch_execz Lis6_%=\n\t"
    "v_fma_f32 v216, -v209, v211, v216\n\t"
    "v_fmac_f32 v217, v209, v210\n\t"
    "v_mfma_f32_16x16x32_bf16 v[116:119], a[8:11], a[120:123], v[116:119]\n\t"
    "ds_read_b128 a[112:115], v238 offset:12288\n\t"
    "v_mul_f32 v218, v210, v210\n\t"
    "v_mfma_f32_16x16x32_bf16 v[116:119], a[12:15], a[124:127], v[116:119]\n\t"
    "ds_read_b128 a[116:119], v239 offset:12288\n\t"
    "v_mul_f32 v219, v210, v211\n\t"
    "v_mfma_f32_16x16x32_bf16 v[112:115], a[4:7], a[124:127], v[112:115]\n\t"
    "s_mov_b64 s[40:41], s[72:73]\n\t"
    "v_fma_f32 v218, -v211, v211, v218\n\t"
    "v_mfma_f32_16x16x32_bf16 v[112:115], a[0:3], a[120:123], v[112:115]\n\t"
    "ds_read_b128 a[120:123], v238 offset:14336\n\t"
    "ds_read_b128 a[124:127], v239 offset:14336\n\t"
    "s_waitcnt lgkmcnt(4)\n\t"
    "v_mfma_f32_16x16x32_bf16 v[0:3], a[32:35], a[64:67], v[0:3]\n\t"
    "v_fmac_f32 v219, v211, v210\n\t"
    "v_mfma_f32_16x16x32_bf16 v[0:3], a[36:39], a[68:71], v[0:3]\n\t"
    "v_mul_f32 v174, v152, v206\n\t"
    "v_mul_f32 v175, v152, v207\n\t"
    "v_mfma_f32_16x16x32_bf16 v[4:7], a[44:47], a[68:71], v[4:7]\n\t"
    "v_fma_f32 v174, -v153, v207, v174\n\t"
    "v_fmac_f32 v175, v153, v206\n\t"
    "v_mfma_f32_16x16x32_bf16 v[4:7], a[40:43], a[64:67], v[4:7]\n\t"
    "v_mul_f32 v176, v156, v206\n\t"
    "v_mul_f32 v177, v156, v207\n\t"
    "v_mfma_f32_16x16x32_bf16 v[8:11], a[48:51], a[64:67], v[8:11]\n\t"
    "v_fma_f32 v176, -v157, v207, v176\n\t"
    "v_mfma_f32_16x16x32_bf16 v[8:11], a[52:55], a[68:71], v[8:11]\n\t"
    "s_waitcnt vmcnt(13) lgkmcnt(0)\n\t"
    "s_barrier\n\t"
    "v_mfma_f32_16x16x32_bf16 v[12:15], a[60:63], a[68:71], v[12:15]\n\t"
    "s_add_u32 m0, s52, 0x4000\n\t"
    "v_fmac_f32 v177, v157, v206\n\t"
    "v_mfma_f32_16x16x32_bf16 v[12:15], a[56:59], a[64:67], v[12:15]\n\t"
    "v_mul_f32 v178, v160, v206\n\t"
    "v_mul_f32 v179, v160, v207\n\t"
    "v_mfma_f32_16x16x32_bf16 v[28:31], a[56:59], a[72:75], v[28:31]\n\t"
    "v_fma_f32 v178, -v161, v207, v178\n\t"
    "v_mfma_f32_16x16x32_bf16 v[28:31], a[60:63], a[76:79], v[28:31]\n\t"
    "buffer_load_dwordx4 v232, s[40:43], s49 offen offset:0 lds\n\t"
    "v_mfma_f32_16x16x32_bf16 v[24:27], a[52:55], a[76:79], v[24:27]\n\t"
    "ds_read_b64_tr_b16 a[10:11], v241 offset:33280\n\t"
    "v_fmac_f32 v179, v161, v206\n\t"
    "v_mfma_f32_16x16x32_bf16 v[24:27], a[48:51], a[72:75], v[24:27]\n\t"
    "buffer_load_dwordx4 v233, s[40:43], s49 offen offset:1024 lds\n\t"
    "v_mfma_f32_16x16x32_bf16 v[20:23], a[40:43], a[72:75], v[20:23]\n\t"
    "ds_read_b64_tr_b16 a[30:31], v243 offset:37376\n\t"
    "v_mul_f32 v182, v152, v208\n\t"
    "v_mfma_f32_16x16x32_bf16 v[20:23], a[44:47], a[76:79], v[20:23]\n\t"
    "v_mul_f32 v183, v152, v209\n\t"
    "v_mfma_f32_16x16x32_bf16 v[16:19], a[36:39], a[76:79], v[16:19]\n\t"
    "ds_read_b128 a[68:71], v237 offset:0\n\t"
    "v_mfma_f32_16x16x32_bf16 v[16:19], a[32:35], a[72:75], v[16:19]\n\t"
    "v_fma_f32 v182, -v153, v209, v182\n\t"
    "v_fmac_f32 v183, v153, v208\n\t"
    "v_mfma_f32_16x16x32_bf16 v[32:35], a[32:35], a[80:83], v[32:35]\n\t"
    "ds_read_b128 a[72:75], v236 offset:2048\n\t"
    "v_mul_f32 v184, v156, v208\n\t"
    "v_mfma_f32_16x16x32_bf16 v[32:35], a[36:39], a[84:87], v[32:35]\n\t"
    "buffer_load_dwordx4 v234, s[40:43], s49 offen offset:2048 lds\n\t"
    "v_mfma_f32_16x16x32_bf16 v[36:39], a[44:47], a[84:87], v[36:39]\n\t"
    "ds_read_b128 a[64:67], v236 offset:0\n\t"
    "ds_read_b64_tr_b16 a[4:5], v240 offset:36864\n\t"
    "v_mfma_f32_16x16x32_bf16 v[36:39], a[40:43], a[80:83], v[36:39]\n\t"
    "buffer_load_dwordx4 v235, s[40:43], s49 offen offset:3072 lds\n\t"
    "v_mfma_f32_16x16x32_bf16 v[40:43], a[48:51], a[80:83], v[40:43]\n\t"
    "v_mul_f32 v185, v156, v209\n\t"
    "v_fma_f32 v184, -v157, v209, v184\n\t"
    "v_mfma_f32_16x16x32_bf16 v[40:43], a[52:55], a[84:87], v[40:43]\n\t"
    "v_fmac_f32 v185, v157, v208\n\t"
    "v_mfma_f32_16x16x32_bf16 v[44:47], a[60:63], a[84:87], v[44:47]\n\t"
    "v_mul_f32 v186, v160, v208\n\t"
    "v_mul_f32 v187, v160, v209\n\t"
    "v_mfma_f32_16x16x32_bf16 v[44:47], a[56:59], a[80:83], v[44:47]\n\t"
    "ds_read_b128 a[76:79], v237 offset:2048\n\t"
    "ds_read_b128 a[84:87], v237 offset:4096\n\t"
    "v_mfma_f32_16x16x32_bf16 v[60:63], a[56:59], a[88:91], v[60:63]\n\t"
    "ds_read_b64_tr_b16 a[0:1], v240 offset:32768\n\t"
    "v_fma_f32 v186, -v161, v209, v186\n\t"
    "v_mfma_f32_16x16x32_bf16 v[60:63], a[60:63], a[92:95], v[60:63]\n\t"
    "ds_read_b64_tr_b16 a[20:21], v242 offset:36864\n\t"
    "ds_read_b64_tr_b16 a[24:25], v243 offset:32768\n\t"
    "v_mfma_f32_16x16x32_bf16 v[56:59], a[52:55], a[92:95], v[56:59]\n\t"
    "v_fmac_f32 v187, v161, v208\n\t"
    "v_mul_f32 v188, v156, v210\n\t"
    "v_mfma_f32_16x16x32_bf16 v[56:59], a[48:51], a[88:91], v[56:59]\n\t"
    "v_mul_f32 v189, v156, v211\n\t"
    "v_fma_f32 v188, -v157, v211, v188\n\t"
    "v_mfma_f32_16x16x32_bf16 v[52:55], a[40:43], a[88:91], v[52:55]\n\t"
    "v_fmac_f32 v189, v157, v210\n\t"
    "v_mul_f32 v190, v156, v212\n\t"
    "v_mfma_f32_16x16x32_bf16 v[52:55], a[44:47], a[92:95], v[52:55]\n\t"
    "v_mul_f32 v191, v156, v213\n\t"
    "v_fma_f32 v190, -v157, v213, v190\n\t"
    "v_mfma_f32_16x16x32_bf16 v[48:51], a[36:39], a[92:95], v[48:51]\n\t"
    "ds_read_b128 a[80:83], v236 offset:4096\n\t"
    "v_fmac_f32 v191, v157, v212\n\t"
    "v_mfma_f32_16x16x32_bf16 v[48:51], a[32:35], a[88:91], v[48:51]\n\t"
    "ds_read_b128 a[92:95], v237 offset:6144\n\t"
    "v_mfma_f32_16x16x32_bf16 v[64:67], a[32:35], a[96:99], v[64:67]\n\t"
    "v_mul_f32 v192, v156, v214\n\t"
    "v_mul_f32 v193, v156, v215\n\t"
    "v_mfma_f32_16x16x32_bf16 v[64:67], a[36:39], a[100:103], v[64:67]\n\t"
    "ds_read_b64_tr_b16 a[8:9], v241 offset:32768\n\t"
    "v_fma_f32 v192, -v157, v215, v192\n\t"
    "v_mfma_f32_16x16x32_bf16 v[68:71], a[44:47], a[100:103], v[68:71]\n\t"
    "v_fmac_f32 v193, v157, v214\n\t"
    "v_mul_f32 v194, v156, v216\n\t"
    "v_mfma_f32_16x16x32_bf16 v[68:71], a[40:43], a[96:99], v[68:71]\n\t"
    "ds_read_b128 a[88:91], v236 offset:6144\n\t"
    "v_mul_f32 v195, v156, v217\n\t"
    "v_mfma_f32_16x16x32_bf16 v[72:75], a[48:51], a[96:99], v[72:75]\n\t"
    "v_fma_f32 v194, -v157, v217, v194\n\t"
    "v_fmac_f32 v195, v157, v216\n\t"
    "v_mfma_f32_16x16x32_bf16 v[72:75], a[52:55], a[100:103], v[72:75]\n\t"
    "v_cvt_pk_f16_f32 v164, v206, v174\n\t"
    "v_mfma_f32_16x16x32_bf16 v[76:79], a[60:63], a[100:103], v[76:79]\n\t"
    "global_load_dwordx4 a[232:235], v244, s[22:23] offset:0\n\t"
    "v_mfma_f32_16x16x32_bf16 v[76:79], a[56:59], a[96:99], v[76:79]\n\t"
    "ds_read_b64_tr_b16 a[16:17], v242 offset:32768\n\t"
    "v_mfma_f32_16x16x32_bf16 v[92:95], a[56:59], a[104:107], v[92:95]\n\t"
    "global_load_dwordx4 a[232:235], v244, s[22:23] offset:1024\n\t"
    "v_mfma_f32_16x16x32_bf16 v[92:95], a[60:63], a[108:111], v[92:95]\n\t"
    "global_load_dwordx4 a[232:235], v220, s[26:27]\n\t"
    "v_add_u32 v220, 0x8000, v220\n\t"
    "v_mfma_f32_16x16x32_bf16 v[88:91], a[52:55], a[108:111], v[88:91]\n\t"
    "global_load_dwordx4 a[232:235], v220, s[26:27]\n\t"
    "v_add_u32 v220, 0x8000, v220\n\t"
    "v_mfma_f32_16x16x32_bf16 v[88:91], a[48:51], a[104:107], v[88:91]\n\t"
    "global_load_dwordx4 a[232:235], v220, s[26:27]\n\t"
    "v_add_u32 v220, 0x8000, v220\n\t"
    "v_mfma_f32_16x16x32_bf16 v[84:87], a[40:43], a[104:107], v[84:87]\n\t"
    "v_cvt_pk_f16_f32 v165, v176, v178\n\t"
    "v_cvt_pk_f16_f32 v166, v208, v182\n\t"
    "v_mfma_f32_16x16x32_bf16 v[84:87], a[44:47], a[108:111], v[84:87]\n\t"
    "global_load_dwordx4 a[232:235], v220, s[26:27]\n\t"
    "v_add_u32 v220, 0x8000, v220\n\t"
    "v_mfma_f32_16x16x32_bf16 v[80:83], a[36:39], a[108:111], v[80:83]\n\t"
    "ds_read_b64_tr_b16 a[26:27], v243 offset:33280\n\t"
    "v_cvt_pk_f16_f32 v167, v184, v186\n\t"
    "v_mfma_f32_16x16x32_bf16 v[80:83], a[32:35], a[104:107], v[80:83]\n\t"
    "ds_read_b128 a[100:103], v237 offset:8192\n\t"
    "v_mfma_f32_16x16x32_bf16 v[96:99], a[32:35], a[112:115], v[96:99]\n\t"
    "ds_read_b64_tr_b16 a[2:3], v240 offset:33280\n\t"
    "ds_read_b64_tr_b16 a[6:7], v240 offset:37376\n\t"
    "v_mfma_f32_16x16x32_bf16 v[96:99], a[36:39], a[116:119], v[96:99]\n\t"
    "ds_read_b128 a[108:111], v237 offset:10240\n\t"
    "v_mfma_f32_16x16x32_bf16 v[100:103], a[44:47], a[116:119], v[100:103]\n\t"
    "ds_write_b128 v132, v[164:167] offset:0\n\t"
    "v_mfma_f32_16x16x32_bf16 v[100:103], a[40:43], a[112:115], v[100:103]\n\t"
    "ds_read_b128 a[96:99], v236 offset:8192\n\t"
    "ds_read_b64_tr_b16 a[18:19], v242 offset:33280\n\t"
    "v_mfma_f32_16x16x32_bf16 v[104:107], a[48:51], a[112:115], v[104:107]\n\t"
    "ds_read_b64_tr_b16 a[28:29], v243 offset:36864\n\t"
    "v_cvt_pk_f16_f32 v168, v210, v188\n\t"
    "v_mfma_f32_16x16x32_bf16 v[104:107], a[52:55], a[116:119], v[104:107]\n\t"
    "ds_read_b64_tr_b16 a[12:13], v241 offset:36864\n\t"
    "v_cvt_pk_f16_f32 v169, v212, v190\n\t"
    "v_mfma_f32_16x16x32_bf16 v[108:111], a[60:63], a[116:119], v[108:111]\n\t"
    "ds_read_b128 a[116:119], v237 offset:12288\n\t"
    "v_cvt_pk_f16_f32 v170, v214, v192\n\t"
    "v_mfma_f32_16x16x32_bf16 v[108:111], a[56:59], a[112:115], v[108:111]\n\t"
    "ds_read_b128 a[104:107], v236 offset:10240\n\t"
    "ds_read_b64_tr_b16 a[14:15], v241 offset:37376\n\t"
    "v_mfma_f32_16x16x32_bf16 v[124:127], a[56:59], a[120:123], v[124:127]\n\t"
    "ds_read_b64_tr_b16 a[22:23], v242 offset:37376\n\t"
    "v_cvt_pk_f16_f32 v171, v216, v194\n\t"
    "v_mfma_f32_16x16x32_bf16 v[124:127], a[60:63], a[124:127], v[124:127]\n\t"
    "ds_write_b128 v133, v[168:171] offset:0\n\t"
    "v_mfma_f32_16x16x32_bf16 v[120:123], a[52:55], a[124:127], v[120:123]\n\t"
    "ds_read_b128 a[112:115], v236 offset:12288\n\t"
    "v_mfma_f32_16x16x32_bf16 v[120:123], a[48:51], a[120:123], v[120:123]\n\t"
    "v_cvt_pk_f16_f32 v164, v207, v175\n\t"
    "v_cvt_pk_f16_f32 v165, v177, v179\n\t"
    "v_mfma_f32_16x16x32_bf16 v[116:119], a[40:43], a[120:123], v[116:119]\n\t"
    "v_mfma_f32_16x16x32_f16 a[192:195], a[144:147], a[128:131], 0\n\t"
    "v_mfma_f32_16x16x32_bf16 v[116:119], a[44:47], a[124:127], v[116:119]\n\t"
    "v_cvt_pk_f16_f32 v166, v209, v183\n\t"
    "v_mfma_f32_16x16x32_bf16 v[112:115], a[36:39], a[124:127], v[112:115]\n\t"
    "ds_read_b128 a[124:127], v237 offset:14336\n\t"
    "v_cvt_pk_f16_f32 v167, v185, v187\n\t"
    "v_mfma_f32_16x16x32_bf16 v[112:115], a[32:35], a[120:123], v[112:115]\n\t"
    "ds_read_b128 a[120:123], v236 offset:14336\n\t"
    "s_sub_u32 s48, s48, 128\n\t"
    "s_waitcnt lgkmcnt(7)\n\t"
    "v_mfma_f32_16x16x32_bf16 v[0:3], a[0:3], a[64:67], v[0:3]\n\t"
    "ds_write_b128 v132, v[164:167] offset:2048\n\t"
    "v_mfma_f32_16x16x32_bf16 v[0:3], a[4:7], a[68:71], v[0:3]\n\t"
    "v_cvt_pk_f16_f32 v168, v211, v189\n\t"
    "v_cvt_pk_f16_f32 v169, v213, v191\n\t"
    "s_waitcnt lgkmcnt(6)\n\t"
    "v_mfma_f32_16x16x32_bf16 v[4:7], a[12:15], a[68:71], v[4:7]\n\t"
    "v_cvt_pk_f16_f32 v170, v215, v193\n\t"
    "v_cvt_pk_f16_f32 v171, v217, v195\n\t"
    "v_mfma_f32_16x16x32_bf16 v[4:7], a[8:11], a[64:67], v[4:7]\n\t"
    "s_and_b32 s48, s48, 1023\n\t"
    "s_lshl_b32 s49, s48, 12\n\t"
    "v_mfma_f32_16x16x32_bf16 v[8:11], a[16:19], a[64:67], v[8:11]\n\t"
    "s_add_u32 m0, s52, 0x8000\n\t"
    "s_waitcnt lgkmcnt(3)\n\t"
    "v_mfma_f32_16x16x32_bf16 v[8:11], a[20:23], a[68:71], v[8:11]\n\t"
    "s_waitcnt vmcnt(10) lgkmcnt(0)\n\t"
    "s_barrier\n\t"
    "v_mfma_f32_16x16x32_bf16 v[12:15], a[28:31], a[68:71], v[12:15]\n\t"
    "ds_write_b128 v133, v[168:171] offset:2048\n\t"
    "v_mfma_f32_16x16x32_bf16 v[12:15], a[24:27], a[64:67], v[12:15]\n\t"
    "ds_read_b128 a[68:71], v237 offset:32768\n\t"
    "ds_read_b64_tr_b16 a[160:161], v134 offset:0\n\t"
    "v_mfma_f32_16x16x32_bf16 v[28:31], a[24:27], a[72:75], v[28:31]\n\t"
    "global_load_dword v252, v245, s[84:85] sc1\n\t"
    "v_mfma_f32_16x16x32_bf16 v[28:31], a[28:31], a[76:79], v[28:31]\n\t"
    "buffer_load_dwordx4 v232, s[40:43], s49 offen offset:0 lds\n\t"
    "v_mfma_f32_16x16x32_bf16 v[24:27], a[20:23], a[76:79], v[24:27]\n\t"
    "buffer_load_dwordx4 v233, s[40:43], s49 offen offset:1024 lds\n\t"
    "v_mfma_f32_16x16x32_bf16 v[24:27], a[16:19], a[72:75], v[24:27]\n\t"
    "ds_read_b64_tr_b16 a[162:163], v134 offset:512\n\t"
    "v_mfma_f32_16x16x32_bf16 v[20:23], a[8:11], a[72:75], v[20:23]\n\t"
    "ds_read_b64_tr_b16 a[164:165], v134 offset:1024\n\t"
    "ds_read_b64_tr_b16 a[166:167], v134 offset:1536\n\t"
    "v_mfma_f32_16x16x32_bf16 v[20:23], a[12:15], a[76:79], v[20:23]\n\t"
    "ds_read_b128 a[64:67], v236 offset:32768\n\t"
    "ds_read_b64_tr_b16 a[56:57], v243 offset:0\n\t"
    "v_mfma_f32_16x16x32_bf16 v[16:19], a[4:7], a[76:79], v[16:19]\n\t"
    "ds_read_b64_tr_b16 a[168:169], v134 offset:2048\n\t"
    "v_mfma_f32_16x16x32_bf16 v[16:19], a[0:3], a[72:75], v[16:19]\n\t"
    "ds_read_b64_tr_b16 a[52:53], v242 offset:4096\n\t"
    "ds_read_b64_tr_b16 a[170:171], v134 offset:2560\n\t"
    "v_mfma_f32_16x16x32_bf16 v[32:35], a[0:3], a[80:83], v[32:35]\n\t"
    "ds_read_b64_tr_b16 a[34:35], v240 offset:512\n\t"
    "ds_read_b64_tr_b16 a[172:173], v134 offset:3072\n\t"
    "v_mfma_f32_16x16x32_bf16 v[32:35], a[4:7], a[84:87], v[32:35]\n\t"
    "buffer_load_dwordx4 v234, s[40:43], s49 offen offset:2048 lds\n\t"
    "v_mfma_f32_16x16x32_bf16 v[36:39], a[12:15], a[84:87], v[36:39]\n\t"
    "ds_read_b64_tr_b16 a[174:175], v134 offset:3584\n\t"
    "v_mfma_f32_16x16x32_bf16 v[36:39], a[8:11], a[80:83], v[36:39]\n\t"
    "v_mul_f32 v196, v212, v210\n\t"
    "v_mfma_f32_16x16x32_bf16 v[40:43], a[16:19], a[80:83], v[40:43]\n\t"
    "ds_read_b128 a[76:79], v237 offset:34816\n\t"
    "v_mul_f32 v197, v212, v211\n\t"
    "v_mfma_f32_16x16x32_bf16 v[40:43], a[20:23], a[84:87], v[40:43]\n\t"
    "v_fma_f32 v196, -v213, v211, v196\n\t"
    "v_fmac_f32 v197, v213, v210\n\t"
    "v_mfma_f32_16x16x32_bf16 v[44:47], a[28:31], a[84:87], v[44:47]\n\t"
    "buffer_load_dwordx4 v235, s[40:43], s49 offen offset:3072 lds\n\t"
    "v_mfma_f32_16x16x32_bf16 v[44:47], a[24:27], a[80:83], v[44:47]\n\t"
    "ds_read_b64_tr_b16 a[46:47], v241 offset:4608\n\t"
    "v_mfma_f32_16x16x32_bf16 v[60:63], a[24:27], a[88:91], v[60:63]\n\t"
    "ds_read_b64_tr_b16 a[38:39], v240 offset:4608\n\t"
    "v_mul_f32 v198, v214, v210\n\t"
    "v_mfma_f32_16x16x32_bf16 v[60:63], a[28:31], a[92:95], v[60:63]\n\t"
    "ds_read_b64_tr_b16 a[50:51], v242 offset:512\n\t"
    "v_mul_f32 v199, v214, v211\n\t"
    "v_mfma_f32_16x16x32_bf16 v[56:59], a[20:23], a[92:95], v[56:59]\n\t"
    "ds_read_b128 a[80:83], v236 offset:36864\n\t"
    "v_fma_f32 v198, -v215, v211, v198\n\t"
    "v_mfma_f32_16x16x32_bf16 v[56:59], a[16:19], a[88:91], v[56:59]\n\t"
    "ds_read_b128 a[72:75], v236 offset:34816\n\t"
    "v_fmac_f32 v199, v215, v210\n\t"
    "v_mfma_f32_16x16x32_bf16 v[52:55], a[8:11], a[88:91], v[52:55]\n\t"
    "v_mul_f32 v200, v216, v210\n\t"
    "v_mul_f32 v201, v216, v211\n\t"
    "v_mfma_f32_16x16x32_bf16 v[52:55], a[12:15], a[92:95], v[52:55]\n\t"
    "v_fma_f32 v200, -v217, v211, v200\n\t"
    "v_fmac_f32 v201, v217, v210\n\t"
    "v_mfma_f32_16x16x32_bf16 v[48:51], a[4:7], a[92:95], v[48:51]\n\t"
    "v_mul_f32 v202, v218, v210\n\t"
    "v_mul_f32 v203, v218, v211\n\t"
    "v_mfma_f32_16x16x32_bf16 v[48:51], a[0:3], a[88:91], v[48:51]\n\t"
    "v_fma_f32 v202, -v219, v211, v202\n\t"
    "v_mfma_f32_16x16x32_bf16 v[64:67], a[0:3], a[96:99], v[64:67]\n\t"
    "v_fmac_f32 v203, v219, v210\n\t"
    "v_mfma_f32_16x16x32_bf16 v[64:67], a[4:7], a[100:103], v[64:67]\n\t"
    "v_mul_f32 v172, v196, v210\n\t"
    "v_mul_f32 v173, v196, v211\n\t"
    "v_mfma_f32_16x16x32_bf16 v[68:71], a[12:15], a[100:103], v[68:71]\n\t"
    "ds_read_b128 a[84:87], v237 offset:36864\n\t"
    "v_mfma_f32_16x16x32_bf16 v[68:71], a[8:11], a[96:99], v[68:71]\n\t"
    "v_fma_f32 v172, -v197, v211, v172\n\t"
    "v_fmac_f32 v173, v197, v210\n\t"
    "v_mfma_f32_16x16x32_bf16 v[72:75], a[16:19], a[96:99], v[72:75]\n\t"
    "v_mul_f32 v174, v198, v210\n\t"
    "v_mul_f32 v175, v198, v211\n\t"
    "v_mfma_f32_16x16x32_bf16 v[72:75], a[20:23], a[100:103], v[72:75]\n\t"
    "v_fma_f32 v174, -v199, v211, v174\n\t"
    "v_mfma_f32_16x16x32_bf16 v[76:79], a[28:31], a[100:103], v[76:79]\n\t"
    "ds_read_b128 a[88:91], v236 offset:38912\n\t"
    "v_fmac_f32 v175, v199, v210\n\t"
    "v_mfma_f32_16x16x32_bf16 v[76:79], a[24:27], a[96:99], v[76:79]\n\t"
    "ds_read_b64_tr_b16 a[32:33], v240 offset:0\n\t"
    "ds_read_b64_tr_b16 a[60:61], v243 offset:4096\n\t"
    "v_mfma_f32_16x16x32_bf16 v[92:95], a[24:27], a[104:107], v[92:95]\n\t"
    "ds_read_b128 a[92:95], v237 offset:38912\n\t"
    "ds_read_b64_tr_b16 a[44:45], v241 offset:4096\n\t"
    "v_mfma_f32_16x16x32_bf16 v[92:95], a[28:31], a[108:111], v[92:95]\n\t"
    "ds_read_b64_tr_b16 a[48:49], v242 offset:0\n\t"
    "v_mul_f32 v176, v200, v210\n\t"
    "v_mfma_f32_16x16x32_bf16 v[88:91], a[20:23], a[108:111], v[88:91]\n\t"
    "ds_read_b64_tr_b16 a[42:43], v241 offset:512\n\t"
    "v_mul_f32 v177, v200, v211\n\t"
    "v_mfma_f32_16x16x32_bf16 v[88:91], a[16:19], a[104:107], v[88:91]\n\t"
    "v_fma_f32 v176, -v201, v211, v176\n\t"
    "v_fmac_f32 v177, v201, v210\n\t"
    "v_mfma_f32_16x16x32_bf16 v[84:87], a[8:11], a[104:107], v[84:87]\n\t"
    "v_mul_f32 v178, v202, v210\n\t"
    "v_mul_f32 v179, v202, v211\n\t"
    "v_mfma_f32_16x16x32_bf16 v[84:87], a[12:15], a[108:111], v[84:87]\n\t"
    "v_fma_f32 v178, -v203, v211, v178\n\t"
    "v_mfma_f32_16x16x32_bf16 v[80:83], a[4:7], a[108:111], v[80:83]\n\t"
    "ds_read_b128 a[96:99], v236 offset:40960\n\t"
    "ds_read_b128 a[100:103], v237 offset:40960\n\t"
    "v_mfma_f32_16x16x32_bf16 v[80:83], a[0:3], a[104:107], v[80:83]\n\t"
    "ds_read_b64_tr_b16 a[40:41], v241 offset:0\n\t"
    "v_mfma_f32_16x16x32_bf16 v[96:99], a[0:3], a[112:115], v[96:99]\n\t"
    "ds_read_b64_tr_b16 a[54:55], v242 offset:4608\n\t"
    "v_fmac_f32 v179, v203, v210\n\t"
    "v_mfma_f32_16x16x32_bf16 v[96:99], a[4:7], a[116:119], v[96:99]\n\t"
    "v_cvt_pk_f16_f32 v164, v218, v196\n\t"
    "v_mfma_f32_16x16x32_bf16 v[100:103], a[12:15], a[116:119], v[100:103]\n\t"
    "ds_read_b128 a[108:111], v237 offset:43008\n\t"
    "v_mfma_f32_16x16x32_bf16 v[100:103], a[8:11], a[112:115], v[100:103]\n\t"
    "v_cvt_pk_f16_f32 v165, v198, v200\n\t"
    "v_cvt_pk_f16_f32 v166, v202, v172\n\t"
    "v_mfma_f32_16x16x32_bf16 v[104:107], a[16:19], a[112:115], v[104:107]\n\t"
    "ds_read_b64_tr_b16 a[58:59], v243 offset:512\n\t"
    "v_cvt_pk_f16_f32 v167, v174, v176\n\t"
    "v_mfma_f32_16x16x32_bf16 v[104:107], a[20:23], a[116:119], v[104:107]\n\t"
    "ds_read_b128 a[104:107], v236 offset:43008\n\t"
    "v_mfma_f32_16x16x32_bf16 v[108:111], a[28:31], a[116:119], v[108:111]\n\t"
    "ds_read_b64_tr_b16 a[36:37], v240 offset:4096\n\t"
    "v_mfma_f32_16x16x32_bf16 v[108:111], a[24:27], a[112:115], v[108:111]\n\t"
    "ds_write_b128 v132, v[164:167] offset:0\n\t"
    "v_mfma_f32_16x16x32_bf16 v[124:127], a[24:27], a[120:123], v[124:127]\n\t"
    "v_cvt_pk_f16_f32 v168, v178, v178\n\t"
    "v_mfma_f32_16x16x32_bf16 v[124:127], a[28:31], a[124:127], v[124:127]\n\t"
    "ds_write_b32 v133, v168 offset:0\n\t"
    "v_mfma_f32_16x16x32_bf16 v[120:123], a[20:23], a[124:127], v[120:123]\n\t"
    "ds_read_b128 a[116:119], v237 offset:45056\n\t"
    "ds_read_b64_tr_b16 a[62:63], v243 offset:4608\n\t"
    "v_mfma_f32_16x16x32_bf16 v[120:123], a[16:19], a[120:123], v[120:123]\n\t"
    "s_sub_u32 s48, s48, 128\n\t"
    "v_cvt_pk_f16_f32 v164, v219, v197\n\t"
    "v_mfma_f32_16x16x32_bf16 v[116:119], a[8:11], a[120:123], v[116:119]\n\t"
    "ds_read_b128 a[112:115], v236 offset:45056\n\t"
    "s_and_b32 s48, s48, 1023\n\t"
    "v_mfma_f32_16x16x32_bf16 v[116:119], a[12:15], a[124:127], v[116:119]\n\t"
    "v_cvt_pk_f16_f32 v165, v199, v201\n\t"
    "v_mfma_f32_16x16x32_bf16 v[112:115], a[4:7], a[124:127], v[112:115]\n\t"
    "ds_read_b128 a[124:127], v237 offset:47104\n\t"
    "v_mfma_f32_16x16x32_bf16 v[112:115], a[0:3], a[120:123], v[112:115]\n\t"
    "ds_read_b128 a[120:123], v236 offset:47104\n\t"
    "v_cvt_pk_f16_f32 v166, v203, v173\n\t"
    "s_waitcnt lgkmcnt(8)\n\t"
    "v_mfma_f32_16x16x32_bf16 v[0:3], a[32:35], a[64:67], v[0:3]\n\t"
    "v_cvt_pk_f16_f32 v167, v175, v177\n\t"
    "s_waitcnt lgkmcnt(7)\n\t"
    "v_mfma_f32_16x16x32_bf16 v[0:3], a[36:39], a[68:71], v[0:3]\n\t"
    "ds_write_b128 v132, v[164:167] offset:2048\n\t"
    "v_mfma_f32_16x16x32_bf16 v[4:7], a[44:47], a[68:71], v[4:7]\n\t"
    "s_lshl_b32 s49, s48, 12\n\t"
    "v_mfma_f32_16x16x32_bf16 v[4:7], a[40:43], a[64:67], v[4:7]\n\t"
    "v_cvt_pk_f16_f32 v168, v179, v179\n\t"
    "v_mfma_f32_16x16x32_bf16 v[8:11], a[48:51], a[64:67], v[8:11]\n\t"
    "ds_write_b32 v133, v168 offset:2048\n\t"
    "v_mfma_f32_16x16x32_bf16 v[8:11], a[52:55], a[68:71], v[8:11]\n\t"
    "s_waitcnt vmcnt(11) lgkmcnt(0)\n\t"
    "s_barrier\n\t"
    "v_mfma_f32_16x16x32_bf16 v[12:15], a[60:63], a[68:71], v[12:15]\n\t"
    "ds_read_b64_tr_b16 a[176:177], v134 offset:0\n\t"
    "v_mfma_f32_16x16x32_bf16 v[12:15], a[56:59], a[64:67], v[12:15]\n\t"
    "ds_read_b64_tr_b16 a[20:21], v242 offset:20480\n\t"
    "s_add_u32 m0, s52, 0x0\n\t"
    "v_mfma_f32_16x16x32_bf16 v[28:31], a[56:59], a[72:75], v[28:31]\n\t"
    "ds_read_b64_tr_b16 a[178:179], v134 offset:512\n\t"
    "v_mfma_f32_16x16x32_bf16 v[28:31], a[60:63], a[76:79], v[28:31]\n\t"
    "buffer_load_dwordx4 v232, s[40:43], s49 offen offset:0 lds\n\t"
    "v_mfma_f32_16x16x32_bf16 v[24:27], a[52:55], a[76:79], v[24:27]\n\t"
    "v_mfma_f32_16x16x32_f16 a[196:199], a[160:163], a[128:131], 0\n\t"
    "v_mfma_f32_16x16x32_bf16 v[24:27], a[48:51], a[72:75], v[24:27]\n\t"
    "ds_read_b64_tr_b16 a[8:9], v241 offset:16384\n\t"
    "v_mfma_f32_16x16x32_bf16 v[20:23], a[40:43], a[72:75], v[20:23]\n\t"
    "ds_read_b64_tr_b16 a[180:181], v134 offset:1024\n\t"
    "v_mfma_f32_16x16x32_bf16 v[20:23], a[44:47], a[76:79], v[20:23]\n\t"
    "buffer_load_dwordx4 v233, s[40:43], s49 offen offset:1024 lds\n\t"
    "v_mfma_f32_16x16x32_bf16 v[16:19], a[36:39], a[76:79], v[16:19]\n\t"
    "ds_read_b64_tr_b16 a[182:183], v134 offset:1536\n\t"
    "ds_read_b64_tr_b16 a[184:185], v134 offset:2048\n\t"
    "v_mfma_f32_16x16x32_bf16 v[16:19], a[32:35], a[72:75], v[16:19]\n\t"
    "ds_read_b64_tr_b16 a[2:3], v240 offset:16896\n\t"
    "v_mfma_f32_16x16x32_bf16 v[32:35], a[32:35], a[80:83], v[32:35]\n\t"
    "ds_read_b64_tr_b16 a[186:187], v134 offset:2560\n\t"
    "v_mfma_f32_16x16x32_bf16 v[32:35], a[36:39], a[84:87], v[32:35]\n\t"
    "buffer_load_dwordx4 v234, s[40:43], s49 offen offset:2048 lds\n\t"
    "v_mfma_f32_16x16x32_bf16 v[36:39], a[44:47], a[84:87], v[36:39]\n\t"
    "ds_read_b64_tr_b16 a[188:189], v134 offset:3072\n\t"
    "ds_read_b64_tr_b16 a[190:191], v134 offset:3584\n\t"
    "v_mfma_f32_16x16x32_bf16 v[36:39], a[40:43], a[80:83], v[36:39]\n\t"
    "buffer_load_dwordx4 v235, s[40:43], s49 offen offset:3072 lds\n\t"
    "v_mfma_f32_16x16x32_bf16 v[40:43], a[48:51], a[80:83], v[40:43]\n\t"
    "s_lshl_b32 s56, s58, 12\n\t"
    "v_mfma_f32_16x16x32_bf16 v[40:43], a[52:55], a[84:87], v[40:43]\n\t"
    "ds_read_b64_tr_b16 a[14:15], v241 offset:20992\n\t"
    "v_mfma_f32_16x16x32_bf16 v[44:47], a[60:63], a[84:87], v[44:47]\n\t"
    "v_mfma_f32_16x16x32_f16 a[196:199], a[164:167], a[132:135], a[196:199]\n\t"
    "v_mfma_f32_16x16x32_bf16 v[44:47], a[56:59], a[80:83], v[44:47]\n\t"
    "ds_read_b64_tr_b16 a[12:13], v241 offset:20480\n\t"
    "v_mfma_f32_16x16x32_bf16 v[60:63], a[56:59], a[88:91], v[60:63]\n\t"
    "s_add_u32 s56, s56, 0xc000\n\t"
    "v_mfma_f32_16x16x32_bf16 v[60:63], a[60:63], a[92:95], v[60:63]\n\t"
    "v_add_u32 v128, s56, v244\n\t"
    "v_mfma_f32_16x16x32_bf16 v[56:59], a[52:55], a[92:95], v[56:59]\n\t"
    "s_min_u32 s56, s58, 2\n\t"
    "v_mfma_f32_16x16x32_bf16 v[56:59], a[48:51], a[88:91], v[56:59]\n\t"
    "ds_read_b64_tr_b16 a[16:17], v242 offset:16384\n\t"
    "v_mfma_f32_16x16x32_bf16 v[52:55], a[40:43], a[88:91], v[52:55]\n\t"
    "ds_read_b64_tr_b16 a[24:25], v243 offset:16384\n\t"
    "v_mfma_f32_16x16x32_bf16 v[52:55], a[44:47], a[92:95], v[52:55]\n\t"
    "s_waitcnt lgkmcnt(6)\n\t"
    "v_mfma_f32_16x16x32_f16 a[200:203], a[128:131], a[176:179], 0\n\t"
    "v_mfma_f32_16x16x32_bf16 v[48:51], a[36:39], a[92:95], v[48:51]\n\t"
    "v_mfma_f32_16x16x32_f16 a[200:203], a[132:135], a[180:183], a[200:203]\n\t"
    "v_mfma_f32_16x16x32_bf16 v[48:51], a[32:35], a[88:91], v[48:51]\n\t"
    "v_mfma_f32_16x16x32_f16 a[200:203], a[136:139], a[184:187], a[200:203]\n\t"
    "v_mfma_f32_16x16x32_bf16 v[64:67], a[32:35], a[96:99], v[64:67]\n\t"
    "v_mfma_f32_16x16x32_f16 a[196:199], a[168:171], a[136:139], a[196:199]\n\t"
    "v_mfma_f32_16x16x32_bf16 v[64:67], a[36:39], a[100:103], v[64:67]\n\t"
    "v_mfma_f32_16x16x32_f16 a[196:199], a[172:175], a[140:143], a[196:199]\n\t"
    "v_mfma_f32_16x16x32_bf16 v[68:71], a[44:47], a[100:103], v[68:71]\n\t"
    "s_lshl_b32 s56, s56, 10\n\t"
    "v_mfma_f32_16x16x32_bf16 v[68:71], a[40:43], a[96:99], v[68:71]\n\t"
    "s_waitcnt lgkmcnt(3)\n\t"
    "v_mfma_f32_16x16x32_f16 a[200:203], a[140:143], a[188:191], a[200:203]\n\t"
    "v_mfma_f32_16x16x32_bf16 v[72:75], a[48:51], a[96:99], v[72:75]\n\t"
    "s_add_u32 s56, s56, 0xc000\n\t"
    "v_mfma_f32_16x16x32_bf16 v[72:75], a[52:55], a[100:103], v[72:75]\n\t"
    "v_mfma_f32_16x16x32_f16 a[192:195], a[148:151], a[132:135], a[192:195]\n\t"
    "v_mfma_f32_16x16x32_bf16 v[76:79], a[60:63], a[100:103], v[76:79]\n\t"
    "v_mfma_f32_16x16x32_f16 a[192:195], a[152:155], a[136:139], a[192:195]\n\t"
    "v_mfma_f32_16x16x32_bf16 v[76:79], a[56:59], a[96:99], v[76:79]\n\t"
    "ds_read_b64_tr_b16 a[18:19], v242 offset:16896\n\t"
    "v_mfma_f32_16x16x32_bf16 v[92:95], a[56:59], a[104:107], v[92:95]\n\t"
    "ds_read_b64_tr_b16 a[22:23], v242 offset:20992\n\t"
    "v_mfma_f32_16x16x32_bf16 v[92:95], a[60:63], a[108:111], v[92:95]\n\t"
    "v_mul_f32 v4, s55, v4\n\t"
    "v_add_u32 v129, s56, v244\n\t"
    "v_mfma_f32_16x16x32_bf16 v[88:91], a[52:55], a[108:111], v[88:91]\n\t"
    "ds_read_b64_tr_b16 a[4:5], v240 offset:20480\n\t"
    "ds_read_b64_tr_b16 a[30:31], v243 offset:20992\n\t"
    "v_mfma_f32_16x16x32_bf16 v[88:91], a[48:51], a[104:107], v[88:91]\n\t"
    "v_mfma_f32_16x16x32_f16 a[192:195], a[156:159], a[140:143], a[192:195]\n\t"
    "v_mfma_f32_16x16x32_bf16 v[84:87], a[40:43], a[104:107], v[84:87]\n\t"
    "ds_read_b64_tr_b16 a[6:7], v240 offset:20992\n\t"
    "v_mul_f32 v3, s55, v3\n\t"
    "v_mfma_f32_16x16x32_bf16 v[84:87], a[44:47], a[108:111], v[84:87]\n\t"
    "v_mul_f32 v1, s55, v1\n\t"
    "v_mfma_f32_16x16x32_bf16 v[80:83], a[36:39], a[108:111], v[80:83]\n\t"
    "v_mul_f32 v5, s55, v5\n\t"
    "v_mfma_f32_16x16x32_bf16 v[80:83], a[32:35], a[104:107], v[80:83]\n\t"
    "ds_read_b64_tr_b16 a[0:1], v240 offset:16384\n\t"
    "v_mfma_f32_16x16x32_bf16 v[96:99], a[32:35], a[112:115], v[96:99]\n\t"
    "ds_read_b64_tr_b16 a[10:11], v241 offset:16896\n\t"
    "v_mfma_f32_16x16x32_bf16 v[96:99], a[36:39], a[116:119], v[96:99]\n\t"
    "ds_read_b64_tr_b16 a[26:27], v243 offset:16896\n\t"
    "v_mfma_f32_16x16x32_bf16 v[100:103], a[44:47], a[116:119], v[100:103]\n\t"
    "ds_write_b128 v128, a[196:199] offset:1024\n\t"
    "v_mfma_f32_16x16x32_bf16 v[100:103], a[40:43], a[112:115], v[100:103]\n\t"
    "ds_write_b128 v128, a[192:195] offset:0\n\t"
    "v_mfma_f32_16x16x32_bf16 v[104:107], a[48:51], a[112:115], v[104:107]\n\t"
    "v_mul_f32 v6, s55, v6\n\t"
    "v_mul_f32 v7, s55, v7\n\t"
    "v_mfma_f32_16x16x32_bf16 v[104:107], a[52:55], a[116:119], v[104:107]\n\t"
    "v_mul_f32 v0, s55, v0\n\t"
    "v_mfma_f32_16x16x32_bf16 v[108:111], a[60:63], a[116:119], v[108:111]\n\t"
    "v_mul_f32 v2, s55, v2\n\t"
    "v_mfma_f32_16x16x32_bf16 v[108:111], a[56:59], a[112:115], v[108:111]\n\t"
    "v_cvt_pk_bf16_f32 v132, v0, v1\n\t"
    "v_mfma_f32_16x16x32_bf16 v[124:127], a[56:59], a[120:123], v[124:127]\n\t"
    "ds_read_b64_tr_b16 a[28:29], v243 offset:20480\n\t"
    "v_mfma_f32_16x16x32_bf16 v[124:127], a[60:63], a[124:127], v[124:127]\n\t"
    "v_cvt_pk_bf16_f32 v133, v2, v3\n\t"
    "v_mfma_f32_16x16x32_bf16 v[120:123], a[52:55], a[124:127], v[120:123]\n\t"
    "ds_write_b128 v128, a[200:203] offset:2048\n\t"
    "v_mfma_f32_16x16x32_bf16 v[120:123], a[48:51], a[120:123], v[120:123]\n\t"
    "s_sub_u32 s48, s48, 128\n\t"
    "s_and_b32 s48, s48, 1023\n\t"
    "v_mfma_f32_16x16x32_bf16 v[116:119], a[40:43], a[120:123], v[116:119]\n\t"
    "v_mul_f32 v8, s55, v8\n\t"
    "v_cvt_pk_bf16_f32 v134, v4, v5\n\t"
    "v_mfma_f32_16x16x32_bf16 v[116:119], a[44:47], a[124:127], v[116:119]\n\t"
    "v_mul_f32 v14, s55, v14\n\t"
    "v_cvt_pk_bf16_f32 v135, v6, v7\n\t"
    "v_mfma_f32_16x16x32_bf16 v[112:115], a[36:39], a[124:127], v[112:115]\n\t"
    "v_mul_f32 v9, s55, v9\n\t"
    "v_mul_f32 v12, s55, v12\n\t"
    "v_mfma_f32_16x16x32_bf16 v[112:115], a[32:35], a[120:123], v[112:115]\n\t"
    "v_mul_f32 v15, s55, v15\n\t"
    "s_lshl_b32 s49, s48, 12\n\t"
    "s_waitcnt lgkmcnt(2)\n\t"
    "v_mfma_f32_16x16x32_bf16 a[128:131], a[64:67], a[0:3], 0\n\t"
    "v_mul_f32 v13, s55, v13\n\t"
    "v_mfma_f32_16x16x32_bf16 a[128:131], a[68:71], a[4:7], a[128:131]\n\t"
    "v_cvt_pk_bf16_f32 v136, v8, v9\n\t"
    "v_mfma_f32_16x16x32_bf16 a[136:139], a[68:71], a[12:15], 0\n\t"
    "v_mul_f32 v10, s55, v10\n\t"
    "v_cvt_pk_bf16_f32 v139, v14, v15\n\t"
    "v_mfma_f32_16x16x32_bf16 a[136:139], a[64:67], a[8:11], a[136:139]\n\t"
    "v_mul_f32 v11, s55, v11\n\t"
    "s_add_u32 m0, s52, 0x4000\n\t"
    "v_mfma_f32_16x16x32_bf16 a[144:147], a[64:67], a[16:19], 0\n\t"
    "v_cvt_pk_bf16_f32 v137, v10, v11\n\t"
    "v_cvt_pk_bf16_f32 v138, v12, v13\n\t"
    "v_mfma_f32_16x16x32_bf16 a[144:147], a[68:71], a[20:23], a[144:147]\n\t"
    "s_waitcnt vmcnt(4) lgkmcnt(0)\n\t"
    "s_barrier\n\t"
    "s_cmp_lg_u64 s[94:95], 0\n\t"
    "s_cbranch_scc1 L_hsL_%=\n\t"
    "global_load_dword v245, v245, s[84:85]\n\t"
    "L_hrL_%=:\n\t"
    "v_mfma_f32_16x16x32_bf16 a[152:155], a[68:71], a[28:31], 0\n\t"
    "ds_read_b128 a[68:71], v237 offset:0\n\t"
    "ds_read_b64_tr_b16 a[34:35], v240 offset:33280\n\t"
    "v_mfma_f32_16x16x32_bf16 a[152:155], a[64:67], a[24:27], a[152:155]\n\t"
    "ds_read_b64_tr_b16 a[46:47], v241 offset:37376\n\t"
    "ds_read_b128 v[160:163], v129 offset:0\n\t"
    "v_mfma_f32_16x16x32_bf16 a[156:159], a[72:75], a[24:27], 0\n\t"
    "buffer_load_dwordx4 v232, s[40:43], s49 offen offset:0 lds\n\t"
    "v_mfma_f32_16x16x32_bf16 a[156:159], a[76:79], a[28:31], a[156:159]\n\t"
    "ds_read_b64_tr_b16 a[58:59], v243 offset:33280\n\t"
    "v_mfma_f32_16x16x32_bf16 a[148:151], a[76:79], a[20:23], 0\n\t"
    "buffer_load_dwordx4 v233, s[40:43], s49 offen offset:1024 lds\n\t"
    "v_mfma_f32_16x16x32_bf16 a[148:151], a[72:75], a[16:19], a[148:151]\n\t"
    "ds_read_b128 a[64:67], v236 offset:0\n\t"
    "ds_read_b64_tr_b16 a[56:57], v243 offset:32768\n\t"
    "v_mfma_f32_16x16x32_bf16 a[140:143], a[72:75], a[8:11], 0\n\t"
    "ds_read_b64_tr_b16 a[50:51], v242 offset:33280\n\t"
    "ds_read_b64_tr_b16 a[54:55], v242 offset:37376\n\t"
    "v_mfma_f32_16x16x32_bf16 a[140:143], a[76:79], a[12:15], a[140:143]\n\t"
    "ds_read_b64_tr_b16 a[38:39], v240 offset:37376\n\t"
    "v_lshrrev_b32 v156, 1, v248\n\t"
    "v_mfma_f32_16x16x32_bf16 a[132:135], a[76:79], a[4:7], 0\n\t"
    "v_mul_f32 v19, s55, v19\n\t"
    "v_mul_f32 v22, s55, v22\n\t"
    "v_mfma_f32_16x16x32_bf16 a[132:135], a[72:75], a[0:3], a[132:135]\n\t"
    "ds_read_b128 a[76:79], v237 offset:2048\n\t"
    "v_and_b32 v157, 1, v248\n\t"
    "v_mfma_f32_16x16x32_bf16 a[160:163], a[80:83], a[0:3], 0\n\t"
    "ds_read_b64_tr_b16 a[40:41], v241 offset:32768\n\t"
    "v_mul_f32 v17, s55, v17\n\t"
    "v_mfma_f32_16x16x32_bf16 a[160:163], a[84:87], a[4:7], a[160:163]\n\t"
    "global_store_dwordx4 v244, v[132:135], s[66:67] offset:-4096 sc1\n\t"
    "v_mfma_f32_16x16x32_bf16 a[168:171], a[84:87], a[12:15], 0\n\t"
    "ds_read_b64_tr_b16 a[32:33], v240 offset:32768\n\t"
    "ds_read_b64_tr_b16 a[36:37], v240 offset:36864\n\t"
    "v_mfma_f32_16x16x32_bf16 a[168:171], a[80:83], a[8:11], a[168:171]\n\t"
    "buffer_load_dwordx4 v234, s[40:43], s49 offen offset:2048 lds\n\t"
    "v_mfma_f32_16x16x32_bf16 a[176:179], a[80:83], a[16:19], 0\n\t"
    "ds_read_b128 a[72:75], v236 offset:2048\n\t"
    "v_mul_f32 v21, s55, v21\n\t"
    "v_mfma_f32_16x16x32_bf16 a[176:179], a[84:87], a[20:23], a[176:179]\n\t"
    "buffer_load_dwordx4 v235, s[40:43], s49 offen offset:3072 lds\n\t"
    "v_mfma_f32_16x16x32_bf16 a[184:187], a[84:87], a[28:31], 0\n\t"
    "ds_read_b128 v[168:171], v129 offset:8192\n\t"
    "v_mul_f32 v16, s55, v16\n\t"
    "v_mfma_f32_16x16x32_bf16 a[184:187], a[80:83], a[24:27], a[184:187]\n\t"
    "ds_read_b64_tr_b16 a[60:61], v243 offset:36864\n\t"
    "v_mul_f32 v23, s55, v23\n\t"
    "v_mfma_f32_16x16x32_bf16 a[188:191], a[88:91], a[24:27], 0\n\t"
    "s_cmp_eq_u32 s58, 1\n\t"
    "Lis3_%=:\n\t"
    "s_cbranch_execz Lis7_%=\n\t"
    "s_cselect_b32 s56, 64, 4\n\t"
    "s_cselect_b32 s57, -1, 0\n\t"
    "v_mfma_f32_16x16x32_bf16 a[188:191], a[92:95], a[28:31], a[188:191]\n\t"
    "ds_read_b64_tr_b16 a[52:53], v242 offset:36864\n\t"
    "s_add_u32 m0, s75, 0x8000\n\t"
    "v_mfma_f32_16x16x32_bf16 a[180:183], a[92:95], a[20:23], 0\n\t"
    "ds_read_b128 a[84:87], v237 offset:4096\n\t"
    "v_mul_f32 v20, s55, v20\n\t"
    "v_mfma_f32_16x16x32_bf16 a[180:183], a[88:91], a[16:19], a[180:183]\n\t"
    "buffer_load_dwordx4 v224, s[36:39], s48 offen offset:0 lds\n\t"
    "v_mfma_f32_16x16x32_bf16 a[172:175], a[88:91], a[8:11], 0\n\t"
    "ds_read_b128 v[172:175], v129 offset:12288\n\t"
    "v_cvt_pk_bf16_f32 v143, v22, v23\n\t"
    "v_mfma_f32_16x16x32_bf16 a[172:175], a[92:95], a[12:15], a[172:175]\n\t"
    "v_mul_f32 v18, s55, v18\n\t"
    "v_cvt_pk_bf16_f32 v140, v16, v17\n\t"
    "v_mfma_f32_16x16x32_bf16 a[164:167], a[92:95], a[4:7], 0\n\t"
    "buffer_load_dwordx4 v225, s[36:39], s48 offen offset:1024 lds\n\t"
    "v_mfma_f32_16x16x32_bf16 a[164:167], a[88:91], a[0:3], a[164:167]\n\t"
    "ds_read_b64_tr_b16 a[42:43], v241 offset:33280\n\t"
    "v_mul_u32_u24 v154, s56, v157\n\t"
    "v_mfma_f32_16x16x32_bf16 a[192:195], a[96:99], a[0:3], 0\n\t"
    "ds_read_b128 a[92:95], v237 offset:6144\n\t"
    "ds_read_b128 v[164:167], v129 offset:4096\n\t"
    "v_mfma_f32_16x16x32_bf16 a[192:195], a[100:103], a[4:7], a[192:195]\n\t"
    "ds_read_b128 a[80:83], v236 offset:4096\n\t"
    "ds_read_b64_tr_b16 a[44:45], v241 offset:36864\n\t"
    "v_mfma_f32_16x16x32_bf16 a[200:203], a[100:103], a[12:15], 0\n\t"
    "ds_read_b128 a[88:91], v236 offset:6144\n\t"
    "v_cvt_pk_bf16_f32 v141, v18, v19\n\t"
    "v_mfma_f32_16x16x32_bf16 a[200:203], a[96:99], a[8:11], a[200:203]\n\t"
    "buffer_load_dwordx4 v226, s[36:39], s48 offen offset:2048 lds\n\t"
    "v_mfma_f32_16x16x32_bf16 a[208:211], a[96:99], a[16:19], 0\n\t"
    "ds_read_b64_tr_b16 a[62:63], v243 offset:37376\n\t"
    "v_and_b32 v158, s57, v156\n\t"
    "v_mfma_f32_16x16x32_bf16 a[208:211], a[100:103], a[20:23], a[208:211]\n\t"
    "s_cmp_ge_u32 s58, 2\n\t"
    "s_cselect_b32 s56, 8, 0x80\n\t"
    "s_cselect_b32 s57, 4, 3\n\t"
    "v_mfma_f32_16x16x32_bf16 a[216:219], a[100:103], a[28:31], 0\n\t"
    "global_store_dwordx4 v244, v[136:139], s[66:67] offset:-3072 sc1\n\t"
    "v_mfma_f32_16x16x32_bf16 a[216:219], a[96:99], a[24:27], a[216:219]\n\t"
    "ds_read_b128 a[96:99], v236 offset:8192\n\t"
    "ds_read_b64_tr_b16 a[48:49], v242 offset:32768\n\t"
    "v_mfma_f32_16x16x32_bf16 a[220:223], a[104:107], a[24:27], 0\n\t"
    "v_cvt_pk_bf16_f32 v142, v20, v21\n\t"
    "v_mad_u32_u24 v154, s56, v156, v154\n\t"
    "v_mfma_f32_16x16x32_bf16 a[220:223], a[108:111], a[28:31], a[220:223]\n\t"
    "buffer_load_dwordx4 v227, s[36:39], s48 offen offset:3072 lds\n\t"
    "v_mfma_f32_16x16x32_bf16 a[212:215], a[108:111], a[20:23], 0\n\t"
    "v_mul_f32 v25, s55, v25\n\t"
    "v_mul_f32 v26, s55, v26\n\t"
    "v_mfma_f32_16x16x32_bf16 a[212:215], a[104:107], a[16:19], a[212:215]\n\t"
    "v_mul_f32 v30, s55, v30\n\t"
    "s_add_u32 m0, s75, 0x9000\n\t"
    "v_mfma_f32_16x16x32_bf16 a[204:207], a[104:107], a[8:11], 0\n\t"
    "buffer_load_dwordx4 v228, s[36:39], s48 offen offset:0 lds\n\t"
    "v_mfma_f32_16x16x32_bf16 a[204:207], a[108:111], a[12:15], a[204:207]\n\t"
    "ds_read_b128 a[100:103], v237 offset:8192\n\t"
    "v_mul_f32 v28, s55, v28\n\t"
    "v_mfma_f32_16x16x32_bf16 a[196:199], a[108:111], a[4:7], 0\n\t"
    "v_mul_f32 v24, s55, v24\n\t"
    "v_mul_f32 v29, s55, v29\n\t"
    "v_mfma_f32_16x16x32_bf16 a[196:199], a[104:107], a[0:3], a[196:199]\n\t"
    "v_mul_f32 v27, s55, v27\n\t"
    "v_mul_f32 v31, s55, v31\n\t"
    "v_mfma_f32_16x16x32_bf16 a[224:227], a[112:115], a[0:3], 0\n\t"
    "v_cvt_pk_bf16_f32 v145, v26, v27\n\t"
    "s_waitcnt lgkmcnt(1)\n\t"
    "v_add_f32 v160, v160, v164\n\t"
    "v_add_f32 v161, v161, v165\n\t"
    "v_mfma_f32_16x16x32_bf16 a[224:227], a[116:119], a[4:7], a[224:227]\n\t"
    "buffer_load_dwordx4 v229, s[36:39], s48 offen offset:1024 lds\n\t"
    "v_mfma_f32_16x16x32_bf16 a[232:235], a[116:119], a[12:15], 0\n\t"
    "ds_read_b128 a[104:107], v236 offset:10240\n\t"
    "v_add_f32 v162, v162, v166\n\t"
    "v_mfma_f32_16x16x32_bf16 a[232:235], a[112:115], a[8:11], a[232:235]\n\t"
    "global_store_dwordx4 v244, v[140:143], s[66:67] offset:0 sc1\n\t"
    "v_mfma_f32_16x16x32_bf16 a[240:243], a[112:115], a[16:19], 0\n\t"
    "s_cmp_eq_u32 s58, 1\n\t"
    "s_cselect_b32 s57, 2, s57\n\t"
    "v_mfma_f32_16x16x32_bf16 a[240:243], a[116:119], a[20:23], a[240:243]\n\t"
    "buffer_load_dwordx4 v230, s[36:39], s48 offen offset:2048 lds\n\t"
    "v_mfma_f32_16x16x32_bf16 a[248:251], a[116:119], a[28:31], 0\n\t"
    "ds_read_b128 a[116:119], v237 offset:12288\n\t"
    "s_lshr_b32 s72, %2, 3\n\t"
    "v_mfma_f32_16x16x32_bf16 a[248:251], a[112:115], a[24:27], a[248:251]\n\t"
    "s_lshl_b32 s72, s72, 2\n\t"
    "v_sub_u32 v159, s57, v158\n\t"
    "v_mfma_f32_16x16x32_bf16 a[252:255], a[120:123], a[24:27], 0\n\t"
    "buffer_load_dwordx4 v231, s[36:39], s48 offen offset:3072 lds\n\t"
    "v_mfma_f32_16x16x32_bf16 a[252:255], a[124:127], a[28:31], a[252:255]\n\t"
    "ds_read_b128 a[108:111], v237 offset:10240\n\t"
    "s_add_u32 s72, s72, s58\n\t"
    "v_mfma_f32_16x16x32_bf16 a[244:247], a[124:127], a[20:23], 0\n\t"
    "ds_read_b128 a[112:115], v236 offset:12288\n\t"
    "s_sub_u32 s48, s48, 128\n\t"
    "v_mfma_f32_16x16x32_bf16 a[244:247], a[120:123], a[16:19], a[244:247]\n\t"
    "v_cvt_pk_bf16_f32 v144, v24, v25\n\t"
    "s_and_b32 s48, s48, 1023\n\t"
    "v_mfma_f32_16x16x32_bf16 a[236:239], a[120:123], a[8:11], 0\n\t"
    "v_mul_f32 v35, s55, v35\n\t"
    "v_add_f32 v163, v163, v167\n\t"
    "v_mfma_f32_16x16x32_bf16 a[236:239], a[124:127], a[12:15], a[236:239]\n\t"
    "v_lshlrev_b32 v159, v159, v247\n\t"
    "v_add_u32 v154, v154, v159\n\t"
    "v_mfma_f32_16x16x32_bf16 a[228:231], a[124:127], a[4:7], 0\n\t"
    "ds_read_b128 a[124:127], v237 offset:14336\n\t"
    "s_min_u32 s56, s58, 2\n\t"
    "v_mfma_f32_16x16x32_bf16 a[228:231], a[120:123], a[0:3], a[228:231]\n\t"
    "ds_read_b128 a[120:123], v236 offset:14336\n\t"
    "s_lshl_b32 s56, s56, 10\n\t"
    "v_mfma_f32_16x16x32_bf16 a[128:131], a[64:67], a[32:35], a[128:131]\n\t"
    "s_lshl_b32 s49, s48, 12\n\t"
    "s_add_u32 s56, s56, 0xe600\n\t"
    "v_mfma_f32_16x16x32_bf16 a[128:131], a[68:71], a[36:39], a[128:131]\n\t"
    "v_mul_f32 v37, s55, v37\n\t"
    "v_mul_f32 v39, s55, v39\n\t"
    "v_mfma_f32_16x16x32_bf16 a[136:139], a[68:71], a[44:47], a[136:139]\n\t"
    "v_mul_f32 v36, s55, v36\n\t"
    "v_cvt_pk_bf16_f32 v147, v30, v31\n\t"
    "s_lshl_b32 s72, s72, 8\n\t"
    "v_mfma_f32_16x16x32_bf16 a[136:139], a[64:67], a[40:43], a[136:139]\n\t"
    "s_add_u32 s72, s72, Lc0_%=-Lpf_%=\n\t"
    "s_add_u32 m0, s52, 0x8000\n\t"
    "v_mfma_f32_16x16x32_bf16 a[144:147], a[64:67], a[48:51], a[144:147]\n\t"
    "v_cvt_pk_bf16_f32 v146, v28, v29\n\t"
    "v_add_f32 v168, v168, v172\n\t"
    "v_mfma_f32_16x16x32_bf16 a[144:147], a[68:71], a[52:55], a[144:147]\n\t"
    "s_waitcnt vmcnt(16) lgkmcnt(0)\n\t"
    "s_barrier\n\t"
    "v_mfma_f32_16x16x32_bf16 a[152:155], a[68:71], a[60:63], a[152:155]\n\t"
    "ds_read_b64_tr_b16 a[20:21], v242 offset:4096\n\t"
    "v_lshl_add_u32 v154, v154, 2, s56\n\t"
    "v_mfma_f32_16x16x32_bf16 a[152:155], a[64:67], a[56:59], a[152:155]\n\t"
    "v_mul_f32 v34, s55, v34\n\t"
    "v_lshrrev_b32 v131, 2, v244\n\t"
    "v_mfma_f32_16x16x32_bf16 a[156:159], a[72:75], a[56:59], a[156:159]\n\t"
    "ds_read_b64_tr_b16 a[30:31], v243 offset:4608\n\t"
    "v_mad_u32_u24 v155, v158, 30, 2\n\t"
    "v_mfma_f32_16x16x32_bf16 a[156:159], a[76:79], a[60:63], a[156:159]\n\t"
    "buffer_load_dwordx4 v232, s[40:43], s49 offen offset:0 lds\n\t"
    "v_mfma_f32_16x16x32_bf16 a[148:151], a[76:79], a[52:55], a[148:151]\n\t"
    "buffer_load_dwordx4 v233, s[40:43], s49 offen offset:1024 lds\n\t"
    "v_mfma_f32_16x16x32_bf16 a[148:151], a[72:75], a[48:51], a[148:151]\n\t"
    "ds_read_b128 a[64:67], v238 offset:0\n\t"
    "v_lshl_add_u32 v155, v155, 2, v154\n\t"
    "v_mfma_f32_16x16x32_bf16 a[140:143], a[72:75], a[40:43], a[140:143]\n\t"
    "ds_read_b64_tr_b16 a[22:23], v242 offset:4608\n\t"
    "v_mul_f32 v32, s55, v32\n\t"
    "v_mfma_f32_16x16x32_bf16 a[140:143], a[76:79], a[44:47], a[140:143]\n\t"
    "ds_read_b64_tr_b16 a[18:19], v242 offset:512\n\t"
    "v_add_f32 v169, v169, v173\n\t"
    "v_mfma_f32_16x16x32_bf16 a[132:135], a[76:79], a[36:39], a[132:135]\n\t"
    "v_add_u32 v131, s72, v131\n\t"
    "s_lshl_b32 s56, s58, 9\n\t"
    "s_sub_u32 s56, 2047, s56\n\t"
    "v_mfma_f32_16x16x32_bf16 a[132:135], a[72:75], a[32:35], a[132:135]\n\t"
    "ds_read_b128 a[68:71], v239 offset:0\n\t"
    "v_lshrrev_b32 v184, 1, v244\n\t"
    "v_mfma_f32_16x16x32_bf16 a[160:163], a[80:83], a[32:35], a[160:163]\n\t"
    "ds_read_b128 a[72:75], v238 offset:2048\n\t"
    "ds_read_b64_tr_b16 a[8:9], v241 offset:0\n\t"
    "v_min_u32 v131, Lkend_fused-Lpf_%=-4, v131\n\t"
    "v_mfma_f32_16x16x32_bf16 a[160:163], a[84:87], a[36:39], a[160:163]\n\t"
    "global_store_dwordx4 v244, v[144:147], s[66:67] offset:1024 sc1\n\t"
    "v_mfma_f32_16x16x32_bf16 a[168:171], a[84:87], a[44:47], a[168:171]\n\t"
    "ds_read_b64_tr_b16 a[14:15], v241 offset:4608\n\t"
    "v_mul_f32 v42, s55, v42\n\t"
    "v_sub_u32 v176, s56, v184\n\t"
    "v_mfma_f32_16x16x32_bf16 a[168:171], a[80:83], a[40:43], a[168:171]\n\t"
    "buffer_load_dwordx4 v234, s[40:43], s49 offen offset:2048 lds\n\t"
    "v_mfma_f32_16x16x32_bf16 a[176:179], a[80:83], a[48:51], a[176:179]\n\t"
    "v_mul_f32 v33, s55, v33\n\t"
    "v_mov_b32 v187, 0x4280\n\t"
    "v_mfma_f32_16x16x32_bf16 a[176:179], a[84:87], a[52:55], a[176:179]\n\t"
    "global_load_dword v131, v131, s[88:89]\n\t"
    "v_mfma_f32_16x16x32_bf16 a[184:187], a[84:87], a[60:63], a[184:187]\n\t"
    "ds_read_b128 a[76:79], v239 offset:2048\n\t"
    "v_mul_f32 v41, s55, v41\n\t"
    "s_movk_i32 s56, 0x80\n\t"
    "v_mfma_f32_16x16x32_bf16 a[184:187], a[80:83], a[56:59], a[184:187]\n\t"
    "buffer_load_dwordx4 v235, s[40:43], s49 offen offset:3072 lds\n\t"
    "v_mfma_f32_16x16x32_bf16 a[188:191], a[88:91], a[56:59], a[188:191]\n\t"
    "v_mul_f32 v38, s55, v38\n\t"
    "v_add_f32 v170, v170, v174\n\t"
    "v_mfma_f32_16x16x32_bf16 a[188:191], a[92:95], a[60:63], a[188:191]\n\t"
    "s_add_u32 s66, s66, 0x4000\n\t"
    "s_addc_u32 s67, s67, 0\n\t"
    "s_add_u32 m0, s75, 0x0\n\t"
    "v_mfma_f32_16x16x32_bf16 a[180:183], a[92:95], a[52:55], a[180:183]\n\t"
    "buffer_load_dwordx4 v224, s[36:39], s48 offen offset:0 lds\n\t"
    "v_mfma_f32_16x16x32_bf16 a[180:183], a[88:91], a[48:51], a[180:183]\n\t"
    "ds_read_b64_tr_b16 a[6:7], v240 offset:4608\n\t"
    "ds_read_b64_tr_b16 a[24:25], v243 offset:0\n\t"
    "v_mfma_f32_16x16x32_bf16 a[172:175], a[88:91], a[40:43], a[172:175]\n\t"
    "v_cvt_pk_bf16_f32 v150, v36, v37\n\t"
    "v_subrev_u32 v177, 1, v176\n\t"
    "v_subrev_u32 v178, 2, v176\n\t"
    "v_mfma_f32_16x16x32_bf16 a[172:175], a[92:95], a[44:47], a[172:175]\n\t"
    "ds_read_b128 a[84:87], v239 offset:4096\n\t"
    "v_add_f32 v171, v171, v175\n\t"
    "v_mfma_f32_16x16x32_bf16 a[164:167], a[92:95], a[36:39], a[164:167]\n\t"
    "buffer_load_dwordx4 v225, s[36:39], s48 offen offset:1024 lds\n\t"
    "v_mfma_f32_16x16x32_bf16 a[164:167], a[88:91], a[32:35], a[164:167]\n\t"
    "v_mul_f32 v44, s55, v44\n\t"
    "v_cvt_pk_bf16_f32 v148, v32, v33\n\t"
    "v_cvt_pk_f16_f32 v160, v160, v168\n\t"
    "v_mfma_f32_16x16x32_bf16 a[192:195], a[96:99], a[32:35], a[192:195]\n\t"
    "ds_read_b128 a[80:83], v238 offset:4096\n\t"
    "v_mul_f32 v46, s55, v46\n\t"
    "v_mfma_f32_16x16x32_bf16 a[192:195], a[100:103], a[36:39], a[192:195]\n\t"
    "buffer_load_dwordx4 v226, s[36:39], s48 offen offset:2048 lds\n\t"
    "v_mfma_f32_16x16x32_bf16 a[200:203], a[100:103], a[44:47], a[200:203]\n\t"
    "v_cvt_pk_bf16_f32 v149, v34, v35\n\t"
    "v_subrev_u32 v179, 3, v176\n\t"
    "v_mfma_f32_16x16x32_bf16 a[200:203], a[96:99], a[40:43], a[200:203]\n\t"
    "ds_read_b128 a[92:95], v239 offset:6144\n\t"
    "v_cvt_pk_bf16_f32 v151, v38, v39\n\t"
    "v_mfma_f32_16x16x32_bf16 a[208:211], a[96:99], a[48:51], a[208:211]\n\t"
    "v_cvt_pk_f16_f32 v161, v161, v169\n\t"
    "v_subrev_u32 v180, 4, v176\n\t"
    "v_subrev_u32 v181, 5, v176\n\t"
    "v_mfma_f32_16x16x32_bf16 a[208:211], a[100:103], a[52:55], a[208:211]\n\t"
    "global_store_dwordx4 v244, v[148:151], s[66:67] offset:-4096 sc1\n\t"
    "v_mfma_f32_16x16x32_bf16 a[216:219], a[100:103], a[60:63], a[216:219]\n\t"
    "ds_read_b128 a[100:103], v239 offset:8192\n\t"
    "ds_read_b64_tr_b16 a[4:5], v240 offset:4096\n\t"
    "v_mfma_f32_16x16x32_bf16 a[216:219], a[96:99], a[56:59], a[216:219]\n\t"
    "buffer_load_dwordx4 v227, s[36:39], s48 offen offset:3072 lds\n\t"
    "v_mfma_f32_16x16x32_bf16 a[220:223], a[104:107], a[56:59], a[220:223]\n\t"
    "s_add_u32 m0, s75, 0x1000\n\t"
    "v_subrev_u32 v182, 6, v176\n\t"
    "v_mfma_f32_16x16x32_bf16 a[220:223], a[108:111], a[60:63], a[220:223]\n\t"
    "ds_read_b128 a[88:91], v238 offset:6144\n\t"
    "ds_read_b128 a[96:99], v238 offset:8192\n\t"
    "v_mfma_f32_16x16x32_bf16 a[212:215], a[108:111], a[52:55], a[212:215]\n\t"
    "v_cvt_pk_f16_f32 v162, v162, v170\n\t"
    "v_subrev_u32 v183, 7, v176\n\t"
    "v_mfma_f32_16x16x32_bf16 a[212:215], a[104:107], a[48:51], a[212:215]\n\t"
    "buffer_load_dwordx4 v228, s[36:39], s48 offen offset:0 lds\n\t"
    "v_mfma_f32_16x16x32_bf16 a[204:207], a[104:107], a[40:43], a[204:207]\n\t"
    "v_mul_f32 v45, s55, v45\n\t"
    "v_cvt_pk_f16_f32 v163, v163, v171\n\t"
    "v_cvt_f32_u32 v184, v176\n\t"
    "v_mfma_f32_16x16x32_bf16 a[204:207], a[108:111], a[44:47], a[204:207]\n\t"
    "v_mul_f32 v43, s55, v43\n\t"
    "v_cvt_pk_bf16_f32 v197, v42, v43\n\t"
    "v_mfma_f32_16x16x32_bf16 a[196:199], a[108:111], a[36:39], a[196:199]\n\t"
    "v_mul_f32 v40, s55, v40\n\t"
    "v_mul_f32 v47, s55, v47\n\t"
    "v_mfma_f32_16x16x32_bf16 a[196:199], a[104:107], a[32:35], a[196:199]\n\t"
    "buffer_load_dwordx4 v229, s[36:39], s48 offen offset:1024 lds\n\t"
    "v_mfma_f32_16x16x32_bf16 a[224:227], a[112:115], a[32:35], a[224:227]\n\t"
    "ds_read_b64_tr_b16 a[2:3], v240 offset:512\n\t"
    "ds_read_b64_tr_b16 a[12:13], v241 offset:4096\n\t"
    "v_cvt_f32_u32 v185, v177\n\t"
    "v_mfma_f32_16x16x32_bf16 a[224:227], a[116:119], a[36:39], a[224:227]\n\t"
    "v_cvt_pk_bf16_f32 v198, v44, v45\n\t"
    "v_cvt_pk_bf16_f32 v199, v46, v47\n\t"
    "v_mfma_f32_16x16x32_bf16 a[232:235], a[116:119], a[44:47], a[232:235]\n\t"
    "ds_read_b64_tr_b16 a[26:27], v243 offset:512\n\t"
    "v_cvt_pk_bf16_f32 v196, v40, v41\n\t"
    "v_mfma_f32_16x16x32_bf16 a[232:235], a[112:115], a[40:43], a[232:235]\n\t"
    "global_store_dwordx4 v244, v[196:199], s[66:67] offset:-3072 sc1\n\t"
    "v_mfma_f32_16x16x32_bf16 a[240:243], a[112:115], a[48:51], a[240:243]\n\t"
    "ds_read_b128 a[108:111], v239 offset:10240\n\t"
    "v_cvt_pk_bf16_f32 v184, v184, v185\n\t"
    "v_mfma_f32_16x16x32_bf16 a[240:243], a[116:119], a[52:55], a[240:243]\n\t"
    "buffer_load_dwordx4 v230, s[36:39], s48 offen offset:2048 lds\n\t"
    "v_mfma_f32_16x16x32_bf16 a[248:251], a[116:119], a[60:63], a[248:251]\n\t"
    "ds_read_b64_tr_b16 a[16:17], v242 offset:0\n\t"
    "v_sub_u32_sdwa v185, v184, v187 dst_sel:DWORD dst_unused:UNUSED_PAD src0_sel:WORD_0 src1_sel:DWORD\n\t"
    "v_mfma_f32_16x16x32_bf16 a[248:251], a[112:115], a[56:59], a[248:251]\n\t"
    "ds_write_b64 v155, v[162:163]\n\t"
    "v_mfma_f32_16x16x32_bf16 a[252:255], a[120:123], a[56:59], a[252:255]\n\t"
    "ds_read_b64_tr_b16 a[28:29], v243 offset:4096\n\t"
    "v_sub_u32_sdwa v186, v184, v187 dst_sel:DWORD dst_unused:UNUSED_PAD src0_sel:WORD_1 src1_sel:DWORD\n\t"
    "v_med3_i32 v176, v185, v176, s56\n\t"
    "v_mfma_f32_16x16x32_bf16 a[252:255], a[124:127], a[60:63], a[252:255]\n\t"
    "ds_read_b64_tr_b16 a[0:1], v240 offset:0\n\t"
    "ds_read_b64_tr_b16 a[10:11], v241 offset:512\n\t"
    "v_mfma_f32_16x16x32_bf16 a[244:247], a[124:127], a[52:55], a[244:247]\n\t"
    "buffer_load_dwordx4 v231, s[36:39], s48 offen offset:3072 lds\n\t"
    "v_mfma_f32_16x16x32_bf16 a[244:247], a[120:123], a[48:51], a[244:247]\n\t"
    "ds_read_b128 a[104:107], v238 offset:10240\n\t"
    "ds_read_b128 a[112:115], v238 offset:12288\n\t"
    "v_med3_i32 v177, v186, v177, s56\n\t"
    "v_mfma_f32_16x16x32_bf16 a[236:239], a[120:123], a[40:43], a[236:239]\n\t"
    "ds_write_b64 v154, v[160:161]\n\t"
    "v_mfma_f32_16x16x32_bf16 a[236:239], a[124:127], a[44:47], a[236:239]\n\t"
    "ds_read_b128 a[116:119], v239 offset:12288\n\t"
    "v_cvt_f32_u32 v184, v178\n\t"
    "v_mfma_f32_16x16x32_bf16 a[228:231], a[124:127], a[36:39], a[228:231]\n\t"
    "ds_read_b128 a[124:127], v239 offset:14336\n\t"
    "s_sub_u32 s48, s48, 128\n\t"
    "v_mfma_f32_16x16x32_bf16 a[228:231], a[120:123], a[32:35], a[228:231]\n\t"
    "ds_read_b128 a[120:123], v238 offset:14336\n\t"
    "v_cvt_f32_u32 v185, v179\n\t"
    "s_waitcnt lgkmcnt(7)\n\t"
    "v_mfma_f32_16x16x32_bf16 a[128:131], a[64:67], a[0:3], a[128:131]\n\t"
    "v_cvt_pk_bf16_f32 v184, v184, v185\n\t"
    "v_sub_u32_sdwa v185, v184, v187 dst_sel:DWORD dst_unused:UNUSED_PAD src0_sel:WORD_0 src1_sel:DWORD\n\t"
    "v_mfma_f32_16x16x32_bf16 a[128:131], a[68:71], a[4:7], a[128:131]\n\t"
    "s_and_b32 s48, s48, 1023\n\t"
    "v_sub_u32_sdwa v186, v184, v187 dst_sel:DWORD dst_unused:UNUSED_PAD src0_sel:WORD_1 src1_sel:DWORD\n\t"
    "v_mfma_f32_16x16x32_bf16 a[136:139], a[68:71], a[12:15], a[136:139]\n\t"
    "v_mul_f32 v50, s55, v50\n\t"
    "v_med3_i32 v178, v185, v178, s56\n\t"
    "s_waitcnt lgkmcnt(6)\n\t"
    "v_mfma_f32_16x16x32_bf16 a[136:139], a[64:67], a[8:11], a[136:139]\n\t"
    "v_mul_f32 v48, s55, v48\n\t"
    "v_med3_i32 v179, v186, v179, s56\n\t"
    "v_mfma_f32_16x16x32_bf16 a[144:147], a[64:67], a[16:19], a[144:147]\n\t"
    "v_mul_f32 v49, s55, v49\n\t"
    "s_lshl_b32 s49, s48, 12\n\t"
    "v_mfma_f32_16x16x32_bf16 a[144:147], a[68:71], a[20:23], a[144:147]\n\t"
    "s_waitcnt vmcnt(16) lgkmcnt(0)\n\t"
    "s_barrier\n\t"
    "v_mfma_f32_16x16x32_bf16 a[152:155], a[68:71], a[28:31], a[152:155]\n\t"
    "v_mul_f32 v53, s55, v53\n\t"
    "s_add_u32 m0, s52, 0x0\n\t"
    "v_mfma_f32_16x16x32_bf16 a[152:155], a[64:67], a[24:27], a[152:155]\n\t"
    "buffer_load_dwordx4 v232, s[40:43], s49 offen offset:0 lds\n\t"
    "v_mfma_f32_16x16x32_bf16 a[156:159], a[72:75], a[24:27], a[156:159]\n\t"
    "ds_read_b64_tr_b16 a[62:63], v243 offset:20992\n\t"
    "v_cvt_f32_u32 v184, v180\n\t"
    "v_mfma_f32_16x16x32_bf16 a[156:159], a[76:79], a[28:31], a[156:159]\n\t"
    "v_mul_f32 v52, s55, v52\n\t"
    "v_cvt_f32_u32 v185, v181\n\t"
    "v_mfma_f32_16x16x32_bf16 a[148:151], a[76:79], a[20:23], a[148:151]\n\t"
    "ds_read_b128 a[64:67], v236 offset:32768\n\t"
    "v_mul_f32 v55, s55, v55\n\t"
    "v_mfma_f32_16x16x32_bf16 a[148:151], a[72:75], a[16:19], a[148:151]\n\t"
    "v_mul_f32 v56, s55, v56\n\t"
    "v_cvt_pk_bf16_f32 v184, v184, v185\n\t"
    "v_mfma_f32_16x16x32_bf16 a[140:143], a[72:75], a[8:11], a[140:143]\n\t"
    "buffer_load_dwordx4 v233, s[40:43], s49 offen offset:1024 lds\n\t"
    "v_mfma_f32_16x16x32_bf16 a[140:143], a[76:79], a[12:15], a[140:143]\n\t"
    "v_cvt_pk_bf16_f32 v200, v48, v49\n\t"
    "v_cvt_pk_bf16_f32 v202, v52, v53\n\t"
    "v_mfma_f32_16x16x32_bf16 a[132:135], a[76:79], a[4:7], a[132:135]\n\t"
    "ds_read_b128 a[68:71], v237 offset:32768\n\t"
    "v_mul_f32 v54, s55, v54\n\t"
    "v_mfma_f32_16x16x32_bf16 a[132:135], a[72:75], a[0:3], a[132:135]\n\t"
    "v_sub_u32_sdwa v185, v184, v187 dst_sel:DWORD dst_unused:UNUSED_PAD src0_sel:WORD_0 src1_sel:DWORD\n\t"
    "v_mfma_f32_16x16x32_bf16 a[160:163], a[80:83], a[0:3], a[160:163]\n\t"
    "ds_read_b128 a[72:75], v236 offset:34816\n\t"
    "v_mul_f32 v51, s55, v51\n\t"
    "v_mfma_f32_16x16x32_bf16 a[160:163], a[84:87], a[4:7], a[160:163]\n\t"
    "buffer_load_dwordx4 v234, s[40:43], s49 offen offset:2048 lds\n\t"
    "v_mfma_f32_16x16x32_bf16 a[168:171], a[84:87], a[12:15], a[168:171]\n\t"
    "ds_read_b64_tr_b16 a[60:61], v243 offset:20480\n\t"
    "v_mul_f32 v59, s55, v59\n\t"
    "v_mfma_f32_16x16x32_bf16 a[168:171], a[80:83], a[8:11], a[168:171]\n\t"
    "v_mul_f32 v63, s55, v63\n\t"
    "v_cvt_pk_bf16_f32 v201, v50, v51\n\t"
    "v_mfma_f32_16x16x32_bf16 a[176:179], a[80:83], a[16:19], a[176:179]\n\t"
    "buffer_load_dwordx4 v235, s[40:43], s49 offen offset:3072 lds\n\t"
    "v_mfma_f32_16x16x32_bf16 a[176:179], a[84:87], a[20:23], a[176:179]\n\t"
    "ds_read_b128 a[76:79], v237 offset:34816\n\t"
    "s_add_u32 m0, s75, 0x10000\n\t"
    "v_mfma_f32_16x16x32_bf16 a[184:187], a[84:87], a[28:31], a[184:187]\n\t"
    "v_cvt_pk_bf16_f32 v203, v54, v55\n\t"
    "v_sub_u32_sdwa v186, v184, v187 dst_sel:DWORD dst_unused:UNUSED_PAD src0_sel:WORD_1 src1_sel:DWORD\n\t"
    "v_mfma_f32_16x16x32_bf16 a[184:187], a[80:83], a[24:27], a[184:187]\n\t"
    "ds_read_b128 a[80:83], v236 offset:36864\n\t"
    "ds_read_b128 a[84:87], v237 offset:36864\n\t"
    "v_mfma_f32_16x16x32_bf16 a[188:191], a[88:91], a[24:27], a[188:191]\n\t"
    "global_store_dwordx4 v244, v[200:203], s[66:67] offset:0 sc1\n\t"
    "v_mfma_f32_16x16x32_bf16 a[188:191], a[92:95], a[28:31], a[188:191]\n\t"
    "v_med3_i32 v180, v185, v180, s56\n\t"
    "v_med3_i32 v181, v186, v181, s56\n\t"
    "v_mfma_f32_16x16x32_bf16 a[180:183], a[92:95], a[20:23], a[180:183]\n\t"
    "buffer_load_dwordx4 v224, s[36:39], s48 offen offset:0 lds\n\t"
    "v_mfma_f32_16x16x32_bf16 a[180:183], a[88:91], a[16:19], a[180:183]\n\t"
    "v_mul_f32 v60, s55, v60\n\t"
    "v_cvt_f32_u32 v184, v182\n\t"
    "v_mfma_f32_16x16x32_bf16 a[172:175], a[88:91], a[8:11], a[172:175]\n\t"
    "ds_read_b64_tr_b16 a[36:37], v240 offset:20480\n\t"
    "ds_read_b64_tr_b16 a[54:55], v242 offset:20992\n\t"
    "v_mfma_f32_16x16x32_bf16 a[172:175], a[92:95], a[12:15], a[172:175]\n\t"
    "ds_read_b64_tr_b16 a[44:45], v241 offset:20480\n\t"
    "ds_read_b64_tr_b16 a[50:51], v242 offset:16896\n\t"
    "v_mfma_f32_16x16x32_bf16 a[164:167], a[92:95], a[4:7], a[164:167]\n\t"
    "v_mul_f32 v62, s55, v62\n\t"
    "v_cvt_f32_u32 v185, v183\n\t"
    "v_mfma_f32_16x16x32_bf16 a[164:167], a[88:91], a[0:3], a[164:167]\n\t"
    "buffer_load_dwordx4 v225, s[36:39], s48 offen offset:1024 lds\n\t"
    "v_mfma_f32_16x16x32_bf16 a[192:195], a[96:99], a[0:3], a[192:195]\n\t"
    "v_cvt_pk_bf16_f32 v184, v184, v185\n\t"
    "v_mfma_f32_16x16x32_bf16 a[192:195], a[100:103], a[4:7], a[192:195]\n\t"
    "v_sub_u32_sdwa v185, v184, v187 dst_sel:DWORD dst_unused:UNUSED_PAD src0_sel:WORD_0 src1_sel:DWORD\n\t"
    "v_sub_u32_sdwa v186, v184, v187 dst_sel:DWORD dst_unused:UNUSED_PAD src0_sel:WORD_1 src1_sel:DWORD\n\t"
    "v_mfma_f32_16x16x32_bf16 a[200:203], a[100:103], a[12:15], a[200:203]\n\t"
    "v_mul_f32 v57, s55, v57\n\t"
    "v_med3_i32 v182, v185, v182, s56\n\t"
    "v_mfma_f32_16x16x32_bf16 a[200:203], a[96:99], a[8:11], a[200:203]\n\t"
    "buffer_load_dwordx4 v226, s[36:39], s48 offen offset:2048 lds\n\t"
    "v_mfma_f32_16x16x32_bf16 a[208:211], a[96:99], a[16:19], a[208:211]\n\t"
    "v_mul_f32 v61, s55, v61\n\t"
    "v_med3_i32 v183, v186, v183, s56\n\t"
    "v_mfma_f32_16x16x32_bf16 a[208:211], a[100:103], a[20:23], a[208:211]\n\t"
    "buffer_load_dwordx4 v227, s[36:39], s48 offen offset:3072 lds\n\t"
    "v_mfma_f32_16x16x32_bf16 a[216:219], a[100:103], a[28:31], a[216:219]\n\t"
    "ds_read_b64_tr_b16 a[42:43], v241 offset:16896\n\t"
    "ds_read_b64_tr_b16 a[56:57], v243 offset:16384\n\t"
    "v_mfma_f32_16x16x32_bf16 a[216:219], a[96:99], a[24:27], a[216:219]\n\t"
    "ds_read_b128 a[92:95], v237 offset:38912\n\t"
    "v_mul_f32 v58, s55, v58\n\t"
    "v_mfma_f32_16x16x32_bf16 a[220:223], a[104:107], a[24:27], a[220:223]\n\t"
    "s_add_u32 m0, s75, 0x11000\n\t"
    "s_mov_b32 s56, 0xe600\n\t"
    "v_mfma_f32_16x16x32_bf16 a[220:223], a[108:111], a[28:31], a[220:223]\n\t"
    "v_lshl_add_u32 v176, v176, 2, s56\n\t"
    "v_lshl_add_u32 v177, v177, 2, s56\n\t"
    "v_mfma_f32_16x16x32_bf16 a[212:215], a[108:111], a[20:23], a[212:215]\n\t"
    "ds_read_b128 a[96:99], v236 offset:40960\n\t"
    "v_cvt_pk_bf16_f32 v207, v62, v63\n\t"
    "v_mfma_f32_16x16x32_bf16 a[212:215], a[104:107], a[16:19], a[212:215]\n\t"
    "ds_read_b128 a[88:91], v236 offset:38912\n\t"
    "v_lshl_add_u32 v178, v178, 2, s56\n\t"
    "v_mfma_f32_16x16x32_bf16 a[204:207], a[104:107], a[8:11], a[204:207]\n\t"
    "buffer_load_dwordx4 v228, s[36:39], s48 offen offset:0 lds\n\t"
    "v_mfma_f32_16x16x32_bf16 a[204:207], a[108:111], a[12:15], a[204:207]\n\t"
    "v_cvt_pk_bf16_f32 v204, v56, v57\n\t"
    "v_cvt_pk_bf16_f32 v206, v60, v61\n\t"
    "v_mfma_f32_16x16x32_bf16 a[196:199], a[108:111], a[4:7], a[196:199]\n\t"
    "v_cvt_pk_bf16_f32 v205, v58, v59\n\t"
    "v_lshl_add_u32 v179, v179, 2, s56\n\t"
    "v_mfma_f32_16x16x32_bf16 a[196:199], a[104:107], a[0:3], a[196:199]\n\t"
    "buffer_load_dwordx4 v229, s[36:39], s48 offen offset:1024 lds\n\t"
    "v_mfma_f32_16x16x32_bf16 a[224:227], a[112:115], a[0:3], a[224:227]\n\t"
    "ds_read_b64_tr_b16 a[34:35], v240 offset:16896\n\t"
    "ds_read_b64_tr_b16 a[40:41], v241 offset:16384\n\t"
    "v_mfma_f32_16x16x32_bf16 a[224:227], a[116:119], a[4:7], a[224:227]\n\t"
    "ds_read_b128 a[100:103], v237 offset:40960\n\t"
    "ds_read_b64_tr_b16 a[38:39], v240 offset:20992\n\t"
    "v_mfma_f32_16x16x32_bf16 a[232:235], a[116:119], a[12:15], a[232:235]\n\t"
    "ds_read_b64_tr_b16 a[46:47], v241 offset:20992\n\t"
    "v_mul_f32 v66, s55, v66\n\t"
    "v_mfma_f32_16x16x32_bf16 a[232:235], a[112:115], a[8:11], a[232:235]\n\t"
    "global_store_dwordx4 v244, v[204:207], s[66:67] offset:1024 sc1\n\t"
    "v_mfma_f32_16x16x32_bf16 a[240:243], a[112:115], a[16:19], a[240:243]\n\t"
    "ds_read_b128 a[104:107], v236 offset:43008\n\t"
    "v_lshl_add_u32 v180, v180, 2, s56\n\t"
    "v_mfma_f32_16x16x32_bf16 a[240:243], a[116:119], a[20:23], a[240:243]\n\t"
    "ds_read_b64_tr_b16 a[52:53], v242 offset:20480\n\t"
    "ds_read_b64_tr_b16 a[58:59], v243 offset:16896\n\t"
    "v_mfma_f32_16x16x32_bf16 a[248:251], a[116:119], a[28:31], a[248:251]\n\t"
    "s_sub_u32 s66, s66, s70\n\t"
    "s_subb_u32 s67, s67, 0\n\t"
    "v_mfma_f32_16x16x32_bf16 a[248:251], a[112:115], a[24:27], a[248:251]\n\t"
    "buffer_load_dwordx4 v230, s[36:39], s48 offen offset:2048 lds\n\t"
    "v_mfma_f32_16x16x32_bf16 a[252:255], a[120:123], a[24:27], a[252:255]\n\t"
    "ds_read_b128 a[116:119], v237 offset:45056\n\t"
    "ds_read_b64_tr_b16 a[32:33], v240 offset:16384\n\t"
    "v_mfma_f32_16x16x32_bf16 a[252:255], a[124:127], a[28:31], a[252:255]\n\t"
    "ds_read_b128 a[108:111], v237 offset:43008\n\t"
    "ds_read_b64_tr_b16 a[48:49], v242 offset:16384\n\t"
    "v_mfma_f32_16x16x32_bf16 a[244:247], a[124:127], a[20:23], a[244:247]\n\t"
    "buffer_load_dwordx4 v231, s[36:39], s48 offen offset:3072 lds\n\t"
    "v_mfma_f32_16x16x32_bf16 a[244:247], a[120:123], a[16:19], a[244:247]\n\t"
    "v_lshl_add_u32 v181, v181, 2, s56\n\t"
    "v_mfma_f32_16x16x32_bf16 a[236:239], a[120:123], a[8:11], a[236:239]\n\t"
    "s_sub_u32 s48, s48, 128\n\t"
    "v_lshl_add_u32 v182, v182, 2, s56\n\t"
    "v_mfma_f32_16x16x32_bf16 a[236:239], a[124:127], a[12:15], a[236:239]\n\t"
    "v_mul_f32 v69, s55, v69\n\t"
    "s_and_b32 s48, s48, 1023\n\t"
    "v_mfma_f32_16x16x32_bf16 a[228:231], a[124:127], a[4:7], a[228:231]\n\t"
    "ds_read_b128 a[124:127], v237 offset:47104\n\t"
    "v_lshl_add_u32 v183, v183, 2, s56\n\t"
    "v_mfma_f32_16x16x32_bf16 a[228:231], a[120:123], a[0:3], a[228:231]\n\t"
    "ds_read_b128 a[112:115], v236 offset:45056\n\t"
    "ds_read_b128 a[120:123], v236 offset:47104\n\t"
    "s_waitcnt lgkmcnt(5)\n\t"
    "v_mfma_f32_16x16x32_bf16 a[128:131], a[64:67], a[32:35], a[128:131]\n\t"
    "v_mul_f32 v70, s55, v70\n\t"
    "s_lshl_b32 s49, s48, 12\n\t"
    "v_mfma_f32_16x16x32_bf16 a[128:131], a[68:71], a[36:39], a[128:131]\n\t"
    "s_lshl2_add_u32 s56, s99, s58\n\t"
    "s_lshl_b32 s56, s56, 12\n\t"
    "v_mfma_f32_16x16x32_bf16 a[136:139], a[68:71], a[44:47], a[136:139]\n\t"
    "v_mul_f32 v65, s55, v65\n\t"
    "s_add_u32 s56, s56, 0xc00000\n\t"
    "v_mfma_f32_16x16x32_bf16 a[136:139], a[64:67], a[40:43], a[136:139]\n\t"
    "v_mul_f32 v71, s55, v71\n\t"
    "s_add_u32 m0, s52, 0x4000\n\t"
    "s_waitcnt lgkmcnt(3)\n\t"
    "v_mfma_f32_16x16x32_bf16 a[144:147], a[64:67], a[48:51], a[144:147]\n\t"
    "v_mul_f32 v64, s55, v64\n\t"
    "v_add_u32 v184, s56, v244\n\t"
    "v_mfma_f32_16x16x32_bf16 a[144:147], a[68:71], a[52:55], a[144:147]\n\t"
    "s_waitcnt vmcnt(14) lgkmcnt(0)\n\t"
    "s_barrier\n\t"
    "v_mfma_f32_16x16x32_bf16 a[152:155], a[68:71], a[60:63], a[152:155]\n\t"
    "buffer_load_dwordx4 v232, s[40:43], s49 offen offset:0 lds\n\t"
    "v_mfma_f32_16x16x32_bf16 a[152:155], a[64:67], a[56:59], a[152:155]\n\t"
    "v_mov_b32 v223, s35\n\t"
    "v_mov_b32 v221, s71\n\t"
    "v_mfma_f32_16x16x32_bf16 a[156:159], a[72:75], a[56:59], a[156:159]\n\t"
    "ds_read_b128 a[64:67], v236 offset:0\n\t"
    "ds_read_b128 a[68:71], v237 offset:0\n\t"
    "v_mfma_f32_16x16x32_bf16 a[156:159], a[76:79], a[60:63], a[156:159]\n\t"
    "ds_read_b64_tr_b16 a[12:13], v241 offset:36864\n\t"
    "v_mfma_f32_16x16x32_bf16 a[148:151], a[76:79], a[52:55], a[148:151]\n\t"
    "s_mov_b32 s27, s71\n\t"
    "v_readfirstlane_b32 s34, v222\n\t"
    "v_mfma_f32_16x16x32_bf16 a[148:151], a[72:75], a[48:51], a[148:151]\n\t"
    "v_mul_f32 v67, s55, v67\n\t"
    "s_mov_b32 s56, 0x05040100\n\t"
    "v_mfma_f32_16x16x32_bf16 a[140:143], a[72:75], a[40:43], a[140:143]\n\t"
    "buffer_load_dwordx4 v233, s[40:43], s49 offen offset:1024 lds\n\t"
    "v_mfma_f32_16x16x32_bf16 a[140:143], a[76:79], a[44:47], a[140:143]\n\t"
    "v_mul_f32 v73, s55, v73\n\t"
    "v_mul_f32 v75, s55, v75\n\t"
    "v_mfma_f32_16x16x32_bf16 a[132:135], a[76:79], a[36:39], a[132:135]\n\t"
    "v_cvt_pk_bf16_f32 v211, v70, v71\n\t"
    "s_mov_b32 s57, 0x07060302\n\t"
    "v_mfma_f32_16x16x32_bf16 a[132:135], a[72:75], a[32:35], a[132:135]\n\t"
    "ds_read_b128 a[76:79], v237 offset:2048\n\t"
    "v_mul_f32 v68, s55, v68\n\t"
    "v_mfma_f32_16x16x32_bf16 a[160:163], a[80:83], a[32:35], a[160:163]\n\t"
    "buffer_load_dwordx4 v234, s[40:43], s49 offen offset:2048 lds\n\t"
    "v_mfma_f32_16x16x32_bf16 a[160:163], a[84:87], a[36:39], a[160:163]\n\t"
    "ds_read_b64_tr_b16 a[8:9], v241 offset:32768\n\t"
    "ds_read_b32 v183, v183\n\t"
    "v_mfma_f32_16x16x32_bf16 a[168:171], a[84:87], a[44:47], a[168:171]\n\t"
    "v_mul_f32 v77, s55, v77\n\t"
    "v_cvt_pk_bf16_f32 v208, v64, v65\n\t"
    "v_mfma_f32_16x16x32_bf16 a[168:171], a[80:83], a[40:43], a[168:171]\n\t"
    "v_cvt_pk_bf16_f32 v209, v66, v67\n\t"
    "v_cvt_pk_bf16_f32 v210, v68, v69\n\t"
    "v_mfma_f32_16x16x32_bf16 a[176:179], a[80:83], a[48:51], a[176:179]\n\t"
    "ds_read_b128 a[72:75], v236 offset:2048\n\t"
    "ds_read_b64_tr_b16 a[18:19], v242 offset:33280\n\t"
    "v_mfma_f32_16x16x32_bf16 a[176:179], a[84:87], a[52:55], a[176:179]\n\t"
    "buffer_load_dwordx4 v235, s[40:43], s49 offen offset:3072 lds\n\t"
    "v_mfma_f32_16x16x32_bf16 a[184:187], a[84:87], a[60:63], a[184:187]\n\t"
    "ds_read_b64_tr_b16 a[24:25], v243 offset:32768\n\t"
    "v_mfma_f32_16x16x32_bf16 a[184:187], a[80:83], a[56:59], a[184:187]\n\t"
    "s_add_u32 m0, s75, 0x8000\n\t"
    "v_mfma_f32_16x16x32_bf16 a[188:191], a[88:91], a[56:59], a[188:191]\n\t"
    "global_store_dwordx4 v244, v[208:211], s[66:67] offset:-4096 sc1\n\t"
    "Lc0_%=:\n\t"
    "v_mfma_f32_16x16x32_bf16 a[188:191], a[92:95], a[60:63], a[188:191]\n\t"
    "ds_read_b32 v179, v179\n\t"
    "v_mul_f32 v74, s55, v74\n\t"
    "v_mfma_f32_16x16x32_bf16 a[180:183], a[92:95], a[52:55], a[180:183]\n\t"
    "buffer_load_dwordx4 v224, s[36:39], s48 offen offset:0 lds\n\t"
    "v_mfma_f32_16x16x32_bf16 a[180:183], a[88:91], a[48:51], a[180:183]\n\t"
    "ds_read_b64_tr_b16 a[22:23], v242 offset:37376\n\t"
    "ds_read_b64_tr_b16 a[28:29], v243 offset:36864\n\t"
    "v_mfma_f32_16x16x32_bf16 a[172:175], a[88:91], a[40:43], a[172:175]\n\t"
    "ds_read_b32 v176, v176\n\t"
    "ds_read_b32 v178, v178\n\t"
    "v_mfma_f32_16x16x32_bf16 a[172:175], a[92:95], a[44:47], a[172:175]\n\t"
    "buffer_load_dwordx4 v225, s[36:39], s48 offen offset:1024 lds\n\t"
    "v_mfma_f32_16x16x32_bf16 a[164:167], a[92:95], a[36:39], a[164:167]\n\t"
    "ds_read_b128 a[92:95], v237 offset:6144\n\t"
    "v_mfma_f32_16x16x32_bf16 a[164:167], a[88:91], a[32:35], a[164:167]\n\t"
    "ds_read_b32 v177, v177\n\t"
    "ds_read_b32 v182, v182\n\t"
    "v_mfma_f32_16x16x32_bf16 a[192:195], a[96:99], a[32:35], a[192:195]\n\t"
    "ds_read_b32 v180, v180\n\t"
    "v_mfma_f32_16x16x32_bf16 a[192:195], a[100:103], a[36:39], a[192:195]\n\t"
    "ds_read_b128 a[84:87], v237 offset:4096\n\t"
    "ds_read_b32 v181, v181\n\t"
    "v_mfma_f32_16x16x32_bf16 a[200:203], a[100:103], a[44:47], a[200:203]\n\t"
    "buffer_load_dwordx4 v226, s[36:39], s48 offen offset:2048 lds\n\t"
    "v_mfma_f32_16x16x32_bf16 a[200:203], a[96:99], a[40:43], a[200:203]\n\t"
    "ds_read_b128 a[80:83], v236 offset:4096\n\t"
    "v_mfma_f32_16x16x32_bf16 a[208:211], a[96:99], a[48:51], a[208:211]\n\t"
    "v_mul_f32 v72, s55, v72\n\t"
    "v_cvt_pk_bf16_f32 v213, v74, v75\n\t"
    "Lis4_%=:\n\t"
    "s_cbranch_execz Lis8_%=\n\t"
    "v_mfma_f32_16x16x32_bf16 a[208:211], a[100:103], a[52:55], a[208:211]\n\t"
    "v_mul_f32 v76, s55, v76\n\t"
    "v_mul_f32 v78, s55, v78\n\t"
    "v_mfma_f32_16x16x32_bf16 a[216:219], a[100:103], a[60:63], a[216:219]\n\t"
    "buffer_load_dwordx4 v227, s[36:39], s48 offen offset:3072 lds\n\t"
    "v_mfma_f32_16x16x32_bf16 a[216:219], a[96:99], a[56:59], a[216:219]\n\t"
    "ds_read_b128 a[88:91], v236 offset:6144\n\t"
    "ds_read_b128 a[100:103], v237 offset:8192\n\t"
    "v_mfma_f32_16x16x32_bf16 a[220:223], a[104:107], a[56:59], a[220:223]\n\t"
    "v_mul_f32 v79, s55, v79\n\t"
    "s_add_u32 m0, s75, 0x9000\n\t"
    "v_mfma_f32_16x16x32_bf16 a[220:223], a[108:111], a[60:63], a[220:223]\n\t"
    "ds_read_b64_tr_b16 a[2:3], v240 offset:33280\n\t"
    "ds_read_b64_tr_b16 a[30:31], v243 offset:37376\n\t"
    "v_mfma_f32_16x16x32_bf16 a[212:215], a[108:111], a[52:55], a[212:215]\n\t"
    "v_cvt_pk_bf16_f32 v215, v78, v79\n\t"
    "s_waitcnt lgkmcnt(6)\n\t"
    "v_perm_b32 v188, v177, v176, s56\n\t"
    "v_mfma_f32_16x16x32_bf16 a[212:215], a[104:107], a[48:51], a[212:215]\n\t"
    "ds_read_b128 a[96:99], v236 offset:8192\n\t"
    "ds_read_b64_tr_b16 a[6:7], v240 offset:37376\n\t"
    "v_mfma_f32_16x16x32_bf16 a[204:207], a[104:107], a[40:43], a[204:207]\n\t"
    "ds_read_b64_tr_b16 a[16:17], v242 offset:32768\n\t"
    "v_perm_b32 v189, v179, v178, s56\n\t"
    "v_mfma_f32_16x16x32_bf16 a[204:207], a[108:111], a[44:47], a[204:207]\n\t"
    "buffer_load_dwordx4 v228, s[36:39], s48 offen offset:0 lds\n\t"
    "v_mfma_f32_16x16x32_bf16 a[196:199], a[108:111], a[36:39], a[196:199]\n\t"
    "s_waitcnt lgkmcnt(7)\n\t"
    "v_perm_b32 v190, v181, v180, s56\n\t"
    "v_mfma_f32_16x16x32_bf16 a[196:199], a[104:107], a[32:35], a[196:199]\n\t"
    "v_cvt_pk_bf16_f32 v214, v76, v77\n\t"
    "v_mfma_f32_16x16x32_bf16 a[224:227], a[112:115], a[32:35], a[224:227]\n\t"
    "buffer_load_dwordx4 v229, s[36:39], s48 offen offset:1024 lds\n\t"
    "v_mfma_f32_16x16x32_bf16 a[224:227], a[116:119], a[36:39], a[224:227]\n\t"
    "ds_read_b128 a[104:107], v236 offset:10240\n\t"
    "v_cvt_pk_bf16_f32 v212, v72, v73\n\t"
    "v_mfma_f32_16x16x32_bf16 a[232:235], a[116:119], a[44:47], a[232:235]\n\t"
    "ds_read_b64_tr_b16 a[10:11], v241 offset:33280\n\t"
    "v_mfma_f32_16x16x32_bf16 a[232:235], a[112:115], a[40:43], a[232:235]\n\t"
    "buffer_load_dwordx4 v230, s[36:39], s48 offen offset:2048 lds\n\t"
    "v_mfma_f32_16x16x32_bf16 a[240:243], a[112:115], a[48:51], a[240:243]\n\t"
    "global_store_dwordx4 v244, v[212:215], s[66:67] offset:-3072 sc1\n\t"
    "v_mfma_f32_16x16x32_bf16 a[240:243], a[116:119], a[52:55], a[240:243]\n\t"
    "ds_read_b64_tr_b16 a[26:27], v243 offset:33280\n\t"
    "v_mfma_f32_16x16x32_bf16 a[248:251], a[116:119], a[60:63], a[248:251]\n\t"
    "ds_read_b64_tr_b16 a[4:5], v240 offset:36864\n\t"
    "ds_read_b64_tr_b16 a[14:15], v241 offset:37376\n\t"
    "v_mfma_f32_16x16x32_bf16 a[248:251], a[112:115], a[56:59], a[248:251]\n\t"
    "buffer_load_dwordx4 v231, s[36:39], s48 offen offset:3072 lds\n\t"
    "v_mfma_f32_16x16x32_bf16 a[252:255], a[120:123], a[56:59], a[252:255]\n\t"
    "ds_read_b128 a[108:111], v237 offset:10240\n\t"
    "v_perm_b32 v191, v183, v182, s56\n\t"
    "v_mfma_f32_16x16x32_bf16 a[252:255], a[124:127], a[60:63], a[252:255]\n\t"
    "ds_read_b128 a[112:115], v236 offset:12288\n\t"
    "ds_read_b64_tr_b16 a[20:21], v242 offset:36864\n\t"
    "v_mfma_f32_16x16x32_bf16 a[244:247], a[124:127], a[52:55], a[244:247]\n\t"
    "ds_read_b64_tr_b16 a[0:1], v240 offset:32768\n\t"
    "s_sub_u32 s48, s48, 128\n\t"
    "v_mfma_f32_16x16x32_bf16 a[244:247], a[120:123], a[48:51], a[244:247]\n\t"
    "s_mov_b64 exec, 1\n\t"
    "global_store_dwordx4 v184, v[220:223], s[84:85] offset:2048 sc1\n\t"
    "s_mov_b64 exec, -1\n\t"
    "v_mfma_f32_16x16x32_bf16 a[236:239], a[120:123], a[40:43], a[236:239]\n\t"
    "v_perm_b32 v192, v177, v176, s57\n\t"
    "v_mfma_f32_16x16x32_bf16 a[236:239], a[124:127], a[44:47], a[236:239]\n\t"
    "s_and_b32 s48, s48, 1023\n\t"
    "v_mfma_f32_16x16x32_bf16 a[228:231], a[124:127], a[36:39], a[228:231]\n\t"
    "ds_read_b128 a[124:127], v237 offset:14336\n\t"
    "v_mfma_f32_16x16x32_bf16 a[228:231], a[120:123], a[32:35], a[228:231]\n\t"
    "ds_read_b128 a[116:119], v237 offset:12288\n\t"
    "ds_read_b128 a[120:123], v236 offset:14336\n\t"
    "s_waitcnt lgkmcnt(3)\n\t"
    "v_mfma_f32_16x16x32_bf16 a[128:131], a[64:67], a[0:3], a[128:131]\n\t"
    "v_perm_b32 v193, v179, v178, s57\n\t"
    "v_mfma_f32_16x16x32_bf16 a[128:131], a[68:71], a[4:7], a[128:131]\n\t"
    "v_mul_f32 v81, s55, v81\n\t"
    "v_mfma_f32_16x16x32_bf16 a[136:139], a[68:71], a[12:15], a[136:139]\n\t"
    "v_perm_b32 v194, v181, v180, s57\n\t"
    "v_mfma_f32_16x16x32_bf16 a[136:139], a[64:67], a[8:11], a[136:139]\n\t"
    "v_mul_f32 v87, s55, v87\n\t"
    "v_perm_b32 v195, v183, v182, s57\n\t"
    "v_mfma_f32_16x16x32_bf16 a[144:147], a[64:67], a[16:19], a[144:147]\n\t"
    "s_lshl_b32 s49, s48, 12\n\t"
    "v_mfma_f32_16x16x32_bf16 a[144:147], a[68:71], a[20:23], a[144:147]\n\t"
    "s_waitcnt vmcnt(15) lgkmcnt(0)\n\t"
    "s_barrier\n\t"
    "v_mfma_f32_16x16x32_bf16 a[152:155], a[68:71], a[28:31], a[152:155]\n\t"
    "s_add_u32 m0, s52, 0x8000\n\t"
    "v_mfma_f32_16x16x32_bf16 a[152:155], a[64:67], a[24:27], a[152:155]\n\t"
    "ds_read_b128 a[68:71], v239 offset:0\n\t"
    "v_mul_f32 v85, s55, v85\n\t"
    "v_mfma_f32_16x16x32_bf16 a[156:159], a[72:75], a[24:27], a[156:159]\n\t"
    "ds_read_b64_tr_b16 a[50:51], v242 offset:512\n\t"
    "v_mfma_f32_16x16x32_bf16 a[156:159], a[76:79], a[28:31], a[156:159]\n\t"
    "buffer_load_dwordx4 v232, s[40:43], s49 offen offset:0 lds\n\t"
    "v_mfma_f32_16x16x32_bf16 a[148:151], a[76:79], a[20:23], a[148:151]\n\t"
    "v_mul_f32 v82, s55, v82\n\t"
    "v_mfma_f32_16x16x32_bf16 a[148:151], a[72:75], a[16:19], a[148:151]\n\t"
    "ds_read_b128 a[64:67], v238 offset:0\n\t"
    "v_mfma_f32_16x16x32_bf16 a[140:143], a[72:75], a[8:11], a[140:143]\n\t"
    "v_mul_f32 v80, s55, v80\n\t"
    "v_mfma_f32_16x16x32_bf16 a[140:143], a[76:79], a[12:15], a[140:143]\n\t"
    "buffer_load_dwordx4 v233, s[40:43], s49 offen offset:1024 lds\n\t"
    "v_mfma_f32_16x16x32_bf16 a[132:135], a[76:79], a[4:7], a[132:135]\n\t"
    "ds_read_b128 a[76:79], v239 offset:2048\n\t"
    "v_mfma_f32_16x16x32_bf16 a[132:135], a[72:75], a[0:3], a[132:135]\n\t"
    "ds_read_b128 a[72:75], v238 offset:2048\n\t"
    "ds_read_b64_tr_b16 a[56:57], v243 offset:0\n\t"
    "v_mfma_f32_16x16x32_bf16 a[160:163], a[80:83], a[0:3], a[160:163]\n\t"
    "buffer_load_dwordx4 v234, s[40:43], s49 offen offset:2048 lds\n\t"
    "v_mfma_f32_16x16x32_bf16 a[160:163], a[84:87], a[4:7], a[160:163]\n\t"
    "ds_read_b64_tr_b16 a[44:45], v241 offset:4096\n\t"
    "v_mul_f32 v83, s55, v83\n\t"
    "v_mfma_f32_16x16x32_bf16 a[168:171], a[84:87], a[12:15], a[168:171]\n\t"
    "v_mul_f32 v86, s55, v86\n\t"
    "v_mfma_f32_16x16x32_bf16 a[168:171], a[80:83], a[8:11], a[168:171]\n\t"
    "ds_read_b64_tr_b16 a[58:59], v243 offset:512\n\t"
    "v_mfma_f32_16x16x32_bf16 a[176:179], a[80:83], a[16:19], a[176:179]\n\t"
    "v_cvt_pk_bf16_f32 v219, v86, v87\n\t"
    "v_mfma_f32_16x16x32_bf16 a[176:179], a[84:87], a[20:23], a[176:179]\n\t"
    "ds_read_b64_tr_b16 a[38:39], v240 offset:4608\n\t"
    "v_mfma_f32_16x16x32_bf16 a[184:187], a[84:87], a[28:31], a[184:187]\n\t"
    "buffer_load_dwordx4 v235, s[40:43], s49 offen offset:3072 lds\n\t"
    "v_mfma_f32_16x16x32_bf16 a[184:187], a[80:83], a[24:27], a[184:187]\n\t"
    "ds_read_b64_tr_b16 a[34:35], v240 offset:512\n\t"
    "v_mul_f32 v84, s55, v84\n\t"
    "v_mfma_f32_16x16x32_bf16 a[188:191], a[88:91], a[24:27], a[188:191]\n\t"
    "s_add_u32 m0, s75, 0x0\n\t"
    "v_mfma_f32_16x16x32_bf16 a[188:191], a[92:95], a[28:31], a[188:191]\n\t"
    "buffer_load_dwordx4 v224, s[36:39], s48 offen offset:0 lds\n\t"
    "v_mfma_f32_16x16x32_bf16 a[180:183], a[92:95], a[20:23], a[180:183]\n\t"
    "v_cvt_pk_bf16_f32 v218, v84, v85\n\t"
    "v_mfma_f32_16x16x32_bf16 a[180:183], a[88:91], a[16:19], a[180:183]\n\t"
    "v_cvt_pk_bf16_f32 v216, v80, v81\n\t"
    "v_mfma_f32_16x16x32_bf16 a[172:175], a[88:91], a[8:11], a[172:175]\n\t"
    "ds_read_b64_tr_b16 a[32:33], v240 offset:0\n\t"
    "v_mfma_f32_16x16x32_bf16 a[172:175], a[92:95], a[12:15], a[172:175]\n\t"
    "ds_read_b64_tr_b16 a[52:53], v242 offset:4096\n\t"
    "v_mfma_f32_16x16x32_bf16 a[164:167], a[92:95], a[4:7], a[164:167]\n\t"
    "buffer_load_dwordx4 v225, s[36:39], s48 offen offset:1024 lds\n\t"
    "v_mfma_f32_16x16x32_bf16 a[164:167], a[88:91], a[0:3], a[164:167]\n\t"
    "ds_read_b128 a[84:87], v239 offset:4096\n\t"
    "v_mfma_f32_16x16x32_bf16 a[192:195], a[96:99], a[0:3], a[192:195]\n\t"
    "ds_read_b128 a[80:83], v238 offset:4096\n\t"
    "v_mfma_f32_16x16x32_bf16 a[192:195], a[100:103], a[4:7], a[192:195]\n\t"
    "buffer_load_dwordx4 v226, s[36:39], s48 offen offset:2048 lds\n\t"
    "v_mfma_f32_16x16x32_bf16 a[200:203], a[100:103], a[12:15], a[200:203]\n\t"
    "v_cvt_pk_bf16_f32 v217, v82, v83\n\t"
    "v_mfma_f32_16x16x32_bf16 a[200:203], a[96:99], a[8:11], a[200:203]\n\t"
    "global_store_dwordx4 v244, v[216:219], s[66:67] offset:0 sc1\n\t"
    "v_mfma_f32_16x16x32_bf16 a[208:211], a[96:99], a[16:19], a[208:211]\n\t"
    "ds_read_b128 a[92:95], v239 offset:6144\n\t"
    "v_mfma_f32_16x16x32_bf16 a[208:211], a[100:103], a[20:23], a[208:211]\n\t"
    "ds_read_b64_tr_b16 a[40:41], v241 offset:0\n\t"
    "v_mfma_f32_16x16x32_bf16 a[216:219], a[100:103], a[28:31], a[216:219]\n\t"
    "ds_read_b128 a[88:91], v238 offset:6144\n\t"
    "v_mfma_f32_16x16x32_bf16 a[216:219], a[96:99], a[24:27], a[216:219]\n\t"
    "buffer_load_dwordx4 v227, s[36:39], s48 offen offset:3072 lds\n\t"
    "v_mfma_f32_16x16x32_bf16 a[220:223], a[104:107], a[24:27], a[220:223]\n\t"
    "ds_read_b64_tr_b16 a[54:55], v242 offset:4608\n\t"
    "v_mfma_f32_16x16x32_bf16 a[220:223], a[108:111], a[28:31], a[220:223]\n\t"
    "ds_read_b64_tr_b16 a[62:63], v243 offset:4608\n\t"
    "v_mfma_f32_16x16x32_bf16 a[212:215], a[108:111], a[20:23], a[212:215]\n\t"
    "s_add_u32 m0, s75, 0x1000\n\t"
    "v_mfma_f32_16x16x32_bf16 a[212:215], a[104:107], a[16:19], a[212:215]\n\t"
    "ds_read_b64_tr_b16 a[48:49], v242 offset:0\n\t"
    "v_mfma_f32_16x16x32_bf16 a[204:207], a[104:107], a[8:11], a[204:207]\n\t"
    "ds_read_b64_tr_b16 a[60:61], v243 offset:4096\n\t"
    "v_mfma_f32_16x16x32_bf16 a[204:207], a[108:111], a[12:15], a[204:207]\n\t"
    "buffer_load_dwordx4 v228, s[36:39], s48 offen offset:0 lds\n\t"
    "v_mfma_f32_16x16x32_bf16 a[196:199], a[108:111], a[4:7], a[196:199]\n\t"
    "ds_read_b128 a[100:103], v239 offset:8192\n\t"
    "v_mfma_f32_16x16x32_bf16 a[196:199], a[104:107], a[0:3], a[196:199]\n\t"
    "buffer_load_dwordx4 v229, s[36:39], s48 offen offset:1024 lds\n\t"
    "v_mfma_f32_16x16x32_bf16 a[224:227], a[112:115], a[0:3], a[224:227]\n\t"
    "ds_read_b128 a[96:99], v238 offset:8192\n\t"
    "v_mfma_f32_16x16x32_bf16 a[224:227], a[116:119], a[4:7], a[224:227]\n\t"
    "ds_read_b128 a[104:107], v238 offset:10240\n\t"
    "v_mfma_f32_16x16x32_bf16 a[232:235], a[116:119], a[12:15], a[232:235]\n\t"
    "ds_read_b64_tr_b16 a[46:47], v241 offset:4608\n\t"
    "v_mfma_f32_16x16x32_bf16 a[232:235], a[112:115], a[8:11], a[232:235]\n\t"
    "ds_read_b64_tr_b16 a[42:43], v241 offset:512\n\t"
    "v_mfma_f32_16x16x32_bf16 a[240:243], a[112:115], a[16:19], a[240:243]\n\t"
    "ds_read_b64_tr_b16 a[36:37], v240 offset:4096\n\t"
    "v_mfma_f32_16x16x32_bf16 a[240:243], a[116:119], a[20:23], a[240:243]\n\t"
    "s_lshl2_add_u32 s56, s98, s61\n\t"
    "v_mfma_f32_16x16x32_bf16 a[248:251], a[116:119], a[28:31], a[248:251]\n\t"
    "ds_read_b128 a[108:111], v239 offset:10240\n\t"
    "v_mfma_f32_16x16x32_bf16 a[248:251], a[112:115], a[24:27], a[248:251]\n\t"
    "buffer_load_dwordx4 v230, s[36:39], s48 offen offset:2048 lds\n\t"
    "v_mfma_f32_16x16x32_bf16 a[252:255], a[120:123], a[24:27], a[252:255]\n\t"
    "buffer_load_dwordx4 v231, s[36:39], s48 offen offset:3072 lds\n\t"
    "v_mfma_f32_16x16x32_bf16 a[252:255], a[124:127], a[28:31], a[252:255]\n\t"
    "s_lshl_b32 s56, s56, 11\n\t"
    "v_mfma_f32_16x16x32_bf16 a[244:247], a[124:127], a[20:23], a[244:247]\n\t"
    "ds_read_b128 a[112:115], v238 offset:12288\n\t"
    "v_mfma_f32_16x16x32_bf16 a[244:247], a[120:123], a[16:19], a[244:247]\n\t"
    "s_mov_b32 s48, s97\n\t"
    "v_mfma_f32_16x16x32_bf16 a[236:239], a[120:123], a[8:11], a[236:239]\n\t"
    "s_lshl_b32 s49, s48, 12\n\t"
    "v_mfma_f32_16x16x32_bf16 a[236:239], a[124:127], a[12:15], a[236:239]\n\t"
    "ds_read_b128 a[116:119], v239 offset:12288\n\t"
    "v_mfma_f32_16x16x32_bf16 a[228:231], a[124:127], a[4:7], a[228:231]\n\t"
    "ds_read_b128 a[124:127], v239 offset:14336\n\t"
    "v_mfma_f32_16x16x32_bf16 a[228:231], a[120:123], a[0:3], a[228:231]\n\t"
    "ds_read_b128 a[120:123], v238 offset:14336\n\t"
    "s_waitcnt lgkmcnt(6)\n\t"
    "v_mfma_f32_16x16x32_bf16 a[128:131], a[64:67], a[32:35], a[128:131]\n\t"
    "s_mov_b64 s[40:41], s[64:65]\n\t"
    "s_waitcnt lgkmcnt(5)\n\t"
    "v_mfma_f32_16x16x32_bf16 a[128:131], a[68:71], a[36:39], a[128:131]\n\t"
    "v_mul_f32 v89, s55, v89\n\t"
    "v_mul_f32 v94, s55, v94\n\t"
    "v_mfma_f32_16x16x32_bf16 a[136:139], a[68:71], a[44:47], a[136:139]\n\t"
    "v_mul_f32 v91, s55, v91\n\t"
    "v_mfma_f32_16x16x32_bf16 a[136:139], a[64:67], a[40:43], a[136:139]\n\t"
    "v_mul_f32 v90, s55, v90\n\t"
    "v_mfma_f32_16x16x32_bf16 a[144:147], a[64:67], a[48:51], a[144:147]\n\t"
    "s_mov_b64 s[36:37], s[62:63]\n\t"
    "v_mfma_f32_16x16x32_bf16 a[144:147], a[68:71], a[52:55], a[144:147]\n\t"
    "s_waitcnt vmcnt(14) lgkmcnt(0)\n\t"
    "s_barrier\n\t"
    "v_mfma_f32_16x16x32_bf16 a[152:155], a[68:71], a[60:63], a[152:155]\n\t"
    "s_add_u32 m0, s52, 0x0\n\t"
    "v_mfma_f32_16x16x32_bf16 a[152:155], a[64:67], a[56:59], a[152:155]\n\t"
    "buffer_load_dwordx4 v232, s[40:43], s49 offen offset:0 lds\n\t"
    "v_mfma_f32_16x16x32_bf16 a[156:159], a[72:75], a[56:59], a[156:159]\n\t"
    "v_cvt_pk_bf16_f32 v161, v90, v91\n\t"
    "v_mfma_f32_16x16x32_bf16 a[156:159], a[76:79], a[60:63], a[156:159]\n\t"
    "ds_read_b64_tr_b16 a[8:9], v241 offset:16384\n\t"
    "v_mfma_f32_16x16x32_bf16 a[148:151], a[76:79], a[52:55], a[148:151]\n\t"
    "global_store_dwordx4 v184, v[188:191], s[84:85] offset:0 sc1\n\t"
    "v_mfma_f32_16x16x32_bf16 a[148:151], a[72:75], a[48:51], a[148:151]\n\t"
    "buffer_load_dwordx4 v233, s[40:43], s49 offen offset:1024 lds\n\t"
    "v_mfma_f32_16x16x32_bf16 a[140:143], a[72:75], a[40:43], a[140:143]\n\t"
    "s_add_u32 s56, s56, 0x8000\n\t"
    "v_mfma_f32_16x16x32_bf16 a[140:143], a[76:79], a[44:47], a[140:143]\n\t"
    "ds_read_b64_tr_b16 a[2:3], v240 offset:16896\n\t"
    "v_mfma_f32_16x16x32_bf16 a[132:135], a[76:79], a[36:39], a[132:135]\n\t"
    "ds_read_b128 a[68:71], v237 offset:32768\n\t"
    "v_mfma_f32_16x16x32_bf16 a[132:135], a[72:75], a[32:35], a[132:135]\n\t"
    "v_mul_f32 v92, s55, v92\n\t"
    "v_mfma_f32_16x16x32_bf16 a[160:163], a[80:83], a[32:35], a[160:163]\n\t"
    "ds_read_b128 a[64:67], v236 offset:32768\n\t"
    "v_mfma_f32_16x16x32_bf16 a[160:163], a[84:87], a[36:39], a[160:163]\n\t"
    "v_mul_f32 v88, s55, v88\n\t"
    "v_mul_f32 v95, s55, v95\n\t"
    "v_mfma_f32_16x16x32_bf16 a[168:171], a[84:87], a[44:47], a[168:171]\n\t"
    "v_cvt_pk_bf16_f32 v160, v88, v89\n\t"
    "v_mfma_f32_16x16x32_bf16 a[168:171], a[80:83], a[40:43], a[168:171]\n\t"
    "buffer_load_dwordx4 v234, s[40:43], s49 offen offset:2048 lds\n\t"
    "v_mfma_f32_16x16x32_bf16 a[176:179], a[80:83], a[48:51], a[176:179]\n\t"
    "ds_read_b128 a[76:79], v237 offset:34816\n\t"
    "ds_read_b64_tr_b16 a[28:29], v243 offset:20480\n\t"
    "v_mfma_f32_16x16x32_bf16 a[176:179], a[84:87], a[52:55], a[176:179]\n\t"
    "buffer_load_dwordx4 v235, s[40:43], s49 offen offset:3072 lds\n\t"
    "v_mfma_f32_16x16x32_bf16 a[184:187], a[84:87], a[60:63], a[184:187]\n\t"
    "ds_read_b64_tr_b16 a[22:23], v242 offset:20992\n\t"
    "v_mfma_f32_16x16x32_bf16 a[184:187], a[80:83], a[56:59], a[184:187]\n\t"
    "ds_read_b64_tr_b16 a[6:7], v240 offset:20992\n\t"
    "v_mfma_f32_16x16x32_bf16 a[188:191], a[88:91], a[56:59], a[188:191]\n\t"
    "s_add_u32 m0, s75, 0x10000\n\t"
    "v_mfma_f32_16x16x32_bf16 a[188:191], a[92:95], a[60:63], a[188:191]\n\t"
    "v_cvt_pk_bf16_f32 v163, v94, v95\n\t"
    "v_mfma_f32_16x16x32_bf16 a[180:183], a[92:95], a[52:55], a[180:183]\n\t"
    "buffer_load_dwordx4 v224, s[36:39], s48 offen offset:0 lds\n\t"
    "v_mfma_f32_16x16x32_bf16 a[180:183], a[88:91], a[48:51], a[180:183]\n\t"
    "ds_read_b128 a[72:75], v236 offset:34816\n\t"
    "v_mfma_f32_16x16x32_bf16 a[172:175], a[88:91], a[40:43], a[172:175]\n\t"
    "ds_read_b64_tr_b16 a[24:25], v243 offset:16384\n\t"
    "v_mfma_f32_16x16x32_bf16 a[172:175], a[92:95], a[44:47], a[172:175]\n\t"
    "ds_read_b128 a[84:87], v237 offset:36864\n\t"
    "v_mfma_f32_16x16x32_bf16 a[164:167], a[92:95], a[36:39], a[164:167]\n\t"
    "ds_read_b64_tr_b16 a[18:19], v242 offset:16896\n\t"
    "v_mul_f32 v93, s55, v93\n\t"
    "v_mfma_f32_16x16x32_bf16 a[164:167], a[88:91], a[32:35], a[164:167]\n\t"
    "ds_read_b64_tr_b16 a[26:27], v243 offset:16896\n\t"
    "v_mfma_f32_16x16x32_bf16 a[192:195], a[96:99], a[32:35], a[192:195]\n\t"
    "buffer_load_dwordx4 v225, s[36:39], s48 offen offset:1024 lds\n\t"
    "v_mfma_f32_16x16x32_bf16 a[192:195], a[100:103], a[36:39], a[192:195]\n\t"
    "ds_read_b64_tr_b16 a[10:11], v241 offset:16896\n\t"
    "v_mfma_f32_16x16x32_bf16 a[200:203], a[100:103], a[44:47], a[200:203]\n\t"
    "ds_read_b64_tr_b16 a[16:17], v242 offset:16384\n\t"
    "v_mfma_f32_16x16x32_bf16 a[200:203], a[96:99], a[40:43], a[200:203]\n\t"
    "ds_read_b128 a[80:83], v236 offset:36864\n\t"
    "v_mfma_f32_16x16x32_bf16 a[208:211], a[96:99], a[48:51], a[208:211]\n\t"
    "ds_read_b128 a[88:91], v236 offset:38912\n\t"
    "v_cvt_pk_bf16_f32 v162, v92, v93\n\t"
    "v_mfma_f32_16x16x32_bf16 a[208:211], a[100:103], a[52:55], a[208:211]\n\t"
    "buffer_load_dwordx4 v226, s[36:39], s48 offen offset:2048 lds\n\t"
    "v_mfma_f32_16x16x32_bf16 a[216:219], a[100:103], a[60:63], a[216:219]\n\t"
    "v_add_u32 v251, s56, v244\n\t"
    "v_mfma_f32_16x16x32_bf16 a[216:219], a[96:99], a[56:59], a[216:219]\n\t"
    "global_store_dwordx4 v244, v[160:163], s[66:67] offset:1024 sc1\n\t"
    "v_mfma_f32_16x16x32_bf16 a[220:223], a[104:107], a[56:59], a[220:223]\n\t"
    "buffer_load_dwordx4 v227, s[36:39], s48 offen offset:3072 lds\n\t"
    "v_mfma_f32_16x16x32_bf16 a[220:223], a[108:111], a[60:63], a[220:223]\n\t"
    "ds_read_b128 a[92:95], v237 offset:38912\n\t"
    "v_mfma_f32_16x16x32_bf16 a[212:215], a[108:111], a[52:55], a[212:215]\n\t"
    "s_add_u32 s66, s66, 0x4000\n\t"
    "s_addc_u32 s67, s67, 0\n\t"
    "v_mfma_f32_16x16x32_bf16 a[212:215], a[104:107], a[48:51], a[212:215]\n\t"
    "ds_read_b128 a[100:103], v237 offset:40960\n\t"
    "v_mfma_f32_16x16x32_bf16 a[204:207], a[104:107], a[40:43], a[204:207]\n\t"
    "s_add_u32 m0, s75, 0x11000\n\t"
    "v_mfma_f32_16x16x32_bf16 a[204:207], a[108:111], a[44:47], a[204:207]\n\t"
    "ds_read_b128 a[96:99], v236 offset:40960\n\t"
    "ds_read_b64_tr_b16 a[30:31], v243 offset:20992\n\t"
    "v_mfma_f32_16x16x32_bf16 a[196:199], a[108:111], a[36:39], a[196:199]\n\t"
    "buffer_load_dwordx4 v228, s[36:39], s48 offen offset:0 lds\n\t"
    "v_mfma_f32_16x16x32_bf16 a[196:199], a[104:107], a[32:35], a[196:199]\n\t"
    "buffer_load_dwordx4 v229, s[36:39], s48 offen offset:1024 lds\n\t"
    "v_mfma_f32_16x16x32_bf16 a[224:227], a[112:115], a[32:35], a[224:227]\n\t"
    "ds_read_b64_tr_b16 a[0:1], v240 offset:16384\n\t"
    "v_mfma_f32_16x16x32_bf16 a[224:227], a[116:119], a[36:39], a[224:227]\n\t"
    "global_store_dwordx4 v184, v[192:195], s[84:85] offset:1024 sc1\n\t"
    "v_mfma_f32_16x16x32_bf16 a[232:235], a[116:119], a[44:47], a[232:235]\n\t"
    "ds_read_b64_tr_b16 a[14:15], v241 offset:20992\n\t"
    "v_mfma_f32_16x16x32_bf16 a[232:235], a[112:115], a[40:43], a[232:235]\n\t"
    "ds_read_b128 a[108:111], v237 offset:43008\n\t"
    "v_mul_f32 v97, s55, v97\n\t"
    "v_mfma_f32_16x16x32_bf16 a[240:243], a[112:115], a[48:51], a[240:243]\n\t"
    "ds_read_b64_tr_b16 a[20:21], v242 offset:20480\n\t"
    "v_mfma_f32_16x16x32_bf16 a[240:243], a[116:119], a[52:55], a[240:243]\n\t"
    "v_mul_f32 v96, s55, v96\n\t"
    "v_mfma_f32_16x16x32_bf16 a[248:251], a[116:119], a[60:63], a[248:251]\n\t"
    "buffer_load_dwordx4 v230, s[36:39], s48 offen offset:2048 lds\n\t"
    "v_mfma_f32_16x16x32_bf16 a[248:251], a[112:115], a[56:59], a[248:251]\n\t"
    "ds_read_b64_tr_b16 a[4:5], v240 offset:20480\n\t"
    "v_mfma_f32_16x16x32_bf16 a[252:255], a[120:123], a[56:59], a[252:255]\n\t"
    "buffer_load_dwordx4 v231, s[36:39], s48 offen offset:3072 lds\n\t"
    "v_mfma_f32_16x16x32_bf16 a[252:255], a[124:127], a[60:63], a[252:255]\n\t"
    "ds_read_b64_tr_b16 a[12:13], v241 offset:20480\n\t"
    "v_mfma_f32_16x16x32_bf16 a[244:247], a[124:127], a[52:55], a[244:247]\n\t"
    "ds_read_b128 a[104:107], v236 offset:43008\n\t"
    "v_mul_f32 v103, s55, v103\n\t"
    "v_mfma_f32_16x16x32_bf16 a[244:247], a[120:123], a[48:51], a[244:247]\n\t"
    "ds_read_b128 a[112:115], v236 offset:45056\n\t"
    "v_mfma_f32_16x16x32_bf16 a[236:239], a[120:123], a[40:43], a[236:239]\n\t"
    "ds_read_b128 a[116:119], v237 offset:45056\n\t"
    "v_mfma_f32_16x16x32_bf16 a[236:239], a[124:127], a[44:47], a[236:239]\n\t"
    "v_mul_f32 v100, s55, v100\n\t"
    "v_mfma_f32_16x16x32_bf16 a[228:231], a[124:127], a[36:39], a[228:231]\n\t"
    "ds_read_b128 a[124:127], v237 offset:47104\n\t"
    "v_mfma_f32_16x16x32_bf16 a[228:231], a[120:123], a[32:35], a[228:231]\n\t"
    "ds_read_b128 a[120:123], v236 offset:47104\n\t"
    "s_waitcnt lgkmcnt(8)\n\t"
    "v_mfma_f32_16x16x32_bf16 a[128:131], a[64:67], a[0:3], a[128:131]\n\t"
    "v_mul_f32 v101, s55, v101\n\t"
    "s_add_u32 s48, s48, 128\n\t"
    "s_waitcnt lgkmcnt(6)\n\t"
    "v_mfma_f32_16x16x32_bf16 a[128:131], a[68:71], a[4:7], a[128:131]\n\t"
    "v_mul_f32 v102, s55, v102\n\t"
    "v_cvt_pk_bf16_f32 v164, v96, v97\n\t"
    "s_waitcnt lgkmcnt(5)\n\t"
    "v_mfma_f32_16x16x32_bf16 a[136:139], a[68:71], a[12:15], a[136:139]\n\t"
    "s_and_b32 s48, s48, 1023\n\t"
    "v_mfma_f32_16x16x32_bf16 a[136:139], a[64:67], a[8:11], a[136:139]\n\t"
    "v_mul_f32 v98, s55, v98\n\t"
    "v_mul_f32 v99, s55, v99\n\t"
    "v_mfma_f32_16x16x32_bf16 a[144:147], a[64:67], a[16:19], a[144:147]\n\t"
    "s_lshl_b32 s49, s48, 12\n\t"
    "v_mfma_f32_16x16x32_bf16 a[144:147], a[68:71], a[20:23], a[144:147]\n\t"
    "s_waitcnt vmcnt(15) lgkmcnt(0)\n\t"
    "s_barrier\n\t"
    "v_mfma_f32_16x16x32_bf16 a[152:155], a[68:71], a[28:31], a[152:155]\n\t"
    "ds_read_b64_tr_b16 a[38:39], v240 offset:37376\n\t"
    "s_add_u32 m0, s52, 0x4000\n\t"
    "v_mfma_f32_16x16x32_bf16 a[152:155], a[64:67], a[24:27], a[152:155]\n\t"
    "ds_read_b128 a[64:67], v236 offset:0\n\t"
    "v_mfma_f32_16x16x32_bf16 a[156:159], a[72:75], a[24:27], a[156:159]\n\t"
    "ds_read_b64_tr_b16 a[54:55], v242 offset:37376\n\t"
    "v_cvt_pk_bf16_f32 v167, v102, v103\n\t"
    "v_mfma_f32_16x16x32_bf16 a[156:159], a[76:79], a[28:31], a[156:159]\n\t"
    "ds_read_b64_tr_b16 a[40:41], v241 offset:32768\n\t"
    "v_cvt_pk_bf16_f32 v166, v100, v101\n\t"
    "v_mfma_f32_16x16x32_bf16 a[148:151], a[76:79], a[20:23], a[148:151]\n\t"
    "buffer_load_dwordx4 v232, s[40:43], s49 offen offset:0 lds\n\t"
    "v_mfma_f32_16x16x32_bf16 a[148:151], a[72:75], a[16:19], a[148:151]\n\t"
    "v_mul_f32 v110, s55, v110\n\t"
    "v_mfma_f32_16x16x32_bf16 a[140:143], a[72:75], a[8:11], a[140:143]\n\t"
    "v_mul_f32 v109, s55, v109\n\t"
    "v_mfma_f32_16x16x32_bf16 a[140:143], a[76:79], a[12:15], a[140:143]\n\t"
    "buffer_load_dwordx4 v233, s[40:43], s49 offen offset:1024 lds\n\t"
    "v_mfma_f32_16x16x32_bf16 a[132:135], a[76:79], a[4:7], a[132:135]\n\t"
    "v_cvt_pk_bf16_f32 v165, v98, v99\n\t"
    "v_mfma_f32_16x16x32_bf16 a[132:135], a[72:75], a[0:3], a[132:135]\n\t"
    "buffer_load_dwordx4 v234, s[40:43], s49 offen offset:2048 lds\n\t"
    "v_mfma_f32_16x16x32_bf16 a[160:163], a[80:83], a[0:3], a[160:163]\n\t"
    "v_mul_f32 v106, s55, v106\n\t"
    "v_mfma_f32_16x16x32_bf16 a[160:163], a[84:87], a[4:7], a[160:163]\n\t"
    "ds_read_b128 a[76:79], v237 offset:2048\n\t"
    "v_mfma_f32_16x16x32_bf16 a[168:171], a[84:87], a[12:15], a[168:171]\n\t"
    "ds_read_b128 a[68:71], v237 offset:0\n\t"
    "v_mfma_f32_16x16x32_bf16 a[168:171], a[80:83], a[8:11], a[168:171]\n\t"
    "ds_read_b128 a[72:75], v236 offset:2048\n\t"
    "v_mfma_f32_16x16x32_bf16 a[176:179], a[80:83], a[16:19], a[176:179]\n\t"
    "v_mul_f32 v107, s55, v107\n\t"
    "v_mfma_f32_16x16x32_bf16 a[176:179], a[84:87], a[20:23], a[176:179]\n\t"
    "buffer_load_dwordx4 v235, s[40:43], s49 offen offset:3072 lds\n\t"
    "v_mfma_f32_16x16x32_bf16 a[184:187], a[84:87], a[28:31], a[184:187]\n\t"
    "s_add_u32 m0, s75, 0x8000\n\t"
    "v_mfma_f32_16x16x32_bf16 a[184:187], a[80:83], a[24:27], a[184:187]\n\t"
    "ds_read_b64_tr_b16 a[60:61], v243 offset:36864\n\t"
    "v_mfma_f32_16x16x32_bf16 a[188:191], a[88:91], a[24:27], a[188:191]\n\t"
    "global_store_dwordx4 v244, v[164:167], s[66:67] offset:-4096 sc1\n\t"
    "v_mfma_f32_16x16x32_bf16 a[188:191], a[92:95], a[28:31], a[188:191]\n\t"
    "buffer_load_dwordx4 v224, s[36:39], s48 offen offset:0 lds\n\t"
    "v_mfma_f32_16x16x32_bf16 a[180:183], a[92:95], a[20:23], a[180:183]\n\t"
    "v_mul_f32 v104, s55, v104\n\t"
    "v_mul_f32 v108, s55, v108\n\t"
    "v_mfma_f32_16x16x32_bf16 a[180:183], a[88:91], a[16:19], a[180:183]\n\t"
    "ds_read_b64_tr_b16 a[36:37], v240 offset:36864\n\t"
    "v_mul_f32 v105, s55, v105\n\t"
    "v_mfma_f32_16x16x32_bf16 a[172:175], a[88:91], a[8:11], a[172:175]\n\t"
    "buffer_load_dwordx4 v225, s[36:39], s48 offen offset:1024 lds\n\t"
    "v_mfma_f32_16x16x32_bf16 a[172:175], a[92:95], a[12:15], a[172:175]\n\t"
    "v_cvt_pk_bf16_f32 v168, v104, v105\n\t"
    "v_mfma_f32_16x16x32_bf16 a[164:167], a[92:95], a[4:7], a[164:167]\n\t"
    "v_mul_f32 v111, s55, v111\n\t"
    "v_mfma_f32_16x16x32_bf16 a[164:167], a[88:91], a[0:3], a[164:167]\n\t"
    "ds_read_b64_tr_b16 a[46:47], v241 offset:37376\n\t"
    "v_mfma_f32_16x16x32_bf16 a[192:195], a[96:99], a[0:3], a[192:195]\n\t"
    "ds_read_b128 a[84:87], v237 offset:4096\n\t"
    "ds_read_b64_tr_b16 a[58:59], v243 offset:33280\n\t"
    "v_mfma_f32_16x16x32_bf16 a[192:195], a[100:103], a[4:7], a[192:195]\n\t"
    "ds_read_b128 a[80:83], v236 offset:4096\n\t"
    "v_mfma_f32_16x16x32_bf16 a[200:203], a[100:103], a[12:15], a[200:203]\n\t"
    "v_cvt_pk_bf16_f32 v170, v108, v109\n\t"
    "v_mfma_f32_16x16x32_bf16 a[200:203], a[96:99], a[8:11], a[200:203]\n\t"
    "buffer_load_dwordx4 v226, s[36:39], s48 offen offset:2048 lds\n\t"
    "v_mfma_f32_16x16x32_bf16 a[208:211], a[96:99], a[16:19], a[208:211]\n\t"
    "ds_read_b64_tr_b16 a[34:35], v240 offset:33280\n\t"
    "v_mfma_f32_16x16x32_bf16 a[208:211], a[100:103], a[20:23], a[208:211]\n\t"
    "buffer_load_dwordx4 v227, s[36:39], s48 offen offset:3072 lds\n\t"
    "v_mfma_f32_16x16x32_bf16 a[216:219], a[100:103], a[28:31], a[216:219]\n\t"
    "s_lshl1_add_u32 s56, s98, s61\n\t"
    "v_mfma_f32_16x16x32_bf16 a[216:219], a[96:99], a[24:27], a[216:219]\n\t"
    "s_add_u32 m0, s75, 0x9000\n\t"
    "v_mfma_f32_16x16x32_bf16 a[220:223], a[104:107], a[24:27], a[220:223]\n\t"
    "ds_read_b128 a[92:95], v237 offset:6144\n\t"
    "ds_read_b64_tr_b16 a[32:33], v240 offset:32768\n\t"
    "v_mfma_f32_16x16x32_bf16 a[220:223], a[108:111], a[28:31], a[220:223]\n\t"
    "ds_read_b64_tr_b16 a[56:57], v243 offset:32768\n\t"
    "v_cvt_pk_bf16_f32 v169, v106, v107\n\t"
    "v_mfma_f32_16x16x32_bf16 a[212:215], a[108:111], a[20:23], a[212:215]\n\t"
    "ds_read_b128 a[88:91], v236 offset:6144\n\t"
    "v_cvt_pk_bf16_f32 v171, v110, v111\n\t"
    "v_mfma_f32_16x16x32_bf16 a[212:215], a[104:107], a[16:19], a[212:215]\n\t"
    "ds_read_b64_tr_b16 a[50:51], v242 offset:33280\n\t"
    "v_mfma_f32_16x16x32_bf16 a[204:207], a[104:107], a[8:11], a[204:207]\n\t"
    "buffer_load_dwordx4 v228, s[36:39], s48 offen offset:0 lds\n\t"
    "v_mfma_f32_16x16x32_bf16 a[204:207], a[108:111], a[12:15], a[204:207]\n\t"
    "ds_read_b64_tr_b16 a[52:53], v242 offset:36864\n\t"
    "v_mfma_f32_16x16x32_bf16 a[196:199], a[108:111], a[4:7], a[196:199]\n\t"
    "ds_read_b128 a[96:99], v236 offset:8192\n\t"
    "v_mfma_f32_16x16x32_bf16 a[196:199], a[104:107], a[0:3], a[196:199]\n\t"
    "buffer_load_dwordx4 v229, s[36:39], s48 offen offset:1024 lds\n\t"
    "v_mfma_f32_16x16x32_bf16 a[224:227], a[112:115], a[0:3], a[224:227]\n\t"
    "ds_read_b128 a[100:103], v237 offset:8192\n\t"
    "ds_read_b64_tr_b16 a[44:45], v241 offset:36864\n\t"
    "v_mfma_f32_16x16x32_bf16 a[224:227], a[116:119], a[4:7], a[224:227]\n\t"
    "ds_read_b128 a[108:111], v237 offset:10240\n\t"
    "v_mul_f32 v113, s55, v113\n\t"
    "v_mfma_f32_16x16x32_bf16 a[232:235], a[116:119], a[12:15], a[232:235]\n\t"
    "ds_read_b64_tr_b16 a[62:63], v243 offset:37376\n\t"
    "v_mfma_f32_16x16x32_bf16 a[232:235], a[112:115], a[8:11], a[232:235]\n\t"
    "ds_read_b128 a[104:107], v236 offset:10240\n\t"
    "v_mfma_f32_16x16x32_bf16 a[240:243], a[112:115], a[16:19], a[240:243]\n\t"
    "ds_read_b64_tr_b16 a[42:43], v241 offset:33280\n\t"
    "ds_read_b64_tr_b16 a[48:49], v242 offset:32768\n\t"
    "v_mfma_f32_16x16x32_bf16 a[240:243], a[116:119], a[20:23], a[240:243]\n\t"
    "global_store_dwordx4 v244, v[168:171], s[66:67] offset:-3072 sc1\n\t"
    "v_mfma_f32_16x16x32_bf16 a[248:251], a[116:119], a[28:31], a[248:251]\n\t"
    "buffer_load_dwordx4 v230, s[36:39], s48 offen offset:2048 lds\n\t"
    "v_mfma_f32_16x16x32_bf16 a[248:251], a[112:115], a[24:27], a[248:251]\n\t"
    "buffer_load_dwordx4 v231, s[36:39], s48 offen offset:3072 lds\n\t"
    "v_mfma_f32_16x16x32_bf16 a[252:255], a[120:123], a[24:27], a[252:255]\n\t"
    "v_mul_f32 v114, s55, v114\n\t"
    "v_mfma_f32_16x16x32_bf16 a[252:255], a[124:127], a[28:31], a[252:255]\n\t"
    "ds_read_b128 a[116:119], v237 offset:12288\n\t"
    "v_mfma_f32_16x16x32_bf16 a[244:247], a[124:127], a[20:23], a[244:247]\n\t"
    "v_mul_f32 v115, s55, v115\n\t"
    "v_mfma_f32_16x16x32_bf16 a[244:247], a[120:123], a[16:19], a[244:247]\n\t"
    "s_add_u32 s48, s48, 128\n\t"
    "v_mfma_f32_16x16x32_bf16 a[236:239], a[120:123], a[8:11], a[236:239]\n\t"
    "ds_read_b128 a[112:115], v236 offset:12288\n\t"
    "v_cvt_pk_bf16_f32 v173, v114, v115\n\t"
    "v_mfma_f32_16x16x32_bf16 a[236:239], a[124:127], a[12:15], a[236:239]\n\t"
    "s_and_b32 s48, s48, 1023\n\t"
    "v_mfma_f32_16x16x32_bf16 a[228:231], a[124:127], a[4:7], a[228:231]\n\t"
    "ds_read_b128 a[124:127], v237 offset:14336\n\t"
    "v_mul_f32 v112, s55, v112\n\t"
    "v_mfma_f32_16x16x32_bf16 a[228:231], a[120:123], a[0:3], a[228:231]\n\t"
    "ds_read_b128 a[120:123], v236 offset:14336\n\t"
    "s_waitcnt lgkmcnt(4)\n\t"
    "v_mfma_f32_16x16x32_bf16 a[128:131], a[64:67], a[32:35], a[128:131]\n\t"
    "v_mul_f32 v118, s55, v118\n\t"
    "v_mfma_f32_16x16x32_bf16 a[128:131], a[68:71], a[36:39], a[128:131]\n\t"
    "v_mul_f32 v116, s55, v116\n\t"
    "v_mfma_f32_16x16x32_bf16 a[136:139], a[68:71], a[44:47], a[136:139]\n\t"
    "s_lshl_b32 s56, s56, 12\n\t"
    "v_mfma_f32_16x16x32_bf16 a[136:139], a[64:67], a[40:43], a[136:139]\n\t"
    "s_lshl_b32 s49, s48, 12\n\t"
    "v_mfma_f32_16x16x32_bf16 a[144:147], a[64:67], a[48:51], a[144:147]\n\t"
    "s_add_u32 m0, s52, 0x8000\n\t"
    "v_mfma_f32_16x16x32_bf16 a[144:147], a[68:71], a[52:55], a[144:147]\n\t"
    "s_waitcnt vmcnt(14) lgkmcnt(0)\n\t"
    "s_barrier\n\t"
    "v_mfma_f32_16x16x32_bf16 a[152:155], a[68:71], a[60:63], a[152:155]\n\t"
    "buffer_load_dwordx4 v232, s[40:43], s49 offen offset:0 lds\n\t"
    "v_mfma_f32_16x16x32_bf16 a[152:155], a[64:67], a[56:59], a[152:155]\n\t"
    "ds_read_b64_tr_b16 a[0:1], v240 offset:0\n\t"
    "s_add_u32 s56, s56, 0x10000\n\t"
    "v_mfma_f32_16x16x32_bf16 a[156:159], a[72:75], a[56:59], a[156:159]\n\t"
    "v_mul_f32 v119, s55, v119\n\t"
    "v_mfma_f32_16x16x32_bf16 a[156:159], a[76:79], a[60:63], a[156:159]\n\t"
    "ds_read_b64_tr_b16 a[24:25], v243 offset:0\n\t"
    "v_mfma_f32_16x16x32_bf16 a[148:151], a[76:79], a[52:55], a[148:151]\n\t"
    "v_mul_f32 v120, s55, v120\n\t"
    "v_mfma_f32_16x16x32_bf16 a[148:151], a[72:75], a[48:51], a[148:151]\n\t"
    "buffer_load_dwordx4 v233, s[40:43], s49 offen offset:1024 lds\n\t"
    "v_mfma_f32_16x16x32_bf16 a[140:143], a[72:75], a[40:43], a[140:143]\n\t"
    "ds_read_b64_tr_b16 a[18:19], v242 offset:512\n\t"
    "v_mfma_f32_16x16x32_bf16 a[140:143], a[76:79], a[44:47], a[140:143]\n\t"
    "ds_read_b64_tr_b16 a[4:5], v240 offset:4096\n\t"
    "v_mfma_f32_16x16x32_bf16 a[132:135], a[76:79], a[36:39], a[132:135]\n\t"
    "ds_read_b128 a[68:71], v239 offset:0\n\t"
    "v_cvt_pk_bf16_f32 v175, v118, v119\n\t"
    "v_mfma_f32_16x16x32_bf16 a[132:135], a[72:75], a[32:35], a[132:135]\n\t"
    "buffer_load_dwordx4 v234, s[40:43], s49 offen offset:2048 lds\n\t"
    "v_mfma_f32_16x16x32_bf16 a[160:163], a[80:83], a[32:35], a[160:163]\n\t"
    "ds_read_b64_tr_b16 a[6:7], v240 offset:4608\n\t"
    "v_mfma_f32_16x16x32_bf16 a[160:163], a[84:87], a[36:39], a[160:163]\n\t"
    "ds_read_b128 a[72:75], v238 offset:2048\n\t"
    "v_cvt_pk_bf16_f32 v172, v112, v113\n\t"
    "v_mfma_f32_16x16x32_bf16 a[168:171], a[84:87], a[44:47], a[168:171]\n\t"
    "ds_read_b128 a[64:67], v238 offset:0\n\t"
    "ds_read_b64_tr_b16 a[14:15], v241 offset:4608\n\t"
    "v_mfma_f32_16x16x32_bf16 a[168:171], a[80:83], a[40:43], a[168:171]\n\t"
    "v_mul_f32 v117, s55, v117\n\t"
    "v_add_u32 v252, s56, v244\n\t"
    "v_mfma_f32_16x16x32_bf16 a[176:179], a[80:83], a[48:51], a[176:179]\n\t"
    "v_mul_f32 v124, s55, v124\n\t"
    "v_cvt_pk_bf16_f32 v174, v116, v117\n\t"
    "v_mfma_f32_16x16x32_bf16 a[176:179], a[84:87], a[52:55], a[176:179]\n\t"
    "ds_read_b128 a[76:79], v239 offset:2048\n\t"
    "v_mfma_f32_16x16x32_bf16 a[184:187], a[84:87], a[60:63], a[184:187]\n\t"
    "buffer_load_dwordx4 v235, s[40:43], s49 offen offset:3072 lds\n\t"
    "v_mfma_f32_16x16x32_bf16 a[184:187], a[80:83], a[56:59], a[184:187]\n\t"
    "ds_read_b128 a[80:83], v238 offset:4096\n\t"
    "v_mfma_f32_16x16x32_bf16 a[188:191], a[88:91], a[56:59], a[188:191]\n\t"
    "Lis5_%=:\n\t"
    "s_cbranch_execz Lis9_%=\n\t"
    "ds_read_b64_tr_b16 a[30:31], v243 offset:4608\n\t"
    "v_mfma_f32_16x16x32_bf16 a[188:191], a[92:95], a[60:63], a[188:191]\n\t"
    "global_store_dwordx4 v244, v[172:175], s[66:67] offset:0 sc1\n\t"
    "v_mfma_f32_16x16x32_bf16 a[180:183], a[92:95], a[52:55], a[180:183]\n\t"
    "s_add_u32 m0, s75, 0x0\n\t"
    "v_mfma_f32_16x16x32_bf16 a[180:183], a[88:91], a[48:51], a[180:183]\n\t"
    "buffer_load_dwordx4 v224, s[36:39], s48 offen offset:0 lds\n\t"
    "v_mfma_f32_16x16x32_bf16 a[172:175], a[88:91], a[40:43], a[172:175]\n\t"
    "ds_read_b128 a[84:87], v239 offset:4096\n\t"
    "v_mfma_f32_16x16x32_bf16 a[172:175], a[92:95], a[44:47], a[172:175]\n\t"
    "buffer_load_dwordx4 v225, s[36:39], s48 offen offset:1024 lds\n\t"
    "v_mfma_f32_16x16x32_bf16 a[164:167], a[92:95], a[36:39], a[164:167]\n\t"
    "v_mul_f32 v126, s55, v126\n\t"
    "v_mfma_f32_16x16x32_bf16 a[164:167], a[88:91], a[32:35], a[164:167]\n\t"
    "ds_read_b128 a[92:95], v239 offset:6144\n\t"
    "v_mfma_f32_16x16x32_bf16 a[192:195], a[96:99], a[32:35], a[192:195]\n\t"
    "ds_read_b64_tr_b16 a[16:17], v242 offset:0\n\t"
    "v_mfma_f32_16x16x32_bf16 a[192:195], a[100:103], a[36:39], a[192:195]\n\t"
    "ds_read_b64_tr_b16 a[22:23], v242 offset:4608\n\t"
    "v_mul_f32 v123, s55, v123\n\t"
    "v_mfma_f32_16x16x32_bf16 a[200:203], a[100:103], a[44:47], a[200:203]\n\t"
    "ds_read_b64_tr_b16 a[10:11], v241 offset:512\n\t"
    "v_mfma_f32_16x16x32_bf16 a[200:203], a[96:99], a[40:43], a[200:203]\n\t"
    "v_mul_f32 v127, s55, v127\n\t"
    "v_mfma_f32_16x16x32_bf16 a[208:211], a[96:99], a[48:51], a[208:211]\n\t"
    "buffer_load_dwordx4 v226, s[36:39], s48 offen offset:2048 lds\n\t"
    "v_mfma_f32_16x16x32_bf16 a[208:211], a[100:103], a[52:55], a[208:211]\n\t"
    "ds_read_b128 a[88:91], v238 offset:6144\n\t"
    "v_mfma_f32_16x16x32_bf16 a[216:219], a[100:103], a[60:63], a[216:219]\n\t"
    "v_mul_f32 v125, s55, v125\n\t"
    "v_mfma_f32_16x16x32_bf16 a[216:219], a[96:99], a[56:59], a[216:219]\n\t"
    "buffer_load_dwordx4 v227, s[36:39], s48 offen offset:3072 lds\n\t"
    "v_mfma_f32_16x16x32_bf16 a[220:223], a[104:107], a[56:59], a[220:223]\n\t"
    "v_mul_f32 v121, s55, v121\n\t"
    "s_add_u32 m0, s75, 0x1000\n\t"
    "v_mfma_f32_16x16x32_bf16 a[220:223], a[108:111], a[60:63], a[220:223]\n\t"
    "ds_read_b128 a[96:99], v238 offset:8192\n\t"
    "v_mfma_f32_16x16x32_bf16 a[212:215], a[108:111], a[52:55], a[212:215]\n\t"
    "v_mul_f32 v122, s55, v122\n\t"
    "v_cvt_pk_bf16_f32 v154, v124, v125\n\t"
    "v_mfma_f32_16x16x32_bf16 a[212:215], a[104:107], a[48:51], a[212:215]\n\t"
    "ds_read_b64_tr_b16 a[8:9], v241 offset:0\n\t"
    "v_mfma_f32_16x16x32_bf16 a[204:207], a[104:107], a[40:43], a[204:207]\n\t"
    "v_cvt_pk_bf16_f32 v155, v126, v127\n\t"
    "v_mfma_f32_16x16x32_bf16 a[204:207], a[108:111], a[44:47], a[204:207]\n\t"
    "ds_read_b64_tr_b16 a[20:21], v242 offset:4096\n\t"
    "v_mfma_f32_16x16x32_bf16 a[196:199], a[108:111], a[36:39], a[196:199]\n\t"
    "buffer_load_dwordx4 v228, s[36:39], s48 offen offset:0 lds\n\t"
    "v_mfma_f32_16x16x32_bf16 a[196:199], a[104:107], a[32:35], a[196:199]\n\t"
    "v_cvt_pk_bf16_f32 v152, v120, v121\n\t"
    "v_cvt_pk_bf16_f32 v153, v122, v123\n\t"
    "v_mfma_f32_16x16x32_bf16 a[224:227], a[112:115], a[32:35], a[224:227]\n\t"
    "ds_read_b128 a[100:103], v239 offset:8192\n\t"
    "v_mfma_f32_16x16x32_bf16 a[224:227], a[116:119], a[36:39], a[224:227]\n\t"
    "ds_read_b64_tr_b16 a[2:3], v240 offset:512\n\t"
    "v_mfma_f32_16x16x32_bf16 a[232:235], a[116:119], a[44:47], a[232:235]\n\t"
    "buffer_load_dwordx4 v229, s[36:39], s48 offen offset:1024 lds\n\t"
    "v_mfma_f32_16x16x32_bf16 a[232:235], a[112:115], a[40:43], a[232:235]\n\t"
    "global_store_dwordx4 v244, v[152:155], s[66:67] offset:1024 sc1\n\t"
    "v_mfma_f32_16x16x32_bf16 a[240:243], a[112:115], a[48:51], a[240:243]\n\t"
    "ds_read_b64_tr_b16 a[26:27], v243 offset:512\n\t"
    "v_mfma_f32_16x16x32_bf16 a[240:243], a[116:119], a[52:55], a[240:243]\n\t"
    "ds_read_b64_tr_b16 a[28:29], v243 offset:4096\n\t"
    "v_mfma_f32_16x16x32_bf16 a[248:251], a[116:119], a[60:63], a[248:251]\n\t"
    "ds_read_b64_tr_b16 a[12:13], v241 offset:4096\n\t"
    "v_mfma_f32_16x16x32_bf16 a[248:251], a[112:115], a[56:59], a[248:251]\n\t"
    "buffer_load_dwordx4 v230, s[36:39], s48 offen offset:2048 lds\n\t"
    "v_mfma_f32_16x16x32_bf16 a[252:255], a[120:123], a[56:59], a[252:255]\n\t"
    "ds_read_b128 a[108:111], v239 offset:10240\n\t"
    "v_mfma_f32_16x16x32_bf16 a[252:255], a[124:127], a[60:63], a[252:255]\n\t"
    "buffer_load_dwordx4 v231, s[36:39], s48 offen offset:3072 lds\n\t"
    "v_mfma_f32_16x16x32_bf16 a[244:247], a[124:127], a[52:55], a[244:247]\n\t"
    "ds_read_b128 a[104:107], v238 offset:10240\n\t"
    "v_mfma_f32_16x16x32_bf16 a[244:247], a[120:123], a[48:51], a[244:247]\n\t"
    "ds_read_b128 a[116:119], v239 offset:12288\n\t"
    "v_mfma_f32_16x16x32_bf16 a[236:239], a[120:123], a[40:43], a[236:239]\n\t"
    "s_add_u32 s48, s48, 128\n\t"
    "v_mfma_f32_16x16x32_bf16 a[236:239], a[124:127], a[44:47], a[236:239]\n\t"
    "ds_read_b128 a[112:115], v238 offset:12288\n\t"
    "v_mfma_f32_16x16x32_bf16 a[228:231], a[124:127], a[36:39], a[228:231]\n\t"
    "ds_read_b128 a[124:127], v239 offset:14336\n\t"
    "v_mfma_f32_16x16x32_bf16 a[228:231], a[120:123], a[32:35], a[228:231]\n\t"
    "ds_read_b128 a[120:123], v238 offset:14336\n\t"
    "s_waitcnt lgkmcnt(9)\n\t"
    "v_mfma_f32_16x16x32_bf16 v[48:51], a[0:3], a[64:67], 0\n\t"
    "s_and_b32 s48, s48, 1023\n\t"
    "v_mfma_f32_16x16x32_bf16 v[48:51], a[4:7], a[68:71], v[48:51]\n\t"
    "v_accvgpr_read_b32 v182, a134\n\t"
    "s_waitcnt lgkmcnt(6)\n\t"
    "v_mfma_f32_16x16x32_bf16 v[52:55], a[12:15], a[68:71], 0\n\t"
    "v_accvgpr_read_b32 v181, a133\n\t"
    "s_lshl_b32 s49, s48, 12\n\t"
    "v_mfma_f32_16x16x32_bf16 v[52:55], a[8:11], a[64:67], v[52:55]\n\t"
    "v_accvgpr_read_b32 v178, a130\n\t"
    "v_mfma_f32_16x16x32_bf16 v[56:59], a[16:19], a[64:67], 0\n\t"
    "v_accvgpr_read_b32 v179, a131\n\t"
    "v_mfma_f32_16x16x32_bf16 v[56:59], a[20:23], a[68:71], v[56:59]\n\t"
    "s_waitcnt vmcnt(14) lgkmcnt(0)\n\t"
    "s_barrier\n\t"
    "v_mfma_f32_16x16x32_bf16 v[60:63], a[28:31], a[68:71], 0\n\t"
    "ds_read_b64_tr_b16 a[50:51], v242 offset:16896\n\t"
    "v_mfma_f32_16x16x32_bf16 v[60:63], a[24:27], a[64:67], v[60:63]\n\t"
    "v_accvgpr_read_b32 v177, a129\n\t"
    "s_add_u32 m0, s52, 0x0\n\t"
    "v_mfma_f32_16x16x32_bf16 v[12:15], a[24:27], a[72:75], 0\n\t"
    "buffer_load_dwordx4 v232, s[40:43], s49 offen offset:0 lds\n\t"
    "v_mfma_f32_16x16x32_bf16 v[12:15], a[28:31], a[76:79], v[12:15]\n\t"
    "ds_read_b128 a[64:67], v236 offset:32768\n\t"
    "v_mfma_f32_16x16x32_bf16 v[8:11], a[20:23], a[76:79], 0\n\t"
    "buffer_load_dwordx4 v233, s[40:43], s49 offen offset:1024 lds\n\t"
    "v_mfma_f32_16x16x32_bf16 v[8:11], a[16:19], a[72:75], v[8:11]\n\t"
    "ds_read_b64_tr_b16 a[60:61], v243 offset:20480\n\t"
    "v_mfma_f32_16x16x32_bf16 v[4:7], a[8:11], a[72:75], 0\n\t"
    "ds_read_b64_tr_b16 a[40:41], v241 offset:16384\n\t"
    "v_mfma_f32_16x16x32_bf16 v[4:7], a[12:15], a[76:79], v[4:7]\n\t"
    "ds_read_b64_tr_b16 a[32:33], v240 offset:16384\n\t"
    "v_mfma_f32_16x16x32_bf16 v[0:3], a[4:7], a[76:79], 0\n\t"
    "ds_read_b64_tr_b16 a[36:37], v240 offset:20480\n\t"
    "v_mfma_f32_16x16x32_bf16 v[0:3], a[0:3], a[72:75], v[0:3]\n\t"
    "ds_read_b128 a[68:71], v237 offset:32768\n\t"
    "v_mfma_f32_16x16x32_bf16 v[16:19], a[0:3], a[80:83], 0\n\t"
    "ds_read_b128 a[76:79], v237 offset:34816\n\t"
    "v_accvgpr_read_b32 v180, a132\n\t"
    "v_mfma_f32_16x16x32_bf16 v[16:19], a[4:7], a[84:87], v[16:19]\n\t"
    "ds_read_b64_tr_b16 a[48:49], v242 offset:16384\n\t"
    "v_mfma_f32_16x16x32_bf16 v[20:23], a[12:15], a[84:87], 0\n\t"
    "ds_read_b64_tr_b16 a[46:47], v241 offset:20992\n\t"
    "v_mfma_f32_16x16x32_bf16 v[20:23], a[8:11], a[80:83], v[20:23]\n\t"
    "buffer_load_dwordx4 v234, s[40:43], s49 offen offset:2048 lds\n\t"
    "v_mfma_f32_16x16x32_bf16 v[24:27], a[16:19], a[80:83], 0\n\t"
    "ds_read_b64_tr_b16 a[34:35], v240 offset:16896\n\t"
    "v_mfma_f32_16x16x32_bf16 v[24:27], a[20:23], a[84:87], v[24:27]\n\t"
    "v_cvt_pk_bf16_f32 v129, v178, v179\n\t"
    "v_mfma_f32_16x16x32_bf16 v[28:31], a[28:31], a[84:87], 0\n\t"
    "buffer_load_dwordx4 v235, s[40:43], s49 offen offset:3072 lds\n\t"
    "v_mfma_f32_16x16x32_bf16 v[28:31], a[24:27], a[80:83], v[28:31]\n\t"
    "v_accvgpr_read_b32 v183, a135\n\t"
    "v_mfma_f32_16x16x32_bf16 v[44:47], a[24:27], a[88:91], 0\n\t"
    "ds_read_b128 a[72:75], v236 offset:34816\n\t"
    "v_mfma_f32_16x16x32_bf16 v[44:47], a[28:31], a[92:95], v[44:47]\n\t"
    "s_add_u32 m0, s75, 0x10000\n\t"
    "v_mfma_f32_16x16x32_bf16 v[40:43], a[20:23], a[92:95], 0\n\t"
    "ds_read_b128 a[80:83], v236 offset:36864\n\t"
    "ds_read_b64_tr_b16 a[42:43], v241 offset:16896\n\t"
    "v_mfma_f32_16x16x32_bf16 v[40:43], a[16:19], a[88:91], v[40:43]\n\t"
    "v_accvgpr_read_b32 v176, a128\n\t"
    "v_mfma_f32_16x16x32_bf16 v[36:39], a[8:11], a[88:91], 0\n\t"
    "buffer_load_dwordx4 v224, s[36:39], s48 offen offset:0 lds\n\t"
    "v_mfma_f32_16x16x32_bf16 v[36:39], a[12:15], a[92:95], v[36:39]\n\t"
    "v_cvt_pk_bf16_f32 v131, v182, v183\n\t"
    "v_mfma_f32_16x16x32_bf16 v[32:35], a[4:7], a[92:95], 0\n\t"
    "v_cvt_pk_bf16_f32 v130, v180, v181\n\t"
    "v_mfma_f32_16x16x32_bf16 v[32:35], a[0:3], a[88:91], v[32:35]\n\t"
    "buffer_load_dwordx4 v225, s[36:39], s48 offen offset:1024 lds\n\t"
    "v_mfma_f32_16x16x32_bf16 v[64:67], a[0:3], a[96:99], 0\n\t"
    "ds_read_b64_tr_b16 a[52:53], v242 offset:20480\n\t"
    "v_mfma_f32_16x16x32_bf16 v[64:67], a[4:7], a[100:103], v[64:67]\n\t"
    "ds_read_b128 a[84:87], v237 offset:36864\n\t"
    "v_mfma_f32_16x16x32_bf16 v[68:71], a[12:15], a[100:103], 0\n\t"
    "v_cvt_pk_bf16_f32 v128, v176, v177\n\t"
    "v_mfma_f32_16x16x32_bf16 v[68:71], a[8:11], a[96:99], v[68:71]\n\t"
    "buffer_load_dwordx4 v226, s[36:39], s48 offen offset:2048 lds\n\t"
    "v_mfma_f32_16x16x32_bf16 v[72:75], a[16:19], a[96:99], 0\n\t"
    "ds_read_b64_tr_b16 a[56:57], v243 offset:16384\n\t"
    "v_mfma_f32_16x16x32_bf16 v[72:75], a[20:23], a[100:103], v[72:75]\n\t"
    "global_store_dwordx4 v244, v[128:131], s[68:69] offset:0 sc1\n\t"
    "v_mfma_f32_16x16x32_bf16 v[76:79], a[28:31], a[100:103], 0\n\t"
    "ds_read_b128 a[100:103], v237 offset:40960\n\t"
    "v_mfma_f32_16x16x32_bf16 v[76:79], a[24:27], a[96:99], v[76:79]\n\t"
    "ds_read_b128 a[88:91], v236 offset:38912\n\t"
    "v_mfma_f32_16x16x32_bf16 v[92:95], a[24:27], a[104:107], 0\n\t"
    "ds_read_b128 a[92:95], v237 offset:38912\n\t"
    "v_mfma_f32_16x16x32_bf16 v[92:95], a[28:31], a[108:111], v[92:95]\n\t"
    "buffer_load_dwordx4 v227, s[36:39], s48 offen offset:3072 lds\n\t"
    "v_mfma_f32_16x16x32_bf16 v[88:91], a[20:23], a[108:111], 0\n\t"
    "s_add_u32 m0, s75, 0x11000\n\t"
    "v_mfma_f32_16x16x32_bf16 v[88:91], a[16:19], a[104:107], v[88:91]\n\t"
    "buffer_load_dwordx4 v228, s[36:39], s48 offen offset:0 lds\n\t"
    "v_mfma_f32_16x16x32_bf16 v[84:87], a[8:11], a[104:107], 0\n\t"
    "ds_read_b64_tr_b16 a[54:55], v242 offset:20992\n\t"
    "v_mfma_f32_16x16x32_bf16 v[84:87], a[12:15], a[108:111], v[84:87]\n\t"
    "ds_read_b64_tr_b16 a[62:63], v243 offset:20992\n\t"
    "v_mfma_f32_16x16x32_bf16 v[80:83], a[4:7], a[108:111], 0\n\t"
    "buffer_load_dwordx4 v229, s[36:39], s48 offen offset:1024 lds\n\t"
    "v_mfma_f32_16x16x32_bf16 v[80:83], a[0:3], a[104:107], v[80:83]\n\t"
    "v_accvgpr_read_b32 v185, a137\n\t"
    "v_accvgpr_read_b32 v188, a140\n\t"
    "v_mfma_f32_16x16x32_bf16 v[96:99], a[0:3], a[112:115], 0\n\t"
    "ds_read_b64_tr_b16 a[38:39], v240 offset:20992\n\t"
    "v_mfma_f32_16x16x32_bf16 v[96:99], a[4:7], a[116:119], v[96:99]\n\t"
    "ds_read_b128 a[96:99], v236 offset:40960\n\t"
    "v_accvgpr_read_b32 v189, a141\n\t"
    "v_mfma_f32_16x16x32_bf16 v[100:103], a[12:15], a[116:119], 0\n\t"
    "v_accvgpr_read_b32 v184, a136\n\t"
    "v_accvgpr_read_b32 v191, a143\n\t"
    "v_mfma_f32_16x16x32_bf16 v[100:103], a[8:11], a[112:115], v[100:103]\n\t"
    "ds_read_b64_tr_b16 a[44:45], v241 offset:20480\n\t"
    "v_mfma_f32_16x16x32_bf16 v[104:107], a[16:19], a[112:115], 0\n\t"
    "buffer_load_dwordx4 v230, s[36:39], s48 offen offset:2048 lds\n\t"
    "v_mfma_f32_16x16x32_bf16 v[104:107], a[20:23], a[116:119], v[104:107]\n\t"
    "ds_read_b64_tr_b16 a[58:59], v243 offset:16896\n\t"
    "v_accvgpr_read_b32 v186, a138\n\t"
    "v_mfma_f32_16x16x32_bf16 v[108:111], a[28:31], a[116:119], 0\n\t"
    "ds_read_b128 a[108:111], v237 offset:43008\n\t"
    "v_mfma_f32_16x16x32_bf16 v[108:111], a[24:27], a[112:115], v[108:111]\n\t"
    "v_accvgpr_read_b32 v190, a142\n\t"
    "v_mfma_f32_16x16x32_bf16 v[124:127], a[24:27], a[120:123], 0\n\t"
    "ds_read_b128 a[104:107], v236 offset:43008\n\t"
    "v_mfma_f32_16x16x32_bf16 v[124:127], a[28:31], a[124:127], v[124:127]\n\t"
    "buffer_load_dwordx4 v231, s[36:39], s48 offen offset:3072 lds\n\t"
    "v_mfma_f32_16x16x32_bf16 v[120:123], a[20:23], a[124:127], 0\n\t"
    "s_add_u32 s48, s48, 128\n\t"
    "s_and_b32 s48, s48, 1023\n\t"
    "v_mfma_f32_16x16x32_bf16 v[120:123], a[16:19], a[120:123], v[120:123]\n\t"
    "v_cvt_pk_bf16_f32 v159, v190, v191\n\t"
    "v_mfma_f32_16x16x32_bf16 v[116:119], a[8:11], a[120:123], 0\n\t"
    "v_cvt_pk_bf16_f32 v158, v188, v189\n\t"
    "v_mfma_f32_16x16x32_bf16 v[116:119], a[12:15], a[124:127], v[116:119]\n\t"
    "ds_read_b128 a[116:119], v237 offset:45056\n\t"
    "s_lshl_b32 s49, s48, 12\n\t"
    "v_mfma_f32_16x16x32_bf16 v[112:115], a[4:7], a[124:127], 0\n\t"
    "ds_read_b128 a[112:115], v236 offset:45056\n\t"
    "ds_read_b128 a[124:127], v237 offset:47104\n\t"
    "v_mfma_f32_16x16x32_bf16 v[112:115], a[0:3], a[120:123], v[112:115]\n\t"
    "ds_read_b128 a[120:123], v236 offset:47104\n\t"
    "s_waitcnt lgkmcnt(6)\n\t"
    "v_mfma_f32_16x16x32_bf16 v[48:51], a[32:35], a[64:67], v[48:51]\n\t"
    "v_accvgpr_read_b32 v187, a139\n\t"
    "v_mfma_f32_16x16x32_bf16 v[48:51], a[36:39], a[68:71], v[48:51]\n\t"
    "s_lshl_b32 s11, s58, 13\n\t"
    "v_cvt_pk_bf16_f32 v156, v184, v185\n\t"
    "v_mfma_f32_16x16x32_bf16 v[52:55], a[44:47], a[68:71], v[52:55]\n\t"
    "s_bfe_u32 s71, %2, 0x30003\n\t"
    "v_mfma_f32_16x16x32_bf16 v[52:55], a[40:43], a[64:67], v[52:55]\n\t"
    "s_add_u32 s12, s11, 0x1000\n\t"
    "s_lshl_b32 s29, s60, 15\n\t"
    "v_mfma_f32_16x16x32_bf16 v[56:59], a[48:51], a[64:67], v[56:59]\n\t"
    "s_add_u32 m0, s52, 0x4000\n\t"
    "v_mfma_f32_16x16x32_bf16 v[56:59], a[52:55], a[68:71], v[56:59]\n\t"
    "s_waitcnt vmcnt(13) lgkmcnt(0)\n\t"
    "s_barrier\n\t"
    "v_mfma_f32_16x16x32_bf16 v[60:63], a[60:63], a[68:71], v[60:63]\n\t"
    "ds_read_b64_tr_b16 a[2:3], v240 offset:33280\n\t"
    "ds_read_b64_tr_b16 a[24:25], v243 offset:32768\n\t"
    "v_mfma_f32_16x16x32_bf16 v[60:63], a[56:59], a[64:67], v[60:63]\n\t"
    "ds_read_b64_tr_b16 a[18:19], v242 offset:33280\n\t"
    "ds_read_b64_tr_b16 a[30:31], v243 offset:37376\n\t"
    "v_mfma_f32_16x16x32_bf16 v[12:15], a[56:59], a[72:75], v[12:15]\n\t"
    "buffer_load_dwordx4 v232, s[40:43], s49 offen offset:0 lds\n\t"
    "v_mfma_f32_16x16x32_bf16 v[12:15], a[60:63], a[76:79], v[12:15]\n\t"
    "ds_read_b64_tr_b16 a[14:15], v241 offset:37376\n\t"
    "v_mfma_f32_16x16x32_bf16 v[8:11], a[52:55], a[76:79], v[8:11]\n\t"
    "ds_read_b64_tr_b16 a[22:23], v242 offset:37376\n\t"
    "v_accvgpr_read_b32 v183, a151\n\t"
    "v_mfma_f32_16x16x32_bf16 v[8:11], a[48:51], a[72:75], v[8:11]\n\t"
    "ds_read_b64_tr_b16 a[10:11], v241 offset:33280\n\t"
    "ds_read_b64_tr_b16 a[12:13], v241 offset:36864\n\t"
    "v_mfma_f32_16x16x32_bf16 v[4:7], a[40:43], a[72:75], v[4:7]\n\t"
    "ds_read_b128 a[64:67], v236 offset:0\n\t"
    "v_mfma_f32_16x16x32_bf16 v[4:7], a[44:47], a[76:79], v[4:7]\n\t"
    "v_accvgpr_read_b32 v179, a147\n\t"
    "v_mfma_f32_16x16x32_bf16 v[0:3], a[36:39], a[76:79], v[0:3]\n\t"
    "buffer_load_dwordx4 v233, s[40:43], s49 offen offset:1024 lds\n\t"
    "v_mfma_f32_16x16x32_bf16 v[0:3], a[32:35], a[72:75], v[0:3]\n\t"
    "ds_read_b128 a[72:75], v236 offset:2048\n\t"
    "ds_read_b64_tr_b16 a[6:7], v240 offset:37376\n\t"
    "v_mfma_f32_16x16x32_bf16 v[16:19], a[32:35], a[80:83], v[16:19]\n\t"
    "ds_read_b64_tr_b16 a[20:21], v242 offset:36864\n\t"
    "s_add_u32 s9, s29, s11\n\t"
    "v_mfma_f32_16x16x32_bf16 v[16:19], a[36:39], a[84:87], v[16:19]\n\t"
    "ds_read_b128 a[68:71], v237 offset:0\n\t"
    "v_cvt_pk_bf16_f32 v157, v186, v187\n\t"
    "v_mfma_f32_16x16x32_bf16 v[20:23], a[44:47], a[84:87], v[20:23]\n\t"
    "v_accvgpr_read_b32 v177, a145\n\t"
    "v_mfma_f32_16x16x32_bf16 v[20:23], a[40:43], a[80:83], v[20:23]\n\t"
    "buffer_load_dwordx4 v234, s[40:43], s49 offen offset:2048 lds\n\t"
    "v_mfma_f32_16x16x32_bf16 v[24:27], a[48:51], a[80:83], v[24:27]\n\t"
    "buffer_load_dwordx4 v235, s[40:43], s49 offen offset:3072 lds\n\t"
    "v_mfma_f32_16x16x32_bf16 v[24:27], a[52:55], a[84:87], v[24:27]\n\t"
    "global_store_dwordx4 v244, v[156:159], s[68:69] offset:1024 sc1\n\t"
    "v_mfma_f32_16x16x32_bf16 v[28:31], a[60:63], a[84:87], v[28:31]\n\t"
    "s_sub_u32 s28, 0x78000, s29\n\t"
    "s_add_u32 m0, s75, 0x8000\n\t"
    "v_mfma_f32_16x16x32_bf16 v[28:31], a[56:59], a[80:83], v[28:31]\n\t"
    "s_sub_u32 s8, s11, 0x8000\n\t"
    "v_mfma_f32_16x16x32_bf16 v[44:47], a[56:59], a[88:91], v[44:47]\n\t"
    "s_lshl_b32 s54, s60, 2\n\t"
    "v_mfma_f32_16x16x32_bf16 v[44:47], a[60:63], a[92:95], v[44:47]\n\t"
    "v_accvgpr_read_b32 v182, a150\n\t"
    "v_mfma_f32_16x16x32_bf16 v[40:43], a[52:55], a[92:95], v[40:43]\n\t"
    "ds_read_b128 a[76:79], v237 offset:2048\n\t"
    "v_mfma_f32_16x16x32_bf16 v[40:43], a[48:51], a[88:91], v[40:43]\n\t"
    "buffer_load_dwordx4 v224, s[36:39], s48 offen offset:0 lds\n\t"
    "v_mfma_f32_16x16x32_bf16 v[36:39], a[40:43], a[88:91], v[36:39]\n\t"
    "ds_read_b64_tr_b16 a[26:27], v243 offset:33280\n\t"
    "v_mfma_f32_16x16x32_bf16 v[36:39], a[44:47], a[92:95], v[36:39]\n\t"
    "ds_read_b128 a[84:87], v237 offset:4096\n\t"
    "v_mfma_f32_16x16x32_bf16 v[32:35], a[36:39], a[92:95], v[32:35]\n\t"
    "buffer_load_dwordx4 v225, s[36:39], s48 offen offset:1024 lds\n\t"
    "v_mfma_f32_16x16x32_bf16 v[32:35], a[32:35], a[88:91], v[32:35]\n\t"
    "ds_read_b128 a[80:83], v236 offset:4096\n\t"
    "ds_read_b64_tr_b16 a[28:29], v243 offset:36864\n\t"
    "v_mfma_f32_16x16x32_bf16 v[64:67], a[32:35], a[96:99], v[64:67]\n\t"
    "v_cvt_pk_bf16_f32 v195, v182, v183\n\t"
    "v_mfma_f32_16x16x32_bf16 v[64:67], a[36:39], a[100:103], v[64:67]\n\t"
    "ds_read_b128 a[92:95], v237 offset:6144\n\t"
    "v_mfma_f32_16x16x32_bf16 v[68:71], a[44:47], a[100:103], v[68:71]\n\t"
    "ds_read_b128 a[88:91], v236 offset:6144\n\t"
    "v_mfma_f32_16x16x32_bf16 v[68:71], a[40:43], a[96:99], v[68:71]\n\t"
    "buffer_load_dwordx4 v226, s[36:39], s48 offen offset:2048 lds\n\t"
    "v_mfma_f32_16x16x32_bf16 v[72:75], a[48:51], a[96:99], v[72:75]\n\t"
    "v_accvgpr_read_b32 v178, a146\n\t"
    "v_accvgpr_read_b32 v181, a149\n\t"
    "v_mfma_f32_16x16x32_bf16 v[72:75], a[52:55], a[100:103], v[72:75]\n\t"
    "buffer_load_dwordx4 v227, s[36:39], s48 offen offset:3072 lds\n\t"
    "v_mfma_f32_16x16x32_bf16 v[76:79], a[60:63], a[100:103], v[76:79]\n\t"
    "ds_read_b64_tr_b16 a[0:1], v240 offset:32768\n\t"
    "v_mfma_f32_16x16x32_bf16 v[76:79], a[56:59], a[96:99], v[76:79]\n\t"
    "ds_read_b128 a[96:99], v236 offset:8192\n\t"
    "s_lshr_b32 s54, 0x87765423, s54\n\t"
    "v_mfma_f32_16x16x32_bf16 v[92:95], a[56:59], a[104:107], v[92:95]\n\t"
    "s_and_b32 s72, %2, 6\n\t"
    "s_add_u32 m0, s75, 0x9000\n\t"
    "v_mfma_f32_16x16x32_bf16 v[92:95], a[60:63], a[108:111], v[92:95]\n\t"
    "v_accvgpr_read_b32 v187, a155\n\t"
    "s_and_b32 s54, s54, 15\n\t"
    "v_mfma_f32_16x16x32_bf16 v[88:91], a[52:55], a[108:111], v[88:91]\n\t"
    "ds_read_b64_tr_b16 a[8:9], v241 offset:32768\n\t"
    "v_accvgpr_read_b32 v180, a148\n\t"
    "v_mfma_f32_16x16x32_bf16 v[88:91], a[48:51], a[104:107], v[88:91]\n\t"
    "buffer_load_dwordx4 v228, s[36:39], s48 offen offset:0 lds\n\t"
    "v_mfma_f32_16x16x32_bf16 v[84:87], a[40:43], a[104:107], v[84:87]\n\t"
    "v_accvgpr_read_b32 v186, a154\n\t"
    "v_cvt_pk_bf16_f32 v193, v178, v179\n\t"
    "v_mfma_f32_16x16x32_bf16 v[84:87], a[44:47], a[108:111], v[84:87]\n\t"
    "s_cmp_eq_u32 s58, 2\n\t"
    "s_cselect_b32 s54, s54, 0x7fff\n\t"
    "v_mfma_f32_16x16x32_bf16 v[80:83], a[36:39], a[108:111], v[80:83]\n\t"
    "v_cvt_pk_bf16_f32 v194, v180, v181\n\t"
    "s_add_u32 s23, s9, 0x8000\n\t"
    "v_mfma_f32_16x16x32_bf16 v[80:83], a[32:35], a[104:107], v[80:83]\n\t"
    "v_accvgpr_read_b32 v176, a144\n\t"
    "v_cvt_pk_bf16_f32 v192, v176, v177\n\t"
    "v_mfma_f32_16x16x32_bf16 v[96:99], a[32:35], a[112:115], v[96:99]\n\t"
    "v_accvgpr_read_b32 v184, a152\n\t"
    "s_add_u32 s8, s8, 0x8000\n\t"
    "v_mfma_f32_16x16x32_bf16 v[96:99], a[36:39], a[116:119], v[96:99]\n\t"
    "ds_read_b128 a[100:103], v237 offset:8192\n\t"
    "ds_read_b64_tr_b16 a[4:5], v240 offset:36864\n\t"
    "v_mfma_f32_16x16x32_bf16 v[100:103], a[44:47], a[116:119], v[100:103]\n\t"
    "buffer_load_dwordx4 v229, s[36:39], s48 offen offset:1024 lds\n\t"
    "v_mfma_f32_16x16x32_bf16 v[100:103], a[40:43], a[112:115], v[100:103]\n\t"
    "global_store_dwordx4 v244, v[192:195], s[68:69] offset:2048 sc1\n\t"
    "v_mfma_f32_16x16x32_bf16 v[104:107], a[48:51], a[112:115], v[104:107]\n\t"
    "ds_read_b128 a[104:107], v236 offset:10240\n\t"
    "v_mfma_f32_16x16x32_bf16 v[104:107], a[52:55], a[116:119], v[104:107]\n\t"
    "ds_read_b128 a[108:111], v237 offset:10240\n\t"
    "ds_read_b64_tr_b16 a[16:17], v242 offset:32768\n\t"
    "v_mfma_f32_16x16x32_bf16 v[108:111], a[60:63], a[116:119], v[108:111]\n\t"
    "s_cmp_eq_u32 s8, s9\n\t"
    "s_cselect_b32 s8, s23, s8\n\t"
    "v_mfma_f32_16x16x32_bf16 v[108:111], a[56:59], a[112:115], v[108:111]\n\t"
    "buffer_load_dwordx4 v230, s[36:39], s48 offen offset:2048 lds\n\t"
    "v_mfma_f32_16x16x32_bf16 v[124:127], a[56:59], a[120:123], v[124:127]\n\t"
    "ds_read_b128 a[112:115], v236 offset:12288\n\t"
    "v_accvgpr_read_b32 v189, a157\n\t"
    "v_mfma_f32_16x16x32_bf16 v[124:127], a[60:63], a[124:127], v[124:127]\n\t"
    "s_lshl_b32 s72, s72, 1\n\t"
    "v_mfma_f32_16x16x32_bf16 v[120:123], a[52:55], a[124:127], v[120:123]\n\t"
    "buffer_load_dwordx4 v231, s[36:39], s48 offen offset:3072 lds\n\t"
    "v_mfma_f32_16x16x32_bf16 v[120:123], a[48:51], a[120:123], v[120:123]\n\t"
    "s_lshl_b32 s25, s60, 2\n\t"
    "s_lshr_b32 s13, 0x301003, s25\n\t"
    "v_mfma_f32_16x16x32_bf16 v[116:119], a[40:43], a[120:123], v[116:119]\n\t"
    "ds_read_b128 a[116:119], v237 offset:12288\n\t"
    "s_add_u32 s48, s48, 128\n\t"
    "v_mfma_f32_16x16x32_bf16 v[116:119], a[44:47], a[124:127], v[116:119]\n\t"
    "v_accvgpr_read_b32 v191, a159\n\t"
    "v_cvt_pk_bf16_f32 v221, v186, v187\n\t"
    "v_mfma_f32_16x16x32_bf16 v[112:115], a[36:39], a[124:127], v[112:115]\n\t"
    "ds_read_b128 a[124:127], v237 offset:14336\n\t"
    "s_and_b32 s48, s48, 1023\n\t"
    "v_mfma_f32_16x16x32_bf16 v[112:115], a[32:35], a[120:123], v[112:115]\n\t"
    "ds_read_b128 a[120:123], v236 offset:14336\n\t"
    "s_and_b32 s13, s13, 7\n\t"
    "s_waitcnt lgkmcnt(4)\n\t"
    "v_mfma_f32_16x16x32_bf16 v[48:51], a[0:3], a[64:67], v[48:51]\n\t"
    "v_accvgpr_read_b32 v188, a156\n\t"
    "v_accvgpr_read_b32 v178, a162\n\t"
    "v_mfma_f32_16x16x32_bf16 v[48:51], a[4:7], a[68:71], v[48:51]\n\t"
    "v_accvgpr_read_b32 v177, a161\n\t"
    "s_lshl_b32 s49, s48, 12\n\t"
    "v_mfma_f32_16x16x32_bf16 v[52:55], a[12:15], a[68:71], v[52:55]\n\t"
    "v_cvt_pk_bf16_f32 v222, v188, v189\n\t"
    "v_accvgpr_read_b32 v181, a165\n\t"
    "v_mfma_f32_16x16x32_bf16 v[52:55], a[8:11], a[64:67], v[52:55]\n\t"
    "v_accvgpr_read_b32 v185, a153\n\t"
    "v_accvgpr_read_b32 v190, a158\n\t"
    "v_mfma_f32_16x16x32_bf16 v[56:59], a[16:19], a[64:67], v[56:59]\n\t"
    "s_lshl_b32 s13, s13, 15\n\t"
    "s_add_u32 m0, s52, 0x8000\n\t"
    "v_mfma_f32_16x16x32_bf16 v[56:59], a[20:23], a[68:71], v[56:59]\n\t"
    "s_waitcnt vmcnt(14) lgkmcnt(0)\n\t"
    "s_barrier\n\t"
    "v_mfma_f32_16x16x32_bf16 v[60:63], a[28:31], a[68:71], v[60:63]\n\t"
    "s_lshr_b32 s64, %2, 6\n\t"
    "v_accvgpr_read_b32 v183, a167\n\t"
    "v_mfma_f32_16x16x32_bf16 v[60:63], a[24:27], a[64:67], v[60:63]\n\t"
    "v_accvgpr_read_b32 v176, a160\n\t"
    "v_cvt_pk_bf16_f32 v132, v176, v177\n\t"
    "v_mfma_f32_16x16x32_bf16 v[12:15], a[24:27], a[72:75], v[12:15]\n\t"
    "v_accvgpr_read_b32 v180, a164\n\t"
    "s_add_u32 s72, s72, s64\n\t"
    "v_mfma_f32_16x16x32_bf16 v[12:15], a[28:31], a[76:79], v[12:15]\n\t"
    "ds_read_b128 a[68:71], v239 offset:0\n\t"
    "ds_read_b64_tr_b16 a[52:53], v242 offset:4096\n\t"
    "v_mfma_f32_16x16x32_bf16 v[8:11], a[20:23], a[76:79], v[8:11]\n\t"
    "buffer_load_dwordx4 v232, s[40:43], s49 offen offset:0 lds\n\t"
    "v_mfma_f32_16x16x32_bf16 v[8:11], a[16:19], a[72:75], v[8:11]\n\t"
    "ds_read_b64_tr_b16 a[40:41], v241 offset:0\n\t"
    "v_cvt_pk_bf16_f32 v220, v184, v185\n\t"
    "v_mfma_f32_16x16x32_bf16 v[4:7], a[8:11], a[72:75], v[4:7]\n\t"
    "buffer_load_dwordx4 v233, s[40:43], s49 offen offset:1024 lds\n\t"
    "v_mfma_f32_16x16x32_bf16 v[4:7], a[12:15], a[76:79], v[4:7]\n\t"
    "ds_read_b64_tr_b16 a[32:33], v240 offset:0\n\t"
    "ds_read_b64_tr_b16 a[56:57], v243 offset:0\n\t"
    "v_mfma_f32_16x16x32_bf16 v[0:3], a[4:7], a[76:79], v[0:3]\n\t"
    "ds_read_b64_tr_b16 a[60:61], v243 offset:4096\n\t"
    "s_and_b32 s64, %2, 1\n\t"
    "v_mfma_f32_16x16x32_bf16 v[0:3], a[0:3], a[72:75], v[0:3]\n\t"
    "buffer_load_dwordx4 v234, s[40:43], s49 offen offset:2048 lds\n\t"
    "v_mfma_f32_16x16x32_bf16 v[16:19], a[0:3], a[80:83], v[16:19]\n\t"
    "v_cvt_pk_bf16_f32 v223, v190, v191\n\t"
    "v_accvgpr_read_b32 v186, a170\n\t"
    "v_mfma_f32_16x16x32_bf16 v[16:19], a[4:7], a[84:87], v[16:19]\n\t"
    "ds_read_b128 a[64:67], v238 offset:0\n\t"
    "v_accvgpr_read_b32 v189, a173\n\t"
    "v_mfma_f32_16x16x32_bf16 v[20:23], a[12:15], a[84:87], v[20:23]\n\t"
    "ds_read_b64_tr_b16 a[38:39], v240 offset:4608\n\t"
    "v_mfma_f32_16x16x32_bf16 v[20:23], a[8:11], a[80:83], v[20:23]\n\t"
    "v_accvgpr_read_b32 v179, a163\n\t"
    "s_lshl1_add_u32 s72, s72, s64\n\t"
    "v_mfma_f32_16x16x32_bf16 v[24:27], a[16:19], a[80:83], v[24:27]\n\t"
    "global_store_dwordx4 v244, v[220:223], s[68:69] offset:3072 sc1\n\t"
    "v_mfma_f32_16x16x32_bf16 v[24:27], a[20:23], a[84:87], v[24:27]\n\t"
    "ds_read_b128 a[76:79], v239 offset:2048\n\t"
    "ds_read_b64_tr_b16 a[58:59], v243 offset:512\n\t"
    "v_mfma_f32_16x16x32_bf16 v[28:31], a[28:31], a[84:87], v[28:31]\n\t"
    "buffer_load_dwordx4 v235, s[40:43], s49 offen offset:3072 lds\n\t"
    "v_mfma_f32_16x16x32_bf16 v[28:31], a[24:27], a[80:83], v[28:31]\n\t"
    "ds_read_b128 a[72:75], v238 offset:2048\n\t"
    "s_add_u32 m0, s75, 0x0\n\t"
    "v_mfma_f32_16x16x32_bf16 v[44:47], a[24:27], a[88:91], v[44:47]\n\t"
    "s_add_u32 s68, s68, 0x4000\n\t"
    "s_addc_u32 s69, s69, 0\n\t"
    "v_mfma_f32_16x16x32_bf16 v[44:47], a[28:31], a[92:95], v[44:47]\n\t"
    "ds_read_b128 a[80:83], v238 offset:4096\n\t"
    "v_accvgpr_read_b32 v182, a166\n\t"
    "v_mfma_f32_16x16x32_bf16 v[40:43], a[20:23], a[92:95], v[40:43]\n\t"
    "ds_read_b64_tr_b16 a[42:43], v241 offset:512\n\t"
    "v_mfma_f32_16x16x32_bf16 v[40:43], a[16:19], a[88:91], v[40:43]\n\t"
    "buffer_load_dwordx4 v224, s[36:39], s48 offen offset:0 lds\n\t"
    "v_mfma_f32_16x16x32_bf16 v[36:39], a[8:11], a[88:91], v[36:39]\n\t"
    "buffer_load_dwordx4 v225, s[36:39], s48 offen offset:1024 lds\n\t"
    "v_mfma_f32_16x16x32_bf16 v[36:39], a[12:15], a[92:95], v[36:39]\n\t"
    "v_accvgpr_read_b32 v184, a168\n\t"
    "v_accvgpr_read_b32 v185, a169\n\t"
    "v_mfma_f32_16x16x32_bf16 v[32:35], a[4:7], a[92:95], v[32:35]\n\t"
    "ds_read_b128 a[92:95], v239 offset:6144\n\t"
    "ds_read_b64_tr_b16 a[50:51], v242 offset:512\n\t"
    "v_mfma_f32_16x16x32_bf16 v[32:35], a[0:3], a[88:91], v[32:35]\n\t"
    "ds_read_b128 a[84:87], v239 offset:4096\n\t"
    "s_lshr_b32 s14, 0x34140341, s25\n\t"
    "v_mfma_f32_16x16x32_bf16 v[64:67], a[0:3], a[96:99], v[64:67]\n\t"
    "v_accvgpr_read_b32 v187, a171\n\t"
    "v_accvgpr_read_b32 v188, a172\n\t"
    "v_mfma_f32_16x16x32_bf16 v[64:67], a[4:7], a[100:103], v[64:67]\n\t"
    "v_cvt_pk_bf16_f32 v133, v178, v179\n\t"
    "s_lshl1_add_u32 s73, s58, s64\n\t"
    "v_mfma_f32_16x16x32_bf16 v[68:71], a[12:15], a[100:103], v[68:71]\n\t"
    "v_accvgpr_read_b32 v190, a174\n\t"
    "v_cvt_pk_bf16_f32 v135, v182, v183\n\t"
    "v_mfma_f32_16x16x32_bf16 v[68:71], a[8:11], a[96:99], v[68:71]\n\t"
    "v_cvt_pk_bf16_f32 v134, v180, v181\n\t"
    "v_mfma_f32_16x16x32_bf16 v[72:75], a[16:19], a[96:99], v[72:75]\n\t"
    "global_store_dwordx4 v244, v[132:135], s[68:69] offset:0 sc1\n\t"
    "v_mfma_f32_16x16x32_bf16 v[72:75], a[20:23], a[100:103], v[72:75]\n\t"
    "buffer_load_dwordx4 v226, s[36:39], s48 offen offset:2048 lds\n\t"
    "v_mfma_f32_16x16x32_bf16 v[76:79], a[28:31], a[100:103], v[76:79]\n\t"
    "buffer_load_dwordx4 v227, s[36:39], s48 offen offset:3072 lds\n\t"
    "v_mfma_f32_16x16x32_bf16 v[76:79], a[24:27], a[96:99], v[76:79]\n\t"
    "s_and_b32 s14, s14, 7\n\t"
    "s_add_u32 m0, s75, 0x1000\n\t"
    "v_mfma_f32_16x16x32_bf16 v[92:95], a[24:27], a[104:107], v[92:95]\n\t"
    "v_accvgpr_read_b32 v191, a175\n\t"
    "s_lshl_b32 s14, s14, 15\n\t"
    "v_mfma_f32_16x16x32_bf16 v[92:95], a[28:31], a[108:111], v[92:95]\n\t"
    "ds_read_b128 a[100:103], v239 offset:8192\n\t"
    "v_cvt_pk_bf16_f32 v137, v186, v187\n\t"
    "v_mfma_f32_16x16x32_bf16 v[88:91], a[20:23], a[108:111], v[88:91]\n\t"
    "ds_read_b128 a[88:91], v238 offset:6144\n\t"
    "v_cvt_pk_bf16_f32 v136, v184, v185\n\t"
    "v_mfma_f32_16x16x32_bf16 v[88:91], a[16:19], a[104:107], v[88:91]\n\t"
    "ds_read_b64_tr_b16 a[44:45], v241 offset:4096\n\t"
    "s_lshr_b32 s10, 0x12034120, s25\n\t"
    "v_mfma_f32_16x16x32_bf16 v[84:87], a[8:11], a[104:107], v[84:87]\n\t"
    "buffer_load_dwordx4 v228, s[36:39], s48 offen offset:0 lds\n\t"
    "v_mfma_f32_16x16x32_bf16 v[84:87], a[12:15], a[108:111], v[84:87]\n\t"
    "ds_read_b128 a[96:99], v238 offset:8192\n\t"
    "v_mfma_f32_16x16x32_bf16 v[80:83], a[4:7], a[108:111], v[80:83]\n\t"
    "s_and_b32 s10, s10, 7\n\t"
    "s_lshl_b32 s10, s10, 15\n\t"
    "v_mfma_f32_16x16x32_bf16 v[80:83], a[0:3], a[104:107], v[80:83]\n\t"
    "buffer_load_dwordx4 v229, s[36:39], s48 offen offset:1024 lds\n\t"
    "v_mfma_f32_16x16x32_bf16 v[96:99], a[0:3], a[112:115], v[96:99]\n\t"
    "ds_read_b64_tr_b16 a[46:47], v241 offset:4608\n\t"
    "s_lshr_b32 s15, 0x43412434, s25\n\t"
    "v_mfma_f32_16x16x32_bf16 v[96:99], a[4:7], a[116:119], v[96:99]\n\t"
    "ds_read_b64_tr_b16 a[36:37], v240 offset:4096\n\t"
    "ds_read_b64_tr_b16 a[54:55], v242 offset:4608\n\t"
    "v_mfma_f32_16x16x32_bf16 v[100:103], a[12:15], a[116:119], v[100:103]\n\t"
    "ds_read_b64_tr_b16 a[62:63], v243 offset:4608\n\t"
    "v_accvgpr_read_b32 v177, a177\n\t"
    "v_mfma_f32_16x16x32_bf16 v[100:103], a[8:11], a[112:115], v[100:103]\n\t"
    "ds_read_b64_tr_b16 a[48:49], v242 offset:0\n\t"
    "v_mfma_f32_16x16x32_bf16 v[104:107], a[16:19], a[112:115], v[104:107]\n\t"
    "ds_read_b128 a[108:111], v239 offset:10240\n\t"
    "v_cvt_pk_bf16_f32 v139, v190, v191\n\t"
    "v_mfma_f32_16x16x32_bf16 v[104:107], a[20:23], a[116:119], v[104:107]\n\t"
    "ds_read_b128 a[104:107], v238 offset:10240\n\t"
    "v_cvt_pk_bf16_f32 v138, v188, v189\n\t"
    "v_mfma_f32_16x16x32_bf16 v[108:111], a[28:31], a[116:119], v[108:111]\n\t"
    "global_store_dwordx4 v244, v[136:139], s[68:69] offset:1024 sc1\n\t"
    "v_mfma_f32_16x16x32_bf16 v[108:111], a[24:27], a[112:115], v[108:111]\n\t"
    "Lis6_%=:\n\t"
    "s_cbranch_execz Lis10_%=\n\t"
    "buffer_load_dwordx4 v230, s[36:39], s48 offen offset:2048 lds\n\t"
    "v_mfma_f32_16x16x32_bf16 v[124:127], a[24:27], a[120:123], v[124:127]\n\t"
    "ds_read_b64_tr_b16 a[34:35], v240 offset:512\n\t"
    "s_and_b32 s15, s15, 7\n\t"
    "v_mfma_f32_16x16x32_bf16 v[124:127], a[28:31], a[124:127], v[124:127]\n\t"
    "buffer_load_dwordx4 v231, s[36:39], s48 offen offset:3072 lds\n\t"
    "v_mfma_f32_16x16x32_bf16 v[120:123], a[20:23], a[124:127], v[120:123]\n\t"
    "s_add_u32 s48, s48, 128\n\t"
    "v_mfma_f32_16x16x32_bf16 v[120:123], a[16:19], a[120:123], v[120:123]\n\t"
    "v_accvgpr_read_b32 v181, a181\n\t"
    "s_lshl_b32 s73, s73, 4\n\t"
    "v_mfma_f32_16x16x32_bf16 v[116:119], a[8:11], a[120:123], v[116:119]\n\t"
    "ds_read_b128 a[112:115], v238 offset:12288\n\t"
    "v_mfma_f32_16x16x32_bf16 v[116:119], a[12:15], a[124:127], v[116:119]\n\t"
    "v_accvgpr_read_b32 v176, a176\n\t"
    "v_accvgpr_read_b32 v178, a178\n\t"
    "v_mfma_f32_16x16x32_bf16 v[112:115], a[4:7], a[124:127], v[112:115]\n\t"
    "ds_read_b128 a[116:119], v239 offset:12288\n\t"
    "s_lshl_b32 s15, s15, 15\n\t"
    "v_mfma_f32_16x16x32_bf16 v[112:115], a[0:3], a[120:123], v[112:115]\n\t"
    "ds_read_b128 a[120:123], v238 offset:14336\n\t"
    "ds_read_b128 a[124:127], v239 offset:14336\n\t"
    "s_waitcnt lgkmcnt(4)\n\t"
    "v_mfma_f32_16x16x32_bf16 v[48:51], a[32:35], a[64:67], v[48:51]\n\t"
    "s_lshl_b32 s64, s73, 15\n\t"
    "v_mfma_f32_16x16x32_bf16 v[48:51], a[36:39], a[68:71], v[48:51]\n\t"
    "v_accvgpr_read_b32 v182, a182\n\t"
    "v_mfma_f32_16x16x32_bf16 v[52:55], a[44:47], a[68:71], v[52:55]\n\t"
    "v_accvgpr_read_b32 v183, a183\n\t"
    "s_and_b32 s48, s48, 1023\n\t"
    "v_mfma_f32_16x16x32_bf16 v[52:55], a[40:43], a[64:67], v[52:55]\n\t"
    "s_lshl_b32 s49, s48, 12\n\t"
    "v_mfma_f32_16x16x32_bf16 v[56:59], a[48:51], a[64:67], v[56:59]\n\t"
    "s_lshr_b32 s16, 0x21223212, s25\n\t"
    "s_add_u32 m0, s52, 0x0\n\t"
    "v_mfma_f32_16x16x32_bf16 v[56:59], a[52:55], a[68:71], v[56:59]\n\t"
    "s_waitcnt vmcnt(15) lgkmcnt(0)\n\t"
    "s_barrier\n\t"
    "v_mfma_f32_16x16x32_bf16 v[60:63], a[60:63], a[68:71], v[60:63]\n\t"
    "ds_read_b64_tr_b16 a[2:3], v240 offset:16896\n\t"
    "ds_read_b64_tr_b16 a[28:29], v243 offset:20480\n\t"
    "v_mfma_f32_16x16x32_bf16 v[60:63], a[56:59], a[64:67], v[60:63]\n\t"
    "ds_read_b128 a[68:71], v237 offset:32768\n\t"
    "ds_read_b64_tr_b16 a[12:13], v241 offset:20480\n\t"
    "v_mfma_f32_16x16x32_bf16 v[12:15], a[56:59], a[72:75], v[12:15]\n\t"
    "buffer_load_dwordx4 v232, s[40:43], s49 offen offset:0 lds\n\t"
    "v_mfma_f32_16x16x32_bf16 v[12:15], a[60:63], a[76:79], v[12:15]\n\t"
    "ds_read_b64_tr_b16 a[10:11], v241 offset:16896\n\t"
    "v_cvt_pk_bf16_f32 v140, v176, v177\n\t"
    "v_mfma_f32_16x16x32_bf16 v[8:11], a[52:55], a[76:79], v[8:11]\n\t"
    "buffer_load_dwordx4 v233, s[40:43], s49 offen offset:1024 lds\n\t"
    "v_mfma_f32_16x16x32_bf16 v[8:11], a[48:51], a[72:75], v[8:11]\n\t"
    "v_accvgpr_read_b32 v186, a186\n\t"
    "s_and_b32 s16, s16, 7\n\t"
    "v_mfma_f32_16x16x32_bf16 v[4:7], a[40:43], a[72:75], v[4:7]\n\t"
    "v_accvgpr_read_b32 v190, a190\n\t"
    "v_mfma_f32_16x16x32_bf16 v[4:7], a[44:47], a[76:79], v[4:7]\n\t"
    "ds_read_b64_tr_b16 a[4:5], v240 offset:20480\n\t"
    "ds_read_b64_tr_b16 a[30:31], v243 offset:20992\n\t"
    "v_mfma_f32_16x16x32_bf16 v[0:3], a[36:39], a[76:79], v[0:3]\n\t"
    "v_accvgpr_read_b32 v179, a179\n\t"
    "v_cvt_pk_bf16_f32 v143, v182, v183\n\t"
    "v_mfma_f32_16x16x32_bf16 v[0:3], a[32:35], a[72:75], v[0:3]\n\t"
    "ds_read_b128 a[76:79], v237 offset:34816\n\t"
    "s_lshl_b32 s16, s16, 15\n\t"
    "v_mfma_f32_16x16x32_bf16 v[16:19], a[32:35], a[80:83], v[16:19]\n\t"
    "v_cvt_pk_bf16_f32 v141, v178, v179\n\t"
    "v_mfma_f32_16x16x32_bf16 v[16:19], a[36:39], a[84:87], v[16:19]\n\t"
    "buffer_load_dwordx4 v234, s[40:43], s49 offen offset:2048 lds\n\t"
    "v_mfma_f32_16x16x32_bf16 v[20:23], a[44:47], a[84:87], v[20:23]\n\t"
    "ds_read_b128 a[64:67], v236 offset:32768\n\t"
    "s_lshl_b32 s65, s71, 7\n\t"
    "v_mfma_f32_16x16x32_bf16 v[20:23], a[40:43], a[80:83], v[20:23]\n\t"
    "v_accvgpr_read_b32 v180, a180\n\t"
    "s_mov_b32 s17, s60\n\t"
    "v_mfma_f32_16x16x32_bf16 v[24:27], a[48:51], a[80:83], v[24:27]\n\t"
    "buffer_load_dwordx4 v235, s[40:43], s49 offen offset:3072 lds\n\t"
    "v_mfma_f32_16x16x32_bf16 v[24:27], a[52:55], a[84:87], v[24:27]\n\t"
    "v_cvt_pk_bf16_f32 v142, v180, v181\n\t"
    "s_add_u32 m0, s75, 0x10000\n\t"
    "v_mfma_f32_16x16x32_bf16 v[28:31], a[60:63], a[84:87], v[28:31]\n\t"
    "v_accvgpr_read_b32 v189, a189\n\t"
    "v_accvgpr_read_b32 v191, a191\n\t"
    "v_mfma_f32_16x16x32_bf16 v[28:31], a[56:59], a[80:83], v[28:31]\n\t"
    "global_store_dwordx4 v244, v[140:143], s[68:69] offset:2048 sc1\n\t"
    "v_mfma_f32_16x16x32_bf16 v[44:47], a[56:59], a[88:91], v[44:47]\n\t"
    "buffer_load_dwordx4 v224, s[36:39], s48 offen offset:0 lds\n\t"
    "v_mfma_f32_16x16x32_bf16 v[44:47], a[60:63], a[92:95], v[44:47]\n\t"
    "ds_read_b128 a[72:75], v236 offset:34816\n\t"
    "v_accvgpr_read_b32 v188, a188\n\t"
    "v_mfma_f32_16x16x32_bf16 v[40:43], a[52:55], a[92:95], v[40:43]\n\t"
    "ds_read_b64_tr_b16 a[16:17], v242 offset:16384\n\t"
    "v_mfma_f32_16x16x32_bf16 v[40:43], a[48:51], a[88:91], v[40:43]\n\t"
    "ds_read_b128 a[84:87], v237 offset:36864\n\t"
    "v_mfma_f32_16x16x32_bf16 v[36:39], a[40:43], a[88:91], v[36:39]\n\t"
    "buffer_load_dwordx4 v225, s[36:39], s48 offen offset:1024 lds\n\t"
    "v_mfma_f32_16x16x32_bf16 v[36:39], a[44:47], a[92:95], v[36:39]\n\t"
    "s_cmp_eq_u32 s60, 7\n\t"
    "s_cselect_b32 s18, 1, -1\n\t"
    "v_mfma_f32_16x16x32_bf16 v[32:35], a[36:39], a[92:95], v[32:35]\n\t"
    "v_accvgpr_read_b32 v187, a187\n\t"
    "v_cvt_pk_bf16_f32 v146, v188, v189\n\t"
    "v_mfma_f32_16x16x32_bf16 v[32:35], a[32:35], a[88:91], v[32:35]\n\t"
    "ds_read_b128 a[80:83], v236 offset:36864\n\t"
    "ds_read_b128 a[88:91], v236 offset:38912\n\t"
    "v_mfma_f32_16x16x32_bf16 v[64:67], a[32:35], a[96:99], v[64:67]\n\t"
    "ds_read_b64_tr_b16 a[20:21], v242 offset:20480\n\t"
    "s_add_u32 s64, s64, s65\n\t"
    "v_mfma_f32_16x16x32_bf16 v[64:67], a[36:39], a[100:103], v[64:67]\n\t"
    "ds_read_b64_tr_b16 a[18:19], v242 offset:16896\n\t"
    "v_mfma_f32_16x16x32_bf16 v[68:71], a[44:47], a[100:103], v[68:71]\n\t"
    "ds_read_b128 a[92:95], v237 offset:38912\n\t"
    "v_cvt_pk_bf16_f32 v147, v190, v191\n\t"
    "v_mfma_f32_16x16x32_bf16 v[68:71], a[40:43], a[96:99], v[68:71]\n\t"
    "buffer_load_dwordx4 v226, s[36:39], s48 offen offset:2048 lds\n\t"
    "v_mfma_f32_16x16x32_bf16 v[72:75], a[48:51], a[96:99], v[72:75]\n\t"
    "v_cvt_pk_bf16_f32 v145, v186, v187\n\t"
    "s_lshl_b32 s25, s59, 12\n\t"
    "v_mfma_f32_16x16x32_bf16 v[72:75], a[52:55], a[100:103], v[72:75]\n\t"
    "ds_read_b64_tr_b16 a[0:1], v240 offset:16384\n\t"
    "s_xor_b32 s26, s58, 2\n\t"
    "v_mfma_f32_16x16x32_bf16 v[76:79], a[60:63], a[100:103], v[76:79]\n\t"
    "v_accvgpr_read_b32 v185, a185\n\t"
    "s_lshl_b32 s26, s26, 17\n\t"
    "v_mfma_f32_16x16x32_bf16 v[76:79], a[56:59], a[96:99], v[76:79]\n\t"
    "ds_read_b128 a[96:99], v236 offset:40960\n\t"
    "ds_read_b64_tr_b16 a[6:7], v240 offset:20992\n\t"
    "v_mfma_f32_16x16x32_bf16 v[92:95], a[56:59], a[104:107], v[92:95]\n\t"
    "buffer_load_dwordx4 v227, s[36:39], s48 offen offset:3072 lds\n\t"
    "v_mfma_f32_16x16x32_bf16 v[92:95], a[60:63], a[108:111], v[92:95]\n\t"
    "ds_read_b64_tr_b16 a[26:27], v243 offset:16896\n\t"
    "s_add_u32 m0, s75, 0x11000\n\t"
    "v_mfma_f32_16x16x32_bf16 v[88:91], a[52:55], a[108:111], v[88:91]\n\t"
    "v_accvgpr_read_b32 v184, a184\n\t"
    "v_cvt_pk_bf16_f32 v144, v184, v185\n\t"
    "v_mfma_f32_16x16x32_bf16 v[88:91], a[48:51], a[104:107], v[88:91]\n\t"
    "buffer_load_dwordx4 v228, s[36:39], s48 offen offset:0 lds\n\t"
    "v_mfma_f32_16x16x32_bf16 v[84:87], a[40:43], a[104:107], v[84:87]\n\t"
    "ds_read_b64_tr_b16 a[14:15], v241 offset:20992\n\t"
    "s_sub_u32 s66, 0x400000, s64\n\t"
    "v_mfma_f32_16x16x32_bf16 v[84:87], a[44:47], a[108:111], v[84:87]\n\t"
    "ds_read_b64_tr_b16 a[24:25], v243 offset:16384\n\t"
    "s_add_u32 s25, s25, s26\n\t"
    "v_mfma_f32_16x16x32_bf16 v[80:83], a[36:39], a[108:111], v[80:83]\n\t"
    "s_add_u32 s25, s25, 0x3000000\n\t"
    "v_mfma_f32_16x16x32_bf16 v[80:83], a[32:35], a[104:107], v[80:83]\n\t"
    "ds_read_b128 a[108:111], v237 offset:43008\n\t"
    "v_mfma_f32_16x16x32_bf16 v[96:99], a[32:35], a[112:115], v[96:99]\n\t"
    "buffer_load_dwordx4 v229, s[36:39], s48 offen offset:1024 lds\n\t"
    "v_mfma_f32_16x16x32_bf16 v[96:99], a[36:39], a[116:119], v[96:99]\n\t"
    "ds_read_b128 a[100:103], v237 offset:40960\n\t"
    "ds_read_b64_tr_b16 a[22:23], v242 offset:20992\n\t"
    "v_mfma_f32_16x16x32_bf16 v[100:103], a[44:47], a[116:119], v[100:103]\n\t"
    "s_add_u32 s62, s82, s64\n\t"
    "s_addc_u32 s63, s83, 0\n\t"
    "v_mfma_f32_16x16x32_bf16 v[100:103], a[40:43], a[112:115], v[100:103]\n\t"
    "global_store_dwordx4 v244, v[144:147], s[68:69] offset:3072 sc1\n\t"
    "v_mfma_f32_16x16x32_bf16 v[104:107], a[48:51], a[112:115], v[104:107]\n\t"
    "v_accvgpr_read_b32 v176, a192\n\t"
    "v_accvgpr_read_b32 v182, a198\n\t"
    "v_mfma_f32_16x16x32_bf16 v[104:107], a[52:55], a[116:119], v[104:107]\n\t"
    "buffer_load_dwordx4 v230, s[36:39], s48 offen offset:2048 lds\n\t"
    "v_mfma_f32_16x16x32_bf16 v[108:111], a[60:63], a[116:119], v[108:111]\n\t"
    "v_accvgpr_read_b32 v178, a194\n\t"
    "v_mfma_f32_16x16x32_bf16 v[108:111], a[56:59], a[112:115], v[108:111]\n\t"
    "ds_read_b64_tr_b16 a[8:9], v241 offset:16384\n\t"
    "v_accvgpr_read_b32 v179, a195\n\t"
    "v_mfma_f32_16x16x32_bf16 v[124:127], a[56:59], a[120:123], v[124:127]\n\t"
    "v_accvgpr_read_b32 v183, a199\n\t"
    "v_mfma_f32_16x16x32_bf16 v[124:127], a[60:63], a[124:127], v[124:127]\n\t"
    "s_sub_u32 s68, s68, s70\n\t"
    "s_subb_u32 s69, s69, 0\n\t"
    "v_mfma_f32_16x16x32_bf16 v[120:123], a[52:55], a[124:127], v[120:123]\n\t"
    "ds_read_b128 a[104:107], v236 offset:43008\n\t"
    "ds_read_b128 a[112:115], v236 offset:45056\n\t"
    "v_mfma_f32_16x16x32_bf16 v[120:123], a[48:51], a[120:123], v[120:123]\n\t"
    "buffer_load_dwordx4 v231, s[36:39], s48 offen offset:3072 lds\n\t"
    "v_mfma_f32_16x16x32_bf16 v[116:119], a[40:43], a[120:123], v[116:119]\n\t"
    "v_accvgpr_read_b32 v177, a193\n\t"
    "v_accvgpr_read_b32 v181, a197\n\t"
    "v_mfma_f32_16x16x32_bf16 v[116:119], a[44:47], a[124:127], v[116:119]\n\t"
    "ds_read_b128 a[116:119], v237 offset:45056\n\t"
    "v_accvgpr_read_b32 v180, a196\n\t"
    "v_mfma_f32_16x16x32_bf16 v[112:115], a[36:39], a[124:127], v[112:115]\n\t"
    "s_add_u32 s48, s48, 128\n\t"
    "s_and_b32 s48, s48, 1023\n\t"
    "v_mfma_f32_16x16x32_bf16 v[112:115], a[32:35], a[120:123], v[112:115]\n\t"
    "ds_read_b128 a[120:123], v236 offset:47104\n\t"
    "ds_read_b128 a[124:127], v237 offset:47104\n\t"
    "s_waitcnt lgkmcnt(6)\n\t"
    "v_mfma_f32_16x16x32_bf16 v[48:51], a[0:3], a[64:67], v[48:51]\n\t"
    "v_cvt_pk_bf16_f32 v130, v180, v181\n\t"
    "s_lshl_b32 s49, s48, 12\n\t"
    "v_mfma_f32_16x16x32_bf16 v[48:51], a[4:7], a[68:71], v[48:51]\n\t"
    "v_cvt_pk_bf16_f32 v131, v182, v183\n\t"
    "s_lshl_b32 s26, s29, 5\n\t"
    "v_mfma_f32_16x16x32_bf16 v[52:55], a[12:15], a[68:71], v[52:55]\n\t"
    "v_cvt_pk_bf16_f32 v129, v178, v179\n\t"
    "s_add_u32 s26, s26, s25\n\t"
    "s_waitcnt lgkmcnt(4)\n\t"
    "v_mfma_f32_16x16x32_bf16 v[52:55], a[8:11], a[64:67], v[52:55]\n\t"
    "s_add_u32 s46, s20, s26\n\t"
    "s_addc_u32 s47, s21, 0\n\t"
    "v_mfma_f32_16x16x32_bf16 v[56:59], a[16:19], a[64:67], v[56:59]\n\t"
    "v_cvt_pk_bf16_f32 v128, v176, v177\n\t"
    "s_add_u32 m0, s52, 0x4000\n\t"
    "v_mfma_f32_16x16x32_bf16 v[56:59], a[20:23], a[68:71], v[56:59]\n\t"
    "s_waitcnt vmcnt(14) lgkmcnt(0)\n\t"
    "s_barrier\n\t"
    "v_mfma_f32_16x16x32_bf16 v[60:63], a[28:31], a[68:71], v[60:63]\n\t"
    "ds_read_b64_tr_b16 a[58:59], v243 offset:33280\n\t"
    "s_lshl_b32 s26, s28, 5\n\t"
    "v_mfma_f32_16x16x32_bf16 v[60:63], a[24:27], a[64:67], v[60:63]\n\t"
    "ds_read_b64_tr_b16 a[34:35], v240 offset:33280\n\t"
    "v_mfma_f32_16x16x32_bf16 v[12:15], a[24:27], a[72:75], v[12:15]\n\t"
    "ds_read_b64_tr_b16 a[50:51], v242 offset:33280\n\t"
    "ds_read_b64_tr_b16 a[60:61], v243 offset:36864\n\t"
    "v_mfma_f32_16x16x32_bf16 v[12:15], a[28:31], a[76:79], v[12:15]\n\t"
    "buffer_load_dwordx4 v232, s[40:43], s49 offen offset:0 lds\n\t"
    "v_mfma_f32_16x16x32_bf16 v[8:11], a[20:23], a[76:79], v[8:11]\n\t"
    "ds_read_b64_tr_b16 a[32:33], v240 offset:32768\n\t"
    "v_accvgpr_read_b32 v191, a207\n\t"
    "v_mfma_f32_16x16x32_bf16 v[8:11], a[16:19], a[72:75], v[8:11]\n\t"
    "buffer_load_dwordx4 v233, s[40:43], s49 offen offset:1024 lds\n\t"
    "v_mfma_f32_16x16x32_bf16 v[4:7], a[8:11], a[72:75], v[4:7]\n\t"
    "ds_read_b64_tr_b16 a[40:41], v241 offset:32768\n\t"
    "ds_read_b64_tr_b16 a[42:43], v241 offset:33280\n\t"
    "v_mfma_f32_16x16x32_bf16 v[4:7], a[12:15], a[76:79], v[4:7]\n\t"
    "ds_read_b64_tr_b16 a[44:45], v241 offset:36864\n\t"
    "s_add_u32 s26, s26, s25\n\t"
    "v_mfma_f32_16x16x32_bf16 v[0:3], a[4:7], a[76:79], v[0:3]\n\t"
    "ds_read_b64_tr_b16 a[38:39], v240 offset:37376\n\t"
    "v_accvgpr_read_b32 v190, a206\n\t"
    "v_mfma_f32_16x16x32_bf16 v[0:3], a[0:3], a[72:75], v[0:3]\n\t"
    "buffer_load_dwordx4 v234, s[40:43], s49 offen offset:2048 lds\n\t"
    "v_mfma_f32_16x16x32_bf16 v[16:19], a[0:3], a[80:83], v[16:19]\n\t"
    "ds_read_b128 a[68:71], v237 offset:0\n\t"
    "ds_read_b64_tr_b16 a[48:49], v242 offset:32768\n\t"
    "v_mfma_f32_16x16x32_bf16 v[16:19], a[4:7], a[84:87], v[16:19]\n\t"
    "ds_read_b128 a[64:67], v236 offset:0\n\t"
    "v_accvgpr_read_b32 v189, a205\n\t"
    "v_mfma_f32_16x16x32_bf16 v[20:23], a[12:15], a[84:87], v[20:23]\n\t"
    "ds_read_b64_tr_b16 a[46:47], v241 offset:37376\n\t"
    "s_add_u32 s26, s26, 0x80000\n\t"
    "v_mfma_f32_16x16x32_bf16 v[20:23], a[8:11], a[80:83], v[20:23]\n\t"
    "buffer_load_dwordx4 v235, s[40:43], s49 offen offset:3072 lds\n\t"
    "v_mfma_f32_16x16x32_bf16 v[24:27], a[16:19], a[80:83], v[24:27]\n\t"
    "ds_read_b128 a[72:75], v236 offset:2048\n\t"
    "v_accvgpr_read_b32 v188, a204\n\t"
    "v_mfma_f32_16x16x32_bf16 v[24:27], a[20:23], a[84:87], v[24:27]\n\t"
    "ds_read_b128 a[76:79], v237 offset:2048\n\t"
    "v_cvt_pk_bf16_f32 v158, v188, v189\n\t"
    "v_mfma_f32_16x16x32_bf16 v[28:31], a[28:31], a[84:87], v[28:31]\n\t"
    "global_store_dwordx4 v244, v[128:131], s[68:69] offset:0 sc1\n\t"
    "v_mfma_f32_16x16x32_bf16 v[28:31], a[24:27], a[80:83], v[28:31]\n\t"
    "v_cvt_pk_bf16_f32 v159, v190, v191\n\t"
    "s_add_u32 m0, s75, 0x8000\n\t"
    "v_mfma_f32_16x16x32_bf16 v[44:47], a[24:27], a[88:91], v[44:47]\n\t"
    "buffer_load_dwordx4 v224, s[36:39], s48 offen offset:0 lds\n\t"
    "v_mfma_f32_16x16x32_bf16 v[44:47], a[28:31], a[92:95], v[44:47]\n\t"
    "ds_read_b128 a[80:83], v236 offset:4096\n\t"
    "ds_read_b64_tr_b16 a[54:55], v242 offset:37376\n\t"
    "v_mfma_f32_16x16x32_bf16 v[40:43], a[20:23], a[92:95], v[40:43]\n\t"
    "buffer_load_dwordx4 v225, s[36:39], s48 offen offset:1024 lds\n\t"
    "v_mfma_f32_16x16x32_bf16 v[40:43], a[16:19], a[88:91], v[40:43]\n\t"
    "ds_read_b64_tr_b16 a[52:53], v242 offset:36864\n\t"
    "v_accvgpr_read_b32 v186, a202\n\t"
    "v_mfma_f32_16x16x32_bf16 v[36:39], a[8:11], a[88:91], v[36:39]\n\t"
    "ds_read_b128 a[84:87], v237 offset:4096\n\t"
    "v_accvgpr_read_b32 v185, a201\n\t"
    "v_mfma_f32_16x16x32_bf16 v[36:39], a[12:15], a[92:95], v[36:39]\n\t"
    "v_accvgpr_read_b32 v184, a200\n\t"
    "s_lshl_b32 s25, s59, 19\n\t"
    "v_mfma_f32_16x16x32_bf16 v[32:35], a[4:7], a[92:95], v[32:35]\n\t"
    "ds_read_b128 a[92:95], v237 offset:6144\n\t"
    "v_accvgpr_read_b32 v187, a203\n\t"
    "v_mfma_f32_16x16x32_bf16 v[32:35], a[0:3], a[88:91], v[32:35]\n\t"
    "ds_read_b64_tr_b16 a[62:63], v243 offset:37376\n\t"
    "v_cvt_pk_bf16_f32 v156, v184, v185\n\t"
    "v_mfma_f32_16x16x32_bf16 v[64:67], a[0:3], a[96:99], v[64:67]\n\t"
    "ds_read_b128 a[88:91], v236 offset:6144\n\t"
    "ds_read_b64_tr_b16 a[56:57], v243 offset:32768\n\t"
    "v_mfma_f32_16x16x32_bf16 v[64:67], a[4:7], a[100:103], v[64:67]\n\t"
    "buffer_load_dwordx4 v226, s[36:39], s48 offen offset:2048 lds\n\t"
    "v_mfma_f32_16x16x32_bf16 v[68:71], a[12:15], a[100:103], v[68:71]\n\t"
    "v_accvgpr_read_b32 v179, a211\n\t"
    "v_accvgpr_read_b32 v182, a214\n\t"
    "v_mfma_f32_16x16x32_bf16 v[68:71], a[8:11], a[96:99], v[68:71]\n\t"
    "ds_read_b64_tr_b16 a[36:37], v240 offset:36864\n\t"
    "v_accvgpr_read_b32 v178, a210\n\t"
    "v_mfma_f32_16x16x32_bf16 v[72:75], a[16:19], a[96:99], v[72:75]\n\t"
    "v_accvgpr_read_b32 v176, a208\n\t"
    "v_accvgpr_read_b32 v183, a215\n\t"
    "v_mfma_f32_16x16x32_bf16 v[72:75], a[20:23], a[100:103], v[72:75]\n\t"
    "v_cvt_pk_bf16_f32 v157, v186, v187\n\t"
    "v_accvgpr_read_b32 v177, a209\n\t"
    "v_mfma_f32_16x16x32_bf16 v[76:79], a[28:31], a[100:103], v[76:79]\n\t"
    "buffer_load_dwordx4 v227, s[36:39], s48 offen offset:3072 lds\n\t"
    "v_mfma_f32_16x16x32_bf16 v[76:79], a[24:27], a[96:99], v[76:79]\n\t"
    "ds_read_b128 a[96:99], v236 offset:8192\n\t"
    "s_add_u32 m0, s75, 0x9000\n\t"
    "v_mfma_f32_16x16x32_bf16 v[92:95], a[24:27], a[104:107], v[92:95]\n\t"
    "v_accvgpr_read_b32 v188, a220\n\t"
    "v_accvgpr_read_b32 v191, a223\n\t"
    "v_mfma_f32_16x16x32_bf16 v[92:95], a[28:31], a[108:111], v[92:95]\n\t"
    "buffer_load_dwordx4 v228, s[36:39], s48 offen offset:0 lds\n\t"
    "v_mfma_f32_16x16x32_bf16 v[88:91], a[20:23], a[108:111], v[88:91]\n\t"
    "v_cvt_pk_bf16_f32 v192, v176, v177\n\t"
    "v_accvgpr_read_b32 v189, a221\n\t"
    "v_mfma_f32_16x16x32_bf16 v[88:91], a[16:19], a[104:107], v[88:91]\n\t"
    "buffer_load_dwordx4 v229, s[36:39], s48 offen offset:1024 lds\n\t"
    "v_mfma_f32_16x16x32_bf16 v[84:87], a[8:11], a[104:107], v[84:87]\n\t"
    "ds_read_b128 a[100:103], v237 offset:8192\n\t"
    "v_accvgpr_read_b32 v184, a216\n\t"
    "v_mfma_f32_16x16x32_bf16 v[84:87], a[12:15], a[108:111], v[84:87]\n\t"
    "v_accvgpr_read_b32 v180, a212\n\t"
    "v_cvt_pk_bf16_f32 v222, v188, v189\n\t"
    "v_mfma_f32_16x16x32_bf16 v[80:83], a[4:7], a[108:111], v[80:83]\n\t"
    "v_accvgpr_read_b32 v185, a217\n\t"
    "v_accvgpr_read_b32 v187, a219\n\t"
    "v_mfma_f32_16x16x32_bf16 v[80:83], a[0:3], a[104:107], v[80:83]\n\t"
    "buffer_load_dwordx4 v230, s[36:39], s48 offen offset:2048 lds\n\t"
    "v_mfma_f32_16x16x32_bf16 v[96:99], a[0:3], a[112:115], v[96:99]\n\t"
    "v_cvt_pk_bf16_f32 v195, v182, v183\n\t"
    "v_cvt_pk_bf16_f32 v220, v184, v185\n\t"
    "v_mfma_f32_16x16x32_bf16 v[96:99], a[4:7], a[116:119], v[96:99]\n\t"
    "ds_read_b128 a[108:111], v237 offset:10240\n\t"
    "v_accvgpr_read_b32 v181, a213\n\t"
    "v_mfma_f32_16x16x32_bf16 v[100:103], a[12:15], a[116:119], v[100:103]\n\t"
    "global_store_dwordx4 v244, v[156:159], s[68:69] offset:1024 sc1\n\t"
    "v_mfma_f32_16x16x32_bf16 v[100:103], a[8:11], a[112:115], v[100:103]\n\t"
    "v_cvt_pk_bf16_f32 v194, v180, v181\n\t"
    "v_accvgpr_read_b32 v189, a237\n\t"
    "v_mfma_f32_16x16x32_bf16 v[104:107], a[16:19], a[112:115], v[104:107]\n\t"
    "ds_read_b128 a[104:107], v236 offset:10240\n\t"
    "v_accvgpr_read_b32 v186, a218\n\t"
    "v_mfma_f32_16x16x32_bf16 v[104:107], a[20:23], a[116:119], v[104:107]\n\t"
    "v_cvt_pk_bf16_f32 v221, v186, v187\n\t"
    "v_accvgpr_read_b32 v177, a225\n\t"
    "v_mfma_f32_16x16x32_bf16 v[108:111], a[28:31], a[116:119], v[108:111]\n\t"
    "v_cvt_pk_bf16_f32 v193, v178, v179\n\t"
    "v_accvgpr_read_b32 v190, a222\n\t"
    "v_mfma_f32_16x16x32_bf16 v[108:111], a[24:27], a[112:115], v[108:111]\n\t"
    "buffer_load_dwordx4 v231, s[36:39], s48 offen offset:3072 lds\n\t"
    "v_mfma_f32_16x16x32_bf16 v[124:127], a[24:27], a[120:123], v[124:127]\n\t"
    "v_accvgpr_read_b32 v179, a227\n\t"
    "v_accvgpr_read_b32 v180, a228\n\t"
    "v_mfma_f32_16x16x32_bf16 v[124:127], a[28:31], a[124:127], v[124:127]\n\t"
    "ds_read_b128 a[112:115], v236 offset:12288\n\t"
    "v_accvgpr_read_b32 v182, a230\n\t"
    "v_mfma_f32_16x16x32_bf16 v[120:123], a[20:23], a[124:127], v[120:123]\n\t"
    "v_cvt_pk_bf16_f32 v223, v190, v191\n\t"
    "v_accvgpr_read_b32 v187, a235\n\t"
    "v_mfma_f32_16x16x32_bf16 v[120:123], a[16:19], a[120:123], v[120:123]\n\t"
    "v_accvgpr_read_b32 v178, a226\n\t"
    "v_accvgpr_read_b32 v186, a234\n\t"
    "v_mfma_f32_16x16x32_bf16 v[116:119], a[8:11], a[120:123], v[116:119]\n\t"
    "ds_read_b128 a[116:119], v237 offset:12288\n\t"
    "v_cvt_pk_bf16_f32 v133, v178, v179\n\t"
    "v_mfma_f32_16x16x32_bf16 v[116:119], a[12:15], a[124:127], v[116:119]\n\t"
    "v_accvgpr_read_b32 v181, a229\n\t"
    "v_cvt_pk_bf16_f32 v134, v180, v181\n\t"
    "v_mfma_f32_16x16x32_bf16 v[112:115], a[4:7], a[124:127], v[112:115]\n\t"
    "ds_read_b128 a[124:127], v237 offset:14336\n\t"
    "v_accvgpr_read_b32 v185, a233\n\t"
    "v_mfma_f32_16x16x32_bf16 v[112:115], a[0:3], a[120:123], v[112:115]\n\t"
    "ds_read_b128 a[120:123], v236 offset:14336\n\t"
    "v_accvgpr_read_b32 v176, a224\n\t"
    "s_waitcnt lgkmcnt(4)\n\t"
    "v_mfma_f32_16x16x32_bf16 v[48:51], a[32:35], a[64:67], v[48:51]\n\t"
    "v_accvgpr_read_b32 v184, a232\n\t"
    "v_accvgpr_read_b32 v180, a244\n\t"
    "v_mfma_f32_16x16x32_bf16 v[48:51], a[36:39], a[68:71], v[48:51]\n\t"
    "v_accvgpr_read_b32 v183, a231\n\t"
    "v_cvt_pk_bf16_f32 v136, v184, v185\n\t"
    "v_mfma_f32_16x16x32_bf16 v[52:55], a[44:47], a[68:71], v[52:55]\n\t"
    "v_accvgpr_read_b32 v190, a238\n\t"
    "s_lshl_b32 s56, s98, 14\n\t"
    "v_mfma_f32_16x16x32_bf16 v[52:55], a[40:43], a[64:67], v[52:55]\n\t"
    "v_accvgpr_read_b32 v181, a245\n\t"
    "v_accvgpr_read_b32 v184, a248\n\t"
    "v_mfma_f32_16x16x32_bf16 v[56:59], a[48:51], a[64:67], v[56:59]\n\t"
    "v_accvgpr_read_b32 v185, a249\n\t"
    "s_lshl_b32 s57, s61, 11\n\t"
    "v_mfma_f32_16x16x32_bf16 v[56:59], a[52:55], a[68:71], v[56:59]\n\t"
    "s_waitcnt vmcnt(14) lgkmcnt(0)\n\t"
    "s_barrier\n\t"
    "v_mfma_f32_16x16x32_bf16 v[60:63], a[60:63], a[68:71], v[60:63]\n\t"
    "v_accvgpr_read_b32 v188, a236\n\t"
    "v_cvt_pk_bf16_f32 v132, v176, v177\n\t"
    "v_mfma_f32_16x16x32_bf16 v[60:63], a[56:59], a[64:67], v[60:63]\n\t"
    "ds_read_b128 a[68:71], v239 offset:0\n\t"
    "v_accvgpr_read_b32 v191, a239\n\t"
    "v_mfma_f32_16x16x32_bf16 v[12:15], a[56:59], a[72:75], v[12:15]\n\t"
    "v_accvgpr_read_b32 v176, a240\n\t"
    "v_accvgpr_read_b32 v179, a243\n\t"
    "v_mfma_f32_16x16x32_bf16 v[12:15], a[60:63], a[76:79], v[12:15]\n\t"
    "ds_write_b128 v251, v[208:211] offset:0\n\t"
    "v_mfma_f32_16x16x32_bf16 v[8:11], a[52:55], a[76:79], v[8:11]\n\t"
    "v_cvt_pk_bf16_f32 v137, v186, v187\n\t"
    "v_cvt_pk_bf16_f32 v138, v188, v189\n\t"
    "v_mfma_f32_16x16x32_bf16 v[8:11], a[48:51], a[72:75], v[8:11]\n\t"
    "ds_read_b128 a[64:67], v238 offset:0\n\t"
    "v_cvt_pk_bf16_f32 v135, v182, v183\n\t"
    "v_mfma_f32_16x16x32_bf16 v[4:7], a[40:43], a[72:75], v[4:7]\n\t"
    "v_cvt_pk_bf16_f32 v139, v190, v191\n\t"
    "s_add_u32 s56, s56, s57\n\t"
    "v_mfma_f32_16x16x32_bf16 v[4:7], a[44:47], a[76:79], v[4:7]\n\t"
    "global_store_dwordx4 v244, v[192:195], s[68:69] offset:2048 sc1\n\t"
    "v_mfma_f32_16x16x32_bf16 v[0:3], a[36:39], a[76:79], v[0:3]\n\t"
    "v_accvgpr_read_b32 v182, a246\n\t"
    "v_accvgpr_read_b32 v183, a247\n\t"
    "v_mfma_f32_16x16x32_bf16 v[0:3], a[32:35], a[72:75], v[0:3]\n\t"
    "ds_write_b128 v251, v[212:215] offset:1024\n\t"
    "v_mfma_f32_16x16x32_bf16 v[16:19], a[32:35], a[80:83], v[16:19]\n\t"
    "global_store_dwordx4 v244, v[220:223], s[68:69] offset:3072 sc1\n\t"
    "v_mfma_f32_16x16x32_bf16 v[16:19], a[36:39], a[84:87], v[16:19]\n\t"
    "s_add_u32 s68, s68, 0x4000\n\t"
    "s_addc_u32 s69, s69, 0\n\t"
    "v_mfma_f32_16x16x32_bf16 v[20:23], a[44:47], a[84:87], v[20:23]\n\t"
    "global_store_dwordx4 v244, v[132:135], s[68:69] offset:0 sc1\n\t"
    "v_mfma_f32_16x16x32_bf16 v[20:23], a[40:43], a[80:83], v[20:23]\n\t"
    "s_add_u32 s56, s56, 0x18000\n\t"
    "v_add_u32 v224, s56, v244\n\t"
    "v_mfma_f32_16x16x32_bf16 v[24:27], a[48:51], a[80:83], v[24:27]\n\t"
    "v_accvgpr_read_b32 v187, a251\n\t"
    "v_accvgpr_read_b32 v189, a253\n\t"
    "v_mfma_f32_16x16x32_bf16 v[24:27], a[52:55], a[84:87], v[24:27]\n\t"
    "ds_write_b128 v251, v[216:219] offset:4096\n\t"
    "v_mfma_f32_16x16x32_bf16 v[28:31], a[60:63], a[84:87], v[28:31]\n\t"
    "ds_read_b128 a[76:79], v239 offset:2048\n\t"
    "v_accvgpr_read_b32 v178, a242\n\t"
    "v_mfma_f32_16x16x32_bf16 v[28:31], a[56:59], a[80:83], v[28:31]\n\t"
    "v_cvt_pk_bf16_f32 v141, v178, v179\n\t"
    "v_accvgpr_read_b32 v190, a254\n\t"
    "v_mfma_f32_16x16x32_bf16 v[44:47], a[56:59], a[88:91], v[44:47]\n\t"
    "global_store_dwordx4 v244, v[136:139], s[68:69] offset:1024 sc1\n\t"
    "v_mfma_f32_16x16x32_bf16 v[44:47], a[60:63], a[92:95], v[44:47]\n\t"
    "v_accvgpr_read_b32 v177, a241\n\t"
    "v_cvt_pk_bf16_f32 v143, v182, v183\n\t"
    "v_mfma_f32_16x16x32_bf16 v[40:43], a[52:55], a[92:95], v[40:43]\n\t"
    "v_cvt_pk_bf16_f32 v140, v176, v177\n\t"
    "v_cvt_pk_bf16_f32 v144, v184, v185\n\t"
    "v_mfma_f32_16x16x32_bf16 v[40:43], a[48:51], a[88:91], v[40:43]\n\t"
    "ds_read_b128 a[72:75], v238 offset:2048\n\t"
    "v_cvt_pk_bf16_f32 v142, v180, v181\n\t"
    "v_mfma_f32_16x16x32_bf16 v[36:39], a[40:43], a[88:91], v[36:39]\n\t"
    "ds_write_b128 v251, v[160:163] offset:5120\n\t"
    "v_mfma_f32_16x16x32_bf16 v[36:39], a[44:47], a[92:95], v[36:39]\n\t"
    "global_store_dwordx4 v244, v[140:143], s[68:69] offset:2048 sc1\n\t"
    "v_mfma_f32_16x16x32_bf16 v[32:35], a[36:39], a[92:95], v[32:35]\n\t"
    "v_accvgpr_read_b32 v191, a255\n\t"
    "s_lshl_b32 s56, s58, 12\n\t"
    "v_mfma_f32_16x16x32_bf16 v[32:35], a[32:35], a[88:91], v[32:35]\n\t"
    "v_accvgpr_read_b32 v186, a250\n\t"
    "v_accvgpr_read_b32 v188, a252\n\t"
    "v_mfma_f32_16x16x32_bf16 v[64:67], a[32:35], a[96:99], v[64:67]\n\t"
    "v_cvt_pk_bf16_f32 v146, v188, v189\n\t"
    "v_cvt_pk_bf16_f32 v147, v190, v191\n\t"
    "v_mfma_f32_16x16x32_bf16 v[64:67], a[36:39], a[100:103], v[64:67]\n\t"
    "v_cvt_pk_bf16_f32 v145, v186, v187\n\t"
    "s_add_u32 s56, s56, 0x18000\n\t"
    "v_mfma_f32_16x16x32_bf16 v[68:71], a[44:47], a[100:103], v[68:71]\n\t"
    "ds_read_b128 a[84:87], v239 offset:4096\n\t"
    "v_add_u32 v225, s56, v244\n\t"
    "v_mfma_f32_16x16x32_bf16 v[68:71], a[40:43], a[96:99], v[68:71]\n\t"
    "ds_read_b128 a[80:83], v238 offset:4096\n\t"
    "v_mfma_f32_16x16x32_bf16 v[72:75], a[48:51], a[96:99], v[72:75]\n\t"
    "global_store_dwordx4 v244, v[144:147], s[68:69] offset:3072 sc1\n\t"
    "v_mfma_f32_16x16x32_bf16 v[72:75], a[52:55], a[100:103], v[72:75]\n\t"
    "ds_write_b128 v251, v[164:167] offset:16384\n\t"
    "v_mfma_f32_16x16x32_bf16 v[76:79], a[60:63], a[100:103], v[76:79]\n\t"
    "ds_read_b64_tr_b16 a[10:11], v241 offset:512\n\t"
    "ds_read_b64_tr_b16 a[22:23], v242 offset:4608\n\t"
    "v_mfma_f32_16x16x32_bf16 v[76:79], a[56:59], a[96:99], v[76:79]\n\t"
    "ds_read_b128 a[92:95], v239 offset:6144\n\t"
    "v_mfma_f32_16x16x32_bf16 v[92:95], a[56:59], a[104:107], v[92:95]\n\t"
    "ds_read_b128 a[96:99], v238 offset:8192\n\t"
    "v_xor_b32 v232, 0x2000, v225\n\t"
    "v_mfma_f32_16x16x32_bf16 v[92:95], a[60:63], a[108:111], v[92:95]\n\t"
    "ds_read_b64_tr_b16 a[16:17], v242 offset:0\n\t"
    "v_mfma_f32_16x16x32_bf16 v[88:91], a[52:55], a[108:111], v[88:91]\n\t"
    "ds_write_b128 v251, v[168:171] offset:17408\n\t"
    "v_mfma_f32_16x16x32_bf16 v[88:91], a[48:51], a[104:107], v[88:91]\n\t"
    "ds_read_b128 a[88:91], v238 offset:6144\n\t"
    "ds_read_b64_tr_b16 a[4:5], v240 offset:4096\n\t"
    "v_mfma_f32_16x16x32_bf16 v[84:87], a[40:43], a[104:107], v[84:87]\n\t"
    "s_lshl_b32 s56, s61, 14\n\t"
    "v_mfma_f32_16x16x32_bf16 v[84:87], a[44:47], a[108:111], v[84:87]\n\t"
    "ds_read_b64_tr_b16 a[26:27], v243 offset:512\n\t"
    "v_add_u32 v227, s56, v244\n\t"
    "v_mfma_f32_16x16x32_bf16 v[80:83], a[36:39], a[108:111], v[80:83]\n\t"
    "v_add_u32 v228, 0x10000, v227\n\t"
    "v_xor_b32 v230, 0x4000, v227\n\t"
    "v_mfma_f32_16x16x32_bf16 v[80:83], a[32:35], a[104:107], v[80:83]\n\t"
    "ds_read_b128 a[100:103], v239 offset:8192\n\t"
    "ds_read_b64_tr_b16 a[8:9], v241 offset:0\n\t"
    "v_mfma_f32_16x16x32_bf16 v[96:99], a[32:35], a[112:115], v[96:99]\n\t"
    "ds_read_b64_tr_b16 a[12:13], v241 offset:4096\n\t"
    "v_mfma_f32_16x16x32_bf16 v[96:99], a[36:39], a[116:119], v[96:99]\n\t"
    "ds_read_b128 a[104:107], v238 offset:10240\n\t"
    "ds_read_b64_tr_b16 a[14:15], v241 offset:4608\n\t"
    "v_mfma_f32_16x16x32_bf16 v[100:103], a[44:47], a[116:119], v[100:103]\n\t"
    "ds_write_b128 v251, v[172:175] offset:20480\n\t"
    "v_mfma_f32_16x16x32_bf16 v[100:103], a[40:43], a[112:115], v[100:103]\n\t"
    "ds_read_b64_tr_b16 a[18:19], v242 offset:512\n\t"
    "v_mfma_f32_16x16x32_bf16 v[104:107], a[48:51], a[112:115], v[104:107]\n\t"
    "ds_read_b128 a[108:111], v239 offset:10240\n\t"
    "ds_read_b64_tr_b16 a[0:1], v240 offset:0\n\t"
    "v_mfma_f32_16x16x32_bf16 v[104:107], a[52:55], a[116:119], v[104:107]\n\t"
    "ds_read_b64_tr_b16 a[20:21], v242 offset:4096\n\t"
    "v_mfma_f32_16x16x32_bf16 v[108:111], a[60:63], a[116:119], v[108:111]\n\t"
    "ds_read_b64_tr_b16 a[6:7], v240 offset:4608\n\t"
    "v_mfma_f32_16x16x32_bf16 v[108:111], a[56:59], a[112:115], v[108:111]\n\t"
    "ds_read_b64_tr_b16 a[24:25], v243 offset:0\n\t"
    "v_xor_b32 v233, 0x4000, v228\n\t"
    "v_mfma_f32_16x16x32_bf16 v[124:127], a[56:59], a[120:123], v[124:127]\n\t"
    "ds_write_b128 v251, v[152:155] offset:21504\n\t"
    "v_mfma_f32_16x16x32_bf16 v[124:127], a[60:63], a[124:127], v[124:127]\n\t"
    "ds_read_b64_tr_b16 a[28:29], v243 offset:4096\n\t"
    "v_mfma_f32_16x16x32_bf16 v[120:123], a[52:55], a[124:127], v[120:123]\n\t"
    "ds_read_b64_tr_b16 a[2:3], v240 offset:512\n\t"
    "ds_read_b64_tr_b16 a[30:31], v243 offset:4608\n\t"
    "v_mfma_f32_16x16x32_bf16 v[120:123], a[48:51], a[120:123], v[120:123]\n\t"
    "s_lshl_b32 s56, s98, 4\n\t"
    "v_mfma_f32_16x16x32_bf16 v[116:119], a[40:43], a[120:123], v[116:119]\n\t"
    "ds_read_b128 a[112:115], v238 offset:12288\n\t"
    "ds_read_b128 a[116:119], v239 offset:12288\n\t"
    "v_mfma_f32_16x16x32_bf16 v[116:119], a[44:47], a[124:127], v[116:119]\n\t"
    "v_lshlrev_b32 v231, 2, v248\n\t"
    "v_mfma_f32_16x16x32_bf16 v[112:115], a[36:39], a[124:127], v[112:115]\n\t"
    "ds_read_b128 a[124:127], v239 offset:14336\n\t"
    "v_mfma_f32_16x16x32_bf16 v[112:115], a[32:35], a[120:123], v[112:115]\n\t"
    "ds_read_b128 a[120:123], v238 offset:14336\n\t"
    "s_waitcnt lgkmcnt(5)\n\t"
    "v_mfma_f32_16x16x32_bf16 v[48:51], a[0:3], a[64:67], v[48:51]\n\t"
    "v_sub_u32 v231, v231, v247\n\t"
    "Lis7_%=:\n\t"
    "s_cbranch_execz Lis11_%=\n\t"
    "v_mfma_f32_16x16x32_bf16 v[48:51], a[4:7], a[68:71], v[48:51]\n\t"
    "v_subrev_u32 v231, s56, v231\n\t"
    "v_mfma_f32_16x16x32_bf16 v[52:55], a[12:15], a[68:71], v[52:55]\n\t"
    "v_mov_b32 v229, 0\n\t"
    "v_mfma_f32_16x16x32_bf16 v[52:55], a[8:11], a[64:67], v[52:55]\n\t"
    "v_mov_b32 v226, 0xff800000\n\t"
    "v_mfma_f32_16x16x32_bf16 v[56:59], a[16:19], a[64:67], v[56:59]\n\t"
    "v_mul_i32_i24 v234, s61, v226\n\t"
    "v_mfma_f32_16x16x32_bf16 v[56:59], a[20:23], a[68:71], v[56:59]\n\t"
    "s_waitcnt vmcnt(0)\n\t"
    "s_waitcnt vmcnt(0) lgkmcnt(0)\n\t"
    "s_barrier\n\t"
    "s_cmp_lg_u32 s58, 0\n\t"
    "s_cbranch_scc1 L_sg22_%=\n\t"
    "s_mov_b64 exec, 3\n\t"
    "global_atomic_add v249, v250, s[84:85] sc1\n\t"
    "s_mov_b64 exec, -1\n\t"
    "L_sg22_%=:\n\t"
    "v_mfma_f32_16x16x32_bf16 v[60:63], a[28:31], a[68:71], v[60:63]\n\t"
    "ds_read_b64_tr_b16 a[62:63], v243 offset:20992\n\t"
    "v_mfma_f32_16x16x32_bf16 v[60:63], a[24:27], a[64:67], v[60:63]\n\t"
    "ds_read_b128 a[168:171], v227 offset:43008\n\t"
    "v_mfma_f32_16x16x32_bf16 v[12:15], a[24:27], a[72:75], v[12:15]\n\t"
    "ds_write_b128 v252, v[128:131] offset:0\n\t"
    "v_mfma_f32_16x16x32_bf16 v[12:15], a[28:31], a[76:79], v[12:15]\n\t"
    "ds_read_b128 a[144:147], v227 offset:36864\n\t"
    "v_mfma_f32_16x16x32_bf16 v[8:11], a[20:23], a[76:79], v[8:11]\n\t"
    "ds_read_b128 a[68:71], v237 offset:32768\n\t"
    "v_mfma_f32_16x16x32_bf16 v[8:11], a[16:19], a[72:75], v[8:11]\n\t"
    "ds_read_b128 a[64:67], v236 offset:32768\n\t"
    "v_mfma_f32_16x16x32_bf16 v[4:7], a[8:11], a[72:75], v[4:7]\n\t"
    "ds_read_b64_tr_b16 a[34:35], v240 offset:16896\n\t"
    "v_mfma_f32_16x16x32_bf16 v[4:7], a[12:15], a[76:79], v[4:7]\n\t"
    "ds_read_b128 a[136:139], v227 offset:34816\n\t"
    "v_mfma_f32_16x16x32_bf16 v[0:3], a[4:7], a[76:79], v[0:3]\n\t"
    "ds_read_b128 a[76:79], v237 offset:34816\n\t"
    "v_mfma_f32_16x16x32_bf16 v[0:3], a[0:3], a[72:75], v[0:3]\n\t"
    "ds_write_b128 v252, v[156:159] offset:1024\n\t"
    "v_mfma_f32_16x16x32_bf16 v[16:19], a[0:3], a[80:83], v[16:19]\n\t"
    "ds_read_b64_tr_b16 a[54:55], v242 offset:20992\n\t"
    "v_mfma_f32_16x16x32_bf16 v[16:19], a[4:7], a[84:87], v[16:19]\n\t"
    "ds_read_b64_tr_b16 a[50:51], v242 offset:16896\n\t"
    "v_mfma_f32_16x16x32_bf16 v[20:23], a[12:15], a[84:87], v[20:23]\n\t"
    "s_add_u32 s36, s20, s25\n\t"
    "s_addc_u32 s37, s21, 0\n\t"
    "v_mfma_f32_16x16x32_bf16 v[20:23], a[8:11], a[80:83], v[20:23]\n\t"
    "s_mov_b32 s38, 0x80000\n\t"
    "v_mfma_f32_16x16x32_bf16 v[24:27], a[16:19], a[80:83], v[24:27]\n\t"
    "s_add_u32 s40, s36, 0x1000000\n\t"
    "s_addc_u32 s41, s37, 0\n\t"
    "v_mfma_f32_16x16x32_bf16 v[24:27], a[20:23], a[84:87], v[24:27]\n\t"
    "ds_write_b128 v252, v[192:195] offset:2048\n\t"
    "v_mfma_f32_16x16x32_bf16 v[28:31], a[28:31], a[84:87], v[28:31]\n\t"
    "s_mov_b32 s42, 0x80000\n\t"
    "v_mfma_f32_16x16x32_bf16 v[28:31], a[24:27], a[80:83], v[28:31]\n\t"
    "ds_read_b128 a[172:175], v227 offset:44032\n\t"
    "v_mfma_f32_16x16x32_bf16 v[44:47], a[24:27], a[88:91], v[44:47]\n\t"
    "ds_write_b128 v252, v[220:223] offset:3072\n\t"
    "v_mfma_f32_16x16x32_bf16 v[44:47], a[28:31], a[92:95], v[44:47]\n\t"
    "ds_read_b128 a[72:75], v236 offset:34816\n\t"
    "v_mfma_f32_16x16x32_bf16 v[40:43], a[20:23], a[92:95], v[40:43]\n\t"
    "s_add_u32 s48, s20, s26\n\t"
    "s_addc_u32 s49, s21, 0\n\t"
    "v_mfma_f32_16x16x32_bf16 v[40:43], a[16:19], a[88:91], v[40:43]\n\t"
    "ds_read_b128 a[80:83], v236 offset:36864\n\t"
    "v_mfma_f32_16x16x32_bf16 v[36:39], a[8:11], a[88:91], v[36:39]\n\t"
    "ds_read_b128 a[180:183], v227 offset:46080\n\t"
    "v_mfma_f32_16x16x32_bf16 v[36:39], a[12:15], a[92:95], v[36:39]\n\t"
    "ds_read_b128 a[140:143], v227 offset:35840\n\t"
    "v_mfma_f32_16x16x32_bf16 v[32:35], a[4:7], a[92:95], v[32:35]\n\t"
    "ds_read_b128 a[84:87], v237 offset:36864\n\t"
    "v_mfma_f32_16x16x32_bf16 v[32:35], a[0:3], a[88:91], v[32:35]\n\t"
    "ds_read_b128 a[92:95], v237 offset:38912\n\t"
    "v_mfma_f32_16x16x32_bf16 v[64:67], a[0:3], a[96:99], v[64:67]\n\t"
    "ds_read_b64_tr_b16 a[38:39], v240 offset:20992\n\t"
    "v_mfma_f32_16x16x32_bf16 v[64:67], a[4:7], a[100:103], v[64:67]\n\t"
    "ds_write_b128 v252, v[132:135] offset:16384\n\t"
    "v_mfma_f32_16x16x32_bf16 v[68:71], a[12:15], a[100:103], v[68:71]\n\t"
    "ds_read_b64_tr_b16 a[42:43], v241 offset:16896\n\t"
    "v_mfma_f32_16x16x32_bf16 v[68:71], a[8:11], a[96:99], v[68:71]\n\t"
    "ds_read_b64_tr_b16 a[48:49], v242 offset:16384\n\t"
    "v_mfma_f32_16x16x32_bf16 v[72:75], a[16:19], a[96:99], v[72:75]\n\t"
    "ds_read_b64_tr_b16 a[58:59], v243 offset:16896\n\t"
    "v_mfma_f32_16x16x32_bf16 v[72:75], a[20:23], a[100:103], v[72:75]\n\t"
    "ds_read_b64_tr_b16 a[40:41], v241 offset:16384\n\t"
    "v_mfma_f32_16x16x32_bf16 v[76:79], a[28:31], a[100:103], v[76:79]\n\t"
    "ds_read_b64_tr_b16 a[44:45], v241 offset:20480\n\t"
    "v_mfma_f32_16x16x32_bf16 v[76:79], a[24:27], a[96:99], v[76:79]\n\t"
    "ds_write_b128 v252, v[136:139] offset:17408\n\t"
    "v_mfma_f32_16x16x32_bf16 v[92:95], a[24:27], a[104:107], v[92:95]\n\t"
    "ds_read_b128 a[88:91], v236 offset:38912\n\t"
    "v_mfma_f32_16x16x32_bf16 v[92:95], a[28:31], a[108:111], v[92:95]\n\t"
    "ds_read_b64_tr_b16 a[60:61], v243 offset:20480\n\t"
    "v_mfma_f32_16x16x32_bf16 v[88:91], a[20:23], a[108:111], v[88:91]\n\t"
    "ds_read_b128 a[100:103], v237 offset:40960\n\t"
    "v_mfma_f32_16x16x32_bf16 v[88:91], a[16:19], a[104:107], v[88:91]\n\t"
    "ds_read_b64_tr_b16 a[32:33], v240 offset:16384\n\t"
    "v_mfma_f32_16x16x32_bf16 v[84:87], a[8:11], a[104:107], v[84:87]\n\t"
    "ds_read_b128 a[132:135], v227 offset:33792\n\t"
    "v_mfma_f32_16x16x32_bf16 v[84:87], a[12:15], a[108:111], v[84:87]\n\t"
    "ds_read_b128 a[96:99], v236 offset:40960\n\t"
    "v_mfma_f32_16x16x32_bf16 v[80:83], a[4:7], a[108:111], v[80:83]\n\t"
    "ds_write_b128 v252, v[140:143] offset:18432\n\t"
    "v_mfma_f32_16x16x32_bf16 v[80:83], a[0:3], a[104:107], v[80:83]\n\t"
    "ds_read_b64_tr_b16 a[52:53], v242 offset:20480\n\t"
    "v_mfma_f32_16x16x32_bf16 v[96:99], a[0:3], a[112:115], v[96:99]\n\t"
    "s_sub_u32 s54, s54, 1\n\t"
    "s_cbranch_scc0 L_k0_%=\n\t"
    "s_waitcnt vmcnt(0)\n\t"
    "v_readfirstlane_b32 s90, v229\n\t"
    "s_mov_b32 s54, 1\n\t"
    "s_cmp_lt_u32 s90, 8\n\t"
    "s_cbranch_scc1 L_j0_%=\n\t"
    "buffer_inv sc1\n\t"
    "s_movk_i32 s54, 0x7fff\n\t"
    "s_branch L_k0_%=\n\t"
    "L_j0_%=:\n\t"
    "global_load_dword v229, v249, s[84:85] sc1\n\t"
    "L_k0_%=:\n\t"
    "v_mfma_f32_16x16x32_bf16 v[96:99], a[4:7], a[116:119], v[96:99]\n\t"
    "ds_read_b64_tr_b16 a[36:37], v240 offset:20480\n\t"
    "v_mfma_f32_16x16x32_bf16 v[100:103], a[12:15], a[116:119], v[100:103]\n\t"
    "ds_read_b128 a[104:107], v236 offset:43008\n\t"
    "v_mfma_f32_16x16x32_bf16 v[100:103], a[8:11], a[112:115], v[100:103]\n\t"
    "ds_read_b128 a[156:159], v227 offset:39936\n\t"
    "v_mfma_f32_16x16x32_bf16 v[104:107], a[16:19], a[112:115], v[104:107]\n\t"
    "ds_read_b64_tr_b16 a[56:57], v243 offset:16384\n\t"
    "v_mfma_f32_16x16x32_bf16 v[104:107], a[20:23], a[116:119], v[104:107]\n\t"
    "ds_write_b128 v252, v[144:147] offset:19456\n\t"
    "v_mfma_f32_16x16x32_bf16 v[108:111], a[28:31], a[116:119], v[108:111]\n\t"
    "ds_read_b128 a[160:163], v227 offset:40960\n\t"
    "v_mfma_f32_16x16x32_bf16 v[108:111], a[24:27], a[112:115], v[108:111]\n\t"
    "ds_read_b64_tr_b16 a[46:47], v241 offset:20992\n\t"
    "v_mfma_f32_16x16x32_bf16 v[124:127], a[24:27], a[120:123], v[124:127]\n\t"
    "ds_read_b128 a[108:111], v237 offset:43008\n\t"
    "v_mfma_f32_16x16x32_bf16 v[124:127], a[28:31], a[124:127], v[124:127]\n\t"
    "ds_read_b128 a[176:179], v227 offset:45056\n\t"
    "v_mfma_f32_16x16x32_bf16 v[120:123], a[20:23], a[124:127], v[120:123]\n\t"
    "ds_read_b128 a[112:115], v236 offset:45056\n\t"
    "v_mfma_f32_16x16x32_bf16 v[120:123], a[16:19], a[120:123], v[120:123]\n\t"
    "ds_read_b128 a[184:187], v227 offset:47104\n\t"
    "v_mfma_f32_16x16x32_bf16 v[116:119], a[8:11], a[120:123], v[116:119]\n\t"
    "ds_read_b128 a[116:119], v237 offset:45056\n\t"
    "v_mfma_f32_16x16x32_bf16 v[116:119], a[12:15], a[124:127], v[116:119]\n\t"
    "ds_read_b128 a[188:191], v227 offset:48128\n\t"
    "v_mfma_f32_16x16x32_bf16 v[112:115], a[4:7], a[124:127], v[112:115]\n\t"
    "ds_read_b128 a[124:127], v237 offset:47104\n\t"
    "v_mfma_f32_16x16x32_bf16 v[112:115], a[0:3], a[120:123], v[112:115]\n\t"
    "ds_read_b128 a[120:123], v236 offset:47104\n\t"
    "s_waitcnt lgkmcnt(12)\n\t"
    "v_mfma_f32_16x16x32_bf16 v[48:51], a[32:35], a[64:67], v[48:51]\n\t"
    "ds_read_b128 a[148:151], v227 offset:37888\n\t"
    "v_mfma_f32_16x16x32_bf16 v[48:51], a[36:39], a[68:71], v[48:51]\n\t"
    "ds_read_b128 a[152:155], v227 offset:38912\n\t"
    "s_waitcnt lgkmcnt(10)\n\t"
    "v_mfma_f32_16x16x32_bf16 v[52:55], a[44:47], a[68:71], v[52:55]\n\t"
    "ds_read_b128 a[128:131], v227 offset:32768\n\t"
    "v_mfma_f32_16x16x32_bf16 v[52:55], a[40:43], a[64:67], v[52:55]\n\t"
    "ds_read_b128 a[164:167], v227 offset:41984\n\t"
    "v_mfma_f32_16x16x32_bf16 v[56:59], a[48:51], a[64:67], v[56:59]\n\t"
    "s_sub_u32 s54, s54, 1\n\t"
    "s_cbranch_scc0 L_k1_%=\n\t"
    "s_waitcnt vmcnt(0)\n\t"
    "v_readfirstlane_b32 s90, v229\n\t"
    "s_mov_b32 s54, 1\n\t"
    "s_cmp_lt_u32 s90, 8\n\t"
    "s_cbranch_scc1 L_j1_%=\n\t"
    "buffer_inv sc1\n\t"
    "s_movk_i32 s54, 0x7fff\n\t"
    "s_branch L_k1_%=\n\t"
    "L_j1_%=:\n\t"
    "global_load_dword v229, v249, s[84:85] sc1\n\t"
    "L_k1_%=:\n\t"
    "v_mfma_f32_16x16x32_bf16 v[56:59], a[52:55], a[68:71], v[56:59]\n\t"
    "v_cvt_pk_bf16_f32 v48, v48, v49\n\t"
    "v_mfma_f32_16x16x32_bf16 v[60:63], a[60:63], a[68:71], v[60:63]\n\t"
    "v_cvt_pk_bf16_f32 v49, v50, v51\n\t"
    "v_cvt_pk_bf16_f32 v50, v52, v53\n\t"
    "v_mfma_f32_16x16x32_bf16 v[60:63], a[56:59], a[64:67], v[60:63]\n\t"
    "v_cvt_pk_bf16_f32 v51, v54, v55\n\t"
    "v_mfma_f32_16x16x32_bf16 v[12:15], a[56:59], a[72:75], v[12:15]\n\t"
    "ds_write_b128 v224, v[48:51] offset:32768\n\t"
    "v_mfma_f32_16x16x32_bf16 v[12:15], a[60:63], a[76:79], v[12:15]\n\t"
    "v_cvt_pk_bf16_f32 v56, v56, v57\n\t"
    "v_cvt_pk_bf16_f32 v57, v58, v59\n\t"
    "v_mfma_f32_16x16x32_bf16 v[8:11], a[52:55], a[76:79], v[8:11]\n\t"
    "s_nop 0\n\t"
    "v_cvt_pk_bf16_f32 v58, v60, v61\n\t"
    "v_mfma_f32_16x16x32_bf16 v[8:11], a[48:51], a[72:75], v[8:11]\n\t"
    "v_cvt_pk_bf16_f32 v59, v62, v63\n\t"
    "v_mfma_f32_16x16x32_bf16 v[4:7], a[40:43], a[72:75], v[4:7]\n\t"
    "ds_write_b128 v224, v[56:59] offset:33792\n\t"
    "v_mfma_f32_16x16x32_bf16 v[4:7], a[44:47], a[76:79], v[4:7]\n\t"
    "s_nop 3\n\t"
    "v_cvt_pk_bf16_f32 v8, v8, v9\n\t"
    "v_cvt_pk_bf16_f32 v9, v10, v11\n\t"
    "v_mfma_f32_16x16x32_bf16 v[0:3], a[36:39], a[76:79], v[0:3]\n\t"
    "v_mov_b32 v57, v234\n\t"
    "v_mfma_f32_16x16x32_bf16 v[0:3], a[32:35], a[72:75], v[0:3]\n\t"
    "v_cvt_pk_bf16_f32 v10, v12, v13\n\t"
    "v_cvt_pk_bf16_f32 v11, v14, v15\n\t"
    "v_mfma_f32_16x16x32_bf16 v[16:19], a[32:35], a[80:83], v[16:19]\n\t"
    "ds_write_b128 v224, v[8:11] offset:37888\n\t"
    "v_mfma_f32_16x16x32_bf16 v[16:19], a[36:39], a[84:87], v[16:19]\n\t"
    "v_mov_b32 v56, v234\n\t"
    "v_mfma_f32_16x16x32_bf16 v[20:23], a[44:47], a[84:87], v[20:23]\n\t"
    "v_mov_b32 v59, v234\n\t"
    "v_mfma_f32_16x16x32_bf16 v[20:23], a[40:43], a[80:83], v[20:23]\n\t"
    "v_cvt_pk_bf16_f32 v0, v0, v1\n\t"
    "v_mfma_f32_16x16x32_bf16 v[24:27], a[48:51], a[80:83], v[24:27]\n\t"
    "v_cvt_pk_bf16_f32 v1, v2, v3\n\t"
    "v_cvt_pk_bf16_f32 v2, v4, v5\n\t"
    "v_mfma_f32_16x16x32_bf16 v[24:27], a[52:55], a[84:87], v[24:27]\n\t"
    "v_cvt_pk_bf16_f32 v16, v16, v17\n\t"
    "v_mfma_f32_16x16x32_bf16 v[28:31], a[60:63], a[84:87], v[28:31]\n\t"
    "v_cvt_pk_bf16_f32 v3, v6, v7\n\t"
    "v_mfma_f32_16x16x32_bf16 v[28:31], a[56:59], a[80:83], v[28:31]\n\t"
    "ds_write_b128 v224, v[0:3] offset:36864\n\t"
    "v_mfma_f32_16x16x32_bf16 v[44:47], a[56:59], a[88:91], v[44:47]\n\t"
    "s_sub_u32 s54, s54, 1\n\t"
    "s_cbranch_scc0 L_k2_%=\n\t"
    "s_waitcnt vmcnt(0)\n\t"
    "v_readfirstlane_b32 s90, v229\n\t"
    "s_mov_b32 s54, 1\n\t"
    "s_cmp_lt_u32 s90, 8\n\t"
    "s_cbranch_scc1 L_j2_%=\n\t"
    "buffer_inv sc1\n\t"
    "s_movk_i32 s54, 0x7fff\n\t"
    "s_branch L_k2_%=\n\t"
    "L_j2_%=:\n\t"
    "global_load_dword v229, v249, s[84:85] sc1\n\t"
    "L_k2_%=:\n\t"
    "v_mfma_f32_16x16x32_bf16 v[44:47], a[60:63], a[92:95], v[44:47]\n\t"
    "v_cvt_pk_bf16_f32 v24, v24, v25\n\t"
    "v_mfma_f32_16x16x32_bf16 v[40:43], a[52:55], a[92:95], v[40:43]\n\t"
    "v_cvt_pk_bf16_f32 v17, v18, v19\n\t"
    "v_cvt_pk_bf16_f32 v25, v26, v27\n\t"
    "v_mfma_f32_16x16x32_bf16 v[40:43], a[48:51], a[88:91], v[40:43]\n\t"
    "v_cvt_pk_bf16_f32 v26, v28, v29\n\t"
    "v_cvt_pk_bf16_f32 v27, v30, v31\n\t"
    "v_mfma_f32_16x16x32_bf16 v[36:39], a[40:43], a[88:91], v[36:39]\n\t"
    "v_cvt_pk_bf16_f32 v18, v20, v21\n\t"
    "v_cvt_pk_bf16_f32 v19, v22, v23\n\t"
    "v_mfma_f32_16x16x32_bf16 v[36:39], a[44:47], a[92:95], v[36:39]\n\t"
    "ds_write_b128 v224, v[16:19] offset:40960\n\t"
    "v_mfma_f32_16x16x32_bf16 v[32:35], a[36:39], a[92:95], v[32:35]\n\t"
    "v_cvt_pk_bf16_f32 v40, v40, v41\n\t"
    "v_mfma_f32_16x16x32_bf16 v[32:35], a[32:35], a[88:91], v[32:35]\n\t"
    "v_cvt_pk_bf16_f32 v41, v42, v43\n\t"
    "v_mfma_f32_16x16x32_bf16 v[64:67], a[32:35], a[96:99], v[64:67]\n\t"
    "v_cvt_pk_bf16_f32 v42, v44, v45\n\t"
    "v_cvt_pk_bf16_f32 v43, v46, v47\n\t"
    "v_mfma_f32_16x16x32_bf16 v[64:67], a[36:39], a[100:103], v[64:67]\n\t"
    "s_nop 2\n\t"
    "v_cvt_pk_bf16_f32 v32, v32, v33\n\t"
    "v_cvt_pk_bf16_f32 v33, v34, v35\n\t"
    "v_mfma_f32_16x16x32_bf16 v[68:71], a[44:47], a[100:103], v[68:71]\n\t"
    "v_mov_b32 v58, v234\n\t"
    "v_mfma_f32_16x16x32_bf16 v[68:71], a[40:43], a[96:99], v[68:71]\n\t"
    "ds_write_b128 v224, v[24:27] offset:41984\n\t"
    "v_mfma_f32_16x16x32_bf16 v[72:75], a[48:51], a[96:99], v[72:75]\n\t"
    "v_cvt_pk_bf16_f32 v34, v36, v37\n\t"
    "v_cvt_pk_bf16_f32 v35, v38, v39\n\t"
    "v_mfma_f32_16x16x32_bf16 v[72:75], a[52:55], a[100:103], v[72:75]\n\t"
    "v_cvt_pk_bf16_f32 v192, v64, v65\n\t"
    "v_cvt_pk_bf16_f32 v193, v66, v67\n\t"
    "v_mfma_f32_16x16x32_bf16 v[76:79], a[60:63], a[100:103], v[76:79]\n\t"
    "ds_write_b128 v224, v[32:35] offset:45056\n\t"
    "v_mfma_f32_16x16x32_bf16 v[76:79], a[56:59], a[96:99], v[76:79]\n\t"
    "v_cvt_pk_bf16_f32 v194, v68, v69\n\t"
    "v_cvt_pk_bf16_f32 v195, v70, v71\n\t"
    "v_mfma_f32_16x16x32_bf16 v[92:95], a[56:59], a[104:107], v[92:95]\n\t"
    "ds_write_b128 v224, v[40:43] offset:46080\n\t"
    "s_waitcnt lgkmcnt(3)\n\t"
    "v_mfma_f32_16x16x32_bf16 v[92:95], a[60:63], a[108:111], v[92:95]\n\t"
    "v_cvt_pk_bf16_f32 v196, v72, v73\n\t"
    "v_cvt_pk_bf16_f32 v197, v74, v75\n\t"
    "v_mfma_f32_16x16x32_bf16 v[88:91], a[52:55], a[108:111], v[88:91]\n\t"
    "v_cvt_pk_bf16_f32 v198, v76, v77\n\t"
    "v_cvt_pk_bf16_f32 v199, v78, v79\n\t"
    "v_mfma_f32_16x16x32_bf16 v[88:91], a[48:51], a[104:107], v[88:91]\n\t"
    "s_waitcnt lgkmcnt(0)\n\t"
    "s_barrier\n\t"
    "v_mfma_f32_16x16x32_bf16 v[84:87], a[40:43], a[104:107], v[84:87]\n\t"
    "s_sub_u32 s54, s54, 1\n\t"
    "s_cbranch_scc0 L_k3_%=\n\t"
    "s_waitcnt vmcnt(0)\n\t"
    "v_readfirstlane_b32 s90, v229\n\t"
    "s_mov_b32 s54, 1\n\t"
    "s_cmp_lt_u32 s90, 8\n\t"
    "s_cbranch_scc1 L_j3_%=\n\t"
    "buffer_inv sc1\n\t"
    "s_movk_i32 s54, 0x7fff\n\t"
    "s_branch L_k3_%=\n\t"
    "L_j3_%=:\n\t"
    "global_load_dword v229, v249, s[84:85] sc1\n\t"
    "L_k3_%=:\n\t"
    "v_mfma_f32_16x16x32_bf16 v[84:87], a[44:47], a[108:111], v[84:87]\n\t"
    "ds_read_b128 a[192:195], v225 offset:49152\n\t"
    "ds_read_b128 a[240:243], v232 offset:49152\n\t"
    "v_mfma_f32_16x16x32_bf16 v[80:83], a[36:39], a[108:111], v[80:83]\n\t"
    "ds_read_b128 a[92:95], v228 offset:11264\n\t"
    "v_cvt_pk_bf16_f32 v204, v88, v89\n\t"
    "v_mfma_f32_16x16x32_bf16 v[80:83], a[32:35], a[104:107], v[80:83]\n\t"
    "ds_read_b128 a[200:203], v225 offset:51200\n\t"
    "ds_read_b128 a[100:103], v228 offset:12288\n\t"
    "v_mfma_f32_16x16x32_bf16 v[96:99], a[32:35], a[112:115], v[96:99]\n\t"
    "ds_read_b128 a[204:207], v225 offset:52224\n\t"
    "ds_read_b128 a[96:99], v228 offset:4096\n\t"
    "v_cvt_pk_bf16_f32 v205, v90, v91\n\t"
    "v_mfma_f32_16x16x32_bf16 v[96:99], a[36:39], a[116:119], v[96:99]\n\t"
    "ds_read_b128 a[236:239], v232 offset:35840\n\t"
    "ds_read_b128 a[88:91], v228 offset:3072\n\t"
    "v_mfma_f32_16x16x32_bf16 v[100:103], a[44:47], a[116:119], v[100:103]\n\t"
    "ds_write_b128 v224, v[192:195] offset:0\n\t"
    "v_mfma_f32_16x16x32_bf16 v[100:103], a[40:43], a[112:115], v[100:103]\n\t"
    "ds_read_b128 a[196:199], v225 offset:50176\n\t"
    "v_cvt_pk_bf16_f32 v200, v80, v81\n\t"
    "v_mfma_f32_16x16x32_bf16 v[104:107], a[48:51], a[112:115], v[104:107]\n\t"
    "ds_write_b128 v224, v[196:199] offset:1024\n\t"
    "v_mfma_f32_16x16x32_bf16 v[104:107], a[52:55], a[116:119], v[104:107]\n\t"
    "v_cvt_pk_bf16_f32 v208, v96, v97\n\t"
    "v_cvt_pk_bf16_f32 v201, v82, v83\n\t"
    "v_cvt_pk_bf16_f32 v206, v92, v93\n\t"
    "v_mfma_f32_16x16x32_bf16 v[108:111], a[60:63], a[116:119], v[108:111]\n\t"
    "ds_read_b128 a[216:219], v225 offset:34816\n\t"
    "ds_read_b128 a[108:111], v228 offset:13312\n\t"
    "v_mfma_f32_16x16x32_bf16 v[108:111], a[56:59], a[112:115], v[108:111]\n\t"
    "v_cvt_pk_bf16_f32 v209, v98, v99\n\t"
    "v_cvt_pk_bf16_f32 v202, v84, v85\n\t"
    "v_cvt_pk_bf16_f32 v203, v86, v87\n\t"
    "v_mfma_f32_16x16x32_bf16 v[124:127], a[56:59], a[120:123], v[124:127]\n\t"
    "ds_read_b128 a[212:215], v225 offset:33792\n\t"
    "v_cvt_pk_bf16_f32 v210, v100, v101\n\t"
    "v_mfma_f32_16x16x32_bf16 v[124:127], a[60:63], a[124:127], v[124:127]\n\t"
    "ds_read_b128 a[104:107], v228 offset:5120\n\t"
    "v_cvt_pk_bf16_f32 v212, v104, v105\n\t"
    "v_cvt_pk_bf16_f32 v211, v102, v103\n\t"
    "v_mfma_f32_16x16x32_bf16 v[120:123], a[52:55], a[124:127], v[120:123]\n\t"
    "ds_read_b128 a[208:211], v225 offset:32768\n\t"
    "v_and_b32 v235, 2, v246\n\t"
    "v_mov_b32 v71, 0x3f803f80\n\t"
    "v_mfma_f32_16x16x32_bf16 v[120:123], a[48:51], a[120:123], v[120:123]\n\t"
    "ds_read_b128 a[64:67], v228 offset:0\n\t"
    "ds_read_b128 a[76:79], v228 offset:9216\n\t"
    "v_cvt_pk_bf16_f32 v207, v94, v95\n\t"
    "v_mfma_f32_16x16x32_bf16 v[116:119], a[40:43], a[120:123], v[116:119]\n\t"
    "ds_read_b128 a[220:223], v225 offset:35840\n\t"
    "ds_read_b128 a[84:87], v228 offset:10240\n\t"
    "v_mfma_f32_16x16x32_bf16 v[116:119], a[44:47], a[124:127], v[116:119]\n\t"
    "s_nop 0\n\t"
    "v_cvt_pk_bf16_f32 v220, v120, v121\n\t"
    "v_cvt_pk_bf16_f32 v213, v106, v107\n\t"
    "v_mfma_f32_16x16x32_bf16 v[112:115], a[36:39], a[124:127], v[112:115]\n\t"
    "ds_read_b128 a[72:75], v228 offset:1024\n\t"
    "v_cvt_pk_bf16_f32 v214, v108, v109\n\t"
    "v_mfma_f32_16x16x32_bf16 v[112:115], a[32:35], a[120:123], v[112:115]\n\t"
    "ds_write_b128 v224, v[204:207] offset:5120\n\t"
    "s_waitcnt lgkmcnt(8)\n\t"
    "v_mfma_f32_16x16x32_bf16 v[0:3], a[128:131], a[192:195], 0\n\t"
    "s_sub_u32 s54, s54, 1\n\t"
    "s_cbranch_scc0 L_k4_%=\n\t"
    "s_waitcnt vmcnt(0)\n\t"
    "v_readfirstlane_b32 s90, v229\n\t"
    "s_mov_b32 s54, 1\n\t"
    "s_cmp_lt_u32 s90, 8\n\t"
    "s_cbranch_scc1 L_j4_%=\n\t"
    "buffer_inv sc1\n\t"
    "s_movk_i32 s54, 0x7fff\n\t"
    "s_branch L_k4_%=\n\t"
    "L_j4_%=:\n\t"
    "global_load_dword v229, v249, s[84:85] sc1\n\t"
    "L_k4_%=:\n\t"
    "v_mfma_f32_16x16x32_bf16 v[0:3], a[132:135], a[196:199], v[0:3]\n\t"
    "ds_read_b128 a[112:115], v228 offset:6144\n\t"
    "v_cvt_pk_bf16_f32 v221, v122, v123\n\t"
    "v_cvt_pk_bf16_f32 v222, v124, v125\n\t"
    "v_mfma_f32_16x16x32_bf16 v[0:3], a[136:139], a[200:203], v[0:3]\n\t"
    "ds_read_b128 a[80:83], v228 offset:2048\n\t"
    "v_cvt_pk_bf16_f32 v216, v112, v113\n\t"
    "v_cvt_pk_bf16_f32 v215, v110, v111\n\t"
    "v_mfma_f32_16x16x32_bf16 v[0:3], a[140:143], a[204:207], v[0:3]\n\t"
    "ds_write_b128 v224, v[212:215] offset:9216\n\t"
    "v_mfma_f32_16x16x32_bf16 v[4:7], a[156:159], a[204:207], 0\n\t"
    "ds_write_b128 v224, v[200:203] offset:4096\n\t"
    "v_mfma_f32_16x16x32_bf16 v[4:7], a[152:155], a[200:203], v[4:7]\n\t"
    "v_cvt_pk_bf16_f32 v223, v126, v127\n\t"
    "v_bfe_i32 v80, v246, 0, 1\n\t"
    "v_cmp_eq_u32 vcc, 0, v235\n\t"
    "v_mfma_f32_16x16x32_bf16 v[4:7], a[148:151], a[196:199], v[4:7]\n\t"
    "ds_read_b128 a[68:71], v228 offset:8192\n\t"
    "v_mov_b32 v96, 0\n\t"
    "v_mov_b32 v97, 0\n\t"
    "v_mfma_f32_16x16x32_bf16 v[4:7], a[144:147], a[192:195], v[4:7]\n\t"
    "v_cndmask_b32 v68, 0, v71, vcc\n\t"
    "v_cndmask_b32 v69, 0, v71, vcc\n\t"
    "v_mfma_f32_16x16x32_bf16 v[8:11], a[160:163], a[192:195], 0\n\t"
    "v_exp_f32 v2, v2\n\t"
    "v_mfma_f32_16x16x32_bf16 v[8:11], a[164:167], a[196:199], v[8:11]\n\t"
    "ds_write_b128 v224, v[208:211] offset:8192\n\t"
    "v_mfma_f32_16x16x32_bf16 v[8:11], a[168:171], a[200:203], v[8:11]\n\t"
    "v_cvt_pk_bf16_f32 v217, v114, v115\n\t"
    "v_exp_f32 v3, v3\n\t"
    "v_mfma_f32_16x16x32_bf16 v[8:11], a[172:175], a[204:207], v[8:11]\n\t"
    "v_cvt_pk_bf16_f32 v218, v116, v117\n\t"
    "v_exp_f32 v0, v0\n\t"
    "v_mfma_f32_16x16x32_bf16 v[12:15], a[188:191], a[204:207], 0\n\t"
    "ds_write_b128 v224, v[220:223] offset:13312\n\t"
    "v_mfma_f32_16x16x32_bf16 v[12:15], a[184:187], a[200:203], v[12:15]\n\t"
    "v_and_b32 v80, 0xaaaaaaaa, v80\n\t"
    "v_exp_f32 v5, v5\n\t"
    "v_mfma_f32_16x16x32_bf16 v[12:15], a[180:183], a[196:199], v[12:15]\n\t"
    "v_exp_f32 v7, v7\n\t"
    "v_mfma_f32_16x16x32_bf16 v[12:15], a[176:179], a[192:195], v[12:15]\n\t"
    "v_cvt_pk_bf16_f32 v219, v118, v119\n\t"
    "v_exp_f32 v1, v1\n\t"
    "s_waitcnt lgkmcnt(2)\n\t"
    "v_mfma_f32_16x16x32_bf16 v[16:19], a[128:131], a[208:211], 0\n\t"
    "ds_write_b128 v224, v[216:219] offset:12288\n\t"
    "v_mfma_f32_16x16x32_bf16 v[16:19], a[132:135], a[212:215], v[16:19]\n\t"
    "v_cvt_pk_bf16_f32 v32, v0, v1\n\t"
    "v_exp_f32 v10, v10\n\t"
    "v_mfma_f32_16x16x32_bf16 v[16:19], a[136:139], a[216:219], v[16:19]\n\t"
    "ds_read_b128 a[232:235], v232 offset:34816\n\t"
    "v_exp_f32 v9, v9\n\t"
    "v_mfma_f32_16x16x32_bf16 v[16:19], a[140:143], a[220:223], v[16:19]\n\t"
    "v_cndmask_b32 v70, 0, v71, vcc\n\t"
    "v_cndmask_b32 v71, 0, v71, vcc\n\t"
    "v_mfma_f32_16x16x32_bf16 v[20:23], a[156:159], a[220:223], 0\n\t"
    "s_sub_u32 s54, s54, 1\n\t"
    "s_cbranch_scc0 L_k5_%=\n\t"
    "s_waitcnt vmcnt(0)\n\t"
    "v_readfirstlane_b32 s90, v229\n\t"
    "s_mov_b32 s54, 1\n\t"
    "s_cmp_lt_u32 s90, 8\n\t"
    "s_cbranch_scc1 L_j5_%=\n\t"
    "buffer_inv sc1\n\t"
    "s_movk_i32 s54, 0x7fff\n\t"
    "s_branch L_k5_%=\n\t"
    "L_j5_%=:\n\t"
    "global_load_dword v229, v249, s[84:85] sc1\n\t"
    "L_k5_%=:\n\t"
    "v_mfma_f32_16x16x32_bf16 v[20:23], a[152:155], a[216:219], v[20:23]\n\t"
    "v_exp_f32 v4, v4\n\t"
    "v_mfma_f32_16x16x32_bf16 v[20:23], a[148:151], a[212:215], v[20:23]\n\t"
    "v_cvt_pk_bf16_f32 v34, v4, v5\n\t"
    "v_exp_f32 v11, v11\n\t"
    "v_mfma_f32_16x16x32_bf16 v[20:23], a[144:147], a[208:211], v[20:23]\n\t"
    "v_cvt_pk_bf16_f32 v33, v2, v3\n\t"
    "v_exp_f32 v8, v8\n\t"
    "v_mfma_f32_16x16x32_bf16 v[24:27], a[160:163], a[208:211], 0\n\t"
    "v_or_b32 v80, 0x44444444, v80\n\t"
    "s_waitcnt lgkmcnt(0)\n\t"
    "s_barrier\n\t"
    "v_mfma_f32_16x16x32_bf16 v[24:27], a[164:167], a[212:215], v[24:27]\n\t"
    "v_cvt_pk_bf16_f32 v36, v8, v9\n\t"
    "v_exp_f32 v15, v15\n\t"
    "v_mfma_f32_16x16x32_bf16 v[24:27], a[168:171], a[216:219], v[24:27]\n\t"
    "v_exp_f32 v12, v12\n\t"
    "v_mfma_f32_16x16x32_bf16 v[24:27], a[172:175], a[220:223], v[24:27]\n\t"
    "ds_read_b128 a[120:123], v228 offset:7168\n\t"
    "v_exp_f32 v6, v6\n\t"
    "v_mfma_f32_16x16x32_bf16 v[28:31], a[188:191], a[220:223], 0\n\t"
    "v_cvt_pk_bf16_f32 v35, v6, v7\n\t"
    "v_exp_f32 v14, v14\n\t"
    "v_mfma_f32_16x16x32_bf16 v[28:31], a[184:187], a[216:219], v[28:31]\n\t"
    "v_exp_f32 v13, v13\n\t"
    "v_mfma_f32_16x16x32_bf16 v[28:31], a[180:183], a[212:215], v[28:31]\n\t"
    "v_cvt_pk_bf16_f32 v37, v10, v11\n\t"
    "v_cvt_pk_bf16_f32 v38, v12, v13\n\t"
    "v_cvt_pk_bf16_f32 v39, v14, v15\n\t"
    "v_mfma_f32_16x16x32_bf16 v[28:31], a[176:179], a[208:211], v[28:31]\n\t"
    "ds_read_b128 a[228:231], v232 offset:33792\n\t"
    "v_exp_f32 v16, v16\n\t"
    "v_smfmac_f32_16x16x64_bf16 v[96:99], v[68:71], v[32:39], v80\n\t"
    "v_exp_f32 v23, v23\n\t"
    "v_mfma_f32_16x16x32_bf16 a[0:3], a[64:67], v[32:35], 0\n\t"
    "ds_read_b128 a[124:127], v228 offset:15360\n\t"
    "v_exp_f32 v24, v24\n\t"
    "v_mfma_f32_16x16x32_bf16 a[0:3], a[68:71], v[36:39], a[0:3]\n\t"
    "ds_read_b128 a[116:119], v228 offset:14336\n\t"
    "ds_read_b128 v[212:215], v232 offset:17408\n\t"
    "v_mfma_f32_16x16x32_bf16 a[4:7], a[72:75], v[32:35], 0\n\t"
    "ds_read_b128 v[192:195], v232 offset:0\n\t"
    "v_exp_f32 v22, v22\n\t"
    "v_mfma_f32_16x16x32_bf16 a[4:7], a[76:79], v[36:39], a[4:7]\n\t"
    "ds_read_b128 v[196:199], v232 offset:1024\n\t"
    "v_mov_b32 v100, 0\n\t"
    "v_mov_b32 v101, 0\n\t"
    "v_mfma_f32_16x16x32_bf16 a[8:11], a[80:83], v[32:35], 0\n\t"
    "ds_read_b128 v[204:207], v232 offset:3072\n\t"
    "v_exp_f32 v29, v29\n\t"
    "v_mfma_f32_16x16x32_bf16 a[8:11], a[84:87], v[36:39], a[8:11]\n\t"
    "v_exp_f32 v28, v28\n\t"
    "v_mfma_f32_16x16x32_bf16 a[12:15], a[88:91], v[32:35], 0\n\t"
    "ds_read_b128 v[200:203], v232 offset:2048\n\t"
    "v_exp_f32 v18, v18\n\t"
    "v_mfma_f32_16x16x32_bf16 a[12:15], a[92:95], v[36:39], a[12:15]\n\t"
    "s_sub_u32 s54, s54, 1\n\t"
    "s_cbranch_scc0 L_k6_%=\n\t"
    "s_waitcnt vmcnt(0)\n\t"
    "v_readfirstlane_b32 s90, v229\n\t"
    "s_mov_b32 s54, 1\n\t"
    "s_cmp_lt_u32 s90, 8\n\t"
    "s_cbranch_scc1 L_j6_%=\n\t"
    "buffer_inv sc1\n\t"
    "s_movk_i32 s54, 0x7fff\n\t"
    "s_branch L_k6_%=\n\t"
    "L_j6_%=:\n\t"
    "global_load_dword v229, v249, s[84:85] sc1\n\t"
    "L_k6_%=:\n\t"
    "v_mfma_f32_16x16x32_bf16 a[16:19], a[96:99], v[32:35], 0\n\t"
    "v_exp_f32 v19, v19\n\t"
    "v_mfma_f32_16x16x32_bf16 a[16:19], a[100:103], v[36:39], a[16:19]\n\t"
    "v_exp_f32 v17, v17\n\t"
    "v_mfma_f32_16x16x32_bf16 a[20:23], a[104:107], v[32:35], 0\n\t"
    "v_exp_f32 v21, v21\n\t"
    "v_mfma_f32_16x16x32_bf16 a[20:23], a[108:111], v[36:39], a[20:23]\n\t"
    "v_exp_f32 v20, v20\n\t"
    "v_mfma_f32_16x16x32_bf16 a[24:27], a[112:115], v[32:35], 0\n\t"
    "v_exp_f32 v26, v26\n\t"
    "s_waitcnt lgkmcnt(1)\n\t"
    "v_mfma_f32_16x16x32_bf16 a[24:27], a[116:119], v[36:39], a[24:27]\n\t"
    "v_cvt_pk_bf16_f32 v40, v16, v17\n\t"
    "v_cvt_pk_bf16_f32 v43, v22, v23\n\t"
    "v_mfma_f32_16x16x32_bf16 a[28:31], a[120:123], v[32:35], 0\n\t"
    "v_cvt_pk_bf16_f32 v41, v18, v19\n\t"
    "v_exp_f32 v31, v31\n\t"
    "v_mfma_f32_16x16x32_bf16 a[28:31], a[124:127], v[36:39], a[28:31]\n\t"
    "v_mov_b32 v121, v244\n\t"
    "v_exp_f32 v30, v30\n\t"
    "v_mfma_f32_16x16x32_bf16 v[0:3], a[128:131], v[192:195], v[56:59]\n\t"
    "v_add_u32 v123, 0x1000, v244\n\t"
    "v_exp_f32 v25, v25\n\t"
    "v_mfma_f32_16x16x32_bf16 v[0:3], a[132:135], v[196:199], v[0:3]\n\t"
    "v_exp_f32 v27, v27\n\t"
    "s_waitcnt lgkmcnt(0)\n\t"
    "v_mfma_f32_16x16x32_bf16 v[0:3], a[136:139], v[200:203], v[0:3]\n\t"
    "v_cvt_pk_bf16_f32 v46, v28, v29\n\t"
    "v_mov_b32 v76, v244\n\t"
    "v_mov_b32 v78, v227\n\t"
    "v_mfma_f32_16x16x32_bf16 v[0:3], a[140:143], v[204:207], v[0:3]\n\t"
    "v_cvt_pk_bf16_f32 v45, v26, v27\n\t"
    "v_add_u32 v81, s13, v227\n\t"
    "v_add_u32 v82, s14, v227\n\t"
    "v_mfma_f32_16x16x32_bf16 v[4:7], a[156:159], v[204:207], v[56:59]\n\t"
    "v_cvt_pk_bf16_f32 v44, v24, v25\n\t"
    "v_cmp_lt_i32 vcc, 0, v231\n\t"
    "v_mfma_f32_16x16x32_bf16 v[4:7], a[152:155], v[200:203], v[4:7]\n\t"
    "ds_read_b128 v[208:211], v232 offset:16384\n\t"
    "v_cndmask_b32 v48, 0, v226, vcc\n\t"
    "v_mfma_f32_16x16x32_bf16 v[4:7], a[148:151], v[196:199], v[4:7]\n\t"
    "v_cvt_pk_bf16_f32 v42, v20, v21\n\t"
    "v_cvt_pk_bf16_f32 v47, v30, v31\n\t"
    "v_mfma_f32_16x16x32_bf16 v[4:7], a[144:147], v[192:195], v[4:7]\n\t"
    "ds_read_b128 a[248:251], v232 offset:51200\n\t"
    "ds_read_b128 v[220:223], v232 offset:19456\n\t"
    "v_cmp_lt_i32 vcc, -1, v231\n\t"
    "v_smfmac_f32_16x16x64_bf16 v[100:103], v[68:71], v[40:47], v80\n\t"
    "v_mov_b32 v64, 0\n\t"
    "v_mov_b32 v65, 0\n\t"
    "v_mov_b32 v66, 0\n\t"
    "v_mfma_f32_16x16x32_bf16 a[32:35], a[64:67], v[40:43], 0\n\t"
    "v_mov_b32 v67, 0\n\t"
    "v_cndmask_b32 v49, 0, v226, vcc\n\t"
    "v_mfma_f32_16x16x32_bf16 a[32:35], a[68:71], v[44:47], a[32:35]\n\t"
    "ds_read_b128 a[224:227], v232 offset:32768\n\t"
    "v_exp_f32 v1, v1\n\t"
    "v_mfma_f32_16x16x32_bf16 a[36:39], a[72:75], v[40:43], 0\n\t"
    "s_sub_u32 s54, s54, 1\n\t"
    "s_cbranch_scc0 L_k7_%=\n\t"
    "s_waitcnt vmcnt(0)\n\t"
    "v_readfirstlane_b32 s90, v229\n\t"
    "s_mov_b32 s54, 1\n\t"
    "s_cmp_lt_u32 s90, 8\n\t"
    "s_cbranch_scc1 L_j7_%=\n\t"
    "buffer_inv sc1\n\t"
    "s_movk_i32 s54, 0x7fff\n\t"
    "s_branch L_k7_%=\n\t"
    "L_j7_%=:\n\t"
    "global_load_dword v229, v249, s[84:85] sc1\n\t"
    "L_k7_%=:\n\t"
    "v_mfma_f32_16x16x32_bf16 a[36:39], a[76:79], v[44:47], a[36:39]\n\t"
    "v_mov_b32 v92, 0\n\t"
    "v_mov_b32 v93, 0\n\t"
    "v_mfma_f32_16x16x32_bf16 a[40:43], a[80:83], v[40:43], 0\n\t"
    "ds_read_b128 v[216:219], v232 offset:18432\n\t"
    "v_exp_f32 v4, v4\n\t"
    "v_mfma_f32_16x16x32_bf16 a[40:43], a[84:87], v[44:47], a[40:43]\n\t"
    "v_mov_b32 v94, 0\n\t"
    "v_mov_b32 v95, 0\n\t"
    "v_mfma_f32_16x16x32_bf16 a[44:47], a[88:91], v[40:43], 0\n\t"
    "v_mov_b32 v116, 0\n\t"
    "v_exp_f32 v7, v7\n\t"
    "v_mfma_f32_16x16x32_bf16 a[44:47], a[92:95], v[44:47], a[44:47]\n\t"
    "v_exp_f32 v3, v3\n\t"
    "v_mfma_f32_16x16x32_bf16 a[48:51], a[96:99], v[40:43], 0\n\t"
    "v_exp_f32 v2, v2\n\t"
    "v_mfma_f32_16x16x32_bf16 a[48:51], a[100:103], v[44:47], a[48:51]\n\t"
    "v_mov_b32 v108, 0\n\t"
    "v_mov_b32 v109, 0\n\t"
    "v_mfma_f32_16x16x32_bf16 a[52:55], a[104:107], v[40:43], 0\n\t"
    "v_mov_b32 v117, 0\n\t"
    "v_mov_b32 v118, 0\n\t"
    "v_cmp_lt_i32 vcc, -2, v231\n\t"
    "v_mfma_f32_16x16x32_bf16 a[52:55], a[108:111], v[44:47], a[52:55]\n\t"
    "v_mov_b32 v119, 0\n\t"
    "v_cndmask_b32 v50, 0, v226, vcc\n\t"
    "v_mfma_f32_16x16x32_bf16 a[56:59], a[112:115], v[40:43], 0\n\t"
    "v_cvt_pk_bf16_f32 v33, v2, v3\n\t"
    "s_lshr_b32 s25, %2, 3\n\t"
    "v_mfma_f32_16x16x32_bf16 a[56:59], a[116:119], v[44:47], a[56:59]\n\t"
    "v_exp_f32 v6, v6\n\t"
    "v_mfma_f32_16x16x32_bf16 a[60:63], a[120:123], v[40:43], 0\n\t"
    "v_exp_f32 v5, v5\n\t"
    "v_mfma_f32_16x16x32_bf16 a[60:63], a[124:127], v[44:47], a[60:63]\n\t"
    "ds_read_b128 a[252:255], v232 offset:52224\n\t"
    "v_cmp_lt_i32 vcc, -3, v231\n\t"
    "s_waitcnt lgkmcnt(1)\n\t"
    "v_mfma_f32_16x16x32_bf16 v[16:19], a[128:131], v[208:211], 0\n\t"
    "Lis8_%=:\n\t"
    "s_cbranch_execz Lis12_%=\n\t"
    "v_exp_f32 v0, v0\n\t"
    "v_mfma_f32_16x16x32_bf16 v[16:19], a[132:135], v[212:215], v[16:19]\n\t"
    "v_cvt_pk_bf16_f32 v32, v0, v1\n\t"
    "s_lshl_b32 s25, s25, 2\n\t"
    "v_mfma_f32_16x16x32_bf16 v[16:19], a[136:139], v[216:219], v[16:19]\n\t"
    "s_add_u32 s25, s25, s58\n\t"
    "s_and_b32 s26, s25, 0x60\n\t"
    "s_add_u32 s25, s25, s26\n\t"
    "v_mfma_f32_16x16x32_bf16 v[16:19], a[140:143], v[220:223], v[16:19]\n\t"
    "ds_read_b128 a[132:135], v230 offset:33792\n\t"
    "ds_read_b128 a[244:247], v232 offset:50176\n\t"
    "v_mfma_f32_16x16x32_bf16 v[20:23], a[156:159], v[220:223], 0\n\t"
    "v_cvt_pk_bf16_f32 v34, v4, v5\n\t"
    "v_cvt_pk_bf16_f32 v35, v6, v7\n\t"
    "v_mfma_f32_16x16x32_bf16 v[20:23], a[152:155], v[216:219], v[20:23]\n\t"
    "ds_read_b128 a[128:131], v230 offset:32768\n\t"
    "v_cndmask_b32 v51, 0, v226, vcc\n\t"
    "v_mfma_f32_16x16x32_bf16 v[20:23], a[148:151], v[212:215], v[20:23]\n\t"
    "s_sub_u32 s54, s54, 1\n\t"
    "s_cbranch_scc0 L_k8_%=\n\t"
    "s_waitcnt vmcnt(0)\n\t"
    "v_readfirstlane_b32 s90, v229\n\t"
    "s_mov_b32 s54, 1\n\t"
    "s_cmp_lt_u32 s90, 8\n\t"
    "s_cbranch_scc1 L_j8_%=\n\t"
    "buffer_inv sc1\n\t"
    "s_movk_i32 s54, 0x7fff\n\t"
    "s_branch L_k8_%=\n\t"
    "L_j8_%=:\n\t"
    "global_load_dword v229, v249, s[84:85] sc1\n\t"
    "L_k8_%=:\n\t"
    "v_mfma_f32_16x16x32_bf16 v[20:23], a[144:147], v[208:211], v[20:23]\n\t"
    "ds_read_b128 a[152:155], v230 offset:38912\n\t"
    "ds_read_b128 a[156:159], v230 offset:39936\n\t"
    "v_mfma_f32_16x16x32_bf16 v[24:27], a[160:163], v[208:211], v[56:59]\n\t"
    "ds_read_b128 a[148:151], v230 offset:37888\n\t"
    "s_and_b32 s26, %2, 1\n\t"
    "v_mfma_f32_16x16x32_bf16 v[24:27], a[164:167], v[212:215], v[24:27]\n\t"
    "v_exp_f32 v16, v16\n\t"
    "v_mfma_f32_16x16x32_bf16 v[24:27], a[168:171], v[216:219], v[24:27]\n\t"
    "v_cmp_lt_i32 vcc, -16, v231\n\t"
    "v_exp_f32 v18, v18\n\t"
    "v_mfma_f32_16x16x32_bf16 v[24:27], a[172:175], v[220:223], v[24:27]\n\t"
    "ds_read_b128 a[136:139], v230 offset:34816\n\t"
    "v_exp_f32 v19, v19\n\t"
    "v_mfma_f32_16x16x32_bf16 v[28:31], a[188:191], v[220:223], v[56:59]\n\t"
    "v_exp_f32 v17, v17\n\t"
    "v_mfma_f32_16x16x32_bf16 v[28:31], a[184:187], v[216:219], v[28:31]\n\t"
    "v_cvt_pk_bf16_f32 v40, v16, v17\n\t"
    "v_exp_f32 v22, v22\n\t"
    "v_mfma_f32_16x16x32_bf16 v[28:31], a[180:183], v[212:215], v[28:31]\n\t"
    "ds_read_b128 a[140:143], v230 offset:35840\n\t"
    "ds_read_b128 a[160:163], v230 offset:40960\n\t"
    "ds_read_b128 a[168:171], v230 offset:43008\n\t"
    "v_mfma_f32_16x16x32_bf16 v[28:31], a[176:179], v[208:211], v[28:31]\n\t"
    "s_lshl_b32 s26, s26, 5\n\t"
    "v_cndmask_b32 v52, 0, v226, vcc\n\t"
    "v_mfma_f32_16x16x32_bf16 v[104:107], v[68:71], v[32:35], 0\n\t"
    "v_exp_f32 v20, v20\n\t"
    "v_mfma_f32_16x16x32_bf16 v[128:131], a[64:67], v[32:35], 0\n\t"
    "ds_read_b128 a[164:167], v230 offset:41984\n\t"
    "ds_read_b128 a[184:187], v230 offset:47104\n\t"
    "v_cmp_lt_i32 vcc, -17, v231\n\t"
    "v_mfma_f32_16x16x32_bf16 v[132:135], a[72:75], v[32:35], 0\n\t"
    "ds_read_b128 a[144:147], v230 offset:36864\n\t"
    "s_add_u32 s25, s25, s26\n\t"
    "s_lshl_b32 s25, s25, 14\n\t"
    "v_mfma_f32_16x16x32_bf16 v[136:139], a[80:83], v[32:35], 0\n\t"
    "s_add_u32 s28, s82, s25\n\t"
    "s_addc_u32 s29, s83, 0\n\t"
    "v_mfma_f32_16x16x32_bf16 v[140:143], a[88:91], v[32:35], 0\n\t"
    "ds_read_b128 a[188:191], v230 offset:48128\n\t"
    "s_movk_i32 s30, 0x4000\n\t"
    "v_mfma_f32_16x16x32_bf16 v[144:147], a[96:99], v[32:35], 0\n\t"
    "ds_read_b128 a[172:175], v230 offset:44032\n\t"
    "v_exp_f32 v24, v24\n\t"
    "v_mfma_f32_16x16x32_bf16 v[148:151], a[104:107], v[32:35], 0\n\t"
    "s_mov_b32 s31, 0x00020000\n\t"
    "v_cndmask_b32 v53, 0, v226, vcc\n\t"
    "v_mfma_f32_16x16x32_bf16 v[152:155], a[112:115], v[32:35], 0\n\t"
    "v_cmp_lt_i32 vcc, -18, v231\n\t"
    "v_exp_f32 v21, v21\n\t"
    "v_mfma_f32_16x16x32_bf16 v[156:159], a[120:123], v[32:35], 0\n\t"
    "v_lshlrev_b32 v125, 3, v244\n\t"
    "v_exp_f32 v29, v29\n\t"
    "s_waitcnt lgkmcnt(4)\n\t"
    "v_mfma_f32_16x16x32_bf16 v[0:3], a[128:131], a[192:195], 0\n\t"
    "v_exp_f32 v30, v30\n\t"
    "v_mfma_f32_16x16x32_bf16 v[0:3], a[132:135], a[196:199], v[0:3]\n\t"
    "s_sub_u32 s54, s54, 1\n\t"
    "s_cbranch_scc0 L_k9_%=\n\t"
    "s_waitcnt vmcnt(0)\n\t"
    "v_readfirstlane_b32 s90, v229\n\t"
    "s_mov_b32 s54, 1\n\t"
    "s_cmp_lt_u32 s90, 8\n\t"
    "s_cbranch_scc1 L_j9_%=\n\t"
    "buffer_inv sc1\n\t"
    "s_movk_i32 s54, 0x7fff\n\t"
    "s_branch L_k9_%=\n\t"
    "L_j9_%=:\n\t"
    "global_load_dword v229, v249, s[84:85] sc1\n\t"
    "L_k9_%=:\n\t"
    "v_mfma_f32_16x16x32_bf16 v[0:3], a[136:139], a[200:203], v[0:3]\n\t"
    "v_exp_f32 v23, v23\n\t"
    "v_mfma_f32_16x16x32_bf16 v[0:3], a[140:143], a[204:207], v[0:3]\n\t"
    "v_exp_f32 v27, v27\n\t"
    "v_mfma_f32_16x16x32_bf16 v[4:7], a[156:159], a[204:207], 0\n\t"
    "ds_read_b128 a[176:179], v230 offset:45056\n\t"
    "ds_read_b128 a[180:183], v230 offset:46080\n\t"
    "v_cvt_pk_bf16_f32 v42, v20, v21\n\t"
    "v_mfma_f32_16x16x32_bf16 v[4:7], a[152:155], a[200:203], v[4:7]\n\t"
    "v_exp_f32 v31, v31\n\t"
    "v_mfma_f32_16x16x32_bf16 v[4:7], a[148:151], a[196:199], v[4:7]\n\t"
    "v_exp_f32 v26, v26\n\t"
    "s_waitcnt lgkmcnt(2)\n\t"
    "v_mfma_f32_16x16x32_bf16 v[4:7], a[144:147], a[192:195], v[4:7]\n\t"
    "v_subrev_u32 v125, 0x18000, v125\n\t"
    "v_cndmask_b32 v54, 0, v226, vcc\n\t"
    "s_lshl_b32 s25, s60, 7\n\t"
    "v_mfma_f32_16x16x32_bf16 v[8:11], a[160:163], a[192:195], 0\n\t"
    "s_add_u32 s25, s25, 0xa000\n\t"
    "v_exp_f32 v28, v28\n\t"
    "v_mfma_f32_16x16x32_bf16 v[8:11], a[164:167], a[196:199], v[8:11]\n\t"
    "v_cvt_pk_bf16_f32 v47, v30, v31\n\t"
    "v_mov_b32 v127, s25\n\t"
    "v_mfma_f32_16x16x32_bf16 v[8:11], a[168:171], a[200:203], v[8:11]\n\t"
    "v_cvt_pk_bf16_f32 v43, v22, v23\n\t"
    "v_cvt_pk_bf16_f32 v46, v28, v29\n\t"
    "v_mfma_f32_16x16x32_bf16 v[8:11], a[172:175], a[204:207], v[8:11]\n\t"
    "v_cvt_pk_bf16_f32 v41, v18, v19\n\t"
    "v_exp_f32 v2, v2\n\t"
    "v_mfma_f32_16x16x32_bf16 v[12:15], a[188:191], a[204:207], 0\n\t"
    "v_exp_f32 v25, v25\n\t"
    "v_mfma_f32_16x16x32_bf16 v[12:15], a[184:187], a[200:203], v[12:15]\n\t"
    "s_cmp_eq_u32 s58, 2\n\t"
    "s_cbranch_scc1 L_pK_%=\n\t"
    "L_rK_%=:\n\t"
    "s_waitcnt lgkmcnt(0)\n\t"
    "s_barrier\n\t"
    "v_mfma_f32_16x16x32_bf16 v[12:15], a[180:183], a[196:199], v[12:15]\n\t"
    "v_cvt_pk_bf16_f32 v44, v24, v25\n\t"
    "v_cvt_pk_bf16_f32 v45, v26, v27\n\t"
    "s_add_u32 m0, s11, s13\n\t"
    "v_mfma_f32_16x16x32_bf16 v[12:15], a[176:179], a[192:195], v[12:15]\n\t"
    "v_exp_f32 v4, v4\n\t"
    "v_smfmac_f32_16x16x64_bf16 v[108:111], v[68:71], v[40:47], v80\n\t"
    "v_exp_f32 v6, v6\n\t"
    "v_mfma_f32_16x16x32_bf16 v[160:163], a[64:67], v[40:43], 0\n\t"
    "buffer_load_dwordx4 v121, s[36:39], s8 offen offset:0 lds\n\t"
    "v_mfma_f32_16x16x32_bf16 v[160:163], a[68:71], v[44:47], v[160:163]\n\t"
    "v_cmp_lt_i32 vcc, -19, v231\n\t"
    "v_exp_f32 v7, v7\n\t"
    "v_mfma_f32_16x16x32_bf16 v[164:167], a[72:75], v[40:43], 0\n\t"
    "v_exp_f32 v1, v1\n\t"
    "v_mfma_f32_16x16x32_bf16 v[164:167], a[76:79], v[44:47], v[164:167]\n\t"
    "buffer_load_dwordx4 v121, s[36:39], s8 offen offset:1024 lds\n\t"
    "v_mfma_f32_16x16x32_bf16 v[168:171], a[80:83], v[40:43], 0\n\t"
    "v_exp_f32 v3, v3\n\t"
    "v_mfma_f32_16x16x32_bf16 v[168:171], a[84:87], v[44:47], v[168:171]\n\t"
    "v_exp_f32 v15, v15\n\t"
    "v_mfma_f32_16x16x32_bf16 v[172:175], a[88:91], v[40:43], 0\n\t"
    "v_exp_f32 v8, v8\n\t"
    "v_mfma_f32_16x16x32_bf16 v[172:175], a[92:95], v[44:47], v[172:175]\n\t"
    "buffer_load_dwordx4 v121, s[36:39], s8 offen offset:2048 lds\n\t"
    "v_mfma_f32_16x16x32_bf16 v[176:179], a[96:99], v[40:43], 0\n\t"
    "v_cvt_pk_bf16_f32 v35, v6, v7\n\t"
    "v_exp_f32 v11, v11\n\t"
    "v_mfma_f32_16x16x32_bf16 v[176:179], a[100:103], v[44:47], v[176:179]\n\t"
    "ds_read_b128 a[68:71], v233 offset:8192\n\t"
    "v_cvt_pk_bf16_f32 v33, v2, v3\n\t"
    "v_mfma_f32_16x16x32_bf16 v[180:183], a[104:107], v[40:43], 0\n\t"
    "buffer_load_dwordx4 v121, s[36:39], s8 offen offset:3072 lds\n\t"
    "v_mfma_f32_16x16x32_bf16 v[180:183], a[108:111], v[44:47], v[180:183]\n\t"
    "ds_read_b128 a[92:95], v233 offset:11264\n\t"
    "s_add_u32 m0, s12, s13\n\t"
    "v_mfma_f32_16x16x32_bf16 v[184:187], a[112:115], v[40:43], 0\n\t"
    "ds_read_b128 a[88:91], v233 offset:3072\n\t"
    "ds_read_b128 a[100:103], v233 offset:12288\n\t"
    "ds_read_b128 a[112:115], v233 offset:6144\n\t"
    "v_mfma_f32_16x16x32_bf16 v[184:187], a[116:119], v[44:47], v[184:187]\n\t"
    "v_exp_f32 v5, v5\n\t"
    "v_mfma_f32_16x16x32_bf16 v[188:191], a[120:123], v[40:43], 0\n\t"
    "v_cvt_pk_bf16_f32 v34, v4, v5\n\t"
    "v_exp_f32 v9, v9\n\t"
    "v_mfma_f32_16x16x32_bf16 v[188:191], a[124:127], v[44:47], v[188:191]\n\t"
    "ds_read_b128 a[84:87], v233 offset:10240\n\t"
    "v_exp_f32 v12, v12\n\t"
    "v_mfma_f32_16x16x32_bf16 v[16:19], a[128:131], a[208:211], 0\n\t"
    "buffer_load_dwordx4 v123, s[36:39], s8 offen offset:0 lds\n\t"
    "v_mfma_f32_16x16x32_bf16 v[16:19], a[132:135], a[212:215], v[16:19]\n\t"
    "ds_read_b128 a[72:75], v233 offset:1024\n\t"
    "ds_read_b128 a[124:127], v233 offset:15360\n\t"
    "v_mfma_f32_16x16x32_bf16 v[16:19], a[136:139], a[216:219], v[16:19]\n\t"
    "ds_read_b128 a[64:67], v233 offset:0\n\t"
    "ds_read_b128 a[76:79], v233 offset:9216\n\t"
    "v_cndmask_b32 v55, 0, v226, vcc\n\t"
    "v_mfma_f32_16x16x32_bf16 v[16:19], a[140:143], a[220:223], v[16:19]\n\t"
    "buffer_load_dwordx4 v123, s[36:39], s8 offen offset:1024 lds\n\t"
    "v_mfma_f32_16x16x32_bf16 v[20:23], a[156:159], a[220:223], 0\n\t"
    "ds_read_b128 a[80:83], v233 offset:2048\n\t"
    "v_exp_f32 v10, v10\n\t"
    "v_mfma_f32_16x16x32_bf16 v[20:23], a[152:155], a[216:219], v[20:23]\n\t"
    "v_exp_f32 v0, v0\n\t"
    "v_mfma_f32_16x16x32_bf16 v[20:23], a[148:151], a[212:215], v[20:23]\n\t"
    "ds_read_b128 a[96:99], v233 offset:4096\n\t"
    "v_exp_f32 v13, v13\n\t"
    "v_mfma_f32_16x16x32_bf16 v[20:23], a[144:147], a[208:211], v[20:23]\n\t"
    "buffer_load_dwordx4 v123, s[36:39], s8 offen offset:2048 lds\n\t"
    "v_mfma_f32_16x16x32_bf16 v[24:27], a[160:163], a[208:211], 0\n\t"
    "v_cvt_pk_bf16_f32 v37, v10, v11\n\t"
    "v_exp_f32 v14, v14\n\t"
    "v_mfma_f32_16x16x32_bf16 v[24:27], a[164:167], a[212:215], v[24:27]\n\t"
    "buffer_load_dwordx4 v123, s[36:39], s8 offen offset:3072 lds\n\t"
    "v_mfma_f32_16x16x32_bf16 v[24:27], a[168:171], a[216:219], v[24:27]\n\t"
    "ds_read_b128 a[116:119], v233 offset:14336\n\t"
    "v_cvt_pk_bf16_f32 v32, v0, v1\n\t"
    "v_cvt_pk_bf16_f32 v36, v8, v9\n\t"
    "v_mfma_f32_16x16x32_bf16 v[24:27], a[172:175], a[220:223], v[24:27]\n\t"
    "v_cvt_pk_bf16_f32 v38, v12, v13\n\t"
    "v_cvt_pk_bf16_f32 v39, v14, v15\n\t"
    "s_add_u32 m0, s11, s14\n\t"
    "v_mfma_f32_16x16x32_bf16 v[28:31], a[188:191], a[220:223], 0\n\t"
    "buffer_load_dwordx4 v121, s[40:43], s8 offen offset:0 lds\n\t"
    "v_mfma_f32_16x16x32_bf16 v[28:31], a[184:187], a[216:219], v[28:31]\n\t"
    "ds_read_b128 a[104:107], v233 offset:5120\n\t"
    "ds_read_b128 a[108:111], v233 offset:13312\n\t"
    "ds_read_b128 a[120:123], v233 offset:7168\n\t"
    "v_mfma_f32_16x16x32_bf16 v[28:31], a[180:183], a[212:215], v[28:31]\n\t"
    "buffer_load_dwordx4 v121, s[40:43], s8 offen offset:1024 lds\n\t"
    "v_mfma_f32_16x16x32_bf16 v[28:31], a[176:179], a[208:211], v[28:31]\n\t"
    "v_exp_f32 v16, v16\n\t"
    "v_smfmac_f32_16x16x64_bf16 v[96:99], v[68:71], v[32:39], v80\n\t"
    "v_exp_f32 v26, v26\n\t"
    "s_waitcnt lgkmcnt(4)\n\t"
    "v_mfma_f32_16x16x32_bf16 a[0:3], a[64:67], v[32:35], a[0:3]\n\t"
    "v_exp_f32 v21, v21\n\t"
    "v_mfma_f32_16x16x32_bf16 a[0:3], a[68:71], v[36:39], a[0:3]\n\t"
    "v_exp_f32 v25, v25\n\t"
    "v_mfma_f32_16x16x32_bf16 a[4:7], a[72:75], v[32:35], a[4:7]\n\t"
    "buffer_load_dwordx4 v121, s[40:43], s8 offen offset:2048 lds\n\t"
    "v_mfma_f32_16x16x32_bf16 a[4:7], a[76:79], v[36:39], a[4:7]\n\t"
    "v_exp_f32 v17, v17\n\t"
    "v_mfma_f32_16x16x32_bf16 a[8:11], a[80:83], v[32:35], a[8:11]\n\t"
    "v_exp_f32 v22, v22\n\t"
    "v_mfma_f32_16x16x32_bf16 a[8:11], a[84:87], v[36:39], a[8:11]\n\t"
    "v_exp_f32 v23, v23\n\t"
    "v_mfma_f32_16x16x32_bf16 a[12:15], a[88:91], v[32:35], a[12:15]\n\t"
    "v_exp_f32 v20, v20\n\t"
    "v_mfma_f32_16x16x32_bf16 a[12:15], a[92:95], v[36:39], a[12:15]\n\t"
    "buffer_load_dwordx4 v121, s[40:43], s8 offen offset:3072 lds\n\t"
    "v_mfma_f32_16x16x32_bf16 a[16:19], a[96:99], v[32:35], a[16:19]\n\t"
    "s_add_u32 m0, s12, s14\n\t"
    "v_exp_f32 v29, v29\n\t"
    "v_mfma_f32_16x16x32_bf16 a[16:19], a[100:103], v[36:39], a[16:19]\n\t"
    "v_exp_f32 v27, v27\n\t"
    "s_waitcnt lgkmcnt(0)\n\t"
    "v_mfma_f32_16x16x32_bf16 a[20:23], a[104:107], v[32:35], a[20:23]\n\t"
    "buffer_load_dwordx4 v123, s[40:43], s8 offen offset:0 lds\n\t"
    "v_mfma_f32_16x16x32_bf16 a[20:23], a[108:111], v[36:39], a[20:23]\n\t"
    "v_cvt_pk_bf16_f32 v42, v20, v21\n\t"
    "v_cvt_pk_bf16_f32 v45, v26, v27\n\t"
    "v_mfma_f32_16x16x32_bf16 a[24:27], a[112:115], v[32:35], a[24:27]\n\t"
    "v_exp_f32 v24, v24\n\t"
    "v_mfma_f32_16x16x32_bf16 a[24:27], a[116:119], v[36:39], a[24:27]\n\t"
    "v_exp_f32 v18, v18\n\t"
    "v_mfma_f32_16x16x32_bf16 a[28:31], a[120:123], v[32:35], a[28:31]\n\t"
    "v_exp_f32 v31, v31\n\t"
    "v_mfma_f32_16x16x32_bf16 a[28:31], a[124:127], v[36:39], a[28:31]\n\t"
    "buffer_load_dwordx4 v123, s[40:43], s8 offen offset:1024 lds\n\t"
    "v_mfma_f32_16x16x32_bf16 v[0:3], a[128:131], v[192:195], v[48:51]\n\t"
    "v_exp_f32 v19, v19\n\t"
    "v_mfma_f32_16x16x32_bf16 v[0:3], a[132:135], v[196:199], v[0:3]\n\t"
    "buffer_load_dwordx4 v123, s[40:43], s8 offen offset:2048 lds\n\t"
    "v_mfma_f32_16x16x32_bf16 v[0:3], a[136:139], v[200:203], v[0:3]\n\t"
    "v_exp_f32 v28, v28\n\t"
    "v_mfma_f32_16x16x32_bf16 v[0:3], a[140:143], v[204:207], v[0:3]\n\t"
    "v_exp_f32 v30, v30\n\t"
    "v_mfma_f32_16x16x32_bf16 v[4:7], a[156:159], v[204:207], v[52:55]\n\t"
    "v_cvt_pk_bf16_f32 v43, v22, v23\n\t"
    "v_cvt_pk_bf16_f32 v46, v28, v29\n\t"
    "v_mfma_f32_16x16x32_bf16 v[4:7], a[152:155], v[200:203], v[4:7]\n\t"
    "v_cvt_pk_bf16_f32 v40, v16, v17\n\t"
    "v_cvt_pk_bf16_f32 v44, v24, v25\n\t"
    "v_mfma_f32_16x16x32_bf16 v[4:7], a[148:151], v[196:199], v[4:7]\n\t"
    "v_cvt_pk_bf16_f32 v41, v18, v19\n\t"
    "v_cvt_pk_bf16_f32 v47, v30, v31\n\t"
    "v_mfma_f32_16x16x32_bf16 v[4:7], a[144:147], v[192:195], v[4:7]\n\t"
    "buffer_load_dwordx4 v123, s[40:43], s8 offen offset:3072 lds\n\t"
    "v_smfmac_f32_16x16x64_bf16 v[100:103], v[68:71], v[40:47], v80\n\t"
    "s_add_u32 s8, s8, 0x8000\n\t"
    "v_mfma_f32_16x16x32_bf16 a[32:35], a[64:67], v[40:43], a[32:35]\n\t"
    "v_exp_f32 v1, v1\n\t"
    "v_mfma_f32_16x16x32_bf16 a[32:35], a[68:71], v[44:47], a[32:35]\n\t"
    "s_cmp_eq_u32 s8, s9\n\t"
    "s_cselect_b32 s8, s23, s8\n\t"
    "v_mfma_f32_16x16x32_bf16 a[36:39], a[72:75], v[40:43], a[36:39]\n\t"
    "v_mfma_f32_16x16x32_bf16 a[36:39], a[76:79], v[44:47], a[36:39]\n\t"
    "v_exp_f32 v5, v5\n\t"
    "v_mfma_f32_16x16x32_bf16 a[40:43], a[80:83], v[40:43], a[40:43]\n\t"
    "v_mfma_f32_16x16x32_bf16 a[40:43], a[84:87], v[44:47], a[40:43]\n\t"
    "v_mfma_f32_16x16x32_bf16 a[44:47], a[88:91], v[40:43], a[44:47]\n\t"
    "v_exp_f32 v7, v7\n\t"
    "v_mfma_f32_16x16x32_bf16 a[44:47], a[92:95], v[44:47], a[44:47]\n\t"
    "v_exp_f32 v2, v2\n\t"
    "v_mfma_f32_16x16x32_bf16 a[48:51], a[96:99], v[40:43], a[48:51]\n\t"
    "v_exp_f32 v0, v0\n\t"
    "v_mfma_f32_16x16x32_bf16 a[48:51], a[100:103], v[44:47], a[48:51]\n\t"
    "v_exp_f32 v6, v6\n\t"
    "v_mfma_f32_16x16x32_bf16 a[52:55], a[104:107], v[40:43], a[52:55]\n\t"
    "v_mfma_f32_16x16x32_bf16 a[52:55], a[108:111], v[44:47], a[52:55]\n\t"
    "v_exp_f32 v4, v4\n\t"
    "v_mfma_f32_16x16x32_bf16 a[56:59], a[112:115], v[40:43], a[56:59]\n\t"
    "v_cvt_pk_bf16_f32 v32, v0, v1\n\t"
    "v_mfma_f32_16x16x32_bf16 a[56:59], a[116:119], v[44:47], a[56:59]\n\t"
    "v_mfma_f32_16x16x32_bf16 a[60:63], a[120:123], v[40:43], a[60:63]\n\t"
    "v_exp_f32 v3, v3\n\t"
    "v_mfma_f32_16x16x32_bf16 a[60:63], a[124:127], v[44:47], a[60:63]\n\t"
    "v_mfma_f32_16x16x32_bf16 v[16:19], a[128:131], v[208:211], 0\n\t"
    "v_mfma_f32_16x16x32_bf16 v[16:19], a[132:135], v[212:215], v[16:19]\n\t"
    "v_cvt_pk_bf16_f32 v33, v2, v3\n\t"
    "v_mfma_f32_16x16x32_bf16 v[16:19], a[136:139], v[216:219], v[16:19]\n\t"
    "v_cvt_pk_bf16_f32 v34, v4, v5\n\t"
    "v_mfma_f32_16x16x32_bf16 v[16:19], a[140:143], v[220:223], v[16:19]\n\t"
    "v_cvt_pk_bf16_f32 v35, v6, v7\n\t"
    "v_mfma_f32_16x16x32_bf16 v[20:23], a[156:159], v[220:223], 0\n\t"
    "v_mfma_f32_16x16x32_bf16 v[20:23], a[152:155], v[216:219], v[20:23]\n\t"
    "v_mfma_f32_16x16x32_bf16 v[20:23], a[148:151], v[212:215], v[20:23]\n\t"
    "v_mfma_f32_16x16x32_bf16 v[20:23], a[144:147], v[208:211], v[20:23]\n\t"
    "s_waitcnt vmcnt(8) lgkmcnt(0)\n\t"
    "s_barrier\n\t"
    "v_mfma_f32_16x16x32_bf16 v[24:27], a[160:163], v[208:211], v[48:51]\n\t"
    "s_add_u32 m0, s11, s10\n\t"
    "v_exp_f32 v17, v17\n\t"
    "v_mfma_f32_16x16x32_bf16 v[24:27], a[164:167], v[212:215], v[24:27]\n\t"
    "ds_read_b128 a[132:135], v81 offset:1024\n\t"
    "ds_read_b128 a[148:151], v81 offset:5120\n\t"
    "v_mfma_f32_16x16x32_bf16 v[24:27], a[168:171], v[216:219], v[24:27]\n\t"
    "buffer_load_dwordx4 v121, s[36:39], s8 offen offset:0 lds\n\t"
    "v_mfma_f32_16x16x32_bf16 v[24:27], a[172:175], v[220:223], v[24:27]\n\t"
    "ds_read_b128 a[136:139], v81 offset:2048\n\t"
    "ds_read_b128 a[172:175], v81 offset:11264\n\t"
    "v_mfma_f32_16x16x32_bf16 v[28:31], a[188:191], v[220:223], v[52:55]\n\t"
    "v_exp_f32 v23, v23\n\t"
    "v_mfma_f32_16x16x32_bf16 v[28:31], a[184:187], v[216:219], v[28:31]\n\t"
    "ds_read_b128 a[164:167], v81 offset:9216\n\t"
    "v_exp_f32 v22, v22\n\t"
    "v_mfma_f32_16x16x32_bf16 v[28:31], a[180:183], v[212:215], v[28:31]\n\t"
    "ds_read_b128 a[128:131], v81 offset:0\n\t"
    "ds_read_b128 a[140:143], v81 offset:3072\n\t"
    "v_mfma_f32_16x16x32_bf16 v[28:31], a[176:179], v[208:211], v[28:31]\n\t"
    "ds_read_b128 a[156:159], v81 offset:7168\n\t"
    "v_exp_f32 v19, v19\n\t"
    "v_mfma_f32_16x16x32_bf16 v[104:107], v[68:71], v[32:35], v[104:107]\n\t"
    "ds_read_b128 a[168:171], v81 offset:10240\n\t"
    "v_exp_f32 v16, v16\n\t"
    "v_mfma_f32_16x16x32_bf16 v[128:131], a[64:67], v[32:35], v[128:131]\n\t"
    "buffer_load_dwordx4 v121, s[36:39], s8 offen offset:1024 lds\n\t"
    "v_mfma_f32_16x16x32_bf16 v[132:135], a[72:75], v[32:35], v[132:135]\n\t"
    "ds_read_b128 a[176:179], v81 offset:12288\n\t"
    "v_exp_f32 v18, v18\n\t"
    "v_mfma_f32_16x16x32_bf16 v[136:139], a[80:83], v[32:35], v[136:139]\n\t"
    "ds_read_b128 a[144:147], v81 offset:4096\n\t"
    "ds_read_b128 a[188:191], v81 offset:15360\n\t"
    "v_mfma_f32_16x16x32_bf16 v[140:143], a[88:91], v[32:35], v[140:143]\n\t"
    "buffer_load_dwordx4 v121, s[36:39], s8 offen offset:2048 lds\n\t"
    "v_mfma_f32_16x16x32_bf16 v[144:147], a[96:99], v[32:35], v[144:147]\n\t"
    "v_cvt_pk_bf16_f32 v40, v16, v17\n\t"
    "v_exp_f32 v31, v31\n\t"
    "v_mfma_f32_16x16x32_bf16 v[148:151], a[104:107], v[32:35], v[148:151]\n\t"
    "ds_read_b128 a[152:155], v81 offset:6144\n\t"
    "v_exp_f32 v26, v26\n\t"
    "v_mfma_f32_16x16x32_bf16 v[152:155], a[112:115], v[32:35], v[152:155]\n\t"
    "v_exp_f32 v21, v21\n\t"
    "v_mfma_f32_16x16x32_bf16 v[156:159], a[120:123], v[32:35], v[156:159]\n\t"
    "ds_read_b128 a[160:163], v81 offset:8192\n\t"
    "v_exp_f32 v29, v29\n\t"
    "s_waitcnt lgkmcnt(6)\n\t"
    "v_mfma_f32_16x16x32_bf16 v[0:3], a[128:131], a[192:195], 0\n\t"
    "v_exp_f32 v24, v24\n\t"
    "v_mfma_f32_16x16x32_bf16 v[0:3], a[132:135], a[196:199], v[0:3]\n\t"
    "v_cvt_pk_bf16_f32 v41, v18, v19\n\t"
    "v_exp_f32 v25, v25\n\t"
    "v_mfma_f32_16x16x32_bf16 v[0:3], a[136:139], a[200:203], v[0:3]\n\t"
    "buffer_load_dwordx4 v121, s[36:39], s8 offen offset:3072 lds\n\t"
    "v_mfma_f32_16x16x32_bf16 v[0:3], a[140:143], a[204:207], v[0:3]\n\t"
    "ds_read_b128 a[184:187], v81 offset:14336\n\t"
    "s_add_u32 m0, s12, s10\n\t"
    "v_mfma_f32_16x16x32_bf16 v[4:7], a[156:159], a[204:207], 0\n\t"
    "v_cvt_pk_bf16_f32 v44, v24, v25\n\t"
    "v_exp_f32 v20, v20\n\t"
    "s_waitcnt lgkmcnt(2)\n\t"
    "v_mfma_f32_16x16x32_bf16 v[4:7], a[152:155], a[200:203], v[4:7]\n\t"
    "ds_read_b128 a[180:183], v81 offset:13312\n\t"
    "v_cvt_pk_bf16_f32 v42, v20, v21\n\t"
    "v_mfma_f32_16x16x32_bf16 v[4:7], a[148:151], a[196:199], v[4:7]\n\t"
    "buffer_load_dwordx4 v123, s[36:39], s8 offen offset:0 lds\n\t"
    "v_mfma_f32_16x16x32_bf16 v[4:7], a[144:147], a[192:195], v[4:7]\n\t"
    "v_exp_f32 v28, v28\n\t"
    "s_waitcnt lgkmcnt(2)\n\t"
    "v_mfma_f32_16x16x32_bf16 v[8:11], a[160:163], a[192:195], 0\n\t"
    "v_cvt_pk_bf16_f32 v43, v22, v23\n\t"
    "v_cvt_pk_bf16_f32 v46, v28, v29\n\t"
    "v_mfma_f32_16x16x32_bf16 v[8:11], a[164:167], a[196:199], v[8:11]\n\t"
    "v_exp_f32 v1, v1\n\t"
    "v_mfma_f32_16x16x32_bf16 v[8:11], a[168:171], a[200:203], v[8:11]\n\t"
    "v_exp_f32 v30, v30\n\t"
    "v_mfma_f32_16x16x32_bf16 v[8:11], a[172:175], a[204:207], v[8:11]\n\t"
    "v_exp_f32 v27, v27\n\t"
    "v_mfma_f32_16x16x32_bf16 v[12:15], a[188:191], a[204:207], 0\n\t"
    "v_cvt_pk_bf16_f32 v45, v26, v27\n\t"
    "v_exp_f32 v5, v5\n\t"
    "s_waitcnt lgkmcnt(1)\n\t"
    "v_mfma_f32_16x16x32_bf16 v[12:15], a[184:187], a[200:203], v[12:15]\n\t"
    "buffer_load_dwordx4 v123, s[36:39], s8 offen offset:1024 lds\n\t"
    "s_waitcnt lgkmcnt(0)\n\t"
    "v_mfma_f32_16x16x32_bf16 v[12:15], a[180:183], a[196:199], v[12:15]\n\t"
    "v_cvt_pk_bf16_f32 v47, v30, v31\n\t"
    "v_exp_f32 v2, v2\n\t"
    "v_mfma_f32_16x16x32_bf16 v[12:15], a[176:179], a[192:195], v[12:15]\n\t"
    "v_mov_b32 v105, 0\n\t"
    "s_waitcnt vmcnt(6)\n\t"
    "s_barrier\n\t"
    "v_smfmac_f32_16x16x64_bf16 v[108:111], v[68:71], v[40:47], v80\n\t"
    "buffer_load_dwordx4 v123, s[36:39], s8 offen offset:2048 lds\n\t"
    "v_mfma_f32_16x16x32_bf16 v[160:163], a[64:67], v[40:43], v[160:163]\n\t"
    "v_exp_f32 v11, v11\n\t"
    "v_mfma_f32_16x16x32_bf16 v[160:163], a[68:71], v[44:47], v[160:163]\n\t"
    "v_exp_f32 v9, v9\n\t"
    "v_mfma_f32_16x16x32_bf16 v[164:167], a[72:75], v[40:43], v[164:167]\n\t"
    "v_exp_f32 v6, v6\n\t"
    "v_mfma_f32_16x16x32_bf16 v[164:167], a[76:79], v[44:47], v[164:167]\n\t"
    "buffer_load_dwordx4 v123, s[36:39], s8 offen offset:3072 lds\n\t"
    "v_mfma_f32_16x16x32_bf16 v[168:171], a[80:83], v[40:43], v[168:171]\n\t"
    "v_exp_f32 v12, v12\n\t"
    "v_mfma_f32_16x16x32_bf16 v[168:171], a[84:87], v[44:47], v[168:171]\n\t"
    "v_exp_f32 v3, v3\n\t"
    "v_mfma_f32_16x16x32_bf16 v[172:175], a[88:91], v[40:43], v[172:175]\n\t"
    "ds_read_b128 a[64:67], v82 offset:0\n\t"
    "ds_read_b128 a[68:71], v82 offset:8192\n\t"
    "v_cvt_pk_bf16_f32 v33, v2, v3\n\t"
    "v_mfma_f32_16x16x32_bf16 v[172:175], a[92:95], v[44:47], v[172:175]\n\t"
    "v_exp_f32 v13, v13\n\t"
    "v_mfma_f32_16x16x32_bf16 v[176:179], a[96:99], v[40:43], v[176:179]\n\t"
    "v_exp_f32 v4, v4\n\t"
    "v_mfma_f32_16x16x32_bf16 v[176:179], a[100:103], v[44:47], v[176:179]\n\t"
    "ds_read_b128 a[72:75], v82 offset:1024\n\t"
    "ds_read_b128 a[76:79], v82 offset:9216\n\t"
    "v_mfma_f32_16x16x32_bf16 v[180:183], a[104:107], v[40:43], v[180:183]\n\t"
    "v_cvt_pk_bf16_f32 v34, v4, v5\n\t"
    "v_exp_f32 v8, v8\n\t"
    "v_mfma_f32_16x16x32_bf16 v[180:183], a[108:111], v[44:47], v[180:183]\n\t"
    "ds_read_b128 a[80:83], v82 offset:2048\n\t"
    "ds_read_b128 a[84:87], v82 offset:10240\n\t"
    "v_cvt_pk_bf16_f32 v38, v12, v13\n\t"
    "v_mfma_f32_16x16x32_bf16 v[184:187], a[112:115], v[40:43], v[184:187]\n\t"
    "v_exp_f32 v0, v0\n\t"
    "v_mfma_f32_16x16x32_bf16 v[184:187], a[116:119], v[44:47], v[184:187]\n\t"
    "v_exp_f32 v15, v15\n\t"
    "v_mfma_f32_16x16x32_bf16 v[188:191], a[120:123], v[40:43], v[188:191]\n\t"
    "v_exp_f32 v14, v14\n\t"
    "s_cmp_eq_u32 s60, 0\n\t"
    "v_mfma_f32_16x16x32_bf16 v[188:191], a[124:127], v[44:47], v[188:191]\n\t"
    "v_cvt_pk_bf16_f32 v39, v14, v15\n\t"
    "v_exp_f32 v7, v7\n\t"
    "s_cbranch_scc1 L_L_%=\n\t"
    "L_A_%=:\n\t"
    "v_mfma_f32_16x16x32_bf16 v[16:19], a[128:131], a[208:211], 0\n\t"
    "ds_read_b128 a[88:91], v82 offset:3072\n\t"
    "ds_read_b128 a[92:95], v82 offset:11264\n\t"
    "v_mfma_f32_16x16x32_bf16 v[16:19], a[132:135], a[212:215], v[16:19]\n\t"
    "ds_read_b128 a[96:99], v82 offset:4096\n\t"
    "ds_read_b128 a[100:103], v82 offset:12288\n\t"
    "v_mfma_f32_16x16x32_bf16 v[16:19], a[136:139], a[216:219], v[16:19]\n\t"
    "ds_read_b128 a[104:107], v82 offset:5120\n\t"
    "ds_read_b128 a[108:111], v82 offset:13312\n\t"
    "v_mfma_f32_16x16x32_bf16 v[16:19], a[140:143], a[220:223], v[16:19]\n\t"
    "v_exp_f32 v10, v10\n\t"
    "v_mfma_f32_16x16x32_bf16 v[20:23], a[156:159], a[220:223], 0\n\t"
    "v_cvt_pk_bf16_f32 v35, v6, v7\n\t"
    "v_cvt_pk_bf16_f32 v37, v10, v11\n\t"
    "v_mfma_f32_16x16x32_bf16 v[20:23], a[152:155], a[216:219], v[20:23]\n\t"
    "ds_read_b128 a[112:115], v82 offset:6144\n\t"
    "ds_read_b128 a[116:119], v82 offset:14336\n\t"
    "v_mfma_f32_16x16x32_bf16 v[20:23], a[148:151], a[212:215], v[20:23]\n\t"
    "ds_read_b128 a[120:123], v82 offset:7168\n\t"
    "v_cvt_pk_bf16_f32 v32, v0, v1\n\t"
    "v_mfma_f32_16x16x32_bf16 v[20:23], a[144:147], a[208:211], v[20:23]\n\t"
    "ds_read_b128 a[124:127], v82 offset:15360\n\t"
    "v_cvt_pk_bf16_f32 v36, v8, v9\n\t"
    "v_mfma_f32_16x16x32_bf16 v[24:27], a[160:163], a[208:211], 0\n\t"
    "s_add_u32 m0, s15, s11\n\t"
    "v_mfma_f32_16x16x32_bf16 v[24:27], a[164:167], a[212:215], v[24:27]\n\t"
    "v_add_u32 v125, 0x2000, v125\n\t"
    "v_mfma_f32_16x16x32_bf16 v[24:27], a[168:171], a[216:219], v[24:27]\n\t"
    "v_exp_f32 v19, v19\n\t"
    "v_mfma_f32_16x16x32_bf16 v[24:27], a[172:175], a[220:223], v[24:27]\n\t"
    "v_exp_f32 v17, v17\n\t"
    "v_mfma_f32_16x16x32_bf16 v[28:31], a[188:191], a[220:223], 0\n\t"
    "v_exp_f32 v16, v16\n\t"
    "v_mfma_f32_16x16x32_bf16 v[28:31], a[184:187], a[216:219], v[28:31]\n\t"
    "buffer_load_dwordx4 v121, s[40:43], s8 offen offset:0 lds\n\t"
    "v_mfma_f32_16x16x32_bf16 v[28:31], a[180:183], a[212:215], v[28:31]\n\t"
    "v_xor_b32 v81, 0x4000, v81\n\t"
    "v_mfma_f32_16x16x32_bf16 v[28:31], a[176:179], a[208:211], v[28:31]\n\t"
    "v_exp_f32 v22, v22\n\t"
    "v_smfmac_f32_16x16x64_bf16 v[96:99], v[68:71], v[32:39], v80\n\t"
    "s_waitcnt lgkmcnt(4)\n\t"
    "v_mfma_f32_16x16x32_bf16 a[0:3], a[64:67], v[32:35], a[0:3]\n\t"
    "v_exp_f32 v18, v18\n\t"
    "v_mfma_f32_16x16x32_bf16 a[0:3], a[68:71], v[36:39], a[0:3]\n\t"
    "v_exp_f32 v27, v27\n\t"
    "v_mfma_f32_16x16x32_bf16 a[4:7], a[72:75], v[32:35], a[4:7]\n\t"
    "v_exp_f32 v23, v23\n\t"
    "v_mfma_f32_16x16x32_bf16 a[4:7], a[76:79], v[36:39], a[4:7]\n\t"
    "v_exp_f32 v24, v24\n\t"
    "v_mfma_f32_16x16x32_bf16 a[8:11], a[80:83], v[32:35], a[8:11]\n\t"
    "v_exp_f32 v29, v29\n\t"
    "v_mfma_f32_16x16x32_bf16 a[8:11], a[84:87], v[36:39], a[8:11]\n\t"
    "v_exp_f32 v20, v20\n\t"
    "v_mfma_f32_16x16x32_bf16 a[12:15], a[88:91], v[32:35], a[12:15]\n\t"
    "buffer_load_dwordx4 v121, s[40:43], s8 offen offset:1024 lds\n\t"
    "v_mfma_f32_16x16x32_bf16 a[12:15], a[92:95], v[36:39], a[12:15]\n\t"
    "v_mfma_f32_16x16x32_bf16 a[16:19], a[96:99], v[32:35], a[16:19]\n\t"
    "v_exp_f32 v28, v28\n\t"
    "v_mfma_f32_16x16x32_bf16 a[16:19], a[100:103], v[36:39], a[16:19]\n\t"
    "v_cvt_pk_bf16_f32 v40, v16, v17\n\t"
    "v_mfma_f32_16x16x32_bf16 a[20:23], a[104:107], v[32:35], a[20:23]\n\t"
    "v_exp_f32 v25, v25\n\t"
    "v_mfma_f32_16x16x32_bf16 a[20:23], a[108:111], v[36:39], a[20:23]\n\t"
    "v_cvt_pk_bf16_f32 v44, v24, v25\n\t"
    "s_waitcnt lgkmcnt(0)\n\t"
    "v_mfma_f32_16x16x32_bf16 a[24:27], a[112:115], v[32:35], a[24:27]\n\t"
    "v_exp_f32 v26, v26\n\t"
    "v_mfma_f32_16x16x32_bf16 a[24:27], a[116:119], v[36:39], a[24:27]\n\t"
    "v_exp_f32 v21, v21\n\t"
    "v_mfma_f32_16x16x32_bf16 a[28:31], a[120:123], v[32:35], a[28:31]\n\t"
    "v_exp_f32 v30, v30\n\t"
    "v_mfma_f32_16x16x32_bf16 a[28:31], a[124:127], v[36:39], a[28:31]\n\t"
    "v_exp_f32 v31, v31\n\t"
    "v_mfma_f32_16x16x32_bf16 v[0:3], a[128:131], v[192:195], 0\n\t"
    "v_cvt_pk_bf16_f32 v45, v26, v27\n\t"
    "v_mfma_f32_16x16x32_bf16 v[0:3], a[132:135], v[196:199], v[0:3]\n\t"
    "v_cvt_pk_bf16_f32 v43, v22, v23\n\t"
    "v_mfma_f32_16x16x32_bf16 v[0:3], a[136:139], v[200:203], v[0:3]\n\t"
    "v_cvt_pk_bf16_f32 v42, v20, v21\n\t"
    "v_mfma_f32_16x16x32_bf16 v[0:3], a[140:143], v[204:207], v[0:3]\n\t"
    "v_cvt_pk_bf16_f32 v41, v18, v19\n\t"
    "v_mfma_f32_16x16x32_bf16 v[4:7], a[156:159], v[204:207], 0\n\t"
    "buffer_load_dwordx4 v121, s[40:43], s8 offen offset:2048 lds\n\t"
    "v_mfma_f32_16x16x32_bf16 v[4:7], a[152:155], v[200:203], v[4:7]\n\t"
    "v_cvt_pk_bf16_f32 v47, v30, v31\n\t"
    "v_mfma_f32_16x16x32_bf16 v[4:7], a[148:151], v[196:199], v[4:7]\n\t"
    "v_xor_b32 v82, 0x4000, v82\n\t"
    "v_mfma_f32_16x16x32_bf16 v[4:7], a[144:147], v[192:195], v[4:7]\n\t"
    "v_cvt_pk_bf16_f32 v46, v28, v29\n\t"
    "v_mfma_f32_16x16x32_bf16 v[8:11], a[160:163], v[192:195], 0\n\t"
    "v_exp_f32 v2, v2\n\t"
    "v_mfma_f32_16x16x32_bf16 v[8:11], a[164:167], v[196:199], v[8:11]\n\t"
    "v_exp_f32 v3, v3\n\t"
    "v_mfma_f32_16x16x32_bf16 v[8:11], a[168:171], v[200:203], v[8:11]\n\t"
    "v_exp_f32 v0, v0\n\t"
    "v_mfma_f32_16x16x32_bf16 v[8:11], a[172:175], v[204:207], v[8:11]\n\t"
    "v_cvt_pk_bf16_f32 v33, v2, v3\n\t"
    "v_mfma_f32_16x16x32_bf16 v[12:15], a[188:191], v[204:207], 0\n\t"
    "v_exp_f32 v7, v7\n\t"
    "v_mfma_f32_16x16x32_bf16 v[12:15], a[184:187], v[200:203], v[12:15]\n\t"
    "v_exp_f32 v4, v4\n\t"
    "v_mfma_f32_16x16x32_bf16 v[12:15], a[180:183], v[196:199], v[12:15]\n\t"
    "buffer_load_dwordx4 v121, s[40:43], s8 offen offset:3072 lds\n\t"
    "v_mfma_f32_16x16x32_bf16 v[12:15], a[176:179], v[192:195], v[12:15]\n\t"
    "v_exp_f32 v1, v1\n\t"
    "v_smfmac_f32_16x16x64_bf16 v[100:103], v[68:71], v[40:47], v80\n\t"
    "v_cvt_pk_bf16_f32 v32, v0, v1\n\t"
    "v_mfma_f32_16x16x32_bf16 a[32:35], a[64:67], v[40:43], a[32:35]\n\t"
    "v_exp_f32 v11, v11\n\t"
    "v_mfma_f32_16x16x32_bf16 a[32:35], a[68:71], v[44:47], a[32:35]\n\t"
    "v_exp_f32 v8, v8\n\t"
    "v_mfma_f32_16x16x32_bf16 a[36:39], a[72:75], v[40:43], a[36:39]\n\t"
    "v_exp_f32 v9, v9\n\t"
    "v_mfma_f32_16x16x32_bf16 a[36:39], a[76:79], v[44:47], a[36:39]\n\t"
    "v_exp_f32 v5, v5\n\t"
    "v_mfma_f32_16x16x32_bf16 a[40:43], a[80:83], v[40:43], a[40:43]\n\t"
    "v_exp_f32 v10, v10\n\t"
    "v_mfma_f32_16x16x32_bf16 a[40:43], a[84:87], v[44:47], a[40:43]\n\t"
    "v_exp_f32 v14, v14\n\t"
    "v_mfma_f32_16x16x32_bf16 a[44:47], a[88:91], v[40:43], a[44:47]\n\t"
    "s_add_u32 m0, s15, s12\n\t"
    "v_mfma_f32_16x16x32_bf16 a[44:47], a[92:95], v[44:47], a[44:47]\n\t"
    "v_exp_f32 v15, v15\n\t"
    "v_mfma_f32_16x16x32_bf16 a[48:51], a[96:99], v[40:43], a[48:51]\n\t"
    "v_exp_f32 v6, v6\n\t"
    "v_mfma_f32_16x16x32_bf16 a[48:51], a[100:103], v[44:47], a[48:51]\n\t"
    "v_exp_f32 v13, v13\n\t"
    "v_mfma_f32_16x16x32_bf16 a[52:55], a[104:107], v[40:43], a[52:55]\n\t"
    "buffer_load_dwordx4 v123, s[40:43], s8 offen offset:0 lds\n\t"
    "v_mfma_f32_16x16x32_bf16 a[52:55], a[108:111], v[44:47], a[52:55]\n\t"
    "v_exp_f32 v12, v12\n\t"
    "v_mfma_f32_16x16x32_bf16 a[56:59], a[112:115], v[40:43], a[56:59]\n\t"
    "v_cvt_pk_bf16_f32 v38, v12, v13\n\t"
    "v_mfma_f32_16x16x32_bf16 a[56:59], a[116:119], v[44:47], a[56:59]\n\t"
    "v_cvt_pk_bf16_f32 v39, v14, v15\n\t"
    "v_mfma_f32_16x16x32_bf16 a[60:63], a[120:123], v[40:43], a[60:63]\n\t"
    "v_cvt_pk_bf16_f32 v37, v10, v11\n\t"
    "v_mfma_f32_16x16x32_bf16 a[60:63], a[124:127], v[44:47], a[60:63]\n\t"
    "v_cvt_pk_bf16_f32 v36, v8, v9\n\t"
    "v_mfma_f32_16x16x32_bf16 v[16:19], a[128:131], v[208:211], 0\n\t"
    "ds_read_b128 a[128:131], v81 offset:0\n\t"
    "v_mfma_f32_16x16x32_bf16 v[16:19], a[132:135], v[212:215], v[16:19]\n\t"
    "ds_read_b128 a[132:135], v81 offset:1024\n\t"
    "v_mfma_f32_16x16x32_bf16 v[16:19], a[136:139], v[216:219], v[16:19]\n\t"
    "ds_read_b128 a[136:139], v81 offset:2048\n\t"
    "v_mfma_f32_16x16x32_bf16 v[16:19], a[140:143], v[220:223], v[16:19]\n\t"
    "v_cvt_pk_bf16_f32 v34, v4, v5\n\t"
    "Lis9_%=:\n\t"
    "s_cbranch_execz Lis13_%=\n\t"
    "v_mfma_f32_16x16x32_bf16 v[20:23], a[156:159], v[220:223], 0\n\t"
    "ds_read_b128 a[140:143], v81 offset:3072\n\t"
    "ds_read_b128 a[156:159], v81 offset:7168\n\t"
    "v_mfma_f32_16x16x32_bf16 v[20:23], a[152:155], v[216:219], v[20:23]\n\t"
    "ds_read_b128 a[152:155], v81 offset:6144\n\t"
    "v_mfma_f32_16x16x32_bf16 v[20:23], a[148:151], v[212:215], v[20:23]\n\t"
    "buffer_load_dwordx4 v123, s[40:43], s8 offen offset:1024 lds\n\t"
    "v_mfma_f32_16x16x32_bf16 v[20:23], a[144:147], v[208:211], v[20:23]\n\t"
    "ds_read_b128 a[148:151], v81 offset:5120\n\t"
    "v_mfma_f32_16x16x32_bf16 v[24:27], a[160:163], v[208:211], 0\n\t"
    "v_cvt_pk_bf16_f32 v35, v6, v7\n\t"
    "v_mfma_f32_16x16x32_bf16 v[24:27], a[164:167], v[212:215], v[24:27]\n\t"
    "ds_read_b128 a[144:147], v81 offset:4096\n\t"
    "ds_read_b128 a[160:163], v81 offset:8192\n\t"
    "v_mfma_f32_16x16x32_bf16 v[24:27], a[168:171], v[216:219], v[24:27]\n\t"
    "ds_read_b128 a[164:167], v81 offset:9216\n\t"
    "v_mfma_f32_16x16x32_bf16 v[24:27], a[172:175], v[220:223], v[24:27]\n\t"
    "ds_read_b128 a[168:171], v81 offset:10240\n\t"
    "v_mfma_f32_16x16x32_bf16 v[28:31], a[188:191], v[220:223], 0\n\t"
    "ds_read_b128 a[172:175], v81 offset:11264\n\t"
    "v_mfma_f32_16x16x32_bf16 v[28:31], a[184:187], v[216:219], v[28:31]\n\t"
    "v_exp_f32 v16, v16\n\t"
    "v_mfma_f32_16x16x32_bf16 v[28:31], a[180:183], v[212:215], v[28:31]\n\t"
    "ds_read_b128 a[188:191], v81 offset:15360\n\t"
    "v_mfma_f32_16x16x32_bf16 v[28:31], a[176:179], v[208:211], v[28:31]\n\t"
    "v_exp_f32 v18, v18\n\t"
    "v_smfmac_f32_16x16x64_bf16 v[104:107], v[68:71], v[32:39], v80\n\t"
    "v_exp_f32 v24, v24\n\t"
    "v_mfma_f32_16x16x32_bf16 v[128:131], a[64:67], v[32:35], v[128:131]\n\t"
    "v_exp_f32 v27, v27\n\t"
    "v_mfma_f32_16x16x32_bf16 v[128:131], a[68:71], v[36:39], v[128:131]\n\t"
    "v_exp_f32 v26, v26\n\t"
    "v_mfma_f32_16x16x32_bf16 v[132:135], a[72:75], v[32:35], v[132:135]\n\t"
    "buffer_load_dwordx4 v123, s[40:43], s8 offen offset:2048 lds\n\t"
    "v_mfma_f32_16x16x32_bf16 v[132:135], a[76:79], v[36:39], v[132:135]\n\t"
    "v_exp_f32 v22, v22\n\t"
    "v_mfma_f32_16x16x32_bf16 v[136:139], a[80:83], v[32:35], v[136:139]\n\t"
    "v_exp_f32 v21, v21\n\t"
    "v_mfma_f32_16x16x32_bf16 v[136:139], a[84:87], v[36:39], v[136:139]\n\t"
    "ds_read_b128 a[184:187], v81 offset:14336\n\t"
    "v_mfma_f32_16x16x32_bf16 v[140:143], a[88:91], v[32:35], v[140:143]\n\t"
    "v_cvt_pk_bf16_f32 v45, v26, v27\n\t"
    "v_mfma_f32_16x16x32_bf16 v[140:143], a[92:95], v[36:39], v[140:143]\n\t"
    "ds_read_b128 a[180:183], v81 offset:13312\n\t"
    "v_mfma_f32_16x16x32_bf16 v[144:147], a[96:99], v[32:35], v[144:147]\n\t"
    "v_exp_f32 v29, v29\n\t"
    "v_mfma_f32_16x16x32_bf16 v[144:147], a[100:103], v[36:39], v[144:147]\n\t"
    "v_exp_f32 v30, v30\n\t"
    "v_mfma_f32_16x16x32_bf16 v[148:151], a[104:107], v[32:35], v[148:151]\n\t"
    "v_exp_f32 v20, v20\n\t"
    "v_mfma_f32_16x16x32_bf16 v[148:151], a[108:111], v[36:39], v[148:151]\n\t"
    "v_cvt_pk_bf16_f32 v42, v20, v21\n\t"
    "v_mfma_f32_16x16x32_bf16 v[152:155], a[112:115], v[32:35], v[152:155]\n\t"
    "buffer_load_dwordx4 v123, s[40:43], s8 offen offset:3072 lds\n\t"
    "v_mfma_f32_16x16x32_bf16 v[152:155], a[116:119], v[36:39], v[152:155]\n\t"
    "ds_read_b128 a[176:179], v81 offset:12288\n\t"
    "s_add_u32 s8, s8, 0x8000\n\t"
    "v_mfma_f32_16x16x32_bf16 v[156:159], a[120:123], v[32:35], v[156:159]\n\t"
    "s_cmp_eq_u32 s8, s9\n\t"
    "s_cselect_b32 s8, s23, s8\n\t"
    "v_mfma_f32_16x16x32_bf16 v[156:159], a[124:127], v[36:39], v[156:159]\n\t"
    "s_waitcnt lgkmcnt(3)\n\t"
    "v_mfma_f32_16x16x32_bf16 v[0:3], a[128:131], a[192:195], 0\n\t"
    "v_exp_f32 v25, v25\n\t"
    "v_mfma_f32_16x16x32_bf16 v[0:3], a[132:135], a[196:199], v[0:3]\n\t"
    "v_exp_f32 v28, v28\n\t"
    "v_mfma_f32_16x16x32_bf16 v[0:3], a[136:139], a[200:203], v[0:3]\n\t"
    "v_exp_f32 v31, v31\n\t"
    "v_mfma_f32_16x16x32_bf16 v[0:3], a[140:143], a[204:207], v[0:3]\n\t"
    "v_exp_f32 v17, v17\n\t"
    "v_mfma_f32_16x16x32_bf16 v[4:7], a[156:159], a[204:207], 0\n\t"
    "v_exp_f32 v23, v23\n\t"
    "v_mfma_f32_16x16x32_bf16 v[4:7], a[152:155], a[200:203], v[4:7]\n\t"
    "v_exp_f32 v19, v19\n\t"
    "v_mfma_f32_16x16x32_bf16 v[4:7], a[148:151], a[196:199], v[4:7]\n\t"
    "v_cvt_pk_bf16_f32 v40, v16, v17\n\t"
    "v_cvt_pk_bf16_f32 v47, v30, v31\n\t"
    "v_mfma_f32_16x16x32_bf16 v[4:7], a[144:147], a[192:195], v[4:7]\n\t"
    "v_cvt_pk_bf16_f32 v43, v22, v23\n\t"
    "v_mfma_f32_16x16x32_bf16 v[8:11], a[160:163], a[192:195], 0\n\t"
    "v_exp_f32 v3, v3\n\t"
    "v_mfma_f32_16x16x32_bf16 v[8:11], a[164:167], a[196:199], v[8:11]\n\t"
    "v_cvt_pk_bf16_f32 v46, v28, v29\n\t"
    "v_mfma_f32_16x16x32_bf16 v[8:11], a[168:171], a[200:203], v[8:11]\n\t"
    "v_exp_f32 v0, v0\n\t"
    "v_mfma_f32_16x16x32_bf16 v[8:11], a[172:175], a[204:207], v[8:11]\n\t"
    "v_cvt_pk_bf16_f32 v41, v18, v19\n\t"
    "v_cvt_pk_bf16_f32 v44, v24, v25\n\t"
    "v_mfma_f32_16x16x32_bf16 v[12:15], a[188:191], a[204:207], 0\n\t"
    "v_exp_f32 v1, v1\n\t"
    "s_waitcnt lgkmcnt(0)\n\t"
    "v_mfma_f32_16x16x32_bf16 v[12:15], a[184:187], a[200:203], v[12:15]\n\t"
    "v_exp_f32 v4, v4\n\t"
    "v_mfma_f32_16x16x32_bf16 v[12:15], a[180:183], a[196:199], v[12:15]\n\t"
    "v_exp_f32 v5, v5\n\t"
    "v_mfma_f32_16x16x32_bf16 v[12:15], a[176:179], a[192:195], v[12:15]\n\t"
    "v_exp_f32 v7, v7\n\t"
    "v_smfmac_f32_16x16x64_bf16 v[108:111], v[68:71], v[40:47], v80\n\t"
    "v_exp_f32 v11, v11\n\t"
    "v_mfma_f32_16x16x32_bf16 v[160:163], a[64:67], v[40:43], v[160:163]\n\t"
    "v_exp_f32 v6, v6\n\t"
    "v_mfma_f32_16x16x32_bf16 v[160:163], a[68:71], v[44:47], v[160:163]\n\t"
    "v_exp_f32 v2, v2\n\t"
    "v_mfma_f32_16x16x32_bf16 v[164:167], a[72:75], v[40:43], v[164:167]\n\t"
    "v_exp_f32 v8, v8\n\t"
    "v_mfma_f32_16x16x32_bf16 v[164:167], a[76:79], v[44:47], v[164:167]\n\t"
    "v_exp_f32 v13, v13\n\t"
    "v_mfma_f32_16x16x32_bf16 v[168:171], a[80:83], v[40:43], v[168:171]\n\t"
    "v_exp_f32 v12, v12\n\t"
    "v_mfma_f32_16x16x32_bf16 v[168:171], a[84:87], v[44:47], v[168:171]\n\t"
    "v_exp_f32 v14, v14\n\t"
    "v_mfma_f32_16x16x32_bf16 v[172:175], a[88:91], v[40:43], v[172:175]\n\t"
    "ds_read_b128 a[64:67], v82 offset:0\n\t"
    "ds_read_b128 a[68:71], v82 offset:8192\n\t"
    "v_mfma_f32_16x16x32_bf16 v[172:175], a[92:95], v[44:47], v[172:175]\n\t"
    "buffer_load_dword v244, v125, s[28:31], 0 offen\n\t"
    "v_mfma_f32_16x16x32_bf16 v[176:179], a[96:99], v[40:43], v[176:179]\n\t"
    "v_exp_f32 v15, v15\n\t"
    "v_mfma_f32_16x16x32_bf16 v[176:179], a[100:103], v[44:47], v[176:179]\n\t"
    "ds_read_b128 a[72:75], v82 offset:1024\n\t"
    "ds_read_b128 a[76:79], v82 offset:9216\n\t"
    "v_mfma_f32_16x16x32_bf16 v[180:183], a[104:107], v[40:43], v[180:183]\n\t"
    "v_cvt_pk_bf16_f32 v33, v2, v3\n\t"
    "v_cvt_pk_bf16_f32 v39, v14, v15\n\t"
    "v_mfma_f32_16x16x32_bf16 v[180:183], a[108:111], v[44:47], v[180:183]\n\t"
    "ds_read_b128 a[80:83], v82 offset:2048\n\t"
    "ds_read_b128 a[84:87], v82 offset:10240\n\t"
    "v_mfma_f32_16x16x32_bf16 v[184:187], a[112:115], v[40:43], v[184:187]\n\t"
    "v_cvt_pk_bf16_f32 v34, v4, v5\n\t"
    "v_cvt_pk_bf16_f32 v38, v12, v13\n\t"
    "v_mfma_f32_16x16x32_bf16 v[184:187], a[116:119], v[44:47], v[184:187]\n\t"
    "s_cmp_eq_u32 s17, s18\n\t"
    "v_mfma_f32_16x16x32_bf16 v[188:191], a[120:123], v[40:43], v[188:191]\n\t"
    "v_exp_f32 v9, v9\n\t"
    "v_mfma_f32_16x16x32_bf16 v[188:191], a[124:127], v[44:47], v[188:191]\n\t"
    "s_cbranch_scc1 L_R_%=\n\t"
    "L_A1_%=:\n\t"
    "v_mfma_f32_16x16x32_bf16 v[16:19], a[128:131], a[208:211], 0\n\t"
    "ds_read_b128 a[88:91], v82 offset:3072\n\t"
    "ds_read_b128 a[92:95], v82 offset:11264\n\t"
    "v_mfma_f32_16x16x32_bf16 v[16:19], a[132:135], a[212:215], v[16:19]\n\t"
    "ds_read_b128 a[96:99], v82 offset:4096\n\t"
    "ds_read_b128 a[100:103], v82 offset:12288\n\t"
    "v_mfma_f32_16x16x32_bf16 v[16:19], a[136:139], a[216:219], v[16:19]\n\t"
    "ds_read_b128 a[104:107], v82 offset:5120\n\t"
    "ds_read_b128 a[108:111], v82 offset:13312\n\t"
    "v_mfma_f32_16x16x32_bf16 v[16:19], a[140:143], a[220:223], v[16:19]\n\t"
    "v_exp_f32 v10, v10\n\t"
    "v_mfma_f32_16x16x32_bf16 v[20:23], a[156:159], a[220:223], 0\n\t"
    "v_cvt_pk_bf16_f32 v35, v6, v7\n\t"
    "v_cvt_pk_bf16_f32 v37, v10, v11\n\t"
    "v_mfma_f32_16x16x32_bf16 v[20:23], a[152:155], a[216:219], v[20:23]\n\t"
    "ds_read_b128 a[112:115], v82 offset:6144\n\t"
    "ds_read_b128 a[116:119], v82 offset:14336\n\t"
    "v_mfma_f32_16x16x32_bf16 v[20:23], a[148:151], a[212:215], v[20:23]\n\t"
    "ds_read_b128 a[120:123], v82 offset:7168\n\t"
    "v_cvt_pk_bf16_f32 v32, v0, v1\n\t"
    "v_mfma_f32_16x16x32_bf16 v[20:23], a[144:147], a[208:211], v[20:23]\n\t"
    "ds_read_b128 a[124:127], v82 offset:15360\n\t"
    "v_cvt_pk_bf16_f32 v36, v8, v9\n\t"
    "v_mfma_f32_16x16x32_bf16 v[24:27], a[160:163], a[208:211], 0\n\t"
    "v_exp_f32 v18, v18\n\t"
    "v_mfma_f32_16x16x32_bf16 v[24:27], a[164:167], a[212:215], v[24:27]\n\t"
    "s_add_u32 m0, s16, s11\n\t"
    "v_mfma_f32_16x16x32_bf16 v[24:27], a[168:171], a[216:219], v[24:27]\n\t"
    "buffer_load_dwordx4 v121, s[36:39], s8 offen offset:0 lds\n\t"
    "v_mfma_f32_16x16x32_bf16 v[24:27], a[172:175], a[220:223], v[24:27]\n\t"
    "v_exp_f32 v17, v17\n\t"
    "v_mfma_f32_16x16x32_bf16 v[28:31], a[188:191], a[220:223], 0\n\t"
    "v_exp_f32 v19, v19\n\t"
    "v_mfma_f32_16x16x32_bf16 v[28:31], a[184:187], a[216:219], v[28:31]\n\t"
    "v_exp_f32 v22, v22\n\t"
    "v_mfma_f32_16x16x32_bf16 v[28:31], a[180:183], a[212:215], v[28:31]\n\t"
    "s_mov_b32 s22, s15\n\t"
    "v_mfma_f32_16x16x32_bf16 v[28:31], a[176:179], a[208:211], v[28:31]\n\t"
    "s_mov_b32 s15, s13\n\t"
    "v_smfmac_f32_16x16x64_bf16 v[96:99], v[68:71], v[32:39], v80\n\t"
    "s_waitcnt lgkmcnt(4)\n\t"
    "v_mfma_f32_16x16x32_bf16 a[0:3], a[64:67], v[32:35], a[0:3]\n\t"
    "v_exp_f32 v25, v25\n\t"
    "v_mfma_f32_16x16x32_bf16 a[0:3], a[68:71], v[36:39], a[0:3]\n\t"
    "v_exp_f32 v23, v23\n\t"
    "v_mfma_f32_16x16x32_bf16 a[4:7], a[72:75], v[32:35], a[4:7]\n\t"
    "s_sub_u32 s17, s17, 1\n\t"
    "v_mfma_f32_16x16x32_bf16 a[4:7], a[76:79], v[36:39], a[4:7]\n\t"
    "v_exp_f32 v27, v27\n\t"
    "v_mfma_f32_16x16x32_bf16 a[8:11], a[80:83], v[32:35], a[8:11]\n\t"
    "v_exp_f32 v16, v16\n\t"
    "v_mfma_f32_16x16x32_bf16 a[8:11], a[84:87], v[36:39], a[8:11]\n\t"
    "v_exp_f32 v20, v20\n\t"
    "v_mfma_f32_16x16x32_bf16 a[12:15], a[88:91], v[32:35], a[12:15]\n\t"
    "v_add_u32 v125, 0x2000, v125\n\t"
    "v_mfma_f32_16x16x32_bf16 a[12:15], a[92:95], v[36:39], a[12:15]\n\t"
    "v_mfma_f32_16x16x32_bf16 a[16:19], a[96:99], v[32:35], a[16:19]\n\t"
    "buffer_load_dwordx4 v121, s[36:39], s8 offen offset:1024 lds\n\t"
    "v_mfma_f32_16x16x32_bf16 a[16:19], a[100:103], v[36:39], a[16:19]\n\t"
    "v_exp_f32 v24, v24\n\t"
    "v_mfma_f32_16x16x32_bf16 a[20:23], a[104:107], v[32:35], a[20:23]\n\t"
    "v_cvt_pk_bf16_f32 v40, v16, v17\n\t"
    "v_mfma_f32_16x16x32_bf16 a[20:23], a[108:111], v[36:39], a[20:23]\n\t"
    "v_exp_f32 v21, v21\n\t"
    "s_waitcnt lgkmcnt(0)\n\t"
    "v_mfma_f32_16x16x32_bf16 a[24:27], a[112:115], v[32:35], a[24:27]\n\t"
    "v_exp_f32 v26, v26\n\t"
    "v_mfma_f32_16x16x32_bf16 a[24:27], a[116:119], v[36:39], a[24:27]\n\t"
    "v_cvt_pk_bf16_f32 v45, v26, v27\n\t"
    "v_mfma_f32_16x16x32_bf16 a[28:31], a[120:123], v[32:35], a[28:31]\n\t"
    "v_cvt_pk_bf16_f32 v43, v22, v23\n\t"
    "v_mfma_f32_16x16x32_bf16 a[28:31], a[124:127], v[36:39], a[28:31]\n\t"
    "s_mov_b32 s13, s10\n\t"
    "v_mfma_f32_16x16x32_bf16 v[0:3], a[128:131], v[192:195], 0\n\t"
    "v_cvt_pk_bf16_f32 v44, v24, v25\n\t"
    "v_mfma_f32_16x16x32_bf16 v[0:3], a[132:135], v[196:199], v[0:3]\n\t"
    "buffer_load_dwordx4 v121, s[36:39], s8 offen offset:2048 lds\n\t"
    "v_mfma_f32_16x16x32_bf16 v[0:3], a[136:139], v[200:203], v[0:3]\n\t"
    "v_exp_f32 v30, v30\n\t"
    "v_mfma_f32_16x16x32_bf16 v[0:3], a[140:143], v[204:207], v[0:3]\n\t"
    "v_cvt_pk_bf16_f32 v41, v18, v19\n\t"
    "v_mfma_f32_16x16x32_bf16 v[4:7], a[156:159], v[204:207], 0\n\t"
    "v_add_u32 v81, s13, v78\n\t"
    "v_mfma_f32_16x16x32_bf16 v[4:7], a[152:155], v[200:203], v[4:7]\n\t"
    "v_exp_f32 v31, v31\n\t"
    "v_mfma_f32_16x16x32_bf16 v[4:7], a[148:151], v[196:199], v[4:7]\n\t"
    "v_cvt_pk_bf16_f32 v42, v20, v21\n\t"
    "v_mfma_f32_16x16x32_bf16 v[4:7], a[144:147], v[192:195], v[4:7]\n\t"
    "v_exp_f32 v28, v28\n\t"
    "v_mfma_f32_16x16x32_bf16 v[8:11], a[160:163], v[192:195], 0\n\t"
    "v_exp_f32 v2, v2\n\t"
    "v_mfma_f32_16x16x32_bf16 v[8:11], a[164:167], v[196:199], v[8:11]\n\t"
    "v_exp_f32 v1, v1\n\t"
    "v_mfma_f32_16x16x32_bf16 v[8:11], a[168:171], v[200:203], v[8:11]\n\t"
    "v_cvt_pk_bf16_f32 v47, v30, v31\n\t"
    "v_mfma_f32_16x16x32_bf16 v[8:11], a[172:175], v[204:207], v[8:11]\n\t"
    "v_exp_f32 v0, v0\n\t"
    "v_mfma_f32_16x16x32_bf16 v[12:15], a[188:191], v[204:207], 0\n\t"
    "v_exp_f32 v29, v29\n\t"
    "v_mfma_f32_16x16x32_bf16 v[12:15], a[184:187], v[200:203], v[12:15]\n\t"
    "buffer_load_dwordx4 v121, s[36:39], s8 offen offset:3072 lds\n\t"
    "v_mfma_f32_16x16x32_bf16 v[12:15], a[180:183], v[196:199], v[12:15]\n\t"
    "v_cvt_pk_bf16_f32 v46, v28, v29\n\t"
    "v_mfma_f32_16x16x32_bf16 v[12:15], a[176:179], v[192:195], v[12:15]\n\t"
    "v_exp_f32 v3, v3\n\t"
    "v_smfmac_f32_16x16x64_bf16 v[100:103], v[68:71], v[40:47], v80\n\t"
    "s_mov_b32 s10, s16\n\t"
    "v_mfma_f32_16x16x32_bf16 a[32:35], a[64:67], v[40:43], a[32:35]\n\t"
    "v_exp_f32 v5, v5\n\t"
    "v_mfma_f32_16x16x32_bf16 a[32:35], a[68:71], v[44:47], a[32:35]\n\t"
    "v_exp_f32 v8, v8\n\t"
    "v_mfma_f32_16x16x32_bf16 a[36:39], a[72:75], v[40:43], a[36:39]\n\t"
    "v_cvt_pk_bf16_f32 v32, v0, v1\n\t"
    "v_mfma_f32_16x16x32_bf16 a[36:39], a[76:79], v[44:47], a[36:39]\n\t"
    "v_cvt_pk_bf16_f32 v33, v2, v3\n\t"
    "v_mfma_f32_16x16x32_bf16 a[40:43], a[80:83], v[40:43], a[40:43]\n\t"
    "v_exp_f32 v13, v13\n\t"
    "v_mfma_f32_16x16x32_bf16 a[40:43], a[84:87], v[44:47], a[40:43]\n\t"
    "v_exp_f32 v12, v12\n\t"
    "v_mfma_f32_16x16x32_bf16 a[44:47], a[88:91], v[40:43], a[44:47]\n\t"
    "v_exp_f32 v10, v10\n\t"
    "v_mfma_f32_16x16x32_bf16 a[44:47], a[92:95], v[44:47], a[44:47]\n\t"
    "s_add_u32 m0, s16, s12\n\t"
    "v_mfma_f32_16x16x32_bf16 a[48:51], a[96:99], v[40:43], a[48:51]\n\t"
    "buffer_load_dwordx4 v123, s[36:39], s8 offen offset:0 lds\n\t"
    "v_mfma_f32_16x16x32_bf16 a[48:51], a[100:103], v[44:47], a[48:51]\n\t"
    "s_mov_b32 s16, s14\n\t"
    "v_mfma_f32_16x16x32_bf16 a[52:55], a[104:107], v[40:43], a[52:55]\n\t"
    "v_cvt_pk_bf16_f32 v38, v12, v13\n\t"
    "v_mfma_f32_16x16x32_bf16 a[52:55], a[108:111], v[44:47], a[52:55]\n\t"
    "v_exp_f32 v4, v4\n\t"
    "v_mfma_f32_16x16x32_bf16 a[56:59], a[112:115], v[40:43], a[56:59]\n\t"
    "v_exp_f32 v9, v9\n\t"
    "v_mfma_f32_16x16x32_bf16 a[56:59], a[116:119], v[44:47], a[56:59]\n\t"
    "v_exp_f32 v15, v15\n\t"
    "v_mfma_f32_16x16x32_bf16 a[60:63], a[120:123], v[40:43], a[60:63]\n\t"
    "v_exp_f32 v11, v11\n\t"
    "v_mfma_f32_16x16x32_bf16 a[60:63], a[124:127], v[44:47], a[60:63]\n\t"
    "s_waitcnt vmcnt(6) lgkmcnt(0)\n\t"
    "s_barrier\n\t"
    "v_mfma_f32_16x16x32_bf16 v[16:19], a[128:131], v[208:211], 0\n\t"
    "v_exp_f32 v14, v14\n\t"
    "v_mfma_f32_16x16x32_bf16 v[16:19], a[132:135], v[212:215], v[16:19]\n\t"
    "v_exp_f32 v6, v6\n\t"
    "v_mfma_f32_16x16x32_bf16 v[16:19], a[136:139], v[216:219], v[16:19]\n\t"
    "v_exp_f32 v7, v7\n\t"
    "v_mfma_f32_16x16x32_bf16 v[16:19], a[140:143], v[220:223], v[16:19]\n\t"
    "ds_read_b128 a[128:131], v81 offset:0\n\t"
    "v_mfma_f32_16x16x32_bf16 v[20:23], a[156:159], v[220:223], 0\n\t"
    "ds_read_b128 a[132:135], v81 offset:1024\n\t"
    "v_cvt_pk_bf16_f32 v35, v6, v7\n\t"
    "v_mfma_f32_16x16x32_bf16 v[20:23], a[152:155], v[216:219], v[20:23]\n\t"
    "ds_read_b128 a[136:139], v81 offset:2048\n\t"
    "ds_read_b128 a[140:143], v81 offset:3072\n\t"
    "v_mfma_f32_16x16x32_bf16 v[20:23], a[148:151], v[212:215], v[20:23]\n\t"
    "buffer_load_dwordx4 v123, s[36:39], s8 offen offset:1024 lds\n\t"
    "v_mfma_f32_16x16x32_bf16 v[20:23], a[144:147], v[208:211], v[20:23]\n\t"
    "v_cvt_pk_bf16_f32 v34, v4, v5\n\t"
    "s_mov_b32 s14, s22\n\t"
    "v_mfma_f32_16x16x32_bf16 v[24:27], a[160:163], v[208:211], 0\n\t"
    "ds_read_b128 a[156:159], v81 offset:7168\n\t"
    "ds_read_b128 a[152:155], v81 offset:6144\n\t"
    "v_mfma_f32_16x16x32_bf16 v[24:27], a[164:167], v[212:215], v[24:27]\n\t"
    "v_cvt_pk_bf16_f32 v36, v8, v9\n\t"
    "v_mfma_f32_16x16x32_bf16 v[24:27], a[168:171], v[216:219], v[24:27]\n\t"
    "ds_read_b128 a[148:151], v81 offset:5120\n\t"
    "v_cvt_pk_bf16_f32 v39, v14, v15\n\t"
    "v_mfma_f32_16x16x32_bf16 v[24:27], a[172:175], v[220:223], v[24:27]\n\t"
    "v_exp_f32 v17, v17\n\t"
    "v_mfma_f32_16x16x32_bf16 v[28:31], a[188:191], v[220:223], 0\n\t"
    "ds_read_b128 a[144:147], v81 offset:4096\n\t"
    "v_mfma_f32_16x16x32_bf16 v[28:31], a[184:187], v[216:219], v[28:31]\n\t"
    "ds_read_b128 a[160:163], v81 offset:8192\n\t"
    "v_cvt_pk_bf16_f32 v37, v10, v11\n\t"
    "v_mfma_f32_16x16x32_bf16 v[28:31], a[180:183], v[212:215], v[28:31]\n\t"
    "ds_read_b128 a[164:167], v81 offset:9216\n\t"
    "ds_read_b128 a[168:171], v81 offset:10240\n\t"
    "v_mfma_f32_16x16x32_bf16 v[28:31], a[176:179], v[208:211], v[28:31]\n\t"
    "v_add_u32 v82, s14, v78\n\t"
    "v_smfmac_f32_16x16x64_bf16 v[104:107], v[68:71], v[32:39], v80\n\t"
    "buffer_load_dwordx4 v123, s[36:39], s8 offen offset:2048 lds\n\t"
    "v_mfma_f32_16x16x32_bf16 v[128:131], a[64:67], v[32:35], v[128:131]\n\t"
    "ds_read_b128 a[172:175], v81 offset:11264\n\t"
    "v_mfma_f32_16x16x32_bf16 v[128:131], a[68:71], v[36:39], v[128:131]\n\t"
    "v_exp_f32 v24, v24\n\t"
    "v_mfma_f32_16x16x32_bf16 v[132:135], a[72:75], v[32:35], v[132:135]\n\t"
    "v_exp_f32 v27, v27\n\t"
    "v_mfma_f32_16x16x32_bf16 v[132:135], a[76:79], v[36:39], v[132:135]\n\t"
    "ds_read_b128 a[188:191], v81 offset:15360\n\t"
    "ds_read_b128 a[184:187], v81 offset:14336\n\t"
    "v_mfma_f32_16x16x32_bf16 v[136:139], a[80:83], v[32:35], v[136:139]\n\t"
    "v_exp_f32 v22, v22\n\t"
    "v_mfma_f32_16x16x32_bf16 v[136:139], a[84:87], v[36:39], v[136:139]\n\t"
    "v_exp_f32 v18, v18\n\t"
    "v_mfma_f32_16x16x32_bf16 v[140:143], a[88:91], v[32:35], v[140:143]\n\t"
    "v_exp_f32 v16, v16\n\t"
    "v_mfma_f32_16x16x32_bf16 v[140:143], a[92:95], v[36:39], v[140:143]\n\t"
    "v_exp_f32 v26, v26\n\t"
    "v_mfma_f32_16x16x32_bf16 v[144:147], a[96:99], v[32:35], v[144:147]\n\t"
    "v_exp_f32 v25, v25\n\t"
    "v_mfma_f32_16x16x32_bf16 v[144:147], a[100:103], v[36:39], v[144:147]\n\t"
    "ds_read_b128 a[180:183], v81 offset:13312\n\t"
    "v_cvt_pk_bf16_f32 v44, v24, v25\n\t"
    "v_mfma_f32_16x16x32_bf16 v[148:151], a[104:107], v[32:35], v[148:151]\n\t"
    "v_exp_f32 v29, v29\n\t"
    "v_mfma_f32_16x16x32_bf16 v[148:151], a[108:111], v[36:39], v[148:151]\n\t"
    "v_exp_f32 v19, v19\n\t"
    "v_mfma_f32_16x16x32_bf16 v[152:155], a[112:115], v[32:35], v[152:155]\n\t"
    "v_cvt_pk_bf16_f32 v41, v18, v19\n\t"
    "v_mfma_f32_16x16x32_bf16 v[152:155], a[116:119], v[36:39], v[152:155]\n\t"
    "buffer_load_dwordx4 v123, s[36:39], s8 offen offset:3072 lds\n\t"
    "v_mfma_f32_16x16x32_bf16 v[156:159], a[120:123], v[32:35], v[156:159]\n\t"
    "v_cvt_pk_bf16_f32 v40, v16, v17\n\t"
    "v_mfma_f32_16x16x32_bf16 v[156:159], a[124:127], v[36:39], v[156:159]\n\t"
    "ds_read_b128 a[176:179], v81 offset:12288\n\t"
    "s_waitcnt lgkmcnt(4)\n\t"
    "v_mfma_f32_16x16x32_bf16 v[0:3], a[128:131], a[192:195], 0\n\t"
    "v_cvt_pk_bf16_f32 v45, v26, v27\n\t"
    "v_mfma_f32_16x16x32_bf16 v[0:3], a[132:135], a[196:199], v[0:3]\n\t"
    "v_exp_f32 v28, v28\n\t"
    "v_mfma_f32_16x16x32_bf16 v[0:3], a[136:139], a[200:203], v[0:3]\n\t"
    "v_exp_f32 v23, v23\n\t"
    "v_mfma_f32_16x16x32_bf16 v[0:3], a[140:143], a[204:207], v[0:3]\n\t"
    "v_exp_f32 v31, v31\n\t"
    "v_mfma_f32_16x16x32_bf16 v[4:7], a[156:159], a[204:207], 0\n\t"
    "v_exp_f32 v20, v20\n\t"
    "v_mfma_f32_16x16x32_bf16 v[4:7], a[152:155], a[200:203], v[4:7]\n\t"
    "v_exp_f32 v21, v21\n\t"
    "v_mfma_f32_16x16x32_bf16 v[4:7], a[148:151], a[196:199], v[4:7]\n\t"
    "v_cvt_pk_bf16_f32 v46, v28, v29\n\t"
    "v_mfma_f32_16x16x32_bf16 v[4:7], a[144:147], a[192:195], v[4:7]\n\t"
    "v_cvt_pk_bf16_f32 v42, v20, v21\n\t"
    "v_mfma_f32_16x16x32_bf16 v[8:11], a[160:163], a[192:195], v[64:67]\n\t"
    "v_exp_f32 v2, v2\n\t"
    "v_mfma_f32_16x16x32_bf16 v[8:11], a[164:167], a[196:199], v[8:11]\n\t"
    "v_exp_f32 v3, v3\n\t"
    "v_mfma_f32_16x16x32_bf16 v[8:11], a[168:171], a[200:203], v[8:11]\n\t"
    "v_exp_f32 v30, v30\n\t"
    "v_mfma_f32_16x16x32_bf16 v[8:11], a[172:175], a[204:207], v[8:11]\n\t"
    "v_cvt_pk_bf16_f32 v47, v30, v31\n\t"
    "v_cvt_pk_bf16_f32 v33, v2, v3\n\t"
    "s_waitcnt lgkmcnt(1)\n\t"
    "v_mfma_f32_16x16x32_bf16 v[12:15], a[188:191], a[204:207], v[92:95]\n\t"
    "v_exp_f32 v5, v5\n\t"
    "v_mfma_f32_16x16x32_bf16 v[12:15], a[184:187], a[200:203], v[12:15]\n\t"
    "v_exp_f32 v0, v0\n\t"
    "v_mfma_f32_16x16x32_bf16 v[12:15], a[180:183], a[196:199], v[12:15]\n\t"
    "v_cvt_pk_bf16_f32 v43, v22, v23\n\t"
    "s_waitcnt lgkmcnt(0)\n\t"
    "v_mfma_f32_16x16x32_bf16 v[12:15], a[176:179], a[192:195], v[12:15]\n\t"
    "buffer_load_dword v244, v125, s[28:31], 0 offen\n\t"
    "v_smfmac_f32_16x16x64_bf16 v[108:111], v[68:71], v[40:47], v80\n\t"
    "v_exp_f32 v1, v1\n\t"
    "v_mfma_f32_16x16x32_bf16 v[160:163], a[64:67], v[40:43], v[160:163]\n\t"
    "v_exp_f32 v9, v9\n\t"
    "v_mfma_f32_16x16x32_bf16 v[160:163], a[68:71], v[44:47], v[160:163]\n\t"
    "v_exp_f32 v7, v7\n\t"
    "v_mfma_f32_16x16x32_bf16 v[164:167], a[72:75], v[40:43], v[164:167]\n\t"
    "v_exp_f32 v4, v4\n\t"
    "v_mfma_f32_16x16x32_bf16 v[164:167], a[76:79], v[44:47], v[164:167]\n\t"
    "v_exp_f32 v12, v12\n\t"
    "v_mfma_f32_16x16x32_bf16 v[168:171], a[80:83], v[40:43], v[168:171]\n\t"
    "v_exp_f32 v8, v8\n\t"
    "v_mfma_f32_16x16x32_bf16 v[168:171], a[84:87], v[44:47], v[168:171]\n\t"
    "v_exp_f32 v13, v13\n\t"
    "v_mfma_f32_16x16x32_bf16 v[172:175], a[88:91], v[40:43], v[172:175]\n\t"
    "ds_read_b128 a[64:67], v82 offset:0\n\t"
    "ds_read_b128 a[68:71], v82 offset:8192\n\t"
    "v_mfma_f32_16x16x32_bf16 v[172:175], a[92:95], v[44:47], v[172:175]\n\t"
    "v_exp_f32 v11, v11\n\t"
    "v_mfma_f32_16x16x32_bf16 v[176:179], a[96:99], v[40:43], v[176:179]\n\t"
    "v_exp_f32 v14, v14\n\t"
    "v_mfma_f32_16x16x32_bf16 v[176:179], a[100:103], v[44:47], v[176:179]\n\t"
    "ds_read_b128 a[72:75], v82 offset:1024\n\t"
    "ds_read_b128 a[76:79], v82 offset:9216\n\t"
    "v_mfma_f32_16x16x32_bf16 v[180:183], a[104:107], v[40:43], v[180:183]\n\t"
    "v_exp_f32 v6, v6\n\t"
    "v_mfma_f32_16x16x32_bf16 v[180:183], a[108:111], v[44:47], v[180:183]\n\t"
    "ds_read_b128 a[80:83], v82 offset:2048\n\t"
    "ds_read_b128 a[84:87], v82 offset:10240\n\t"
    "v_mfma_f32_16x16x32_bf16 v[184:187], a[112:115], v[40:43], v[184:187]\n\t"
    "v_exp_f32 v15, v15\n\t"
    "v_mfma_f32_16x16x32_bf16 v[184:187], a[116:119], v[44:47], v[184:187]\n\t"
    "v_cvt_pk_bf16_f32 v38, v12, v13\n\t"
    "v_cvt_pk_bf16_f32 v39, v14, v15\n\t"
    "v_mfma_f32_16x16x32_bf16 v[188:191], a[120:123], v[40:43], v[188:191]\n\t"
    "v_cvt_pk_bf16_f32 v34, v4, v5\n\t"
    "s_cmp_gt_u32 s17, 0\n\t"
    "v_mfma_f32_16x16x32_bf16 v[188:191], a[124:127], v[44:47], v[188:191]\n\t"
    "s_cbranch_scc1 L_A_%=\n\t"
    "L_L_%=:\n\t"
    "s_add_u32 m0, s15, s11\n\t"
    "v_add_u32 v125, 0x1000, v125\n\t"
    "buffer_load_dwordx4 v121, s[40:43], s8 offen offset:0 lds\n\t"
    "v_add_f32 v74, v104, v105\n\t"
    "v_add_f32 v75, v108, v109\n\t"
    "v_rcp_f32 v124, v74\n\t"
    "v_rcp_f32 v126, v75\n\t"
    "v_subrev_u32 v114, 0x1f800000, v74\n\t"
    "v_subrev_u32 v115, 0x1f800000, v75\n\t"
    "buffer_load_dwordx4 v121, s[40:43], s8 offen offset:1024 lds\n\t"
    "v_max_u32 v112, v114, v115\n\t"
    "v_cmp_lt_u32 vcc, 0x40000000, v112\n\t"
    "v_pk_mul_f32 v[128:129], v[128:129], v[124:125] op_sel_hi:[1,0]\n\t"
    "v_pk_mul_f32 v[130:131], v[130:131], v[124:125] op_sel_hi:[1,0]\n\t"
    "v_pk_mul_f32 v[132:133], v[132:133], v[124:125] op_sel_hi:[1,0]\n\t"
    "buffer_load_dwordx4 v121, s[40:43], s8 offen offset:2048 lds\n\t"
    "v_pk_mul_f32 v[134:135], v[134:135], v[124:125] op_sel_hi:[1,0]\n\t"
    "v_cvt_pk_bf16_f32 v192, v128, v129\n\t"
    "v_cvt_pk_bf16_f32 v193, v130, v131\n\t"
    "v_cvt_pk_bf16_f32 v194, v132, v133\n\t"
    "v_cvt_pk_bf16_f32 v195, v134, v135\n\t"
    "global_store_dwordx4 v76, v[192:195], s[46:47] offset:0 sc1\n\t"
    "buffer_load_dwordx4 v121, s[40:43], s8 offen offset:3072 lds\n\t"
    "v_mov_b32 v104, 0\n\t"
    "v_mov_b32 v105, 0\n\t"
    "v_pk_mul_f32 v[136:137], v[136:137], v[124:125] op_sel_hi:[1,0]\n\t"
    "v_pk_mul_f32 v[138:139], v[138:139], v[124:125] op_sel_hi:[1,0]\n\t"
    "v_pk_mul_f32 v[140:141], v[140:141], v[124:125] op_sel_hi:[1,0]\n\t"
    "v_pk_mul_f32 v[142:143], v[142:143], v[124:125] op_sel_hi:[1,0]\n\t"
    "s_add_u32 m0, s15, s12\n\t"
    "v_add_u32 v125, 0x1000, v125\n\t"
    "buffer_load_dwordx4 v123, s[40:43], s8 offen offset:0 lds\n\t"
    "v_cvt_pk_bf16_f32 v196, v136, v137\n\t"
    "v_cvt_pk_bf16_f32 v197, v138, v139\n\t"
    "v_cvt_pk_bf16_f32 v198, v140, v141\n\t"
    "v_cvt_pk_bf16_f32 v199, v142, v143\n\t"
    "v_mfma_f32_32x32x16_bf16 v[128:143], v[116:119], v[116:119], 0\n\t"
    "global_store_dwordx4 v76, v[196:199], s[46:47] offset:1024 sc1\n\t"
    "buffer_load_dwordx4 v123, s[40:43], s8 offen offset:1024 lds\n\t"
    "v_pk_mul_f32 v[144:145], v[144:145], v[124:125] op_sel_hi:[1,0]\n\t"
    "v_pk_mul_f32 v[146:147], v[146:147], v[124:125] op_sel_hi:[1,0]\n\t"
    "v_pk_mul_f32 v[148:149], v[148:149], v[124:125] op_sel_hi:[1,0]\n\t"
    "v_pk_mul_f32 v[150:151], v[150:151], v[124:125] op_sel_hi:[1,0]\n\t"
    "v_cvt_pk_bf16_f32 v200, v144, v145\n\t"
    "buffer_load_dwordx4 v123, s[40:43], s8 offen offset:2048 lds\n\t"
    "v_cvt_pk_bf16_f32 v201, v146, v147\n\t"
    "v_cvt_pk_bf16_f32 v202, v148, v149\n\t"
    "v_cvt_pk_bf16_f32 v203, v150, v151\n\t"
    "global_store_dwordx4 v76, v[200:203], s[46:47] offset:2048 sc1\n\t"
    "v_pk_mul_f32 v[152:153], v[152:153], v[124:125] op_sel_hi:[1,0]\n\t"
    "v_pk_mul_f32 v[154:155], v[154:155], v[124:125] op_sel_hi:[1,0]\n\t"
    "buffer_load_dwordx4 v123, s[40:43], s8 offen offset:3072 lds\n\t"
    "s_add_u32 s8, s8, 0x8000\n\t"
    "s_cmp_eq_u32 s8, s9\n\t"
    "s_cselect_b32 s8, s23, s8\n\t"
    "v_pk_mul_f32 v[156:157], v[156:157], v[124:125] op_sel_hi:[1,0]\n\t"
    "v_pk_mul_f32 v[158:159], v[158:159], v[124:125] op_sel_hi:[1,0]\n\t"
    "v_cvt_pk_bf16_f32 v204, v152, v153\n\t"
    "v_cvt_pk_bf16_f32 v205, v154, v155\n\t"
    "v_cvt_pk_bf16_f32 v206, v156, v157\n\t"
    "v_cvt_pk_bf16_f32 v207, v158, v159\n\t"
    "v_mfma_f32_32x32x16_bf16 v[144:159], v[116:119], v[116:119], 0\n\t"
    "global_store_dwordx4 v76, v[204:207], s[46:47] offset:3072 sc1\n\t"
    "s_cmp_lg_u64 vcc, 0\n\t"
    "s_cselect_b32 s24, 2, 0\n\t"
    "s_add_u32 s46, s46, 0x80000\n\t"
    "s_addc_u32 s47, s47, 0\n\t"
    "v_pk_mul_f32 v[160:161], v[160:161], v[126:127] op_sel_hi:[1,0]\n\t"
    "v_pk_mul_f32 v[162:163], v[162:163], v[126:127] op_sel_hi:[1,0]\n\t"
    "v_pk_mul_f32 v[164:165], v[164:165], v[126:127] op_sel_hi:[1,0]\n\t"
    "v_pk_mul_f32 v[166:167], v[166:167], v[126:127] op_sel_hi:[1,0]\n\t"
    "v_cvt_pk_bf16_f32 v208, v160, v161\n\t"
    "v_cvt_pk_bf16_f32 v209, v162, v163\n\t"
    "v_cvt_pk_bf16_f32 v210, v164, v165\n\t"
    "v_cvt_pk_bf16_f32 v211, v166, v167\n\t"
    "global_store_dwordx4 v76, v[208:211], s[46:47] offset:0 sc1\n\t"
    "v_mov_b32 v108, 0\n\t"
    "v_mov_b32 v109, 0\n\t"
    "v_pk_mul_f32 v[168:169], v[168:169], v[126:127] op_sel_hi:[1,0]\n\t"
    "v_pk_mul_f32 v[170:171], v[170:171], v[126:127] op_sel_hi:[1,0]\n\t"
    "v_pk_mul_f32 v[172:173], v[172:173], v[126:127] op_sel_hi:[1,0]\n\t"
    "v_pk_mul_f32 v[174:175], v[174:175], v[126:127] op_sel_hi:[1,0]\n\t"
    "v_cvt_pk_bf16_f32 v212, v168, v169\n\t"
    "v_cvt_pk_bf16_f32 v213, v170, v171\n\t"
    "v_cvt_pk_bf16_f32 v214, v172, v173\n\t"
    "v_cvt_pk_bf16_f32 v215, v174, v175\n\t"
    "v_mfma_f32_32x32x16_bf16 v[160:175], v[116:119], v[116:119], 0\n\t"
    "global_store_dwordx4 v76, v[212:215], s[46:47] offset:1024 sc1\n\t"
    "v_pk_mul_f32 v[176:177], v[176:177], v[126:127] op_sel_hi:[1,0]\n\t"
    "v_pk_mul_f32 v[178:179], v[178:179], v[126:127] op_sel_hi:[1,0]\n\t"
    "v_pk_mul_f32 v[180:181], v[180:181], v[126:127] op_sel_hi:[1,0]\n\t"
    "v_pk_mul_f32 v[182:183], v[182:183], v[126:127] op_sel_hi:[1,0]\n\t"
    "v_cvt_pk_bf16_f32 v216, v176, v177\n\t"
    "v_cvt_pk_bf16_f32 v217, v178, v179\n\t"
    "v_cvt_pk_bf16_f32 v218, v180, v181\n\t"
    "v_cvt_pk_bf16_f32 v219, v182, v183\n\t"
    "global_store_dwordx4 v76, v[216:219], s[46:47] offset:2048 sc1\n\t"
    "v_pk_mul_f32 v[184:185], v[184:185], v[126:127] op_sel_hi:[1,0]\n\t"
    "v_pk_mul_f32 v[186:187], v[186:187], v[126:127] op_sel_hi:[1,0]\n\t"
    "v_pk_mul_f32 v[188:189], v[188:189], v[126:127] op_sel_hi:[1,0]\n\t"
    "v_pk_mul_f32 v[190:191], v[190:191], v[126:127] op_sel_hi:[1,0]\n\t"
    "v_cvt_pk_bf16_f32 v220, v184, v185\n\t"
    "v_cvt_pk_bf16_f32 v221, v186, v187\n\t"
    "v_cvt_pk_bf16_f32 v222, v188, v189\n\t"
    "v_cvt_pk_bf16_f32 v223, v190, v191\n\t"
    "v_mfma_f32_32x32x16_bf16 v[176:191], v[116:119], v[116:119], 0\n\t"
    "global_store_dwordx4 v76, v[220:223], s[46:47] offset:3072 sc1\n\t"
    "buffer_load_dword v244, v125, s[28:31], 0 offen\n\t"
    "s_lshl_b32 s17, s60, 1\n\t"
    "s_sub_u32 s17, 14, s17\n\t"
    "s_cmp_eq_u32 s17, 0\n\t"
    "s_cbranch_scc1 L_E_%=\n\t"
    "L_N_%=:\n\t"
    "v_mfma_f32_16x16x32_bf16 v[16:19], a[128:131], a[208:211], 0\n\t"
    "ds_read_b128 a[88:91], v82 offset:3072\n\t"
    "ds_read_b128 a[92:95], v82 offset:11264\n\t"
    "v_mfma_f32_16x16x32_bf16 v[16:19], a[132:135], a[212:215], v[16:19]\n\t"
    "ds_read_b128 a[96:99], v82 offset:4096\n\t"
    "ds_read_b128 a[100:103], v82 offset:12288\n\t"
    "v_mfma_f32_16x16x32_bf16 v[16:19], a[136:139], a[216:219], v[16:19]\n\t"
    "ds_read_b128 a[104:107], v82 offset:5120\n\t"
    "ds_read_b128 a[108:111], v82 offset:13312\n\t"
    "v_mfma_f32_16x16x32_bf16 v[16:19], a[140:143], a[220:223], v[16:19]\n\t"
    "v_exp_f32 v10, v10\n\t"
    "v_mfma_f32_16x16x32_bf16 v[20:23], a[156:159], a[220:223], 0\n\t"
    "v_cvt_pk_bf16_f32 v35, v6, v7\n\t"
    "v_cvt_pk_bf16_f32 v37, v10, v11\n\t"
    "v_mfma_f32_16x16x32_bf16 v[20:23], a[152:155], a[216:219], v[20:23]\n\t"
    "ds_read_b128 a[112:115], v82 offset:6144\n\t"
    "ds_read_b128 a[116:119], v82 offset:14336\n\t"
    "v_mfma_f32_16x16x32_bf16 v[20:23], a[148:151], a[212:215], v[20:23]\n\t"
    "ds_read_b128 a[120:123], v82 offset:7168\n\t"
    "v_cvt_pk_bf16_f32 v32, v0, v1\n\t"
    "v_mfma_f32_16x16x32_bf16 v[20:23], a[144:147], a[208:211], v[20:23]\n\t"
    "ds_read_b128 a[124:127], v82 offset:15360\n\t"
    "v_cvt_pk_bf16_f32 v36, v8, v9\n\t"
    "v_mfma_f32_16x16x32_bf16 v[24:27], a[160:163], a[208:211], 0\n\t"
    "s_add_u32 m0, s16, s11\n\t"
    "v_mfma_f32_16x16x32_bf16 v[24:27], a[164:167], a[212:215], v[24:27]\n\t"
    "v_exp_f32 v18, v18\n\t"
    "v_mfma_f32_16x16x32_bf16 v[24:27], a[168:171], a[216:219], v[24:27]\n\t"
    "v_exp_f32 v16, v16\n\t"
    "v_mfma_f32_16x16x32_bf16 v[24:27], a[172:175], a[220:223], v[24:27]\n\t"
    "buffer_load_dwordx4 v121, s[36:39], s8 offen offset:0 lds\n\t"
    "v_mfma_f32_16x16x32_bf16 v[28:31], a[188:191], a[220:223], 0\n\t"
    "v_exp_f32 v20, v20\n\t"
    "v_mfma_f32_16x16x32_bf16 v[28:31], a[184:187], a[216:219], v[28:31]\n\t"
    "v_exp_f32 v19, v19\n\t"
    "v_mfma_f32_16x16x32_bf16 v[28:31], a[180:183], a[212:215], v[28:31]\n\t"
    "s_mov_b32 s22, s15\n\t"
    "v_mfma_f32_16x16x32_bf16 v[28:31], a[176:179], a[208:211], v[28:31]\n\t"
    "v_exp_f32 v23, v23\n\t"
    "v_smfmac_f32_16x16x64_bf16 v[96:99], v[68:71], v[32:39], v80\n\t"
    "s_waitcnt lgkmcnt(4)\n\t"
    "v_mfma_f32_16x16x32_bf16 a[0:3], a[64:67], v[32:35], a[0:3]\n\t"
    "s_sub_u32 s17, s17, 1\n\t"
    "v_mfma_f32_16x16x32_bf16 a[0:3], a[68:71], v[36:39], a[0:3]\n\t"
    "buffer_load_dwordx4 v121, s[36:39], s8 offen offset:1024 lds\n\t"
    "v_mfma_f32_16x16x32_bf16 a[4:7], a[72:75], v[32:35], a[4:7]\n\t"
    "v_exp_f32 v25, v25\n\t"
    "v_mfma_f32_16x16x32_bf16 a[4:7], a[76:79], v[36:39], a[4:7]\n\t"
    "v_exp_f32 v30, v30\n\t"
    "v_mfma_f32_16x16x32_bf16 a[8:11], a[80:83], v[32:35], a[8:11]\n\t"
    "Lis10_%=:\n\t"
    "s_cbranch_execz Lis14_%=\n\t"
    "v_exp_f32 v21, v21\n\t"
    "v_mfma_f32_16x16x32_bf16 a[8:11], a[84:87], v[36:39], a[8:11]\n\t"
    "v_exp_f32 v17, v17\n\t"
    "v_mfma_f32_16x16x32_bf16 a[12:15], a[88:91], v[32:35], a[12:15]\n\t"
    "v_exp_f32 v29, v29\n\t"
    "v_mfma_f32_16x16x32_bf16 a[12:15], a[92:95], v[36:39], a[12:15]\n\t"
    "v_cvt_pk_bf16_f32 v40, v16, v17\n\t"
    "v_mfma_f32_16x16x32_bf16 a[16:19], a[96:99], v[32:35], a[16:19]\n\t"
    "v_exp_f32 v22, v22\n\t"
    "v_mfma_f32_16x16x32_bf16 a[16:19], a[100:103], v[36:39], a[16:19]\n\t"
    "v_exp_f32 v26, v26\n\t"
    "v_mfma_f32_16x16x32_bf16 a[20:23], a[104:107], v[32:35], a[20:23]\n\t"
    "buffer_load_dwordx4 v121, s[36:39], s8 offen offset:2048 lds\n\t"
    "v_mfma_f32_16x16x32_bf16 a[20:23], a[108:111], v[36:39], a[20:23]\n\t"
    "v_exp_f32 v31, v31\n\t"
    "s_waitcnt lgkmcnt(0)\n\t"
    "v_mfma_f32_16x16x32_bf16 a[24:27], a[112:115], v[32:35], a[24:27]\n\t"
    "v_cvt_pk_bf16_f32 v41, v18, v19\n\t"
    "s_mov_b32 s15, s13\n\t"
    "v_mfma_f32_16x16x32_bf16 a[24:27], a[116:119], v[36:39], a[24:27]\n\t"
    "v_exp_f32 v24, v24\n\t"
    "v_mfma_f32_16x16x32_bf16 a[28:31], a[120:123], v[32:35], a[28:31]\n\t"
    "v_exp_f32 v28, v28\n\t"
    "v_mfma_f32_16x16x32_bf16 a[28:31], a[124:127], v[36:39], a[28:31]\n\t"
    "v_cvt_pk_bf16_f32 v43, v22, v23\n\t"
    "v_cvt_pk_bf16_f32 v44, v24, v25\n\t"
    "v_mfma_f32_16x16x32_bf16 v[0:3], a[128:131], a[224:227], 0\n\t"
    "v_exp_f32 v27, v27\n\t"
    "v_mfma_f32_16x16x32_bf16 v[0:3], a[132:135], a[228:231], v[0:3]\n\t"
    "buffer_load_dwordx4 v121, s[36:39], s8 offen offset:3072 lds\n\t"
    "v_mfma_f32_16x16x32_bf16 v[0:3], a[136:139], a[232:235], v[0:3]\n\t"
    "v_add_u32 v125, 0x2000, v125\n\t"
    "v_mfma_f32_16x16x32_bf16 v[0:3], a[140:143], a[236:239], v[0:3]\n\t"
    "v_cvt_pk_bf16_f32 v47, v30, v31\n\t"
    "s_add_u32 m0, s16, s12\n\t"
    "v_mfma_f32_16x16x32_bf16 v[4:7], a[156:159], a[236:239], 0\n\t"
    "s_mov_b32 s13, s10\n\t"
    "s_mov_b32 s10, s16\n\t"
    "v_mfma_f32_16x16x32_bf16 v[4:7], a[152:155], a[232:235], v[4:7]\n\t"
    "v_cvt_pk_bf16_f32 v45, v26, v27\n\t"
    "v_mfma_f32_16x16x32_bf16 v[4:7], a[148:151], a[228:231], v[4:7]\n\t"
    "buffer_load_dwordx4 v123, s[36:39], s8 offen offset:0 lds\n\t"
    "v_mfma_f32_16x16x32_bf16 v[4:7], a[144:147], a[224:227], v[4:7]\n\t"
    "v_add_u32 v81, s13, v78\n\t"
    "v_mfma_f32_16x16x32_bf16 v[8:11], a[160:163], a[224:227], 0\n\t"
    "v_exp_f32 v2, v2\n\t"
    "v_mfma_f32_16x16x32_bf16 v[8:11], a[164:167], a[228:231], v[8:11]\n\t"
    "v_cvt_pk_bf16_f32 v46, v28, v29\n\t"
    "s_mov_b32 s16, s14\n\t"
    "v_mfma_f32_16x16x32_bf16 v[8:11], a[168:171], a[232:235], v[8:11]\n\t"
    "v_exp_f32 v3, v3\n\t"
    "v_mfma_f32_16x16x32_bf16 v[8:11], a[172:175], a[236:239], v[8:11]\n\t"
    "v_exp_f32 v0, v0\n\t"
    "v_mfma_f32_16x16x32_bf16 v[12:15], a[188:191], a[236:239], 0\n\t"
    "s_mov_b32 s14, s22\n\t"
    "v_mfma_f32_16x16x32_bf16 v[12:15], a[184:187], a[232:235], v[12:15]\n\t"
    "v_cvt_pk_bf16_f32 v42, v20, v21\n\t"
    "v_mfma_f32_16x16x32_bf16 v[12:15], a[180:183], a[228:231], v[12:15]\n\t"
    "buffer_load_dwordx4 v123, s[36:39], s8 offen offset:1024 lds\n\t"
    "v_mfma_f32_16x16x32_bf16 v[12:15], a[176:179], a[224:227], v[12:15]\n\t"
    "v_exp_f32 v6, v6\n\t"
    "v_smfmac_f32_16x16x64_bf16 v[100:103], v[68:71], v[40:47], v80\n\t"
    "v_exp_f32 v4, v4\n\t"
    "v_mfma_f32_16x16x32_bf16 a[32:35], a[64:67], v[40:43], a[32:35]\n\t"
    "v_exp_f32 v7, v7\n\t"
    "v_mfma_f32_16x16x32_bf16 a[32:35], a[68:71], v[44:47], a[32:35]\n\t"
    "v_exp_f32 v1, v1\n\t"
    "v_mfma_f32_16x16x32_bf16 a[36:39], a[72:75], v[40:43], a[36:39]\n\t"
    "v_cvt_pk_bf16_f32 v33, v2, v3\n\t"
    "v_add_u32 v82, s14, v78\n\t"
    "v_mfma_f32_16x16x32_bf16 a[36:39], a[76:79], v[44:47], a[36:39]\n\t"
    "buffer_load_dwordx4 v123, s[36:39], s8 offen offset:2048 lds\n\t"
    "v_mfma_f32_16x16x32_bf16 a[40:43], a[80:83], v[40:43], a[40:43]\n\t"
    "v_exp_f32 v14, v14\n\t"
    "v_mfma_f32_16x16x32_bf16 a[40:43], a[84:87], v[44:47], a[40:43]\n\t"
    "v_exp_f32 v12, v12\n\t"
    "v_mfma_f32_16x16x32_bf16 a[44:47], a[88:91], v[40:43], a[44:47]\n\t"
    "v_exp_f32 v13, v13\n\t"
    "v_mfma_f32_16x16x32_bf16 a[44:47], a[92:95], v[44:47], a[44:47]\n\t"
    "v_exp_f32 v11, v11\n\t"
    "v_mfma_f32_16x16x32_bf16 a[48:51], a[96:99], v[40:43], a[48:51]\n\t"
    "v_cvt_pk_bf16_f32 v32, v0, v1\n\t"
    "v_cvt_pk_bf16_f32 v35, v6, v7\n\t"
    "v_mfma_f32_16x16x32_bf16 a[48:51], a[100:103], v[44:47], a[48:51]\n\t"
    "v_exp_f32 v5, v5\n\t"
    "v_mfma_f32_16x16x32_bf16 a[52:55], a[104:107], v[40:43], a[52:55]\n\t"
    "v_exp_f32 v10, v10\n\t"
    "v_mfma_f32_16x16x32_bf16 a[52:55], a[108:111], v[44:47], a[52:55]\n\t"
    "v_exp_f32 v15, v15\n\t"
    "v_mfma_f32_16x16x32_bf16 a[56:59], a[112:115], v[40:43], a[56:59]\n\t"
    "buffer_load_dwordx4 v123, s[36:39], s8 offen offset:3072 lds\n\t"
    "v_mfma_f32_16x16x32_bf16 a[56:59], a[116:119], v[44:47], a[56:59]\n\t"
    "v_exp_f32 v9, v9\n\t"
    "v_mfma_f32_16x16x32_bf16 a[60:63], a[120:123], v[40:43], a[60:63]\n\t"
    "v_cvt_pk_bf16_f32 v38, v12, v13\n\t"
    "v_mfma_f32_16x16x32_bf16 a[60:63], a[124:127], v[44:47], a[60:63]\n\t"
    "s_waitcnt vmcnt(9) lgkmcnt(0)\n\t"
    "s_barrier\n\t"
    "v_mfma_f32_16x16x32_bf16 v[16:19], a[128:131], a[240:243], 0\n\t"
    "v_exp_f32 v8, v8\n\t"
    "v_mfma_f32_16x16x32_bf16 v[16:19], a[132:135], a[244:247], v[16:19]\n\t"
    "s_add_u32 m0, s15, s11\n\t"
    "v_mfma_f32_16x16x32_bf16 v[16:19], a[136:139], a[248:251], v[16:19]\n\t"
    "ds_read_b128 a[128:131], v81 offset:0\n\t"
    "ds_read_b128 a[132:135], v81 offset:1024\n\t"
    "v_mfma_f32_16x16x32_bf16 v[16:19], a[140:143], a[252:255], v[16:19]\n\t"
    "v_cvt_pk_bf16_f32 v34, v4, v5\n\t"
    "v_cvt_pk_bf16_f32 v36, v8, v9\n\t"
    "v_mfma_f32_16x16x32_bf16 v[20:23], a[156:159], a[252:255], 0\n\t"
    "ds_read_b128 a[136:139], v81 offset:2048\n\t"
    "ds_read_b128 a[140:143], v81 offset:3072\n\t"
    "v_mfma_f32_16x16x32_bf16 v[20:23], a[152:155], a[248:251], v[20:23]\n\t"
    "buffer_load_dwordx4 v121, s[40:43], s8 offen offset:0 lds\n\t"
    "v_mfma_f32_16x16x32_bf16 v[20:23], a[148:151], a[244:247], v[20:23]\n\t"
    "ds_read_b128 a[156:159], v81 offset:7168\n\t"
    "ds_read_b128 a[152:155], v81 offset:6144\n\t"
    "v_mfma_f32_16x16x32_bf16 v[20:23], a[144:147], a[240:243], v[20:23]\n\t"
    "ds_read_b128 a[148:151], v81 offset:5120\n\t"
    "v_mfma_f32_16x16x32_bf16 v[24:27], a[160:163], a[240:243], 0\n\t"
    "v_exp_f32 v18, v18\n\t"
    "v_mfma_f32_16x16x32_bf16 v[24:27], a[164:167], a[244:247], v[24:27]\n\t"
    "buffer_load_dwordx4 v121, s[40:43], s8 offen offset:1024 lds\n\t"
    "v_mfma_f32_16x16x32_bf16 v[24:27], a[168:171], a[248:251], v[24:27]\n\t"
    "ds_read_b128 a[144:147], v81 offset:4096\n\t"
    "ds_read_b128 a[160:163], v81 offset:8192\n\t"
    "v_mfma_f32_16x16x32_bf16 v[24:27], a[172:175], a[252:255], v[24:27]\n\t"
    "v_exp_f32 v17, v17\n\t"
    "v_mfma_f32_16x16x32_bf16 v[28:31], a[188:191], a[252:255], 0\n\t"
    "ds_read_b128 a[164:167], v81 offset:9216\n\t"
    "ds_read_b128 a[168:171], v81 offset:10240\n\t"
    "v_mfma_f32_16x16x32_bf16 v[28:31], a[184:187], a[248:251], v[28:31]\n\t"
    "ds_read_b128 a[172:175], v81 offset:11264\n\t"
    "ds_read_b128 a[188:191], v81 offset:15360\n\t"
    "v_mfma_f32_16x16x32_bf16 v[28:31], a[180:183], a[244:247], v[28:31]\n\t"
    "v_cvt_pk_bf16_f32 v37, v10, v11\n\t"
    "v_cvt_pk_bf16_f32 v39, v14, v15\n\t"
    "v_mfma_f32_16x16x32_bf16 v[28:31], a[176:179], a[240:243], v[28:31]\n\t"
    "v_exp_f32 v16, v16\n\t"
    "v_smfmac_f32_16x16x64_bf16 v[104:107], v[68:71], v[32:39], v80\n\t"
    "buffer_load_dwordx4 v121, s[40:43], s8 offen offset:2048 lds\n\t"
    "v_mfma_f32_16x16x32_bf16 v[128:131], a[64:67], v[32:35], v[128:131]\n\t"
    "v_exp_f32 v20, v20\n\t"
    "v_mfma_f32_16x16x32_bf16 v[128:131], a[68:71], v[36:39], v[128:131]\n\t"
    "ds_read_b128 a[184:187], v81 offset:14336\n\t"
    "ds_read_b128 a[180:183], v81 offset:13312\n\t"
    "v_mfma_f32_16x16x32_bf16 v[132:135], a[72:75], v[32:35], v[132:135]\n\t"
    "v_exp_f32 v24, v24\n\t"
    "v_mfma_f32_16x16x32_bf16 v[132:135], a[76:79], v[36:39], v[132:135]\n\t"
    "v_exp_f32 v21, v21\n\t"
    "v_mfma_f32_16x16x32_bf16 v[136:139], a[80:83], v[32:35], v[136:139]\n\t"
    "v_exp_f32 v25, v25\n\t"
    "v_mfma_f32_16x16x32_bf16 v[136:139], a[84:87], v[36:39], v[136:139]\n\t"
    "buffer_load_dwordx4 v121, s[40:43], s8 offen offset:3072 lds\n\t"
    "v_mfma_f32_16x16x32_bf16 v[140:143], a[88:91], v[32:35], v[140:143]\n\t"
    "v_exp_f32 v19, v19\n\t"
    "v_mfma_f32_16x16x32_bf16 v[140:143], a[92:95], v[36:39], v[140:143]\n\t"
    "v_exp_f32 v29, v29\n\t"
    "v_mfma_f32_16x16x32_bf16 v[144:147], a[96:99], v[32:35], v[144:147]\n\t"
    "v_exp_f32 v27, v27\n\t"
    "v_mfma_f32_16x16x32_bf16 v[144:147], a[100:103], v[36:39], v[144:147]\n\t"
    "v_exp_f32 v26, v26\n\t"
    "v_mfma_f32_16x16x32_bf16 v[148:151], a[104:107], v[32:35], v[148:151]\n\t"
    "v_cvt_pk_bf16_f32 v42, v20, v21\n\t"
    "s_add_u32 m0, s15, s12\n\t"
    "v_mfma_f32_16x16x32_bf16 v[148:151], a[108:111], v[36:39], v[148:151]\n\t"
    "buffer_load_dwordx4 v123, s[40:43], s8 offen offset:0 lds\n\t"
    "v_mfma_f32_16x16x32_bf16 v[152:155], a[112:115], v[32:35], v[152:155]\n\t"
    "v_exp_f32 v28, v28\n\t"
    "v_mfma_f32_16x16x32_bf16 v[152:155], a[116:119], v[36:39], v[152:155]\n\t"
    "v_exp_f32 v31, v31\n\t"
    "v_mfma_f32_16x16x32_bf16 v[156:159], a[120:123], v[32:35], v[156:159]\n\t"
    "ds_read_b128 a[176:179], v81 offset:12288\n\t"
    "v_cvt_pk_bf16_f32 v44, v24, v25\n\t"
    "v_mfma_f32_16x16x32_bf16 v[156:159], a[124:127], v[36:39], v[156:159]\n\t"
    "v_cvt_pk_bf16_f32 v41, v18, v19\n\t"
    "s_waitcnt lgkmcnt(1)\n\t"
    "v_mfma_f32_16x16x32_bf16 v[0:3], a[128:131], a[192:195], 0\n\t"
    "buffer_load_dwordx4 v123, s[40:43], s8 offen offset:1024 lds\n\t"
    "v_mfma_f32_16x16x32_bf16 v[0:3], a[132:135], a[196:199], v[0:3]\n\t"
    "v_exp_f32 v23, v23\n\t"
    "v_mfma_f32_16x16x32_bf16 v[0:3], a[136:139], a[200:203], v[0:3]\n\t"
    "v_exp_f32 v22, v22\n\t"
    "v_mfma_f32_16x16x32_bf16 v[0:3], a[140:143], a[204:207], v[0:3]\n\t"
    "v_cvt_pk_bf16_f32 v45, v26, v27\n\t"
    "v_cvt_pk_bf16_f32 v46, v28, v29\n\t"
    "v_mfma_f32_16x16x32_bf16 v[4:7], a[156:159], a[204:207], 0\n\t"
    "v_cvt_pk_bf16_f32 v43, v22, v23\n\t"
    "v_mfma_f32_16x16x32_bf16 v[4:7], a[152:155], a[200:203], v[4:7]\n\t"
    "buffer_load_dwordx4 v123, s[40:43], s8 offen offset:2048 lds\n\t"
    "v_mfma_f32_16x16x32_bf16 v[4:7], a[148:151], a[196:199], v[4:7]\n\t"
    "v_exp_f32 v30, v30\n\t"
    "v_mfma_f32_16x16x32_bf16 v[4:7], a[144:147], a[192:195], v[4:7]\n\t"
    "v_cvt_pk_bf16_f32 v40, v16, v17\n\t"
    "v_mfma_f32_16x16x32_bf16 v[8:11], a[160:163], a[192:195], v[64:67]\n\t"
    "v_exp_f32 v1, v1\n\t"
    "v_mfma_f32_16x16x32_bf16 v[8:11], a[164:167], a[196:199], v[8:11]\n\t"
    "v_exp_f32 v3, v3\n\t"
    "v_mfma_f32_16x16x32_bf16 v[8:11], a[168:171], a[200:203], v[8:11]\n\t"
    "v_exp_f32 v2, v2\n\t"
    "v_mfma_f32_16x16x32_bf16 v[8:11], a[172:175], a[204:207], v[8:11]\n\t"
    "v_cvt_pk_bf16_f32 v47, v30, v31\n\t"
    "v_cvt_pk_bf16_f32 v33, v2, v3\n\t"
    "v_mfma_f32_16x16x32_bf16 v[12:15], a[188:191], a[204:207], v[92:95]\n\t"
    "buffer_load_dwordx4 v123, s[40:43], s8 offen offset:3072 lds\n\t"
    "v_mfma_f32_16x16x32_bf16 v[12:15], a[184:187], a[200:203], v[12:15]\n\t"
    "v_exp_f32 v4, v4\n\t"
    "v_mfma_f32_16x16x32_bf16 v[12:15], a[180:183], a[196:199], v[12:15]\n\t"
    "v_exp_f32 v5, v5\n\t"
    "s_waitcnt lgkmcnt(0)\n\t"
    "v_mfma_f32_16x16x32_bf16 v[12:15], a[176:179], a[192:195], v[12:15]\n\t"
    "v_exp_f32 v6, v6\n\t"
    "v_smfmac_f32_16x16x64_bf16 v[108:111], v[68:71], v[40:47], v80\n\t"
    "v_cvt_pk_bf16_f32 v34, v4, v5\n\t"
    "s_add_u32 s8, s8, 0x8000\n\t"
    "v_mfma_f32_16x16x32_bf16 v[160:163], a[64:67], v[40:43], v[160:163]\n\t"
    "s_cmp_eq_u32 s8, s9\n\t"
    "s_cselect_b32 s8, s23, s8\n\t"
    "v_mfma_f32_16x16x32_bf16 v[160:163], a[68:71], v[44:47], v[160:163]\n\t"
    "v_exp_f32 v0, v0\n\t"
    "v_mfma_f32_16x16x32_bf16 v[164:167], a[72:75], v[40:43], v[164:167]\n\t"
    "v_exp_f32 v7, v7\n\t"
    "v_mfma_f32_16x16x32_bf16 v[164:167], a[76:79], v[44:47], v[164:167]\n\t"
    "v_exp_f32 v8, v8\n\t"
    "v_mfma_f32_16x16x32_bf16 v[168:171], a[80:83], v[40:43], v[168:171]\n\t"
    "v_exp_f32 v9, v9\n\t"
    "v_mfma_f32_16x16x32_bf16 v[168:171], a[84:87], v[44:47], v[168:171]\n\t"
    "buffer_load_dword v244, v125, s[28:31], 0 offen\n\t"
    "v_mfma_f32_16x16x32_bf16 v[172:175], a[88:91], v[40:43], v[172:175]\n\t"
    "ds_read_b128 a[64:67], v82 offset:0\n\t"
    "ds_read_b128 a[68:71], v82 offset:8192\n\t"
    "v_mfma_f32_16x16x32_bf16 v[172:175], a[92:95], v[44:47], v[172:175]\n\t"
    "v_exp_f32 v12, v12\n\t"
    "v_mfma_f32_16x16x32_bf16 v[176:179], a[96:99], v[40:43], v[176:179]\n\t"
    "v_exp_f32 v15, v15\n\t"
    "v_mfma_f32_16x16x32_bf16 v[176:179], a[100:103], v[44:47], v[176:179]\n\t"
    "ds_read_b128 a[72:75], v82 offset:1024\n\t"
    "ds_read_b128 a[76:79], v82 offset:9216\n\t"
    "s_cmp_gt_u32 s17, 1\n\t"
    "v_mfma_f32_16x16x32_bf16 v[180:183], a[104:107], v[40:43], v[180:183]\n\t"
    "v_exp_f32 v13, v13\n\t"
    "v_mfma_f32_16x16x32_bf16 v[180:183], a[108:111], v[44:47], v[180:183]\n\t"
    "ds_read_b128 a[80:83], v82 offset:2048\n\t"
    "ds_read_b128 a[84:87], v82 offset:10240\n\t"
    "v_mfma_f32_16x16x32_bf16 v[184:187], a[112:115], v[40:43], v[184:187]\n\t"
    "v_exp_f32 v14, v14\n\t"
    "v_mfma_f32_16x16x32_bf16 v[184:187], a[116:119], v[44:47], v[184:187]\n\t"
    "v_cvt_pk_bf16_f32 v38, v12, v13\n\t"
    "v_cvt_pk_bf16_f32 v39, v14, v15\n\t"
    "v_mfma_f32_16x16x32_bf16 v[188:191], a[120:123], v[40:43], v[188:191]\n\t"
    "v_exp_f32 v11, v11\n\t"
    "v_mfma_f32_16x16x32_bf16 v[188:191], a[124:127], v[44:47], v[188:191]\n\t"
    "s_cbranch_scc1 L_N_%=\n\t"
    "s_cmp_eq_u32 s17, 0\n\t"
    "s_cbranch_scc1 L_E_%=\n\t"
    "L_R_%=:\n\t"
    "global_load_dword v77, v127, s[84:85] sc1\n\t"
    "v_mov_b32 v64, v48\n\t"
    "v_mov_b32 v65, v49\n\t"
    "v_mov_b32 v66, v50\n\t"
    "v_mov_b32 v67, v51\n\t"
    "v_mov_b32 v92, v52\n\t"
    "v_mov_b32 v93, v53\n\t"
    "v_mov_b32 v94, v54\n\t"
    "v_mov_b32 v95, v55\n\t"
    "s_lshr_b32 s22, %2, 3\n\t"
    "s_lshl_b32 s22, s22, 2\n\t"
    "s_add_u32 s22, s22, s58\n\t"
    "s_lshl_b32 s22, s22, 8\n\t"
    "s_add_u32 s22, s22, L_E_%=-Lpf_%=\n\t"
    "v_lshrrev_b32 v248, 2, v76\n\t"
    "v_add_u32 v248, s22, v248\n\t"
    "v_min_u32 v248, Lkend_fused-Lpf_%=-4, v248\n\t"
    "global_load_dword v248, v248, s[88:89]\n\t"
    "s_mov_b64 s[36:37], s[62:63]\n\t"
    "s_add_u32 s40, s62, 0x10000\n\t"
    "s_addc_u32 s41, s63, 0\n\t"
    "s_mov_b32 s38, s66\n\t"
    "s_sub_u32 s42, s66, 0x10000\n\t"
    "s_mov_b32 s8, 0\n\t"
    "v_and_b32 v123, 0x380, v76\n\t"
    "v_mad_u32_u24 v121, v123, 31, v76\n\t"
    "v_add_u32 v123, 0x8000, v121\n\t"
    "s_cmp_eq_u32 s60, 7\n\t"
    "s_cbranch_scc1 L_A1_%=\n\t"
    "s_branch L_N_%=\n\t"
    "L_E_%=:\n\t"
    "v_mfma_f32_16x16x32_bf16 v[16:19], a[128:131], a[240:243], 0\n\t"
    "ds_read_b128 a[88:91], v82 offset:3072\n\t"
    "ds_read_b128 a[92:95], v82 offset:11264\n\t"
    "v_mfma_f32_16x16x32_bf16 v[16:19], a[132:135], a[244:247], v[16:19]\n\t"
    "ds_read_b128 a[96:99], v82 offset:4096\n\t"
    "ds_read_b128 a[100:103], v82 offset:12288\n\t"
    "v_mfma_f32_16x16x32_bf16 v[16:19], a[136:139], a[248:251], v[16:19]\n\t"
    "ds_read_b128 a[104:107], v82 offset:5120\n\t"
    "ds_read_b128 a[108:111], v82 offset:13312\n\t"
    "v_mfma_f32_16x16x32_bf16 v[16:19], a[140:143], a[252:255], v[16:19]\n\t"
    "v_exp_f32 v10, v10\n\t"
    "v_mfma_f32_16x16x32_bf16 v[20:23], a[156:159], a[252:255], 0\n\t"
    "v_cvt_pk_bf16_f32 v35, v6, v7\n\t"
    "v_cvt_pk_bf16_f32 v37, v10, v11\n\t"
    "v_mfma_f32_16x16x32_bf16 v[20:23], a[152:155], a[248:251], v[20:23]\n\t"
    "ds_read_b128 a[112:115], v82 offset:6144\n\t"
    "ds_read_b128 a[116:119], v82 offset:14336\n\t"
    "v_mfma_f32_16x16x32_bf16 v[20:23], a[148:151], a[244:247], v[20:23]\n\t"
    "ds_read_b128 a[120:123], v82 offset:7168\n\t"
    "v_cvt_pk_bf16_f32 v32, v0, v1\n\t"
    "v_mfma_f32_16x16x32_bf16 v[20:23], a[144:147], a[240:243], v[20:23]\n\t"
    "ds_read_b128 a[124:127], v82 offset:15360\n\t"
    "v_cvt_pk_bf16_f32 v36, v8, v9\n\t"
    "v_mfma_f32_16x16x32_bf16 v[24:27], a[160:163], a[240:243], v[56:59]\n\t"
    "s_mov_b64 s[40:41], s[62:63]\n\t"
    "s_mov_b32 s42, s66\n\t"
    "s_lshl_b32 s45, s58, 13\n\t"
    "s_waitcnt vmcnt(17)\n\t"
    "v_readfirstlane_b32 s90, v77\n\t"
    "v_mfma_f32_16x16x32_bf16 v[24:27], a[164:167], a[244:247], v[24:27]\n\t"
    "v_add_f32 v108, v108, v109\n\t"
    "v_and_b32 v254, 0xc0, v76\n\t"
    "v_and_b32 v255, 0x30, v76\n\t"
    "s_cmp_lt_u32 s90, 32\n\t"
    "s_cbranch_scc1 L_pU_%=\n\t"
    "L_rU_%=:\n\t"
    "v_mfma_f32_16x16x32_bf16 v[24:27], a[168:171], a[248:251], v[24:27]\n\t"
    "s_lshl1_add_u32 s25, s99, s61\n\t"
    "s_lshl_b32 s25, s25, 12\n\t"
    "s_add_u32 s25, s25, 0x6400000\n\t"
    "s_add_u32 s64, s20, s25\n\t"
    "s_addc_u32 s65, s21, 0\n\t"
    "v_mfma_f32_16x16x32_bf16 v[24:27], a[172:175], a[252:255], v[24:27]\n\t"
    "v_add_f32 v96, v96, v97\n\t"
    "v_lshl_add_u32 v254, v254, 4, v255\n\t"
    "s_movk_i32 s66, 0x1000\n\t"
    "s_mov_b32 s67, 0x00020000\n\t"
    "s_lshl_b32 s25, s98, 11\n\t"
    "v_mfma_f32_16x16x32_bf16 v[28:31], a[188:191], a[252:255], v[56:59]\n\t"
    "v_lshl_add_u32 v252, v76, 1, s25\n\t"
    "v_exp_f32 v18, v18\n\t"
    "v_exp_f32 v23, v23\n\t"
    "v_mfma_f32_16x16x32_bf16 v[28:31], a[184:187], a[248:251], v[28:31]\n\t"
    "buffer_load_dwordx4 v[228:231], v252, s[64:67], 0 offen offset:0 sc1\n\t"
    "v_subrev_u32 v253, 16, v252\n\t"
    "v_mfma_f32_16x16x32_bf16 v[28:31], a[180:183], a[244:247], v[28:31]\n\t"
    "v_lshrrev_b32 v255, 8, v76\n\t"
    "v_lshl_add_u32 v254, v255, 7, v254\n\t"
    "v_add_u32 v81, s45, v254\n\t"
    "v_exp_f32 v21, v21\n\t"
    "v_mfma_f32_16x16x32_bf16 v[28:31], a[176:179], a[240:243], v[28:31]\n\t"
    "v_add_u32 v86, 0x10000, v81\n\t"
    "v_lshrrev_b32 v254, 2, v76\n\t"
    "v_and_b32 v254, 64, v254\n\t"
    "v_xor_b32 v254, v254, v76\n\t"
    "v_and_b32 v255, 0x380, v76\n\t"
    "v_mfma_f32_16x16x32_bf16 v[96:99], v[68:71], v[32:35], v[96:99]\n\t"
    "buffer_load_dwordx4 v[232:235], v252, s[64:67], 0 offen offset:16 sc1\n\t"
    "v_exp_f32 v22, v22\n\t"
    "v_mfma_f32_16x16x32_bf16 v[96:99], v[68:71], v[36:39], v[96:99]\n\t"
    "v_add_f32 v100, v100, v101\n\t"
    "v_mad_u32_u24 v64, v255, 7, v254\n\t"
    "v_add_u32 v65, 0x1c00, v64\n\t"
    "v_add_u32 v66, 0x3800, v64\n\t"
    "v_add_u32 v67, 0x5400, v64\n\t"
    "s_waitcnt lgkmcnt(0)\n\t"
    "v_mfma_f32_16x16x32_bf16 a[0:3], a[64:67], v[32:35], a[0:3]\n\t"
    "buffer_load_dwordx4 v[224:227], v253, s[64:67], 0 offen offset:0 sc1\n\t"
    "v_exp_f32 v17, v17\n\t"
    "v_mfma_f32_16x16x32_bf16 a[0:3], a[68:71], v[36:39], a[0:3]\n\t"
    "v_add_f32 v104, v104, v105\n\t"
    "s_lshl2_add_u32 s26, s99, s58\n\t"
    "s_mov_b32 s44, 0x20000\n\t"
    "v_exp_f32 v20, v20\n\t"
    "v_mfma_f32_16x16x32_bf16 a[4:7], a[72:75], v[32:35], a[4:7]\n\t"
    "v_cvt_pk_bf16_f32 v42, v20, v21\n\t"
    "s_waitcnt vmcnt(16)\n\t"
    "s_add_u32 s22, s45, 0x10000\n\t"
    "v_exp_f32 v16, v16\n\t"
    "v_mfma_f32_16x16x32_bf16 a[4:7], a[76:79], v[36:39], a[4:7]\n\t"
    "ds_read_b64_tr_b16 a[194:195], v86 offset:33280\n\t"
    "ds_read_b64_tr_b16 a[202:203], v86 offset:33344\n\t"
    "s_lshl_b32 s26, s26, 12\n\t"
    "s_add_u32 s26, s26, 0xc00000\n\t"
    "v_add_u32 v252, s26, v76\n\t"
    "v_mfma_f32_16x16x32_bf16 a[8:11], a[80:83], v[32:35], a[8:11]\n\t"
    "global_load_dwordx4 v[244:247], v252, s[84:85] offset:0 sc1\n\t"
    "s_waitcnt lgkmcnt(0)\n\t"
    "s_barrier\n\t"
    "v_mfma_f32_16x16x32_bf16 a[8:11], a[84:87], v[36:39], a[8:11]\n\t"
    "ds_read_b64_tr_b16 a[200:201], v86 offset:32832\n\t"
    "s_add_u32 m0, s45, 0x0\n\t"
    "v_add_u32 v84, s22, v76\n\t"
    "v_exp_f32 v24, v24\n\t"
    "v_mfma_f32_16x16x32_bf16 a[12:15], a[88:91], v[32:35], a[12:15]\n\t"
    "buffer_load_dwordx4 v64, s[40:43], s44 offen offset:0 lds\n\t"
    "ds_read_b64_tr_b16 a[196:197], v86 offset:32776\n\t"
    "ds_read_b64_tr_b16 a[206:207], v86 offset:33352\n\t"
    "v_mfma_f32_16x16x32_bf16 a[12:15], a[92:95], v[36:39], a[12:15]\n\t"
    "global_load_dwordx4 v[248:251], v252, s[84:85] offset:1024 sc1\n\t"
    "ds_read_b64_tr_b16 a[192:193], v86 offset:32768\n\t"
    "v_lshrrev_b32 v82, 2, v76\n\t"
    "v_mfma_f32_16x16x32_bf16 a[16:19], a[96:99], v[32:35], a[16:19]\n\t"
    "ds_read_b64_tr_b16 a[204:205], v86 offset:32840\n\t"
    "s_add_u32 s22, s45, 0x1000\n\t"
    "v_add_u32 v82, s22, v82\n\t"
    "v_exp_f32 v27, v27\n\t"
    "v_mfma_f32_16x16x32_bf16 a[16:19], a[100:103], v[36:39], a[16:19]\n\t"
    "buffer_load_dwordx4 v65, s[40:43], s44 offen offset:1024 lds\n\t"
    "ds_read_b64_tr_b16 a[198:199], v86 offset:33288\n\t"
    "v_xor_b32 v87, 0x4000, v84\n\t"
    "v_mfma_f32_16x16x32_bf16 a[20:23], a[104:107], v[32:35], a[20:23]\n\t"
    "v_and_b32 v77, 0x3c0, v76\n\t"
    "v_exp_f32 v19, v19\n\t"
    "v_exp_f32 v26, v26\n\t"
    "v_mfma_f32_16x16x32_bf16 a[20:23], a[108:111], v[36:39], a[20:23]\n\t"
    "v_cvt_pk_bf16_f32 v41, v18, v19\n\t"
    "s_waitcnt vmcnt(16)\n\t"
    "v_xor_b32 v83, 0x4000, v82\n\t"
    "v_add_u32 v85, 0x10000, v84\n\t"
    "v_add_u32 v88, 0x10000, v87\n\t"
    "v_mfma_f32_16x16x32_bf16 a[24:27], a[112:115], v[32:35], a[24:27]\n\t"
    "ds_read_b64_tr_b16 a[240:241], v86 offset:36864\n\t"
    "ds_read_b64_tr_b16 a[254:255], v86 offset:37448\n\t"
    "v_add_u32 v77, v77, v76\n\t"
    "v_exp_f32 v31, v31\n\t"
    "v_mfma_f32_16x16x32_bf16 a[24:27], a[116:119], v[36:39], a[24:27]\n\t"
    "ds_write_b128 v84, a[12:15] offset:3072\n\t"
    "v_mfma_f32_16x16x32_bf16 a[28:31], a[120:123], v[32:35], a[28:31]\n\t"
    "buffer_load_dwordx4 v66, s[40:43], s44 offen offset:2048 lds\n\t"
    "v_cvt_pk_bf16_f32 v45, v26, v27\n\t"
    "v_lshrrev_b32 v78, 1, v76\n\t"
    "v_mfma_f32_16x16x32_bf16 a[28:31], a[124:127], v[36:39], a[28:31]\n\t"
    "ds_write_b128 v84, a[16:19] offset:4096\n\t"
    "v_mfma_f32_16x16x32_bf16 v[0:3], a[128:131], a[208:211], v[48:51]\n\t"
    "ds_read_b64_tr_b16 a[246:247], v86 offset:37384\n\t"
    "v_cvt_pk_bf16_f32 v40, v16, v17\n\t"
    "v_and_b32 v78, 64, v78\n\t"
    "v_exp_f32 v30, v30\n\t"
    "v_mfma_f32_16x16x32_bf16 v[0:3], a[132:135], a[212:215], v[0:3]\n\t"
    "ds_write_b128 v84, a[20:23] offset:5120\n\t"
    "v_mfma_f32_16x16x32_bf16 v[0:3], a[136:139], a[216:219], v[0:3]\n\t"
    "buffer_load_dwordx4 v67, s[40:43], s44 offen offset:3072 lds\n\t"
    "ds_read_b64_tr_b16 a[248:249], v86 offset:36928\n\t"
    "ds_read_b64_tr_b16 a[250:251], v86 offset:37440\n\t"
    "v_mfma_f32_16x16x32_bf16 v[0:3], a[140:143], a[220:223], v[0:3]\n\t"
    "ds_write_b128 v84, a[24:27] offset:6144\n\t"
    "v_mfma_f32_16x16x32_bf16 v[4:7], a[156:159], a[220:223], v[52:55]\n\t"
    "ds_write_b128 v84, a[0:3] offset:0\n\t"
    "v_mfma_f32_16x16x32_bf16 v[4:7], a[152:155], a[216:219], v[4:7]\n\t"
    "ds_read_b64_tr_b16 a[252:253], v86 offset:36936\n\t"
    "v_cvt_pk_bf16_f32 v43, v22, v23\n\t"
    "v_add3_u32 v77, v77, v78, s45\n\t"
    "s_add_u32 s44, s44, 0x8000\n\t"
    "s_waitcnt lgkmcnt(8)\n\t"
    "s_add_u32 m0, s45, 0x18000\n\t"
    "v_mfma_f32_16x16x32_bf16 v[4:7], a[148:151], a[212:215], v[4:7]\n\t"
    "buffer_load_dwordx4 v64, s[40:43], s44 offen offset:0 lds\n\t"
    "v_exp_f32 v29, v29\n\t"
    "v_mfma_f32_16x16x32_bf16 v[4:7], a[144:147], a[208:211], v[4:7]\n\t"
    "ds_write_b128 v84, a[28:31] offset:7168\n\t"
    "v_mfma_f32_16x16x32_bf16 v[8:11], a[128:131], a[224:227], v[56:59]\n\t"
    "ds_write_b128 v84, a[8:11] offset:2048\n\t"
    "v_mfma_f32_16x16x32_bf16 v[8:11], a[132:135], a[228:231], v[8:11]\n\t"
    "ds_write_b128 v84, a[4:7] offset:1024\n\t"
    "v_mfma_f32_16x16x32_bf16 v[8:11], a[136:139], a[232:235], v[8:11]\n\t"
    "buffer_load_dwordx4 v65, s[40:43], s44 offen offset:1024 lds\n\t"
    "ds_read_b64_tr_b16 a[244:245], v86 offset:36872\n\t"
    "s_waitcnt vmcnt(16)\n\t"
    "v_mfma_f32_16x16x32_bf16 v[8:11], a[140:143], a[236:239], v[8:11]\n\t"
    "ds_read_b64_tr_b16 a[172:173], v81 offset:32840\n\t"
    "v_xor_b32 v78, 64, v77\n\t"
    "v_add_u32 v79, 0x10000, v77\n\t"
    "v_exp_f32 v25, v25\n\t"
    "v_mfma_f32_16x16x32_bf16 v[12:15], a[156:159], a[236:239], v[56:59]\n\t"
    "ds_read_b64_tr_b16 a[242:243], v86 offset:37376\n\t"
    "ds_read_b64_tr_b16 a[166:167], v81 offset:33288\n\t"
    "v_cvt_pk_bf16_f32 v44, v24, v25\n\t"
    "v_exp_f32 v28, v28\n\t"
    "v_mfma_f32_16x16x32_bf16 v[12:15], a[152:155], a[232:235], v[12:15]\n\t"
    "buffer_load_dwordx4 v66, s[40:43], s44 offen offset:2048 lds\n\t"
    "v_exp_f32 v0, v0\n\t"
    "v_mfma_f32_16x16x32_bf16 v[12:15], a[148:151], a[228:231], v[12:15]\n\t"
    "ds_read_b64_tr_b16 a[160:161], v81 offset:32768\n\t"
    "ds_read_b64_tr_b16 a[164:165], v81 offset:32776\n\t"
    "ds_read_b64_tr_b16 a[168:169], v81 offset:32832\n\t"
    "v_cvt_pk_bf16_f32 v46, v28, v29\n\t"
    "v_cvt_pk_bf16_f32 v47, v30, v31\n\t"
    "v_mfma_f32_16x16x32_bf16 v[12:15], a[144:147], a[224:227], v[12:15]\n\t"
    "ds_write_b32 v82, v96 offset:0\n\t"
    "ds_read_b64_tr_b16 a[170:171], v81 offset:33344\n\t"
    "v_add_u32 v80, 0x10000, v78\n\t"
    "v_mfma_f32_16x16x32_bf16 v[108:111], v[68:71], v[40:43], v[108:111]\n\t"
    "buffer_load_dwordx4 v67, s[40:43], s44 offen offset:3072 lds\n\t"
    "v_exp_f32 v1, v1\n\t"
    "v_mfma_f32_16x16x32_bf16 v[108:111], v[68:71], v[44:47], v[108:111]\n\t"
    "ds_read_b64_tr_b16 a[162:163], v81 offset:33280\n\t"
    "s_add_u32 s44, s44, 0x8000\n\t"
    "s_waitcnt lgkmcnt(7)\n\t"
    "s_add_u32 m0, s45, 0x19000\n\t"
    "v_exp_f32 v5, v5\n\t"
    "v_mfma_f32_16x16x32_bf16 v[160:163], a[64:67], v[40:43], v[160:163]\n\t"
    "v_cvt_pk_bf16_f32 v32, v0, v1\n\t"
    "v_exp_f32 v10, v10\n\t"
    "s_waitcnt lgkmcnt(0)\n\t"
    "s_barrier\n\t"
    "v_mfma_f32_16x16x32_bf16 v[160:163], a[68:71], v[44:47], v[160:163]\n\t"
    "ds_read_b32 v115, v83 offset:0\n\t"
    "ds_read_b128 v[192:195], v87 offset:0\n\t"
    "ds_read_b128 v[200:203], v87 offset:2048\n\t"
    "v_exp_f32 v11, v11\n\t"
    "v_mfma_f32_16x16x32_bf16 v[164:167], a[72:75], v[40:43], v[164:167]\n\t"
    "buffer_load_dwordx4 v64, s[40:43], s44 offen offset:0 lds\n\t"
    "ds_read_b128 v[208:211], v87 offset:4096\n\t"
    "v_cvt_pk_bf16_f32 v37, v10, v11\n\t"
    "v_mfma_f32_16x16x32_bf16 v[164:167], a[76:79], v[44:47], v[164:167]\n\t"
    "ds_read_b128 v[196:199], v87 offset:1024\n\t"
    "v_exp_f32 v4, v4\n\t"
    "v_exp_f32 v6, v6\n\t"
    "v_mfma_f32_16x16x32_bf16 v[168:171], a[80:83], v[40:43], v[168:171]\n\t"
    "ds_read_b128 v[204:207], v87 offset:3072\n\t"
    "ds_read_b128 v[212:215], v87 offset:5120\n\t"
    "ds_read_b128 v[220:223], v87 offset:7168\n\t"
    "v_exp_f32 v3, v3\n\t"
    "v_mfma_f32_16x16x32_bf16 v[168:171], a[84:87], v[44:47], v[168:171]\n\t"
    "v_cvt_pk_bf16_f32 v34, v4, v5\n\t"
    "v_exp_f32 v7, v7\n\t"
    "v_exp_f32 v13, v13\n\t"
    "v_mfma_f32_16x16x32_bf16 v[172:175], a[88:91], v[40:43], v[172:175]\n\t"
    "buffer_load_dwordx4 v65, s[40:43], s44 offen offset:1024 lds\n\t"
    "v_exp_f32 v8, v8\n\t"
    "v_mfma_f32_16x16x32_bf16 v[172:175], a[92:95], v[44:47], v[172:175]\n\t"
    "s_waitcnt vmcnt(16)\n\t"
    "s_waitcnt lgkmcnt(6)\n\t"
    "v_add_f32 v75, v108, v115\n\t"
    "v_exp_f32 v12, v12\n\t"
    "v_rcp_f32 v126, v75\n\t"
    "v_mfma_f32_16x16x32_bf16 v[176:179], a[96:99], v[40:43], v[176:179]\n\t"
    "v_subrev_u32 v115, 0x1f800000, v75\n\t"
    "v_exp_f32 v2, v2\n\t"
    "v_exp_f32 v9, v9\n\t"
    "v_mfma_f32_16x16x32_bf16 v[176:179], a[100:103], v[44:47], v[176:179]\n\t"
    "ds_read_b64_tr_b16 a[174:175], v81 offset:33352\n\t"
    "v_add_f32 v160, v160, v192\n\t"
    "v_add_f32 v161, v161, v193\n\t"
    "v_exp_f32 v14, v14\n\t"
    "v_mfma_f32_16x16x32_bf16 v[180:183], a[104:107], v[40:43], v[180:183]\n\t"
    "buffer_load_dwordx4 v66, s[40:43], s44 offen offset:2048 lds\n\t"
    "ds_read_b64_tr_b16 a[180:181], v81 offset:36872\n\t"
    "ds_read_b128 v[216:219], v87 offset:6144\n\t"
    "v_mfma_f32_16x16x32_bf16 v[180:183], a[108:111], v[44:47], v[180:183]\n\t"
    "v_cvt_pk_bf16_f32 v33, v2, v3\n\t"
    "v_add_f32 v162, v162, v194\n\t"
    "v_add_f32 v163, v163, v195\n\t"
    "s_waitcnt lgkmcnt(3)\n\t"
    "v_add_f32 v164, v164, v196\n\t"
    "v_add_f32 v165, v165, v197\n\t"
    "v_mfma_f32_16x16x32_bf16 v[184:187], a[112:115], v[40:43], v[184:187]\n\t"
    "ds_read_b64_tr_b16 a[178:179], v81 offset:37376\n\t"
    "v_cvt_pk_bf16_f32 v35, v6, v7\n\t"
    "v_cvt_pk_bf16_f32 v36, v8, v9\n\t"
    "v_add_f32 v166, v166, v198\n\t"
    "v_add_f32 v167, v167, v199\n\t"
    "v_mfma_f32_16x16x32_bf16 v[184:187], a[116:119], v[44:47], v[184:187]\n\t"
    "v_mul_f32 v160, v160, v126\n\t"
    "v_mul_f32 v161, v161, v126\n\t"
    "v_mul_f32 v162, v162, v126\n\t"
    "v_mul_f32 v163, v163, v126\n\t"
    "v_mul_f32 v164, v164, v126\n\t"
    "v_mfma_f32_16x16x32_bf16 v[188:191], a[120:123], v[40:43], v[188:191]\n\t"
    "buffer_load_dwordx4 v67, s[40:43], s44 offen offset:3072 lds\n\t"
    "ds_read_b64_tr_b16 a[176:177], v81 offset:36864\n\t"
    "s_add_u32 s44, s44, 0x8000\n\t"
    "v_mfma_f32_16x16x32_bf16 v[188:191], a[124:127], v[44:47], v[188:191]\n\t"
    "v_mul_f32 v165, v165, v126\n\t"
    "v_mul_f32 v166, v166, v126\n\t"
    "v_mul_f32 v167, v167, v126\n\t"
    "v_cvt_pk_bf16_f32 v160, v160, v161\n\t"
    "v_cvt_pk_bf16_f32 v161, v162, v163\n\t"
    "v_cvt_pk_bf16_f32 v162, v164, v165\n\t"
    "v_mfma_f32_16x16x32_bf16 v[100:103], v[68:71], v[32:35], v[100:103]\n\t"
    "ds_read_b64_tr_b16 a[184:185], v81 offset:36928\n\t"
    "ds_read_b64_tr_b16 a[190:191], v81 offset:37448\n\t"
    "v_cvt_pk_bf16_f32 v38, v12, v13\n\t"
    "s_waitcnt lgkmcnt(3)\n\t"
    "s_add_u32 m0, s45, 0x8000\n\t"
    "v_cvt_pk_bf16_f32 v163, v166, v167\n\t"
    "v_mfma_f32_16x16x32_bf16 a[32:35], a[64:67], v[32:35], a[32:35]\n\t"
    "global_store_dwordx4 v76, v[160:163], s[48:49] offset:0 sc1\n\t"
    "v_exp_f32 v15, v15\n\t"
    "v_mfma_f32_16x16x32_bf16 a[36:39], a[72:75], v[32:35], a[36:39]\n\t"
    "buffer_load_dwordx4 v64, s[40:43], s44 offen offset:0 lds\n\t"
    "v_add_f32 v168, v168, v200\n\t"
    "v_add_f32 v169, v169, v201\n\t"
    "v_mfma_f32_16x16x32_bf16 a[40:43], a[80:83], v[32:35], a[40:43]\n\t"
    "v_add_f32 v170, v170, v202\n\t"
    "v_add_f32 v171, v171, v203\n\t"
    "v_add_f32 v172, v172, v204\n\t"
    "v_add_f32 v173, v173, v205\n\t"
    "v_add_f32 v174, v174, v206\n\t"
    "v_mfma_f32_16x16x32_bf16 a[44:47], a[88:91], v[32:35], a[44:47]\n\t"
    "ds_read_b64_tr_b16 a[188:189], v81 offset:36936\n\t"
    "v_add_f32 v175, v175, v207\n\t"
    "v_mul_f32 v168, v168, v126\n\t"
    "v_mul_f32 v169, v169, v126\n\t"
    "v_mul_f32 v170, v170, v126\n\t"
    "v_mfma_f32_16x16x32_bf16 a[48:51], a[96:99], v[32:35], a[48:51]\n\t"
    "buffer_load_dwordx4 v65, s[40:43], s44 offen offset:1024 lds\n\t"
    "v_mul_f32 v171, v171, v126\n\t"
    "v_mul_f32 v172, v172, v126\n\t"
    "v_mfma_f32_16x16x32_bf16 a[52:55], a[104:107], v[32:35], a[52:55]\n\t"
    "ds_read_b64_tr_b16 a[186:187], v81 offset:37440\n\t"
    "v_cvt_pk_bf16_f32 v39, v14, v15\n\t"
    "v_mul_f32 v173, v173, v126\n\t"
    "v_mul_f32 v174, v174, v126\n\t"
    "v_mul_f32 v175, v175, v126\n\t"
    "v_mfma_f32_16x16x32_bf16 a[56:59], a[112:115], v[32:35], a[56:59]\n\t"
    "ds_read_b64_tr_b16 a[182:183], v81 offset:37384\n\t"
    "v_cvt_pk_bf16_f32 v168, v168, v169\n\t"
    "v_cvt_pk_bf16_f32 v169, v170, v171\n\t"
    "v_cvt_pk_bf16_f32 v170, v172, v173\n\t"
    "v_cvt_pk_bf16_f32 v171, v174, v175\n\t"
    "v_mfma_f32_16x16x32_bf16 a[60:63], a[120:123], v[32:35], a[60:63]\n\t"
    "ds_write_b128 v85, a[36:39] offset:1024\n\t"
    "v_mfma_f32_16x16x32_bf16 v[104:107], v[68:71], v[36:39], v[104:107]\n\t"
    "buffer_load_dwordx4 v66, s[40:43], s44 offen offset:2048 lds\n\t"
    "Lis11_%=:\n\t"
    "s_cbranch_execz Lis15_%=\n\t"
    "global_store_dwordx4 v76, v[168:171], s[48:49] offset:1024 sc1\n\t"
    "v_mfma_f32_16x16x32_bf16 v[128:131], a[64:67], v[36:39], v[128:131]\n\t"
    "v_add_f32 v176, v176, v208\n\t"
    "v_add_f32 v177, v177, v209\n\t"
    "v_add_f32 v178, v178, v210\n\t"
    "v_add_f32 v179, v179, v211\n\t"
    "v_add_f32 v180, v180, v212\n\t"
    "v_add_f32 v181, v181, v213\n\t"
    "v_mfma_f32_16x16x32_bf16 v[132:135], a[72:75], v[36:39], v[132:135]\n\t"
    "ds_write_b128 v85, a[40:43] offset:2048\n\t"
    "v_mfma_f32_16x16x32_bf16 v[136:139], a[80:83], v[36:39], v[136:139]\n\t"
    "ds_write_b128 v85, a[32:35] offset:0\n\t"
    "v_add_f32 v182, v182, v214\n\t"
    "v_mfma_f32_16x16x32_bf16 v[140:143], a[88:91], v[36:39], v[140:143]\n\t"
    "v_add_f32 v183, v183, v215\n\t"
    "v_mul_f32 v176, v176, v126\n\t"
    "v_mul_f32 v177, v177, v126\n\t"
    "v_mul_f32 v178, v178, v126\n\t"
    "v_mul_f32 v179, v179, v126\n\t"
    "v_mul_f32 v180, v180, v126\n\t"
    "v_mfma_f32_16x16x32_bf16 v[144:147], a[96:99], v[36:39], v[144:147]\n\t"
    "buffer_load_dwordx4 v67, s[40:43], s44 offen offset:3072 lds\n\t"
    "s_add_u32 s44, s44, 0x8000\n\t"
    "v_mul_f32 v181, v181, v126\n\t"
    "v_mfma_f32_16x16x32_bf16 v[148:151], a[104:107], v[36:39], v[148:151]\n\t"
    "ds_write_b128 v85, a[44:47] offset:3072\n\t"
    "v_mfma_f32_16x16x32_bf16 v[152:155], a[112:115], v[36:39], v[152:155]\n\t"
    "v_mul_f32 v182, v182, v126\n\t"
    "v_mul_f32 v183, v183, v126\n\t"
    "v_cvt_pk_bf16_f32 v176, v176, v177\n\t"
    "v_cvt_pk_bf16_f32 v177, v178, v179\n\t"
    "v_cvt_pk_bf16_f32 v178, v180, v181\n\t"
    "v_cvt_pk_bf16_f32 v179, v182, v183\n\t"
    "v_mfma_f32_16x16x32_bf16 v[156:159], a[120:123], v[36:39], v[156:159]\n\t"
    "ds_write_b32 v82, v100 offset:256\n\t"
    "global_store_dwordx4 v76, v[176:179], s[48:49] offset:2048 sc1\n\t"
    "ds_write_b128 v85, a[48:51] offset:4096\n\t"
    "s_waitcnt vmcnt(15)\n\t"
    "ds_read_b64_tr_b16 a[208:209], v77 offset:0\n\t"
    "ds_read_b64_tr_b16 a[210:211], v77 offset:2048\n\t"
    "ds_read_b64_tr_b16 a[212:213], v77 offset:8\n\t"
    "ds_read_b64_tr_b16 a[214:215], v77 offset:2056\n\t"
    "ds_read_b64_tr_b16 a[216:217], v78 offset:0\n\t"
    "ds_read_b64_tr_b16 a[218:219], v78 offset:2048\n\t"
    "ds_read_b64_tr_b16 a[220:221], v78 offset:8\n\t"
    "ds_read_b64_tr_b16 a[222:223], v78 offset:2056\n\t"
    "s_waitcnt lgkmcnt(11)\n\t"
    "s_add_u32 m0, s45, 0x9000\n\t"
    "s_nop 0\n\t"
    "buffer_load_dwordx4 v64, s[40:43], s44 offen offset:0 lds\n\t"
    "v_and_b32 v236, 0x7fff7fff, v228\n\t"
    "v_and_b32 v237, 0x7fff7fff, v229\n\t"
    "v_and_b32 v238, 0x7fff7fff, v230\n\t"
    "v_and_b32 v239, 0x7fff7fff, v231\n\t"
    "v_and_b32 v240, 0x7fff7fff, v232\n\t"
    "ds_write_b128 v85, a[52:55] offset:5120\n\t"
    "v_and_b32 v241, 0x7fff7fff, v233\n\t"
    "v_and_b32 v242, 0x7fff7fff, v234\n\t"
    "v_and_b32 v243, 0x7fff7fff, v235\n\t"
    "v_pk_max_u16 v236, v236, v240\n\t"
    "v_pk_max_u16 v237, v237, v241\n\t"
    "v_pk_max_u16 v238, v238, v242\n\t"
    "v_pk_max_u16 v239, v239, v243\n\t"
    "v_pk_max_u16 v236, v236, v238\n\t"
    "v_pk_max_u16 v237, v237, v239\n\t"
    "v_pk_max_u16 v236, v236, v237\n\t"
    "ds_write_b128 v85, a[56:59] offset:6144\n\t"
    "buffer_load_dwordx4 v65, s[40:43], s44 offen offset:1024 lds\n\t"
    "v_max_u32_sdwa v236, v236, v236 dst_sel:DWORD dst_unused:UNUSED_PAD src0_sel:WORD_0 src1_sel:WORD_1\n\t"
    "ds_write_b32 v82, v236 offset:1024\n\t"
    "v_xor_b32 v237, 0x2000, v82\n\t"
    "v_pk_add_f32 v[184:185], v[184:185], v[216:217]\n\t"
    "v_pk_add_f32 v[186:187], v[186:187], v[218:219]\n\t"
    "v_pk_add_f32 v[188:189], v[188:189], v[220:221]\n\t"
    "v_pk_add_f32 v[190:191], v[190:191], v[222:223]\n\t"
    "v_pk_mul_f32 v[184:185], v[184:185], v[126:127] op_sel_hi:[1,0]\n\t"
    "v_pk_mul_f32 v[186:187], v[186:187], v[126:127] op_sel_hi:[1,0]\n\t"
    "ds_write_b128 v85, a[60:63] offset:7168\n\t"
    "v_pk_mul_f32 v[188:189], v[188:189], v[126:127] op_sel_hi:[1,0]\n\t"
    "v_pk_mul_f32 v[190:191], v[190:191], v[126:127] op_sel_hi:[1,0]\n\t"
    "v_cvt_pk_bf16_f32 v184, v184, v185\n\t"
    "v_cvt_pk_bf16_f32 v185, v186, v187\n\t"
    "v_cvt_pk_bf16_f32 v186, v188, v189\n\t"
    "v_cvt_pk_bf16_f32 v187, v190, v191\n\t"
    "global_store_dwordx4 v76, v[184:187], s[48:49] offset:3072 sc1\n\t"
    "s_sub_u32 s48, s48, 0x80000\n\t"
    "s_subb_u32 s49, s49, 0\n\t"
    "s_waitcnt lgkmcnt(0)\n\t"
    "s_barrier\n\t"
    "buffer_load_dwordx4 v66, s[40:43], s44 offen offset:2048 lds\n\t"
    "ds_read_b32 v114, v83 offset:256\n\t"
    "ds_read_b128 v[192:195], v88 offset:0\n\t"
    "ds_read_b128 v[196:199], v88 offset:1024\n\t"
    "ds_read_b128 v[200:203], v88 offset:2048\n\t"
    "ds_read_b128 v[204:207], v88 offset:3072\n\t"
    "ds_read_b128 v[208:211], v88 offset:4096\n\t"
    "ds_read_b128 v[212:215], v88 offset:5120\n\t"
    "ds_read_b128 v[216:219], v88 offset:6144\n\t"
    "ds_read_b128 v[220:223], v88 offset:7168\n\t"
    "ds_read_b32 v237, v237 offset:1024\n\t"
    "s_waitcnt lgkmcnt(9)\n\t"
    "v_add_f32 v74, v104, v114\n\t"
    "v_rcp_f32 v124, v74\n\t"
    "v_subrev_u32 v114, 0x1f800000, v74\n\t"
    "s_waitcnt lgkmcnt(8)\n\t"
    "v_pk_add_f32 v[128:129], v[128:129], v[192:193]\n\t"
    "v_pk_add_f32 v[130:131], v[130:131], v[194:195]\n\t"
    "s_waitcnt lgkmcnt(7)\n\t"
    "v_pk_add_f32 v[132:133], v[132:133], v[196:197]\n\t"
    "v_pk_add_f32 v[134:135], v[134:135], v[198:199]\n\t"
    "v_pk_mul_f32 v[128:129], v[128:129], v[124:125] op_sel_hi:[1,0]\n\t"
    "v_pk_mul_f32 v[130:131], v[130:131], v[124:125] op_sel_hi:[1,0]\n\t"
    "v_pk_mul_f32 v[132:133], v[132:133], v[124:125] op_sel_hi:[1,0]\n\t"
    "v_pk_mul_f32 v[134:135], v[134:135], v[124:125] op_sel_hi:[1,0]\n\t"
    "v_cvt_pk_bf16_f32 v128, v128, v129\n\t"
    "v_cvt_pk_bf16_f32 v129, v130, v131\n\t"
    "v_cvt_pk_bf16_f32 v130, v132, v133\n\t"
    "v_cvt_pk_bf16_f32 v131, v134, v135\n\t"
    "global_store_dwordx4 v76, v[128:131], s[48:49] offset:0 sc1\n\t"
    "s_waitcnt lgkmcnt(6)\n\t"
    "v_pk_add_f32 v[136:137], v[136:137], v[200:201]\n\t"
    "v_pk_add_f32 v[138:139], v[138:139], v[202:203]\n\t"
    "s_waitcnt lgkmcnt(5)\n\t"
    "v_pk_add_f32 v[140:141], v[140:141], v[204:205]\n\t"
    "v_pk_add_f32 v[142:143], v[142:143], v[206:207]\n\t"
    "v_pk_mul_f32 v[136:137], v[136:137], v[124:125] op_sel_hi:[1,0]\n\t"
    "v_pk_mul_f32 v[138:139], v[138:139], v[124:125] op_sel_hi:[1,0]\n\t"
    "v_pk_mul_f32 v[140:141], v[140:141], v[124:125] op_sel_hi:[1,0]\n\t"
    "v_pk_mul_f32 v[142:143], v[142:143], v[124:125] op_sel_hi:[1,0]\n\t"
    "v_cvt_pk_bf16_f32 v136, v136, v137\n\t"
    "v_cvt_pk_bf16_f32 v137, v138, v139\n\t"
    "v_cvt_pk_bf16_f32 v138, v140, v141\n\t"
    "v_cvt_pk_bf16_f32 v139, v142, v143\n\t"
    "buffer_load_dwordx4 v67, s[40:43], s44 offen offset:3072 lds\n\t"
    "s_add_u32 s44, s44, 0x8000\n\t"
    "global_store_dwordx4 v76, v[136:139], s[48:49] offset:1024 sc1\n\t"
    "s_waitcnt lgkmcnt(0)\n\t"
    "s_barrier\n\t"
    "v_max_u32 v236, v236, v237\n\t"
    "v_max_u32 v112, v114, v115\n\t"
    "v_cmp_lt_u32 vcc, 0x40000000, v112\n\t"
    "v_pk_add_f32 v[144:145], v[144:145], v[208:209]\n\t"
    "v_pk_add_f32 v[146:147], v[146:147], v[210:211]\n\t"
    "v_pk_add_f32 v[148:149], v[148:149], v[212:213]\n\t"
    "v_pk_add_f32 v[150:151], v[150:151], v[214:215]\n\t"
    "v_pk_mul_f32 v[144:145], v[144:145], v[124:125] op_sel_hi:[1,0]\n\t"
    "v_pk_mul_f32 v[146:147], v[146:147], v[124:125] op_sel_hi:[1,0]\n\t"
    "v_pk_mul_f32 v[148:149], v[148:149], v[124:125] op_sel_hi:[1,0]\n\t"
    "v_pk_mul_f32 v[150:151], v[150:151], v[124:125] op_sel_hi:[1,0]\n\t"
    "v_cvt_pk_bf16_f32 v144, v144, v145\n\t"
    "v_cvt_pk_bf16_f32 v145, v146, v147\n\t"
    "v_cvt_pk_bf16_f32 v146, v148, v149\n\t"
    "v_cvt_pk_bf16_f32 v147, v150, v151\n\t"
    "global_store_dwordx4 v76, v[144:147], s[48:49] offset:2048 sc1\n\t"
    "v_pk_add_f32 v[152:153], v[152:153], v[216:217]\n\t"
    "v_pk_add_f32 v[154:155], v[154:155], v[218:219]\n\t"
    "v_pk_add_f32 v[156:157], v[156:157], v[220:221]\n\t"
    "v_pk_add_f32 v[158:159], v[158:159], v[222:223]\n\t"
    "v_pk_mul_f32 v[152:153], v[152:153], v[124:125] op_sel_hi:[1,0]\n\t"
    "v_pk_mul_f32 v[154:155], v[154:155], v[124:125] op_sel_hi:[1,0]\n\t"
    "v_pk_mul_f32 v[156:157], v[156:157], v[124:125] op_sel_hi:[1,0]\n\t"
    "s_add_u32 m0, s45, 0x1000\n\t"
    "s_nop 0\n\t"
    "buffer_load_dwordx4 v64, s[40:43], s44 offen offset:0 lds\n\t"
    "v_pk_mul_f32 v[158:159], v[158:159], v[124:125] op_sel_hi:[1,0]\n\t"
    "v_cvt_pk_bf16_f32 v152, v152, v153\n\t"
    "v_cvt_pk_bf16_f32 v153, v154, v155\n\t"
    "v_cvt_pk_bf16_f32 v154, v156, v157\n\t"
    "v_cvt_pk_bf16_f32 v155, v158, v159\n\t"
    "global_store_dwordx4 v76, v[152:155], s[48:49] offset:3072 sc1\n\t"
    "buffer_load_dwordx4 v65, s[40:43], s44 offen offset:1024 lds\n\t"
    "buffer_load_dwordx4 v66, s[40:43], s44 offen offset:2048 lds\n\t"
    "buffer_load_dwordx4 v67, s[40:43], s44 offen offset:3072 lds\n\t"
    "s_add_u32 s44, s44, 0x8000\n\t"
    "s_cmp_lg_u64 vcc, 0\n\t"
    "s_cselect_b32 s25, 1, 0\n\t"
    "s_or_b32 s24, s24, s25\n\t"
    "s_cbranch_scc1 L_end_%=\n\t"
    "Ltail_fused:\n\t"
    "v_max_u32_dpp v236, v236, v236 row_shr:1 row_mask:0xf bank_mask:0xf\n\t"
    "s_lshl_b32 s25, s58, 10\n\t"
    "s_add_u32 s25, s25, 0x14100\n\t"
    "v_add_u32 v75, s25, v76\n\t"
    "v_max_u32_dpp v236, v236, v236 row_shr:2 row_mask:0xf bank_mask:0xf\n\t"
    "ds_write_b128 v75, v[244:247]\n\t"
    "v_add_u32 v75, 0x10000, v75\n\t"
    "v_lshrrev_b32 v73, 4, v76\n\t"
    "v_lshrrev_b32 v74, 8, v76\n\t"
    "v_max_u32_dpp v236, v236, v236 row_shr:4 row_mask:0xf bank_mask:0xf\n\t"
    "v_and_b32 v73, 15, v73\n\t"
    "s_lshl_b32 s16, s61, 16\n\t"
    "s_add_u32 s16, s16, 0x10000\n\t"
    "v_bfe_u32 v188, v73, 1, 2\n\t"
    "v_max_u32_dpp v236, v236, v236 row_shr:8 row_mask:0xf bank_mask:0xf\n\t"
    "v_mul_u32_u24 v188, 4160, v188\n\t"
    "v_and_b32 v189, 16, v76\n\t"
    "v_add_u32 v188, v188, v189\n\t"
    "ds_write_b128 v75, v[248:251]\n\t"
    "v_max_u32_dpp v236, v236, v236 row_bcast:15 row_mask:0xa bank_mask:0xf\n\t"
    "v_add_u32 v189, 0x5100, v76\n\t"
    "s_mov_b32 vcc_lo, -1\n\t"
    "s_mov_b32 vcc_hi, 0\n\t"
    "v_mov_b32 v58, 0\n\t"
    "v_max_u32_dpp v236, v236, v236 row_bcast:31 row_mask:0xc bank_mask:0xf\n\t"
    "v_mov_b32 v59, 0\n\t"
    "v_cndmask_b32 v188, v188, v189, vcc\n\t"
    "v_mov_b32 v60, 0\n\t"
    "v_mov_b32 v61, 0\n\t"
    "v_add_u32 v188, s16, v188\n\t"
    "v_readlane_b32 s25, v236, 63\n\t"
    "ds_write_b128 v188, v[58:61]\n\t"
    "v_mov_b32 v94, s34\n\t"
    "v_mov_b32 v93, s27\n\t"
    "s_lshr_b32 s25, s25, 7\n\t"
    "s_sub_i32 s25, 141, s25\n\t"
    "s_min_i32 s25, s25, 126\n\t"
    "s_sub_i32 s35, s35, s25\n\t"
    "s_sub_i32 s17, 127, s25\n\t"
    "s_lshl_b32 s17, s17, 23\n\t"
    "s_add_u32 s25, s25, 127\n\t"
    "s_lshl_b32 s14, s25, 23\n\t"
    "s_ashr_i32 s25, s35, 1\n\t"
    "s_sub_i32 s26, s35, s25\n\t"
    "s_add_u32 s25, s25, 1\n\t"
    "v_ldexp_f32 v94, v94, s25\n\t"
    "s_add_u32 s26, s26, 127\n\t"
    "s_lshl_b32 s15, s26, 23\n\t"
    "v_mov_b32 v92, s15\n\t"
    "v_mov_b32 v95, s17\n\t"
    "v_add_u32 v71, s16, v76\n\t"
    "s_lshl_b32 s26, s98, 11\n\t"
    "s_add_u32 s26, s26, s16\n\t"
    "s_add_u32 s26, s26, 32\n\t"
    "v_lshl_add_u32 v68, v76, 1, s26\n\t"
    "v_lshlrev_b32 v184, 16, v226\n\t"
    "v_and_b32 v185, 0xffff0000, v226\n\t"
    "v_mul_f32 v184, s14, v184\n\t"
    "v_mul_f32 v185, s14, v185\n\t"
    "v_cvt_pk_f16_f32 v32, v184, v185\n\t"
    "v_lshlrev_b32 v186, 16, v227\n\t"
    "v_and_b32 v187, 0xffff0000, v227\n\t"
    "v_mul_f32 v186, s14, v186\n\t"
    "v_mul_f32 v187, s14, v187\n\t"
    "v_cvt_pk_f16_f32 v33, v186, v187\n\t"
    "v_cvt_pk_f16_f32 v42, v186, v187\n\t"
    "v_alignbit_b32 v176, v33, v32, 16\n\t"
    "ds_write_b128 v71, v[92:95] offset:21248\n\t"
    "v_lshlrev_b32 v184, 16, v228\n\t"
    "v_and_b32 v185, 0xffff0000, v228\n\t"
    "v_mul_f32 v184, s14, v184\n\t"
    "v_mul_f32 v185, s14, v185\n\t"
    "v_cvt_pk_f16_f32 v34, v184, v185\n\t"
    "v_cvt_pk_f16_f32 v43, v184, v185\n\t"
    "v_alignbit_b32 v177, v34, v33, 16\n\t"
    "v_alignbit_b32 v50, v34, v33, 16\n\t"
    "v_lshlrev_b32 v186, 16, v229\n\t"
    "v_and_b32 v187, 0xffff0000, v229\n\t"
    "v_mul_f32 v186, s14, v186\n\t"
    "v_mul_f32 v187, s14, v187\n\t"
    "v_cvt_pk_f16_f32 v35, v186, v187\n\t"
    "v_cvt_pk_f16_f32 v44, v186, v187\n\t"
    "v_alignbit_b32 v178, v35, v34, 16\n\t"
    "v_alignbit_b32 v51, v35, v34, 16\n\t"
    "v_lshlrev_b32 v184, 16, v230\n\t"
    "v_and_b32 v185, 0xffff0000, v230\n\t"
    "v_mul_f32 v184, s14, v184\n\t"
    "v_mul_f32 v185, s14, v185\n\t"
    "v_cvt_pk_f16_f32 v36, v184, v185\n\t"
    "v_cvt_pk_f16_f32 v45, v184, v185\n\t"
    "v_alignbit_b32 v179, v36, v35, 16\n\t"
    "v_alignbit_b32 v52, v36, v35, 16\n\t"
    "ds_write_b128 v68, v[42:45] offset:8320\n\t"
    "v_lshlrev_b32 v186, 16, v231\n\t"
    "v_and_b32 v187, 0xffff0000, v231\n\t"
    "v_mul_f32 v186, s14, v186\n\t"
    "v_mul_f32 v187, s14, v187\n\t"
    "v_cvt_pk_f16_f32 v37, v186, v187\n\t"
    "v_cvt_pk_f16_f32 v46, v186, v187\n\t"
    "v_alignbit_b32 v180, v37, v36, 16\n\t"
    "v_alignbit_b32 v53, v37, v36, 16\n\t"
    "ds_write_b128 v68, v[176:179] offset:12480\n\t"
    "v_lshlrev_b32 v184, 16, v232\n\t"
    "v_and_b32 v185, 0xffff0000, v232\n\t"
    "v_mul_f32 v184, s14, v184\n\t"
    "v_mul_f32 v185, s14, v185\n\t"
    "v_cvt_pk_f16_f32 v38, v184, v185\n\t"
    "v_cvt_pk_f16_f32 v47, v184, v185\n\t"
    "v_alignbit_b32 v181, v38, v37, 16\n\t"
    "v_alignbit_b32 v54, v38, v37, 16\n\t"
    "ds_write_b128 v68, v[34:37] offset:0\n\t"
    "v_lshlrev_b32 v186, 16, v233\n\t"
    "v_and_b32 v187, 0xffff0000, v233\n\t"
    "v_mul_f32 v186, s14, v186\n\t"
    "v_mul_f32 v187, s14, v187\n\t"
    "v_cvt_pk_f16_f32 v39, v186, v187\n\t"
    "v_cvt_pk_f16_f32 v48, v186, v187\n\t"
    "v_alignbit_b32 v182, v39, v38, 16\n\t"
    "v_alignbit_b32 v55, v39, v38, 16\n\t"
    "v_lshlrev_b32 v184, 16, v234\n\t"
    "v_and_b32 v185, 0xffff0000, v234\n\t"
    "v_mul_f32 v184, s14, v184\n\t"
    "v_mul_f32 v185, s14, v185\n\t"
    "v_cvt_pk_f16_f32 v40, v184, v185\n\t"
    "v_cvt_pk_f16_f32 v49, v184, v185\n\t"
    "v_alignbit_b32 v183, v40, v39, 16\n\t"
    "v_alignbit_b32 v56, v40, v39, 16\n\t"
    "ds_write_b128 v68, v[50:53] offset:4160\n\t"
    "v_lshlrev_b32 v186, 16, v235\n\t"
    "v_and_b32 v187, 0xffff0000, v235\n\t"
    "v_mul_f32 v186, s14, v186\n\t"
    "v_mul_f32 v187, s14, v187\n\t"
    "v_cvt_pk_f16_f32 v41, v186, v187\n\t"
    "v_alignbit_b32 v57, v41, v40, 16\n\t"
    "ds_write_b128 v68, v[46:49] offset:8336\n\t"
    "v_sub_u32 v184, 3, v73\n\t"
    "v_and_b32 v184, 3, v184\n\t"
    "v_mul_u32_u24 v69, 4162, v184\n\t"
    "v_lshl_add_u32 v69, v73, 1, v69\n\t"
    "v_lshl_add_u32 v69, v74, 4, v69\n\t"
    "s_lshl_b32 s26, s98, 8\n\t"
    "s_add_u32 s26, s26, s16\n\t"
    "s_add_u32 s25, s26, 2\n\t"
    "ds_write_b128 v68, v[180:183] offset:12496\n\t"
    "v_add_u32 v69, s25, v69\n\t"
    "s_add_u32 s25, s26, 0x42e0\n\t"
    "v_lshl_add_u32 v70, v74, 4, s25\n\t"
    "v_lshlrev_b32 v184, 5, v73\n\t"
    "v_sub_u32 v70, v70, v184\n\t"
    "s_add_u32 s25, s45, 0x18000\n\t"
    "v_add_u32 v71, s25, v76\n\t"
    "s_lshl_b32 s25, s58, 11\n\t"
    "ds_write_b128 v68, v[38:41] offset:16\n\t"
    "s_add_u32 s25, s25, 0x18000\n\t"
    "v_add_u32 v72, s25, v76\n\t"
    "s_lshl_b32 s25, s72, 7\n\t"
    "s_add_u32 s25, s25, 0x9000\n\t"
    "v_mov_b32 v87, s25\n\t"
    "s_lshl_b32 s25, s60, 8\n\t"
    "s_and_b32 s26, s59, 4\n\t"
    "s_lshl_b32 s26, s26, 5\n\t"
    "ds_write_b128 v68, v[54:57] offset:4176\n\t"
    "s_add_u32 s26, s26, 0x9000\n\t"
    "s_add_u32 s52, s26, s25\n\t"
    "s_sub_u32 s26, s26, s25\n\t"
    "s_add_u32 s26, s26, 0xf00\n\t"
    "v_mov_b32 v254, s52\n\t"
    "v_mov_b32 v255, 1\n\t"
    "v_writelane_b32 v254, s26, 1\n\t"
    "s_waitcnt vmcnt(0)\n\t"
    "s_waitcnt lgkmcnt(0)\n\t"
    "s_barrier\n\t"
    "s_cmp_lg_u32 s58, 0\n\t"
    "s_cbranch_scc1 L_ns_%=\n\t"
    "s_mov_b64 exec, 3\n\t"
    "global_atomic_add v254, v255, s[84:85] sc1\n\t"
    "s_mov_b64 exec, -1\n\t"
    "L_ns_%=:\n\t"
    "s_bitcmp0_b32 s72, 0\n\t"
    "s_cbranch_scc1 L_v0_%=\n\t"
    "L_v1_%=:\n\t"
    "ds_read_b64 v[0:1], v69 offset:0\n\t"
    "ds_read_b64 v[2:3], v69 offset:8\n\t"
    "ds_read_b128 v[4:7], v70 offset:3584\n\t"
    "ds_read_b128 v[8:11], v70 offset:3072\n\t"
    "ds_read_b128 v[12:15], v70 offset:2560\n\t"
    "ds_read_b128 v[16:19], v70 offset:2048\n\t"
    "ds_read_b128 v[20:23], v70 offset:1536\n\t"
    "ds_read_b128 v[24:27], v70 offset:1024\n\t"
    "ds_read_b128 v[28:31], v70 offset:512\n\t"
    "ds_read_b128 v[32:35], v70 offset:0\n\t"
    "ds_read_b64 v[36:37], v69 offset:512\n\t"
    "ds_read_b64 v[38:39], v69 offset:520\n\t"
    "s_waitcnt lgkmcnt(9)\n\t"
    "v_mfma_f32_16x16x32_f16 v[224:227], v[0:3], v[4:7], 0\n\t"
    "s_lshr_b32 s25, s72, 1\n\t"
    "s_lshl_b32 s25, s25, 20\n\t"
    "s_waitcnt lgkmcnt(8)\n\t"
    "v_mfma_f32_16x16x32_f16 v[228:231], v[0:3], v[8:11], 0\n\t"
    "ds_read_b64 v[40:41], v69 offset:1024\n\t"
    "ds_read_b64_tr_b16 a[232:233], v80 offset:32768\n\t"
    "s_add_u32 m0, s45, 0x0\n\t"
    "s_waitcnt lgkmcnt(9)\n\t"
    "v_mfma_f32_16x16x32_f16 v[232:235], v[0:3], v[12:15], 0\n\t"
    "ds_read_b64 v[42:43], v69 offset:1032\n\t"
    "ds_read_b64_tr_b16 a[236:237], v80 offset:32776\n\t"
    "s_lshl_b32 s26, s73, 10\n\t"
    "s_waitcnt lgkmcnt(10)\n\t"
    "v_mfma_f32_16x16x32_f16 v[236:239], v[0:3], v[16:19], 0\n\t"
    "buffer_load_dwordx4 v64, s[40:43], s44 offen offset:0 lds\n\t"
    "s_waitcnt lgkmcnt(9)\n\t"
    "v_mfma_f32_16x16x32_f16 v[240:243], v[0:3], v[20:23], 0\n\t"
    "ds_read_b64 v[46:47], v69 offset:1544\n\t"
    "ds_read_b64_tr_b16 a[230:231], v79 offset:34824\n\t"
    "ds_read_b64_tr_b16 a[238:239], v80 offset:34824\n\t"
    "s_waitcnt lgkmcnt(11)\n\t"
    "v_mfma_f32_16x16x32_f16 v[244:247], v[0:3], v[24:27], 0\n\t"
    "buffer_load_dwordx4 v65, s[40:43], s44 offen offset:1024 lds\n\t"
    "s_waitcnt lgkmcnt(10)\n\t"
    "v_mfma_f32_16x16x32_f16 v[248:251], v[0:3], v[28:31], 0\n\t"
    "ds_read_b64 v[52:53], v69 offset:2560\n\t"
    "ds_read_b64_tr_b16 a[234:235], v80 offset:34816\n\t"
    "s_waitcnt lgkmcnt(11)\n\t"
    "v_mfma_f32_16x16x32_f16 v[252:255], v[0:3], v[32:35], 0\n\t"
    "ds_read_b64_tr_b16 a[224:225], v79 offset:32768\n\t"
    "ds_read_b64_tr_b16 a[228:229], v79 offset:32776\n\t"
    "ds_read_b64_tr_b16 a[134:135], v79 offset:38920\n\t"
    "s_waitcnt lgkmcnt(12)\n\t"
    "v_mfma_f32_16x16x32_f16 v[228:231], v[36:39], v[4:7], v[228:231]\n\t"
    "ds_read_b64_tr_b16 a[128:129], v79 offset:36864\n\t"
    "ds_read_b64_tr_b16 a[138:139], v80 offset:38912\n\t"
    "v_mfma_f32_16x16x32_f16 v[232:235], v[36:39], v[8:11], v[232:235]\n\t"
    "ds_read_b64 v[56:57], v69 offset:3072\n\t"
    "ds_read_b64_tr_b16 a[140:141], v80 offset:36872\n\t"
    "ds_read_b64_tr_b16 a[142:143], v80 offset:38920\n\t"
    "v_mfma_f32_16x16x32_f16 v[236:239], v[36:39], v[12:15], v[236:239]\n\t"
    "buffer_load_dwordx4 v66, s[40:43], s44 offen offset:2048 lds\n\t"
    "v_mfma_f32_16x16x32_f16 v[240:243], v[36:39], v[16:19], v[240:243]\n\t"
    "ds_read_b64 v[0:1], v69 offset:64\n\t"
    "ds_read_b64_tr_b16 a[132:133], v79 offset:36872\n\t"
    "s_add_u32 s25, s25, s26\n\t"
    "v_mfma_f32_16x16x32_f16 v[244:247], v[36:39], v[20:23], v[244:247]\n\t"
    "ds_read_b64 v[44:45], v69 offset:1536\n\t"
    "ds_read_b64_tr_b16 a[226:227], v79 offset:34816\n\t"
    "s_add_u32 s25, s25, 0x3000000\n\t"
    "v_mfma_f32_16x16x32_f16 v[248:251], v[36:39], v[24:27], v[248:251]\n\t"
    "buffer_load_dwordx4 v67, s[40:43], s44 offen offset:3072 lds\n\t"
    "v_mfma_f32_16x16x32_f16 v[252:255], v[36:39], v[28:31], v[252:255]\n\t"
    "ds_read_b64 v[60:61], v69 offset:3584\n\t"
    "ds_read_b64 v[62:63], v69 offset:3592\n\t"
    "ds_read_b64_tr_b16 a[136:137], v80 offset:36864\n\t"
    "s_waitcnt lgkmcnt(13)\n\t"
    "v_mfma_f32_16x16x32_f16 v[232:235], v[40:43], v[4:7], v[232:235]\n\t"
    "ds_read_b64 v[50:51], v69 offset:2056\n\t"
    "ds_read_b64 v[2:3], v69 offset:72\n\t"
    "v_mfma_f32_16x16x32_f16 v[236:239], v[40:43], v[8:11], v[236:239]\n\t"
    "ds_read_b64_tr_b16 a[130:131], v79 offset:38912\n\t"
    "ds_read_b64_tr_b16 a[146:147], v77 offset:34816\n\t"
    "ds_read_b64_tr_b16 a[158:159], v78 offset:34824\n\t"
    "v_mfma_f32_16x16x32_f16 v[240:243], v[40:43], v[12:15], v[240:243]\n\t"
    "ds_read_b64 v[48:49], v69 offset:2048\n\t"
    "ds_read_b128 v[28:31], v70 offset:3136\n\t"
    "ds_read_b128 v[36:39], v70 offset:2624\n\t"
    "v_mfma_f32_16x16x32_f16 v[244:247], v[40:43], v[16:19], v[244:247]\n\t"
    "ds_read_b64 v[58:59], v69 offset:3080\n\t"
    "s_add_u32 s44, s44, 0x8000\n\t"
    "v_mfma_f32_16x16x32_f16 v[248:251], v[40:43], v[20:23], v[248:251]\n\t"
    "ds_read_b64 v[54:55], v69 offset:2568\n\t"
    "ds_read_b64_tr_b16 a[148:149], v77 offset:32776\n\t"
    "ds_read_b64_tr_b16 a[156:157], v78 offset:32776\n\t"
    "v_mfma_f32_16x16x32_f16 v[252:255], v[40:43], v[24:27], v[252:255]\n\t"
    "s_add_u32 s36, s20, s25\n\t"
    "s_addc_u32 s37, s21, 0\n\t"
    "s_waitcnt lgkmcnt(15)\n\t"
    "s_add_u32 m0, s45, 0x18000\n\t"
    "v_mfma_f32_16x16x32_f16 v[236:239], v[44:47], v[4:7], v[236:239]\n\t"
    "ds_read_b128 v[32:35], v70 offset:3648\n\t"
    "ds_read_b128 v[40:43], v70 offset:1600\n\t"
    "ds_read_b64_tr_b16 a[150:151], v77 offset:34824\n\t"
    "v_mfma_f32_16x16x32_f16 v[240:243], v[44:47], v[8:11], v[240:243]\n\t"
    "buffer_load_dwordx4 v64, s[40:43], s44 offen offset:0 lds\n\t"
    "v_mfma_f32_16x16x32_f16 v[244:247], v[44:47], v[12:15], v[244:247]\n\t"
    "buffer_load_dwordx4 v65, s[40:43], s44 offen offset:1024 lds\n\t"
    "v_mfma_f32_16x16x32_f16 v[248:251], v[44:47], v[16:19], v[248:251]\n\t"
    "ds_read_b128 v[24:27], v70 offset:2112\n\t"
    "ds_read_b64_tr_b16 a[152:153], v78 offset:32768\n\t"
    "ds_read_b64_tr_b16 a[154:155], v78 offset:34816\n\t"
    "v_mfma_f32_16x16x32_f16 v[252:255], v[44:47], v[20:23], v[252:255]\n\t"
    "buffer_load_dwordx4 v66, s[40:43], s44 offen offset:2048 lds\n\t"
    "s_waitcnt lgkmcnt(10)\n\t"
    "v_mfma_f32_16x16x32_f16 v[240:243], v[48:51], v[4:7], v[240:243]\n\t"
    "ds_read_b128 v[44:47], v70 offset:576\n\t"
    "ds_read_b64_tr_b16 a[144:145], v77 offset:32768\n\t"
    "s_mov_b32 s52, 0x0\n\t"
    "v_mfma_f32_16x16x32_f16 v[244:247], v[48:51], v[8:11], v[244:247]\n\t"
    "buffer_load_dwordx4 v67, s[40:43], s44 offen offset:3072 lds\n\t"
    "v_mfma_f32_16x16x32_f16 v[248:251], v[48:51], v[12:15], v[248:251]\n\t"
    "s_mov_b32 s53, 0x1000\n\t"
    "s_add_u32 s44, s44, 0x8000\n\t"
    "v_mfma_f32_16x16x32_f16 v[252:255], v[48:51], v[16:19], v[252:255]\n\t"
    "ds_read_b128 v[16:19], v70 offset:64\n\t"
    "ds_read_b64_tr_b16 a[10:11], v78 offset:38912\n\t"
    "s_add_u32 m0, s45, 0x19000\n\t"
    "s_waitcnt lgkmcnt(8)\n\t"
    "v_mfma_f32_16x16x32_f16 v[244:247], v[52:55], v[4:7], v[244:247]\n\t"
    "buffer_load_dwordx4 v64, s[40:43], s44 offen offset:0 lds\n\t"
    "v_mfma_f32_16x16x32_f16 v[248:251], v[52:55], v[8:11], v[248:251]\n\t"
    "buffer_load_dwordx4 v65, s[40:43], s44 offen offset:1024 lds\n\t"
    "v_mfma_f32_16x16x32_f16 v[252:255], v[52:55], v[12:15], v[252:255]\n\t"
    "ds_read_b128 v[20:23], v70 offset:1088\n\t"
    "ds_read_b64_tr_b16 a[8:9], v78 offset:36864\n\t"
    "ds_read_b64_tr_b16 a[14:15], v78 offset:38920\n\t"
    "v_mfma_f32_16x16x32_f16 v[248:251], v[56:59], v[4:7], v[248:251]\n\t"
    "ds_read_b64 v[50:51], v69 offset:584\n\t"
    "ds_read_b64 v[12:13], v69 offset:1088\n\t"
    "ds_read_b64_tr_b16 a[2:3], v77 offset:38912\n\t"
    "v_mfma_f32_16x16x32_f16 v[252:255], v[56:59], v[8:11], v[252:255]\n\t"
    "ds_read_b64 v[48:49], v69 offset:576\n\t"
    "ds_read_b64 v[10:11], v69 offset:2120\n\t"
    "ds_read_b64_tr_b16 a[6:7], v77 offset:38920\n\t"
    "v_mfma_f32_16x16x32_f16 v[252:255], v[60:63], v[4:7], v[252:255]\n\t"
    "ds_read_b64_tr_b16 a[0:1], v77 offset:36864\n\t"
    "ds_read_b64_tr_b16 a[12:13], v78 offset:36872\n\t"
    "s_mov_b32 s54, 0x2000\n\t"
    "v_mfma_f32_16x16x32_f16 v[224:227], v[0:3], v[32:35], v[224:227]\n\t"
    "buffer_load_dwordx4 v66, s[40:43], s44 offen offset:2048 lds\n\t"
    "v_mfma_f32_16x16x32_f16 v[228:231], v[0:3], v[28:31], v[228:231]\n\t"
    "buffer_load_dwordx4 v67, s[40:43], s44 offen offset:3072 lds\n\t"
    "v_mfma_f32_16x16x32_f16 v[232:235], v[0:3], v[36:39], v[232:235]\n\t"
    "ds_read_b64 v[14:15], v69 offset:1096\n\t"
    "ds_read_b64_tr_b16 a[4:5], v77 offset:36872\n\t"
    "s_mov_b32 s55, 0x3000\n\t"
    "s_waitcnt lgkmcnt(13)\n\t"
    "v_mfma_f32_16x16x32_f16 v[236:239], v[0:3], v[24:27], v[236:239]\n\t"
    "s_add_u32 s25, s58, 0\n\t"
    "s_add_u32 s44, s44, 0x8000\n\t"
    "s_add_u32 m0, s45, 0x8000\n\t"
    "v_mfma_f32_16x16x32_f16 v[240:243], v[0:3], v[40:43], v[240:243]\n\t"
    "global_load_dword v88, v87, s[84:85] sc1\n\t"
    "s_waitcnt lgkmcnt(9)\n\t"
    "v_mfma_f32_16x16x32_f16 v[244:247], v[0:3], v[20:23], v[244:247]\n\t"
    "ds_read_b64 v[54:55], v69 offset:1608\n\t"
    "v_mfma_f32_16x16x32_f16 v[248:251], v[0:3], v[44:47], v[248:251]\n\t"
    "buffer_load_dwordx4 v64, s[40:43], s44 offen offset:0 lds\n\t"
    "v_mfma_f32_16x16x32_f16 v[252:255], v[0:3], v[16:19], v[252:255]\n\t"
    "ds_read_b64 v[52:53], v69 offset:1600\n\t"
    "ds_read_b64_tr_b16 a[26:27], v78 offset:6144\n\t"
    "ds_read_b64_tr_b16 a[28:29], v78 offset:4104\n\t"
    "s_waitcnt lgkmcnt(7)\n\t"
    "v_mfma_f32_16x16x32_f16 v[228:231], v[48:51], v[32:35], v[228:231]\n\t"
    "ds_read_b64 v[4:5], v69 offset:3136\n\t"
    "ds_read_b64_tr_b16 a[24:25], v78 offset:4096\n\t"
    "v_mfma_f32_16x16x32_f16 v[232:235], v[48:51], v[28:31], v[232:235]\n\t"
    "buffer_load_dwordx4 v65, s[40:43], s44 offen offset:1024 lds\n\t"
    "v_mfma_f32_16x16x32_f16 v[236:239], v[48:51], v[36:39], v[236:239]\n\t"
    "ds_read_b64 v[60:61], v69 offset:3648\n\t"
    "ds_read_b64_tr_b16 a[30:31], v78 offset:6152\n\t"
    "s_and_b32 s25, s25, 3\n\t"
    "v_mfma_f32_16x16x32_f16 v[240:243], v[48:51], v[24:27], v[240:243]\n\t"
    "ds_read_b64 v[58:59], v69 offset:2632\n\t"
    "ds_read_b64_tr_b16 a[18:19], v77 offset:6144\n\t"
    "s_waitcnt vmcnt(11)\n\t"
    "v_mfma_f32_16x16x32_f16 v[244:247], v[48:51], v[40:43], v[244:247]\n\t"
    "buffer_load_dwordx4 v66, s[40:43], s44 offen offset:2048 lds\n\t"
    "v_mfma_f32_16x16x32_f16 v[248:251], v[48:51], v[20:23], v[248:251]\n\t"
    "ds_read_b64_tr_b16 a[16:17], v77 offset:4096\n\t"
    "s_lshl_b32 s25, s25, 17\n\t"
    "v_add_u32 v82, s25, v76\n\t"
    "v_mfma_f32_16x16x32_f16 v[252:255], v[48:51], v[44:47], v[252:255]\n\t"
    "ds_read_b64_tr_b16 a[22:23], v77 offset:6152\n\t"
    "ds_read_b64_tr_b16 a[42:43], v78 offset:2048\n\t"
    "s_add_u32 s25, s58, 1\n\t"
    "s_waitcnt lgkmcnt(10)\n\t"
    "v_mfma_f32_16x16x32_f16 v[232:235], v[12:15], v[32:35], v[232:235]\n\t"
    "buffer_load_dwordx4 v67, s[40:43], s44 offen offset:3072 lds\n\t"
    "v_mfma_f32_16x16x32_f16 v[236:239], v[12:15], v[28:31], v[236:239]\n\t"
    "ds_read_b128 v[44:47], v70 offset:3200\n\t"
    "s_add_u32 s44, s44, 0x8000\n\t"
    "s_add_u32 m0, s45, 0x9000\n\t"
    "v_mfma_f32_16x16x32_f16 v[240:243], v[12:15], v[36:39], v[240:243]\n\t"
    "ds_read_b64 v[8:9], v69 offset:2112\n\t"
    "ds_read_b128 v[16:19], v70 offset:3712\n\t"
    "ds_read_b64_tr_b16 a[20:21], v77 offset:4104\n\t"
    "v_mfma_f32_16x16x32_f16 v[244:247], v[12:15], v[24:27], v[244:247]\n\t"
    "ds_read_b64 v[56:57], v69 offset:2624\n\t"
    "ds_read_b64_tr_b16 a[32:33], v77 offset:0\n\t"
    "ds_read_b64_tr_b16 a[36:37], v77 offset:8\n\t"
    "v_mfma_f32_16x16x32_f16 v[248:251], v[12:15], v[40:43], v[248:251]\n\t"
    "buffer_load_dwordx4 v64, s[40:43], s44 offen offset:0 lds\n\t"
    "v_mfma_f32_16x16x32_f16 v[252:255], v[12:15], v[20:23], v[252:255]\n\t"
    "ds_read_b64 v[6:7], v69 offset:3144\n\t"
    "ds_read_b64_tr_b16 a[38:39], v77 offset:2056\n\t"
    "v_mfma_f32_16x16x32_f16 v[236:239], v[52:55], v[32:35], v[236:239]\n\t"
    "ds_read_b64_tr_b16 a[34:35], v77 offset:2048\n\t"
    "ds_read_b64_tr_b16 a[44:45], v78 offset:8\n\t"
    "v_mfma_f32_16x16x32_f16 v[240:243], v[52:55], v[28:31], v[240:243]\n\t"
    "buffer_load_dwordx4 v65, s[40:43], s44 offen offset:1024 lds\n\t"
    "v_mfma_f32_16x16x32_f16 v[244:247], v[52:55], v[36:39], v[244:247]\n\t"
    "ds_read_b64 v[62:63], v69 offset:3656\n\t"
    "ds_read_b64_tr_b16 a[40:41], v78 offset:0\n\t"
    "ds_read_b64_tr_b16 a[46:47], v78 offset:2056\n\t"
    "v_mfma_f32_16x16x32_f16 v[248:251], v[52:55], v[24:27], v[248:251]\n\t"
    "buffer_load_dwordx4 v66, s[40:43], s44 offen offset:2048 lds\n\t"
    "v_mfma_f32_16x16x32_f16 v[252:255], v[52:55], v[40:43], v[252:255]\n\t"
    "ds_read_b64 v[2:3], v69 offset:136\n\t"
    "ds_read_b128 v[52:55], v70 offset:640\n\t"
    "s_and_b32 s25, s25, 3\n\t"
    "s_waitcnt lgkmcnt(12)\n\t"
    "v_mfma_f32_16x16x32_f16 v[240:243], v[8:11], v[32:35], v[240:243]\n\t"
    "ds_read_b64 v[0:1], v69 offset:128\n\t"
    "ds_read_b128 v[40:43], v70 offset:1152\n\t"
    "s_waitcnt vmcnt(12)\n\t"
    "v_mfma_f32_16x16x32_f16 v[244:247], v[8:11], v[28:31], v[244:247]\n\t"
    "buffer_load_dwordx4 v67, s[40:43], s44 offen offset:3072 lds\n\t"
    "v_mfma_f32_16x16x32_f16 v[248:251], v[8:11], v[36:39], v[248:251]\n\t"
    "ds_read_b128 v[12:15], v70 offset:2176\n\t"
    "ds_read_b64_tr_b16 a[58:59], v80 offset:34816\n\t"
    "Lis12_%=:\n\t"
    "s_cbranch_execz Lis16_%=\n\t"
    "v_mfma_f32_16x16x32_f16 v[252:255], v[8:11], v[24:27], v[252:255]\n\t"
    "ds_read_b128 v[48:51], v70 offset:2688\n\t"
    "s_add_u32 s44, s44, 0x8000\n\t"
    "s_add_u32 m0, s45, 0x1000\n\t"
    "s_waitcnt lgkmcnt(10)\n\t"
    "v_mfma_f32_16x16x32_f16 v[244:247], v[56:59], v[32:35], v[244:247]\n\t"
    "ds_read_b64_tr_b16 a[52:53], v79 offset:32776\n\t"
    "ds_read_b64_tr_b16 a[54:55], v79 offset:34824\n\t"
    "ds_read_b64_tr_b16 a[56:57], v80 offset:32768\n\t"
    "v_mfma_f32_16x16x32_f16 v[248:251], v[56:59], v[28:31], v[248:251]\n\t"
    "ds_read_b128 v[20:23], v70 offset:1664\n\t"
    "ds_read_b64_tr_b16 a[48:49], v79 offset:32768\n\t"
    "s_lshl_b32 s25, s25, 17\n\t"
    "v_mfma_f32_16x16x32_f16 v[252:255], v[56:59], v[36:39], v[252:255]\n\t"
    "ds_read_b128 v[8:11], v70 offset:128\n\t"
    "ds_read_b64 v[58:59], v69 offset:1672\n\t"
    "ds_read_b64_tr_b16 a[62:63], v80 offset:34824\n\t"
    "v_mfma_f32_16x16x32_f16 v[248:251], v[4:7], v[32:35], v[248:251]\n\t"
    "buffer_load_dwordx4 v64, s[40:43], s44 offen offset:0 lds\n\t"
    "v_mfma_f32_16x16x32_f16 v[252:255], v[4:7], v[28:31], v[252:255]\n\t"
    "ds_read_b64 v[24:25], v69 offset:640\n\t"
    "v_add_u32 v83, s25, v76\n\t"
    "s_waitcnt lgkmcnt(12)\n\t"
    "v_mfma_f32_16x16x32_f16 v[252:255], v[60:63], v[32:35], v[252:255]\n\t"
    "ds_read_b64 v[26:27], v69 offset:648\n\t"
    "ds_read_b64_tr_b16 a[60:61], v80 offset:32776\n\t"
    "s_add_u32 s25, s58, 2\n\t"
    "v_mfma_f32_16x16x32_f16 v[224:227], v[0:3], v[16:19], v[224:227]\n\t"
    "buffer_load_dwordx4 v65, s[40:43], s44 offen offset:1024 lds\n\t"
    "v_mfma_f32_16x16x32_f16 v[228:231], v[0:3], v[44:47], v[228:231]\n\t"
    "ds_read_b64_tr_b16 a[50:51], v79 offset:34816\n\t"
    "s_waitcnt vmcnt(11)\n\t"
    "s_waitcnt lgkmcnt(11)\n\t"
    "v_mfma_f32_16x16x32_f16 v[232:235], v[0:3], v[48:51], v[232:235]\n\t"
    "buffer_load_dwordx4 v66, s[40:43], s44 offen offset:2048 lds\n\t"
    "v_mfma_f32_16x16x32_f16 v[236:239], v[0:3], v[12:15], v[236:239]\n\t"
    "ds_read_b64_tr_b16 a[64:65], v79 offset:36864\n\t"
    "ds_read_b64_tr_b16 a[76:77], v80 offset:36872\n\t"
    "s_waitcnt lgkmcnt(9)\n\t"
    "v_mfma_f32_16x16x32_f16 v[240:243], v[0:3], v[20:23], v[240:243]\n\t"
    "ds_read_b64 v[36:37], v69 offset:1152\n\t"
    "ds_read_b64 v[38:39], v69 offset:1160\n\t"
    "v_mfma_f32_16x16x32_f16 v[244:247], v[0:3], v[40:43], v[244:247]\n\t"
    "buffer_load_dwordx4 v67, s[40:43], s44 offen offset:3072 lds\n\t"
    "v_mfma_f32_16x16x32_f16 v[248:251], v[0:3], v[52:55], v[248:251]\n\t"
    "ds_read_b64_tr_b16 a[72:73], v80 offset:36864\n\t"
    "s_and_b32 s25, s25, 3\n\t"
    "s_lshl_b32 s25, s25, 17\n\t"
    "s_waitcnt lgkmcnt(8)\n\t"
    "v_mfma_f32_16x16x32_f16 v[252:255], v[0:3], v[8:11], v[252:255]\n\t"
    "ds_read_b64 v[62:63], v69 offset:3720\n\t"
    "v_add_u32 v84, s25, v76\n\t"
    "s_waitcnt lgkmcnt(8)\n\t"
    "v_mfma_f32_16x16x32_f16 v[228:231], v[24:27], v[16:19], v[228:231]\n\t"
    "ds_read_b64_tr_b16 a[70:71], v79 offset:38920\n\t"
    "ds_read_b64_tr_b16 a[78:79], v80 offset:38920\n\t"
    "v_mfma_f32_16x16x32_f16 v[232:235], v[24:27], v[44:47], v[232:235]\n\t"
    "ds_read_b64 v[6:7], v69 offset:2184\n\t"
    "ds_read_b64_tr_b16 a[74:75], v80 offset:38912\n\t"
    "v_mfma_f32_16x16x32_f16 v[236:239], v[24:27], v[48:51], v[236:239]\n\t"
    "ds_read_b64_tr_b16 a[68:69], v79 offset:36872\n\t"
    "s_add_u32 s25, s58, 3\n\t"
    "v_mfma_f32_16x16x32_f16 v[240:243], v[24:27], v[12:15], v[240:243]\n\t"
    "ds_read_b128 v[8:11], v70 offset:3776\n\t"
    "ds_read_b64_tr_b16 a[66:67], v79 offset:38912\n\t"
    "s_and_b32 s25, s25, 3\n\t"
    "v_mfma_f32_16x16x32_f16 v[244:247], v[24:27], v[20:23], v[244:247]\n\t"
    "ds_read_b64 v[56:57], v69 offset:1664\n\t"
    "s_lshl_b32 s25, s25, 17\n\t"
    "v_mfma_f32_16x16x32_f16 v[248:251], v[24:27], v[40:43], v[248:251]\n\t"
    "ds_read_b64 v[60:61], v69 offset:3712\n\t"
    "v_add_u32 v85, s25, v76\n\t"
    "v_mfma_f32_16x16x32_f16 v[252:255], v[24:27], v[52:55], v[252:255]\n\t"
    "ds_read_b64 v[32:33], v69 offset:3200\n\t"
    "s_waitcnt vmcnt(8)\n\t"
    "s_waitcnt lgkmcnt(11)\n\t"
    "v_mfma_f32_16x16x32_f16 v[232:235], v[36:39], v[16:19], v[232:235]\n\t"
    "ds_read_b64 v[4:5], v69 offset:2176\n\t"
    "s_add_u32 s44, s44, 0x8000\n\t"
    "v_mfma_f32_16x16x32_f16 v[236:239], v[36:39], v[44:47], v[236:239]\n\t"
    "ds_read_b64_tr_b16 a[82:83], v77 offset:34816\n\t"
    "ds_read_b64_tr_b16 a[84:85], v77 offset:32776\n\t"
    "v_mfma_f32_16x16x32_f16 v[240:243], v[36:39], v[48:51], v[240:243]\n\t"
    "ds_read_b64_tr_b16 a[92:93], v78 offset:32776\n\t"
    "ds_read_b64_tr_b16 a[94:95], v78 offset:34824\n\t"
    "v_mfma_f32_16x16x32_f16 v[244:247], v[36:39], v[12:15], v[244:247]\n\t"
    "ds_read_b64 v[28:29], v69 offset:2688\n\t"
    "ds_read_b64 v[30:31], v69 offset:2696\n\t"
    "ds_read_b128 v[24:27], v70 offset:3264\n\t"
    "v_mfma_f32_16x16x32_f16 v[248:251], v[36:39], v[20:23], v[248:251]\n\t"
    "ds_read_b64_tr_b16 a[86:87], v77 offset:34824\n\t"
    "v_readfirstlane_b32 s90, v88\n\t"
    "v_mfma_f32_16x16x32_f16 v[252:255], v[36:39], v[40:43], v[252:255]\n\t"
    "s_cmp_ge_u32 s90, 16\n\t"
    "s_cselect_b32 s38, 0xe8000, 0\n\t"
    "s_waitcnt lgkmcnt(11)\n\t"
    "v_mfma_f32_16x16x32_f16 v[236:239], v[56:59], v[16:19], v[236:239]\n\t"
    "buffer_load_dwordx4 v[96:99], v83, s[36:39], s52 offen offset:0 sc1\n\t"
    "v_mfma_f32_16x16x32_f16 v[240:243], v[56:59], v[44:47], v[240:243]\n\t"
    "ds_read_b64 v[34:35], v69 offset:3208\n\t"
    "ds_read_b64_tr_b16 a[88:89], v78 offset:32768\n\t"
    "v_mfma_f32_16x16x32_f16 v[244:247], v[56:59], v[48:51], v[244:247]\n\t"
    "buffer_load_dwordx4 v[100:103], v83, s[36:39], s52 offen offset:1024 sc1\n\t"
    "v_mfma_f32_16x16x32_f16 v[248:251], v[56:59], v[12:15], v[248:251]\n\t"
    "ds_read_b128 v[52:55], v70 offset:2752\n\t"
    "ds_read_b64_tr_b16 a[80:81], v77 offset:32768\n\t"
    "ds_read_b64_tr_b16 a[90:91], v78 offset:34816\n\t"
    "v_mfma_f32_16x16x32_f16 v[252:255], v[56:59], v[20:23], v[252:255]\n\t"
    "ds_read_b64 v[2:3], v69 offset:200\n\t"
    "ds_read_b128 v[56:59], v70 offset:704\n\t"
    "s_waitcnt lgkmcnt(11)\n\t"
    "v_mfma_f32_16x16x32_f16 v[240:243], v[4:7], v[16:19], v[240:243]\n\t"
    "buffer_load_dwordx4 v[104:107], v83, s[36:39], s52 offen offset:2048 sc1\n\t"
    "v_mfma_f32_16x16x32_f16 v[244:247], v[4:7], v[44:47], v[244:247]\n\t"
    "ds_read_b64 v[0:1], v69 offset:192\n\t"
    "s_waitcnt vmcnt(7)\n\t"
    "v_mfma_f32_16x16x32_f16 v[248:251], v[4:7], v[48:51], v[248:251]\n\t"
    "buffer_load_dwordx4 v[108:111], v83, s[36:39], s52 offen offset:3072 sc1\n\t"
    "v_mfma_f32_16x16x32_f16 v[252:255], v[4:7], v[12:15], v[252:255]\n\t"
    "ds_read_b128 v[36:39], v70 offset:2240\n\t"
    "ds_read_b128 v[40:43], v70 offset:1728\n\t"
    "s_waitcnt lgkmcnt(10)\n\t"
    "v_mfma_f32_16x16x32_f16 v[244:247], v[28:31], v[16:19], v[244:247]\n\t"
    "ds_read_b128 v[20:23], v70 offset:1216\n\t"
    "ds_read_b64_tr_b16 a[98:99], v77 offset:38912\n\t"
    "v_mfma_f32_16x16x32_f16 v[248:251], v[28:31], v[44:47], v[248:251]\n\t"
    "buffer_load_dwordx4 v[112:115], v83, s[36:39], s53 offen offset:0 sc1\n\t"
    "v_mfma_f32_16x16x32_f16 v[252:255], v[28:31], v[48:51], v[252:255]\n\t"
    "ds_read_b128 v[4:7], v70 offset:192\n\t"
    "ds_read_b64_tr_b16 a[104:105], v78 offset:36864\n\t"
    "s_waitcnt lgkmcnt(12)\n\t"
    "v_mfma_f32_16x16x32_f16 v[248:251], v[32:35], v[16:19], v[248:251]\n\t"
    "buffer_load_dwordx4 v[116:119], v83, s[36:39], s53 offen offset:1024 sc1\n\t"
    "v_mfma_f32_16x16x32_f16 v[252:255], v[32:35], v[44:47], v[252:255]\n\t"
    "ds_read_b64 v[14:15], v69 offset:712\n\t"
    "ds_read_b64_tr_b16 a[102:103], v77 offset:38920\n\t"
    "v_mfma_f32_16x16x32_f16 v[252:255], v[60:63], v[16:19], v[252:255]\n\t"
    "ds_read_b64 v[12:13], v69 offset:704\n\t"
    "ds_read_b64_tr_b16 a[108:109], v78 offset:36872\n\t"
    "s_waitcnt lgkmcnt(10)\n\t"
    "v_mfma_f32_16x16x32_f16 v[224:227], v[0:3], v[8:11], v[224:227]\n\t"
    "buffer_load_dwordx4 v[120:123], v83, s[36:39], s53 offen offset:2048 sc1\n\t"
    "v_mfma_f32_16x16x32_f16 v[228:231], v[0:3], v[24:27], v[228:231]\n\t"
    "ds_read_b64_tr_b16 a[100:101], v77 offset:36872\n\t"
    "ds_read_b64_tr_b16 a[106:107], v78 offset:38912\n\t"
    "v_mfma_f32_16x16x32_f16 v[232:235], v[0:3], v[52:55], v[232:235]\n\t"
    "buffer_load_dwordx4 v[124:127], v83, s[36:39], s53 offen offset:3072 sc1\n\t"
    "s_waitcnt lgkmcnt(11)\n\t"
    "v_mfma_f32_16x16x32_f16 v[236:239], v[0:3], v[36:39], v[236:239]\n\t"
    "ds_read_b64_tr_b16 a[96:97], v77 offset:36864\n\t"
    "ds_read_b64_tr_b16 a[110:111], v78 offset:38920\n\t"
    "s_waitcnt lgkmcnt(11)\n\t"
    "v_mfma_f32_16x16x32_f16 v[240:243], v[0:3], v[40:43], v[240:243]\n\t"
    "ds_read_b64 v[28:29], v69 offset:1216\n\t"
    "s_waitcnt vmcnt(8)\n\t"
    "v_mfma_f32_16x16x32_f16 v[244:247], v[0:3], v[20:23], v[244:247]\n\t"
    "buffer_load_dwordx4 v[128:131], v83, s[36:39], s54 offen offset:0 sc1\n\t"
    "v_mfma_f32_16x16x32_f16 v[248:251], v[0:3], v[56:59], v[248:251]\n\t"
    "ds_read_b64 v[30:31], v69 offset:1224\n\t"
    "ds_read_b64 v[50:51], v69 offset:1736\n\t"
    "s_waitcnt lgkmcnt(11)\n\t"
    "v_mfma_f32_16x16x32_f16 v[252:255], v[0:3], v[4:7], v[252:255]\n\t"
    "ds_read_b64 v[48:49], v69 offset:1728\n\t"
    "ds_read_b64_tr_b16 a[122:123], v78 offset:6144\n\t"
    "s_waitcnt lgkmcnt(10)\n\t"
    "v_mfma_f32_16x16x32_f16 v[228:231], v[12:15], v[8:11], v[228:231]\n\t"
    "buffer_load_dwordx4 v[132:135], v83, s[36:39], s54 offen offset:1024 sc1\n\t"
    "v_mfma_f32_16x16x32_f16 v[232:235], v[12:15], v[24:27], v[232:235]\n\t"
    "ds_read_b64 v[32:33], v69 offset:2240\n\t"
    "v_mfma_f32_16x16x32_f16 v[236:239], v[12:15], v[52:55], v[236:239]\n\t"
    "buffer_load_dwordx4 v[136:139], v83, s[36:39], s54 offen offset:2048 sc1\n\t"
    "v_mfma_f32_16x16x32_f16 v[240:243], v[12:15], v[36:39], v[240:243]\n\t"
    "ds_read_b64_tr_b16 a[112:113], v77 offset:4096\n\t"
    "ds_read_b64_tr_b16 a[114:115], v77 offset:6144\n\t"
    "v_mfma_f32_16x16x32_f16 v[244:247], v[12:15], v[40:43], v[244:247]\n\t"
    "buffer_load_dwordx4 v[140:143], v83, s[36:39], s54 offen offset:3072 sc1\n\t"
    "v_mfma_f32_16x16x32_f16 v[248:251], v[12:15], v[20:23], v[248:251]\n\t"
    "ds_read_b64_tr_b16 a[120:121], v78 offset:4096\n\t"
    "v_mfma_f32_16x16x32_f16 v[252:255], v[12:15], v[56:59], v[252:255]\n\t"
    "ds_read_b64_tr_b16 a[124:125], v78 offset:4104\n\t"
    "ds_read_b64_tr_b16 a[126:127], v78 offset:6152\n\t"
    "s_waitcnt lgkmcnt(9)\n\t"
    "v_mfma_f32_16x16x32_f16 v[232:235], v[28:31], v[8:11], v[232:235]\n\t"
    "buffer_load_dwordx4 v[144:147], v83, s[36:39], s55 offen offset:0 sc1\n\t"
    "v_mfma_f32_16x16x32_f16 v[236:239], v[28:31], v[24:27], v[236:239]\n\t"
    "ds_read_b64_tr_b16 a[116:117], v77 offset:4104\n\t"
    "ds_read_b64_tr_b16 a[118:119], v77 offset:6152\n\t"
    "v_mfma_f32_16x16x32_f16 v[240:243], v[28:31], v[52:55], v[240:243]\n\t"
    "ds_read_b64 v[34:35], v69 offset:2248\n\t"
    "ds_read_b64 v[62:63], v69 offset:3784\n\t"
    "v_mfma_f32_16x16x32_f16 v[244:247], v[28:31], v[36:39], v[244:247]\n\t"
    "buffer_load_dwordx4 v[148:151], v83, s[36:39], s55 offen offset:1024 sc1\n\t"
    "v_mfma_f32_16x16x32_f16 v[248:251], v[28:31], v[40:43], v[248:251]\n\t"
    "ds_read_b64 v[44:45], v69 offset:2752\n\t"
    "v_mfma_f32_16x16x32_f16 v[252:255], v[28:31], v[20:23], v[252:255]\n\t"
    "buffer_load_dwordx4 v[152:155], v84, s[36:39], s52 offen offset:0 sc1\n\t"
    "s_waitcnt lgkmcnt(10)\n\t"
    "v_mfma_f32_16x16x32_f16 v[236:239], v[48:51], v[8:11], v[236:239]\n\t"
    "ds_read_b64 v[46:47], v69 offset:2760\n\t"
    "v_mfma_f32_16x16x32_f16 v[240:243], v[48:51], v[24:27], v[240:243]\n\t"
    "buffer_load_dwordx4 v[156:159], v84, s[36:39], s52 offen offset:1024 sc1\n\t"
    "v_mfma_f32_16x16x32_f16 v[244:247], v[48:51], v[52:55], v[244:247]\n\t"
    "ds_read_b64 v[18:19], v69 offset:3272\n\t"
    "v_mfma_f32_16x16x32_f16 v[248:251], v[48:51], v[36:39], v[248:251]\n\t"
    "ds_read_b64 v[16:17], v69 offset:3264\n\t"
    "v_mfma_f32_16x16x32_f16 v[252:255], v[48:51], v[40:43], v[252:255]\n\t"
    "ds_read_b64 v[60:61], v69 offset:3776\n\t"
    "s_waitcnt lgkmcnt(6)\n\t"
    "v_mfma_f32_16x16x32_f16 v[240:243], v[32:35], v[8:11], v[240:243]\n\t"
    "buffer_load_dwordx4 v[160:163], v84, s[36:39], s52 offen offset:2048 sc1\n\t"
    "v_mfma_f32_16x16x32_f16 v[244:247], v[32:35], v[24:27], v[244:247]\n\t"
    "buffer_load_dwordx4 v[164:167], v84, s[36:39], s52 offen offset:3072 sc1\n\t"
    "v_mfma_f32_16x16x32_f16 v[248:251], v[32:35], v[52:55], v[248:251]\n\t"
    "ds_write_b128 v71, v[232:235] offset:2048\n\t"
    "v_mfma_f32_16x16x32_f16 v[252:255], v[32:35], v[36:39], v[252:255]\n\t"
    "ds_write_b128 v71, v[236:239] offset:3072\n\t"
    "s_waitcnt lgkmcnt(5)\n\t"
    "v_mfma_f32_16x16x32_f16 v[244:247], v[44:47], v[8:11], v[244:247]\n\t"
    "buffer_load_dwordx4 v[168:171], v84, s[36:39], s53 offen offset:0 sc1\n\t"
    "v_mfma_f32_16x16x32_f16 v[248:251], v[44:47], v[24:27], v[248:251]\n\t"
    "ds_write_b128 v71, v[228:231] offset:1024\n\t"
    "v_mfma_f32_16x16x32_f16 v[252:255], v[44:47], v[52:55], v[252:255]\n\t"
    "buffer_load_dwordx4 v[172:175], v84, s[36:39], s53 offen offset:1024 sc1\n\t"
    "s_waitcnt lgkmcnt(4)\n\t"
    "v_mfma_f32_16x16x32_f16 v[248:251], v[16:19], v[8:11], v[248:251]\n\t"
    "s_nop 0\n\t"
    "ds_write_b128 v71, v[244:247] offset:5120\n\t"
    "v_mfma_f32_16x16x32_f16 v[252:255], v[16:19], v[24:27], v[252:255]\n\t"
    "ds_write_b128 v71, v[224:227] offset:0\n\t"
    "s_waitcnt lgkmcnt(5)\n\t"
    "v_mfma_f32_16x16x32_f16 v[252:255], v[60:63], v[8:11], v[252:255]\n\t"
    "ds_write_b128 v71, v[240:243] offset:4096\n\t"
    "s_nop 0\n\t"
    "ds_write_b128 v71, v[248:251] offset:6144\n\t"
    "s_nop 4\n\t"
    "ds_write_b128 v71, v[252:255] offset:7168\n\t"
    "s_waitcnt lgkmcnt(0)\n\t"
    "s_barrier\n\t"
    "ds_read_b128 v[224:227], v72 offset:0\n\t"
    "ds_read_b128 v[240:243], v72 offset:8192\n\t"
    "ds_read_b128 v[228:231], v72 offset:16384\n\t"
    "ds_read_b128 v[244:247], v72 offset:24576\n\t"
    "ds_read_b128 v[232:235], v72 offset:1024\n\t"
    "ds_read_b128 v[248:251], v72 offset:9216\n\t"
    "ds_read_b128 v[236:239], v72 offset:17408\n\t"
    "ds_read_b128 v[252:255], v72 offset:25600\n\t"
    "s_cmp_eq_u32 s38, 0\n\t"
    "s_cbranch_scc1 L_pC1_%=\n\t"
    "L_rC1_%=:\n\t"
    "s_waitcnt vmcnt(18)\n\t"
    "v_mfma_f32_16x16x32_bf16 v[0:3], a[192:195], v[96:99], 0\n\t"
    "v_mfma_f32_16x16x32_bf16 v[0:3], a[240:243], v[100:103], v[0:3]\n\t"
    "buffer_load_dwordx4 v[176:179], v84, s[36:39], s53 offen offset:2048 sc1\n\t"
    "v_mfma_f32_16x16x32_bf16 v[4:7], a[244:247], v[100:103], 0\n\t"
    "v_add_u32 v75, 0x10000, v76\n\t"
    "v_add_u32 v68, 0x20000, v76\n\t"
    "v_mfma_f32_16x16x32_bf16 v[4:7], a[196:199], v[96:99], v[4:7]\n\t"
    "ds_read_b128 v[48:51], v75 offset:21248\n\t"
    "ds_read_b128 v[52:55], v68 offset:21248\n\t"
    "s_lshl_b32 s25, s58, 10\n\t"
    "s_add_u32 s25, s25, 0x10000\n\t"
    "v_mfma_f32_16x16x32_bf16 v[8:11], a[200:203], v[96:99], 0\n\t"
    "v_lshl_add_u32 v75, v73, 5, s25\n\t"
    "v_lshl_add_u32 v75, v74, 3, v75\n\t"
    "v_add_u32 v68, 0x10000, v75\n\t"
    "s_waitcnt lgkmcnt(8)\n\t"
    "v_add_f32 v224, v224, v240\n\t"
    "v_mfma_f32_16x16x32_bf16 v[8:11], a[248:251], v[100:103], v[8:11]\n\t"
    "v_add_f32 v225, v225, v241\n\t"
    "v_add_f32 v226, v226, v242\n\t"
    "v_add_f32 v227, v227, v243\n\t"
    "v_mfma_f32_16x16x32_bf16 v[12:15], a[252:255], v[100:103], 0\n\t"
    "buffer_load_dwordx4 v[180:183], v84, s[36:39], s53 offen offset:3072 sc1\n\t"
    "v_mfma_f32_16x16x32_bf16 v[12:15], a[204:207], v[96:99], v[12:15]\n\t"
    "ds_read_b64 v[242:243], v75 offset:32\n\t"
    "s_waitcnt lgkmcnt(3)\n\t"
    "v_add_f32 v228, v228, v244\n\t"
    "s_waitcnt vmcnt(18)\n\t"
    "v_mfma_f32_16x16x32_bf16 v[0:3], a[160:163], v[104:107], v[0:3]\n\t"
    "buffer_load_dwordx4 v[184:187], v84, s[36:39], s54 offen offset:0 sc1\n\t"
    "v_mfma_f32_16x16x32_bf16 v[0:3], a[176:179], v[108:111], v[0:3]\n\t"
    "v_add_f32 v229, v229, v245\n\t"
    "v_add_f32 v230, v230, v246\n\t"
    "v_add_f32 v231, v231, v247\n\t"
    "v_mfma_f32_16x16x32_bf16 v[4:7], a[180:183], v[108:111], v[4:7]\n\t"
    "ds_read_b64 v[246:247], v68 offset:32\n\t"
    "v_add_f32 v232, v232, v248\n\t"
    "v_mfma_f32_16x16x32_bf16 v[4:7], a[164:167], v[104:107], v[4:7]\n\t"
    "buffer_load_dwordx4 v[188:191], v84, s[36:39], s54 offen offset:1024 sc1\n\t"
    "v_mfma_f32_16x16x32_bf16 v[8:11], a[168:171], v[104:107], v[8:11]\n\t"
    "v_add_f32 v233, v233, v249\n\t"
    "v_add_f32 v234, v234, v250\n\t"
    "v_add_f32 v235, v235, v251\n\t"
    "v_mfma_f32_16x16x32_bf16 v[8:11], a[184:187], v[108:111], v[8:11]\n\t"
    "ds_read_b64 v[250:251], v75 offset:544\n\t"
    "v_add_f32 v236, v236, v252\n\t"
    "v_add_f32 v237, v237, v253\n\t"
    "v_add_f32 v238, v238, v254\n\t"
    "v_mfma_f32_16x16x32_bf16 v[12:15], a[188:191], v[108:111], v[12:15]\n\t"
    "buffer_load_dwordx4 v[192:195], v84, s[36:39], s54 offen offset:2048 sc1\n\t"
    "v_add_f32 v239, v239, v255\n\t"
    "v_mfma_f32_16x16x32_bf16 v[12:15], a[172:175], v[104:107], v[12:15]\n\t"
    "ds_read_b64 v[254:255], v68 offset:544\n\t"
    "s_lshl2_add_u32 s25, s99, s58\n\t"
    "s_waitcnt vmcnt(19)\n\t"
    "v_mfma_f32_16x16x32_bf16 v[0:3], a[208:211], v[112:115], v[0:3]\n\t"
    "s_lshl_b32 s25, s25, 11\n\t"
    "s_add_u32 s25, s25, 0x400000\n\t"
    "v_lshl_add_u32 v72, v73, 6, s25\n\t"
    "v_mfma_f32_16x16x32_bf16 v[0:3], a[224:227], v[116:119], v[0:3]\n\t"
    "v_lshl_add_u32 v72, v74, 4, v72\n\t"
    "s_waitcnt lgkmcnt(2)\n\t"
    "v_mul_f32 v224, v224, v50\n\t"
    "v_mul_f32 v225, v225, v50\n\t"
    "v_mfma_f32_16x16x32_bf16 v[4:7], a[228:231], v[116:119], v[4:7]\n\t"
    "buffer_load_dwordx4 v[196:199], v84, s[36:39], s54 offen offset:3072 sc1\n\t"
    "v_mfma_f32_16x16x32_bf16 v[4:7], a[212:215], v[112:115], v[4:7]\n\t"
    "v_mul_f32 v224, v48, v224\n\t"
    "v_mul_f32 v225, v48, v225\n\t"
    "v_cvt_pk_bf16_f32 v225, v224, v225\n\t"
    "v_lshlrev_b32 v224, 16, v225\n\t"
    "v_mfma_f32_16x16x32_bf16 v[8:11], a[216:219], v[112:115], v[8:11]\n\t"
    "buffer_load_dwordx4 v[200:203], v84, s[36:39], s55 offen offset:0 sc1\n\t"
    "v_mfma_f32_16x16x32_bf16 v[8:11], a[232:235], v[116:119], v[8:11]\n\t"
    "v_and_b32 v225, 0xffff0000, v225\n\t"
    "v_mul_f32 v226, v226, v50\n\t"
    "v_mul_f32 v227, v227, v50\n\t"
    "v_mfma_f32_16x16x32_bf16 v[12:15], a[236:239], v[116:119], v[12:15]\n\t"
    "v_mul_f32 v226, v48, v226\n\t"
    "v_mul_f32 v227, v48, v227\n\t"
    "v_cvt_pk_bf16_f32 v227, v226, v227\n\t"
    "v_mfma_f32_16x16x32_bf16 v[12:15], a[220:223], v[112:115], v[12:15]\n\t"
    "buffer_load_dwordx4 v[204:207], v84, s[36:39], s55 offen offset:1024 sc1\n\t"
    "s_waitcnt vmcnt(20)\n\t"
    "v_mfma_f32_16x16x32_bf16 v[0:3], a[128:131], v[120:123], v[0:3]\n\t"
    "v_lshlrev_b32 v226, 16, v227\n\t"
    "v_and_b32 v227, 0xffff0000, v227\n\t"
    "v_mul_f32 v228, v228, v54\n\t"
    "v_mfma_f32_16x16x32_bf16 v[0:3], a[144:147], v[124:127], v[0:3]\n\t"
    "v_mul_f32 v229, v229, v54\n\t"
    "v_mul_f32 v228, v52, v228\n\t"
    "v_mul_f32 v229, v52, v229\n\t"
    "v_cvt_pk_bf16_f32 v229, v228, v229\n\t"
    "v_mfma_f32_16x16x32_bf16 v[4:7], a[148:151], v[124:127], v[4:7]\n\t"
    "buffer_load_dwordx4 v[208:211], v85, s[36:39], s52 offen offset:0 sc1\n\t"
    "v_mfma_f32_16x16x32_bf16 v[4:7], a[132:135], v[120:123], v[4:7]\n\t"
    "v_lshlrev_b32 v228, 16, v229\n\t"
    "v_and_b32 v229, 0xffff0000, v229\n\t"
    "v_mul_f32 v230, v230, v54\n\t"
    "v_mfma_f32_16x16x32_bf16 v[8:11], a[136:139], v[120:123], v[8:11]\n\t"
    "v_mul_f32 v231, v231, v54\n\t"
    "v_mul_f32 v230, v52, v230\n\t"
    "v_mul_f32 v231, v52, v231\n\t"
    "v_mfma_f32_16x16x32_bf16 v[8:11], a[152:155], v[124:127], v[8:11]\n\t"
    "buffer_load_dwordx4 v[212:215], v85, s[36:39], s52 offen offset:1024 sc1\n\t"
    "v_mfma_f32_16x16x32_bf16 v[12:15], a[156:159], v[124:127], v[12:15]\n\t"
    "v_cvt_pk_bf16_f32 v231, v230, v231\n\t"
    "v_lshlrev_b32 v230, 16, v231\n\t"
    "v_and_b32 v231, 0xffff0000, v231\n\t"
    "v_mfma_f32_16x16x32_bf16 v[12:15], a[140:143], v[120:123], v[12:15]\n\t"
    "buffer_load_dwordx4 v[216:219], v85, s[36:39], s52 offen offset:2048 sc1\n\t"
    "s_waitcnt vmcnt(21)\n\t"
    "v_mfma_f32_16x16x32_bf16 v[0:3], a[0:3], v[128:131], v[0:3]\n\t"
    "v_mul_f32 v232, v232, v50\n\t"
    "v_mul_f32 v233, v233, v50\n\t"
    "v_mul_f32 v232, v48, v232\n\t"
    "v_mfma_f32_16x16x32_bf16 v[0:3], a[16:19], v[132:135], v[0:3]\n\t"
    "v_add_u32 v92, s45, v76\n\t"
    "v_mul_f32 v233, v48, v233\n\t"
    "v_cvt_pk_bf16_f32 v233, v232, v233\n\t"
    "v_lshlrev_b32 v232, 16, v233\n\t"
    "v_mfma_f32_16x16x32_bf16 v[4:7], a[20:23], v[132:135], v[4:7]\n\t"
    "buffer_load_dwordx4 v[220:223], v85, s[36:39], s52 offen offset:3072 sc1\n\t"
    "v_mfma_f32_16x16x32_bf16 v[4:7], a[4:7], v[128:131], v[4:7]\n\t"
    "v_and_b32 v233, 0xffff0000, v233\n\t"
    "v_mul_f32 v234, v234, v50\n\t"
    "v_mul_f32 v235, v235, v50\n\t"
    "v_mfma_f32_16x16x32_bf16 v[8:11], a[8:11], v[128:131], v[8:11]\n\t"
    "v_mul_f32 v234, v48, v234\n\t"
    "v_mul_f32 v235, v48, v235\n\t"
    "v_cvt_pk_bf16_f32 v235, v234, v235\n\t"
    "v_lshlrev_b32 v234, 16, v235\n\t"
    "v_mfma_f32_16x16x32_bf16 v[8:11], a[24:27], v[132:135], v[8:11]\n\t"
    "v_and_b32 v235, 0xffff0000, v235\n\t"
    "v_mul_f32 v236, v236, v54\n\t"
    "v_mul_f32 v237, v237, v54\n\t"
    "v_mfma_f32_16x16x32_bf16 v[12:15], a[28:31], v[132:135], v[12:15]\n\t"
    "buffer_load_dwordx4 v[96:99], v85, s[36:39], s53 offen offset:0 sc1\n\t"
    "v_mfma_f32_16x16x32_bf16 v[12:15], a[12:15], v[128:131], v[12:15]\n\t"
    "v_xor_b32 v93, 0x4000, v92\n\t"
    "v_mul_f32 v236, v52, v236\n\t"
    "v_mul_f32 v237, v52, v237\n\t"
    "s_waitcnt vmcnt(21)\n\t"
    "v_mfma_f32_16x16x32_bf16 v[0:3], a[32:35], v[136:139], v[0:3]\n\t"
    "v_cvt_pk_bf16_f32 v237, v236, v237\n\t"
    "v_lshlrev_b32 v236, 16, v237\n\t"
    "v_and_b32 v237, 0xffff0000, v237\n\t"
    "v_mfma_f32_16x16x32_bf16 v[0:3], a[48:51], v[140:143], v[0:3]\n\t"
    "v_mul_f32 v238, v238, v54\n\t"
    "v_mul_f32 v239, v239, v54\n\t"
    "v_mul_f32 v238, v52, v238\n\t"
    "v_mul_f32 v239, v52, v239\n\t"
    "v_mfma_f32_16x16x32_bf16 v[4:7], a[52:55], v[140:143], v[4:7]\n\t"
    "buffer_load_dwordx4 v[100:103], v85, s[36:39], s53 offen offset:1024 sc1\n\t"
    "v_mfma_f32_16x16x32_bf16 v[4:7], a[36:39], v[136:139], v[4:7]\n\t"
    "buffer_load_dwordx4 v[104:107], v85, s[36:39], s53 offen offset:2048 sc1\n\t"
    "v_mfma_f32_16x16x32_bf16 v[8:11], a[40:43], v[136:139], v[8:11]\n\t"
    "v_cvt_pk_bf16_f32 v239, v238, v239\n\t"
    "v_lshlrev_b32 v238, 16, v239\n\t"
    "v_and_b32 v239, 0xffff0000, v239\n\t"
    "v_mfma_f32_16x16x32_bf16 v[8:11], a[56:59], v[140:143], v[8:11]\n\t"
    "v_fma_mix_f32 v240, v242, v51, 0 op_sel_hi:[1,0,0]\n\t"
    "v_fma_mix_f32 v241, v242, v51, 0 op_sel:[1,0,0] op_sel_hi:[1,0,0]\n\t"
    "v_mul_f32 v240, v240, v49\n\t"
    "v_mul_f32 v241, v241, v49\n\t"
    "v_mfma_f32_16x16x32_bf16 v[12:15], a[60:63], v[140:143], v[12:15]\n\t"
    "v_cvt_pk_bf16_f32 v241, v240, v241\n\t"
    "v_lshlrev_b32 v240, 16, v241\n\t"
    "v_and_b32 v241, 0xffff0000, v241\n\t"
    "v_add_f32 v224, v224, v240\n\t"
    "v_mfma_f32_16x16x32_bf16 v[12:15], a[44:47], v[136:139], v[12:15]\n\t"
    "buffer_load_dwordx4 v[108:111], v85, s[36:39], s53 offen offset:3072 sc1\n\t"
    "s_waitcnt vmcnt(22)\n\t"
    "v_mfma_f32_16x16x32_bf16 v[0:3], a[64:67], v[144:147], v[0:3]\n\t"
    "v_add_f32 v225, v225, v241\n\t"
    "v_fma_mix_f32 v244, v246, v55, 0 op_sel_hi:[1,0,0]\n\t"
    "v_fma_mix_f32 v245, v246, v55, 0 op_sel:[1,0,0] op_sel_hi:[1,0,0]\n\t"
    "v_mfma_f32_16x16x32_bf16 v[0:3], a[80:83], v[148:151], v[0:3]\n\t"
    "v_mul_f32 v244, v244, v53\n\t"
    "v_mul_f32 v245, v245, v53\n\t"
    "v_cvt_pk_bf16_f32 v245, v244, v245\n\t"
    "v_mfma_f32_16x16x32_bf16 v[4:7], a[84:87], v[148:151], v[4:7]\n\t"
    "buffer_load_dwordx4 v[112:115], v85, s[36:39], s54 offen offset:0 sc1\n\t"
    "v_mfma_f32_16x16x32_bf16 v[4:7], a[68:71], v[144:147], v[4:7]\n\t"
    "v_add_u32 v94, 0x2000, v92\n\t"
    "v_lshlrev_b32 v244, 16, v245\n\t"
    "v_and_b32 v245, 0xffff0000, v245\n\t"
    "v_add_f32 v228, v228, v244\n\t"
    "v_mfma_f32_16x16x32_bf16 v[8:11], a[72:75], v[144:147], v[8:11]\n\t"
    "buffer_load_dwordx4 v[116:119], v85, s[36:39], s54 offen offset:1024 sc1\n\t"
    "v_mfma_f32_16x16x32_bf16 v[8:11], a[88:91], v[148:151], v[8:11]\n\t"
    "v_add_f32 v229, v229, v245\n\t"
    "v_cvt_pk_bf16_f32 v224, v224, v228\n\t"
    "v_cvt_pk_bf16_f32 v225, v225, v229\n\t"
    "v_fma_mix_f32 v242, v243, v51, 0 op_sel_hi:[1,0,0]\n\t"
    "v_mfma_f32_16x16x32_bf16 v[12:15], a[92:95], v[148:151], v[12:15]\n\t"
    "v_fma_mix_f32 v243, v243, v51, 0 op_sel:[1,0,0] op_sel_hi:[1,0,0]\n\t"
    "v_mul_f32 v242, v242, v49\n\t"
    "v_mul_f32 v243, v243, v49\n\t"
    "v_cvt_pk_bf16_f32 v243, v242, v243\n\t"
    "v_mfma_f32_16x16x32_bf16 v[12:15], a[76:79], v[144:147], v[12:15]\n\t"
    "v_and_b32 v94, 0x7fff, v94\n\t"
    "v_lshlrev_b32 v242, 16, v243\n\t"
    "s_waitcnt vmcnt(22)\n\t"
    "v_mfma_f32_16x16x32_bf16 v[16:19], a[192:195], v[152:155], 0\n\t"
    "v_and_b32 v243, 0xffff0000, v243\n\t"
    "v_add_f32 v226, v226, v242\n\t"
    "v_add_f32 v227, v227, v243\n\t"
    "v_mfma_f32_16x16x32_bf16 v[16:19], a[240:243], v[156:159], v[16:19]\n\t"
    "buffer_load_dwordx4 v[120:123], v85, s[36:39], s54 offen offset:2048 sc1\n\t"
    "v_mfma_f32_16x16x32_bf16 v[20:23], a[244:247], v[156:159], 0\n\t"
    "buffer_load_dwordx4 v[124:127], v85, s[36:39], s54 offen offset:3072 sc1\n\t"
    "v_mfma_f32_16x16x32_bf16 v[20:23], a[196:199], v[152:155], v[20:23]\n\t"
    "v_fma_mix_f32 v246, v247, v55, 0 op_sel_hi:[1,0,0]\n\t"
    "v_fma_mix_f32 v247, v247, v55, 0 op_sel:[1,0,0] op_sel_hi:[1,0,0]\n\t"
    "v_mul_f32 v246, v246, v53\n\t"
    "v_mfma_f32_16x16x32_bf16 v[24:27], a[200:203], v[152:155], 0\n\t"
    "v_mul_f32 v247, v247, v53\n\t"
    "v_cvt_pk_bf16_f32 v247, v246, v247\n\t"
    "v_lshlrev_b32 v246, 16, v247\n\t"
    "v_and_b32 v247, 0xffff0000, v247\n\t"
    "v_mfma_f32_16x16x32_bf16 v[24:27], a[248:251], v[156:159], v[24:27]\n\t"
    "v_add_f32 v230, v230, v246\n\t"
    "v_add_f32 v231, v231, v247\n\t"
    "v_cvt_pk_bf16_f32 v226, v226, v230\n\t"
    "v_cvt_pk_bf16_f32 v227, v227, v231\n\t"
    "v_mfma_f32_16x16x32_bf16 v[28:31], a[252:255], v[156:159], 0\n\t"
    "ds_write_b128 v92, v[0:3] offset:0\n\t"
    "v_mfma_f32_16x16x32_bf16 v[28:31], a[204:207], v[152:155], v[28:31]\n\t"
    "buffer_load_dwordx4 v[128:131], v85, s[36:39], s55 offen offset:0 sc1\n\t"
    "s_waitcnt vmcnt(23)\n\t"
    "v_mfma_f32_16x16x32_bf16 v[16:19], a[160:163], v[160:163], v[16:19]\n\t"
    "ds_write_b128 v92, v[4:7] offset:1024\n\t"
    "v_mfma_f32_16x16x32_bf16 v[16:19], a[176:179], v[164:167], v[16:19]\n\t"
    "global_store_dwordx4 v72, v[224:227], s[84:85] offset:0 sc1\n\t"
    "s_waitcnt lgkmcnt(2)\n\t"
    "v_fma_mix_f32 v248, v250, v51, 0 op_sel_hi:[1,0,0]\n\t"
    "v_mfma_f32_16x16x32_bf16 v[20:23], a[180:183], v[164:167], v[20:23]\n\t"
    "buffer_load_dwordx4 v[132:135], v85, s[36:39], s55 offen offset:1024 sc1\n\t"
    "v_fma_mix_f32 v249, v250, v51, 0 op_sel:[1,0,0] op_sel_hi:[1,0,0]\n\t"
    "v_mfma_f32_16x16x32_bf16 v[20:23], a[164:167], v[160:163], v[20:23]\n\t"
    "v_mul_f32 v248, v248, v49\n\t"
    "v_mul_f32 v249, v249, v49\n\t"
    "v_cvt_pk_bf16_f32 v249, v248, v249\n\t"
    "v_lshlrev_b32 v248, 16, v249\n\t"
    "v_and_b32 v249, 0xffff0000, v249\n\t"
    "v_mfma_f32_16x16x32_bf16 v[24:27], a[168:171], v[160:163], v[24:27]\n\t"
    "ds_write_b128 v92, v[8:11] offset:2048\n\t"
    "v_mfma_f32_16x16x32_bf16 v[24:27], a[184:187], v[164:167], v[24:27]\n\t"
    "buffer_load_dwordx4 v[136:139], v82, s[36:39], s52 offen offset:0 sc1\n\t"
    "v_add_f32 v232, v232, v248\n\t"
    "v_mfma_f32_16x16x32_bf16 v[28:31], a[188:191], v[164:167], v[28:31]\n\t"
    "buffer_load_dwordx4 v[140:143], v82, s[36:39], s52 offen offset:1024 sc1\n\t"
    "v_add_f32 v233, v233, v249\n\t"
    "v_mfma_f32_16x16x32_bf16 v[28:31], a[172:175], v[160:163], v[28:31]\n\t"
    "v_fma_mix_f32 v252, v254, v55, 0 op_sel_hi:[1,0,0]\n\t"
    "v_fma_mix_f32 v253, v254, v55, 0 op_sel:[1,0,0] op_sel_hi:[1,0,0]\n\t"
    "v_mul_f32 v252, v252, v53\n\t"
    "v_mul_f32 v253, v253, v53\n\t"
    "s_waitcnt vmcnt(25)\n\t"
    "v_mfma_f32_16x16x32_bf16 v[16:19], a[208:211], v[168:171], v[16:19]\n\t"
    "v_cvt_pk_bf16_f32 v253, v252, v253\n\t"
    "v_lshlrev_b32 v252, 16, v253\n\t"
    "v_and_b32 v253, 0xffff0000, v253\n\t"
    "v_add_f32 v236, v236, v252\n\t"
    "v_mfma_f32_16x16x32_bf16 v[16:19], a[224:227], v[172:175], v[16:19]\n\t"
    "ds_write_b128 v92, v[12:15] offset:3072\n\t"
    "v_mfma_f32_16x16x32_bf16 v[20:23], a[228:231], v[172:175], v[20:23]\n\t"
    "v_add_f32 v237, v237, v253\n\t"
    "v_cvt_pk_bf16_f32 v232, v232, v236\n\t"
    "v_cvt_pk_bf16_f32 v233, v233, v237\n\t"
    "v_fma_mix_f32 v250, v251, v51, 0 op_sel_hi:[1,0,0]\n\t"
    "v_mfma_f32_16x16x32_bf16 v[20:23], a[212:215], v[168:171], v[20:23]\n\t"
    "buffer_load_dwordx4 v[144:147], v82, s[36:39], s52 offen offset:2048 sc1\n\t"
    "v_fma_mix_f32 v251, v251, v51, 0 op_sel:[1,0,0] op_sel_hi:[1,0,0]\n\t"
    "v_mfma_f32_16x16x32_bf16 v[24:27], a[216:219], v[168:171], v[24:27]\n\t"
    "buffer_load_dwordx4 v[148:151], v82, s[36:39], s52 offen offset:3072 sc1\n\t"
    "v_mul_f32 v250, v250, v49\n\t"
    "v_mfma_f32_16x16x32_bf16 v[24:27], a[232:235], v[172:175], v[24:27]\n\t"
    "v_mul_f32 v251, v251, v49\n\t"
    "v_cvt_pk_bf16_f32 v251, v250, v251\n\t"
    "v_lshlrev_b32 v250, 16, v251\n\t"
    "v_and_b32 v251, 0xffff0000, v251\n\t"
    "v_mfma_f32_16x16x32_bf16 v[28:31], a[236:239], v[172:175], v[28:31]\n\t"
    "v_add_f32 v234, v234, v250\n\t"
    "v_add_f32 v235, v235, v251\n\t"
    "v_fma_mix_f32 v254, v255, v55, 0 op_sel_hi:[1,0,0]\n\t"
    "v_fma_mix_f32 v255, v255, v55, 0 op_sel:[1,0,0] op_sel_hi:[1,0,0]\n\t"
    "v_mfma_f32_16x16x32_bf16 v[28:31], a[220:223], v[168:171], v[28:31]\n\t"
    "v_mul_f32 v254, v254, v53\n\t"
    "v_mul_f32 v255, v255, v53\n\t"
    "v_cvt_pk_bf16_f32 v255, v254, v255\n\t"
    "s_waitcnt vmcnt(25)\n\t"
    "v_mfma_f32_16x16x32_bf16 v[16:19], a[128:131], v[176:179], v[16:19]\n\t"
    "buffer_load_dwordx4 v[152:155], v82, s[36:39], s53 offen offset:0 sc1\n\t"
    "v_lshlrev_b32 v254, 16, v255\n\t"
    "v_mfma_f32_16x16x32_bf16 v[16:19], a[144:147], v[180:183], v[16:19]\n\t"
    "v_and_b32 v255, 0xffff0000, v255\n\t"
    "v_add_f32 v238, v238, v254\n\t"
    "v_add_f32 v239, v239, v255\n\t"
    "v_cvt_pk_bf16_f32 v234, v234, v238\n\t"
    "v_cvt_pk_bf16_f32 v235, v235, v239\n\t"
    "v_mfma_f32_16x16x32_bf16 v[20:23], a[148:151], v[180:183], v[20:23]\n\t"
    "global_store_dwordx4 v72, v[232:235], s[84:85] offset:1024 sc1\n\t"
    "v_mfma_f32_16x16x32_bf16 v[20:23], a[132:135], v[176:179], v[20:23]\n\t"
    "buffer_load_dwordx4 v[156:159], v82, s[36:39], s53 offen offset:1024 sc1\n\t"
    "v_mfma_f32_16x16x32_bf16 v[24:27], a[136:139], v[176:179], v[24:27]\n\t"
    "s_waitcnt lgkmcnt(0)\n\t"
    "s_barrier\n\t"
    "v_mfma_f32_16x16x32_bf16 v[24:27], a[152:155], v[180:183], v[24:27]\n\t"
    "buffer_load_dwordx4 v[160:163], v82, s[36:39], s53 offen offset:2048 sc1\n\t"
    "v_mfma_f32_16x16x32_bf16 v[28:31], a[156:159], v[180:183], v[28:31]\n\t"
    "ds_read_b128 v[4:7], v93 offset:1024\n\t"
    "v_mfma_f32_16x16x32_bf16 v[28:31], a[140:143], v[176:179], v[28:31]\n\t"
    "s_waitcnt vmcnt(27)\n\t"
    "v_mfma_f32_16x16x32_bf16 v[16:19], a[0:3], v[184:187], v[16:19]\n\t"
    "v_mfma_f32_16x16x32_bf16 v[16:19], a[16:19], v[188:191], v[16:19]\n\t"
    "buffer_load_dwordx4 v[164:167], v82, s[36:39], s53 offen offset:3072 sc1\n\t"
    "v_mfma_f32_16x16x32_bf16 v[20:23], a[20:23], v[188:191], v[20:23]\n\t"
    "v_mfma_f32_16x16x32_bf16 v[20:23], a[4:7], v[184:187], v[20:23]\n\t"
    "buffer_load_dwordx4 v[168:171], v82, s[36:39], s54 offen offset:0 sc1\n\t"
    "v_mfma_f32_16x16x32_bf16 v[24:27], a[8:11], v[184:187], v[24:27]\n\t"
    "ds_read_b128 v[8:11], v93 offset:2048\n\t"
    "v_mfma_f32_16x16x32_bf16 v[24:27], a[24:27], v[188:191], v[24:27]\n\t"
    "v_mfma_f32_16x16x32_bf16 v[28:31], a[28:31], v[188:191], v[28:31]\n\t"
    "s_lshl_b32 s56, s60, 7\n\t"
    "v_mfma_f32_16x16x32_bf16 v[28:31], a[12:15], v[184:187], v[28:31]\n\t"
    "s_waitcnt vmcnt(27)\n\t"
    "v_mfma_f32_16x16x32_bf16 v[16:19], a[32:35], v[192:195], v[16:19]\n\t"
    "buffer_load_dwordx4 v[172:175], v82, s[36:39], s54 offen offset:1024 sc1\n\t"
    "v_mfma_f32_16x16x32_bf16 v[16:19], a[48:51], v[196:199], v[16:19]\n\t"
    "ds_read_b128 v[0:3], v93 offset:0\n\t"
    "v_mfma_f32_16x16x32_bf16 v[20:23], a[52:55], v[196:199], v[20:23]\n\t"
    "buffer_load_dwordx4 v[48:51], v82, s[36:39], s54 offen offset:2048 sc1\n\t"
    "v_mfma_f32_16x16x32_bf16 v[20:23], a[36:39], v[192:195], v[20:23]\n\t"
    "ds_read_b128 v[12:15], v93 offset:3072\n\t"
    "v_mfma_f32_16x16x32_bf16 v[24:27], a[40:43], v[192:195], v[24:27]\n\t"
    "buffer_load_dwordx4 v[52:55], v82, s[36:39], s54 offen offset:3072 sc1\n\t"
    "v_mfma_f32_16x16x32_bf16 v[24:27], a[56:59], v[196:199], v[24:27]\n\t"
    "v_mfma_f32_16x16x32_bf16 v[28:31], a[60:63], v[196:199], v[28:31]\n\t"
    "v_mfma_f32_16x16x32_bf16 v[28:31], a[44:47], v[192:195], v[28:31]\n\t"
    "s_waitcnt vmcnt(28)\n\t"
    "v_mfma_f32_16x16x32_bf16 v[16:19], a[64:67], v[200:203], v[16:19]\n\t"
    "buffer_load_dwordx4 v[56:59], v82, s[36:39], s55 offen offset:0 sc1\n\t"
    "v_mfma_f32_16x16x32_bf16 v[16:19], a[80:83], v[204:207], v[16:19]\n\t"
    "v_mfma_f32_16x16x32_bf16 v[20:23], a[84:87], v[204:207], v[20:23]\n\t"
    "Lis13_%=:\n\t"
    "s_cbranch_execz Lis99_%=\n\t"
    "v_mfma_f32_16x16x32_bf16 v[20:23], a[68:71], v[200:203], v[20:23]\n\t"
    "v_mfma_f32_16x16x32_bf16 v[24:27], a[72:75], v[200:203], v[24:27]\n\t"
    "s_add_u32 s56, s56, 0xa400\n\t"
    "v_mfma_f32_16x16x32_bf16 v[24:27], a[88:91], v[204:207], v[24:27]\n\t"
    "v_mfma_f32_16x16x32_bf16 v[28:31], a[92:95], v[204:207], v[28:31]\n\t"
    "v_mfma_f32_16x16x32_bf16 v[28:31], a[76:79], v[200:203], v[28:31]\n\t"
    "s_waitcnt vmcnt(27)\n\t"
    "s_waitcnt lgkmcnt(1)\n\t"
    "v_mfma_f32_16x16x32_bf16 v[0:3], a[192:195], v[208:211], v[0:3]\n\t"
    "v_mov_b32 v81, s56\n\t"
    "v_mfma_f32_16x16x32_bf16 v[0:3], a[240:243], v[212:215], v[0:3]\n\t"
    "v_mfma_f32_16x16x32_bf16 v[4:7], a[244:247], v[212:215], v[4:7]\n\t"
    "v_mfma_f32_16x16x32_bf16 v[4:7], a[196:199], v[208:211], v[4:7]\n\t"
    "ds_write_b128 v92, v[16:19] offset:4096\n\t"
    "v_mfma_f32_16x16x32_bf16 v[8:11], a[200:203], v[208:211], v[8:11]\n\t"
    "v_mfma_f32_16x16x32_bf16 v[8:11], a[248:251], v[212:215], v[8:11]\n\t"
    "s_waitcnt lgkmcnt(1)\n\t"
    "v_mfma_f32_16x16x32_bf16 v[12:15], a[252:255], v[212:215], v[12:15]\n\t"
    "v_mfma_f32_16x16x32_bf16 v[12:15], a[204:207], v[208:211], v[12:15]\n\t"
    "s_waitcnt vmcnt(25)\n\t"
    "v_mfma_f32_16x16x32_bf16 v[0:3], a[160:163], v[216:219], v[0:3]\n\t"
    "v_mfma_f32_16x16x32_bf16 v[0:3], a[176:179], v[220:223], v[0:3]\n\t"
    "v_mfma_f32_16x16x32_bf16 v[4:7], a[180:183], v[220:223], v[4:7]\n\t"
    "ds_write_b128 v92, v[20:23] offset:5120\n\t"
    "v_mfma_f32_16x16x32_bf16 v[4:7], a[164:167], v[216:219], v[4:7]\n\t"
    "v_mfma_f32_16x16x32_bf16 v[8:11], a[168:171], v[216:219], v[8:11]\n\t"
    "v_mfma_f32_16x16x32_bf16 v[8:11], a[184:187], v[220:223], v[8:11]\n\t"
    "v_mfma_f32_16x16x32_bf16 v[12:15], a[188:191], v[220:223], v[12:15]\n\t"
    "v_mfma_f32_16x16x32_bf16 v[12:15], a[172:175], v[216:219], v[12:15]\n\t"
    "s_waitcnt vmcnt(23)\n\t"
    "v_mfma_f32_16x16x32_bf16 v[0:3], a[208:211], v[96:99], v[0:3]\n\t"
    "ds_write_b128 v92, v[24:27] offset:6144\n\t"
    "v_mfma_f32_16x16x32_bf16 v[0:3], a[224:227], v[100:103], v[0:3]\n\t"
    "v_mfma_f32_16x16x32_bf16 v[4:7], a[228:231], v[100:103], v[4:7]\n\t"
    "ds_write_b128 v92, v[28:31] offset:7168\n\t"
    "v_mfma_f32_16x16x32_bf16 v[4:7], a[212:215], v[96:99], v[4:7]\n\t"
    "v_mfma_f32_16x16x32_bf16 v[8:11], a[216:219], v[96:99], v[8:11]\n\t"
    "v_mfma_f32_16x16x32_bf16 v[8:11], a[232:235], v[100:103], v[8:11]\n\t"
    "v_mfma_f32_16x16x32_bf16 v[12:15], a[236:239], v[100:103], v[12:15]\n\t"
    "v_mfma_f32_16x16x32_bf16 v[12:15], a[220:223], v[96:99], v[12:15]\n\t"
    "s_waitcnt vmcnt(21)\n\t"
    "v_mfma_f32_16x16x32_bf16 v[0:3], a[128:131], v[104:107], v[0:3]\n\t"
    "v_mov_b32 v95, 1\n\t"
    "v_mfma_f32_16x16x32_bf16 v[0:3], a[144:147], v[108:111], v[0:3]\n\t"
    "v_mfma_f32_16x16x32_bf16 v[4:7], a[148:151], v[108:111], v[4:7]\n\t"
    "v_mfma_f32_16x16x32_bf16 v[4:7], a[132:135], v[104:107], v[4:7]\n\t"
    "v_mfma_f32_16x16x32_bf16 v[8:11], a[136:139], v[104:107], v[8:11]\n\t"
    "s_waitcnt vmcnt(0)\n\t"
    "s_waitcnt lgkmcnt(0)\n\t"
    "s_barrier\n\t"
    "s_cmp_lg_u32 s58, 1\n\t"
    "s_cbranch_scc1 L_ny1_%=\n\t"
    "s_mov_b64 exec, 1\n\t"
    "global_atomic_add v81, v95, s[84:85] sc1\n\t"
    "s_mov_b64 exec, -1\n\t"
    "L_ny1_%=:\n\t"
    "v_mfma_f32_16x16x32_bf16 v[8:11], a[152:155], v[108:111], v[8:11]\n\t"
    "buffer_load_dwordx4 v[60:63], v82, s[36:39], s55 offen offset:1024 sc1\n\t"
    "v_mfma_f32_16x16x32_bf16 v[12:15], a[156:159], v[108:111], v[12:15]\n\t"
    "s_xor_b32 s52, s52, 0x80000\n\t"
    "v_mfma_f32_16x16x32_bf16 v[12:15], a[140:143], v[104:107], v[12:15]\n\t"
    "v_mfma_f32_16x16x32_bf16 v[0:3], a[0:3], v[112:115], v[0:3]\n\t"
    "ds_read_b128 v[28:31], v93 offset:7168\n\t"
    "v_mfma_f32_16x16x32_bf16 v[0:3], a[16:19], v[116:119], v[0:3]\n\t"
    "buffer_load_dwordx4 v[224:227], v83, s[36:39], s52 offen offset:0 sc1\n\t"
    "v_mfma_f32_16x16x32_bf16 v[4:7], a[20:23], v[116:119], v[4:7]\n\t"
    "ds_read_b128 v[20:23], v93 offset:5120\n\t"
    "v_mfma_f32_16x16x32_bf16 v[4:7], a[4:7], v[112:115], v[4:7]\n\t"
    "ds_read_b128 v[24:27], v93 offset:6144\n\t"
    "v_mfma_f32_16x16x32_bf16 v[8:11], a[8:11], v[112:115], v[8:11]\n\t"
    "buffer_load_dwordx4 v[228:231], v83, s[36:39], s52 offen offset:1024 sc1\n\t"
    "v_mfma_f32_16x16x32_bf16 v[8:11], a[24:27], v[116:119], v[8:11]\n\t"
    "ds_read_b128 v[16:19], v93 offset:4096\n\t"
    "v_mfma_f32_16x16x32_bf16 v[12:15], a[28:31], v[116:119], v[12:15]\n\t"
    "v_mfma_f32_16x16x32_bf16 v[12:15], a[12:15], v[112:115], v[12:15]\n\t"
    "v_mfma_f32_16x16x32_bf16 v[0:3], a[32:35], v[120:123], v[0:3]\n\t"
    "buffer_load_dwordx4 v[232:235], v83, s[36:39], s52 offen offset:2048 sc1\n\t"
    "v_mfma_f32_16x16x32_bf16 v[0:3], a[48:51], v[124:127], v[0:3]\n\t"
    "buffer_load_dwordx4 v[236:239], v83, s[36:39], s52 offen offset:3072 sc1\n\t"
    "v_mfma_f32_16x16x32_bf16 v[4:7], a[52:55], v[124:127], v[4:7]\n\t"
    "s_xor_b32 s53, s53, 0x80000\n\t"
    "v_mfma_f32_16x16x32_bf16 v[4:7], a[36:39], v[120:123], v[4:7]\n\t"
    "v_mfma_f32_16x16x32_bf16 v[8:11], a[40:43], v[120:123], v[8:11]\n\t"
    "buffer_load_dwordx4 v[240:243], v83, s[36:39], s53 offen offset:0 sc1\n\t"
    "v_mfma_f32_16x16x32_bf16 v[8:11], a[56:59], v[124:127], v[8:11]\n\t"
    "v_mfma_f32_16x16x32_bf16 v[12:15], a[60:63], v[124:127], v[12:15]\n\t"
    "s_lshl_b32 s26, s99, 7\n\t"
    "v_mfma_f32_16x16x32_bf16 v[12:15], a[44:47], v[120:123], v[12:15]\n\t"
    "v_mfma_f32_16x16x32_bf16 v[0:3], a[64:67], v[128:131], v[0:3]\n\t"
    "buffer_load_dwordx4 v[244:247], v83, s[36:39], s53 offen offset:1024 sc1\n\t"
    "v_mfma_f32_16x16x32_bf16 v[0:3], a[80:83], v[132:135], v[0:3]\n\t"
    "s_add_u32 s26, s26, 0xc000\n\t"
    "v_mfma_f32_16x16x32_bf16 v[4:7], a[84:87], v[132:135], v[4:7]\n\t"
    "v_mfma_f32_16x16x32_bf16 v[4:7], a[68:71], v[128:131], v[4:7]\n\t"
    "buffer_load_dwordx4 v[248:251], v83, s[36:39], s53 offen offset:2048 sc1\n\t"
    "v_mfma_f32_16x16x32_bf16 v[8:11], a[72:75], v[128:131], v[8:11]\n\t"
    "v_mov_b32 v81, s26\n\t"
    "v_mfma_f32_16x16x32_bf16 v[8:11], a[88:91], v[132:135], v[8:11]\n\t"
    "buffer_load_dwordx4 v[252:255], v83, s[36:39], s53 offen offset:3072 sc1\n\t"
    "v_mfma_f32_16x16x32_bf16 v[12:15], a[92:95], v[132:135], v[12:15]\n\t"
    "v_mov_b32 v95, 32\n\t"
    "v_mfma_f32_16x16x32_bf16 v[12:15], a[76:79], v[128:131], v[12:15]\n\t"
    "s_waitcnt lgkmcnt(0)\n\t"
    "v_mfma_f32_16x16x32_bf16 v[16:19], a[192:195], v[136:139], v[16:19]\n\t"
    "s_xor_b32 s54, s54, 0x80000\n\t"
    "v_mfma_f32_16x16x32_bf16 v[16:19], a[240:243], v[140:143], v[16:19]\n\t"
    "buffer_load_dwordx4 v[176:179], v83, s[36:39], s54 offen offset:0 sc1\n\t"
    "v_mfma_f32_16x16x32_bf16 v[20:23], a[244:247], v[140:143], v[20:23]\n\t"
    "v_mfma_f32_16x16x32_bf16 v[20:23], a[196:199], v[136:139], v[20:23]\n\t"
    "buffer_load_dwordx4 v[180:183], v83, s[36:39], s54 offen offset:1024 sc1\n\t"
    "v_mfma_f32_16x16x32_bf16 v[24:27], a[200:203], v[136:139], v[24:27]\n\t"
    "v_mfma_f32_16x16x32_bf16 v[24:27], a[248:251], v[140:143], v[24:27]\n\t"
    "ds_write_b128 v92, v[0:3] offset:0\n\t"
    "v_mfma_f32_16x16x32_bf16 v[28:31], a[252:255], v[140:143], v[28:31]\n\t"
    "v_mfma_f32_16x16x32_bf16 v[28:31], a[204:207], v[136:139], v[28:31]\n\t"
    "v_mfma_f32_16x16x32_bf16 v[16:19], a[160:163], v[144:147], v[16:19]\n\t"
    "buffer_load_dwordx4 v[184:187], v83, s[36:39], s54 offen offset:2048 sc1\n\t"
    "v_mfma_f32_16x16x32_bf16 v[16:19], a[176:179], v[148:151], v[16:19]\n\t"
    "v_mfma_f32_16x16x32_bf16 v[20:23], a[180:183], v[148:151], v[20:23]\n\t"
    "buffer_load_dwordx4 v[188:191], v83, s[36:39], s54 offen offset:3072 sc1\n\t"
    "v_mfma_f32_16x16x32_bf16 v[20:23], a[164:167], v[144:147], v[20:23]\n\t"
    "ds_write_b128 v92, v[4:7] offset:1024\n\t"
    "v_mfma_f32_16x16x32_bf16 v[24:27], a[168:171], v[144:147], v[24:27]\n\t"
    "s_xor_b32 s55, s55, 0x80000\n\t"
    "v_mfma_f32_16x16x32_bf16 v[24:27], a[184:187], v[148:151], v[24:27]\n\t"
    "ds_write_b128 v92, v[8:11] offset:2048\n\t"
    "v_mfma_f32_16x16x32_bf16 v[28:31], a[188:191], v[148:151], v[28:31]\n\t"
    "buffer_load_dwordx4 v[192:195], v83, s[36:39], s55 offen offset:0 sc1\n\t"
    "v_mfma_f32_16x16x32_bf16 v[28:31], a[172:175], v[144:147], v[28:31]\n\t"
    "v_mfma_f32_16x16x32_bf16 v[16:19], a[208:211], v[152:155], v[16:19]\n\t"
    "buffer_load_dwordx4 v[196:199], v83, s[36:39], s55 offen offset:1024 sc1\n\t"
    "v_mfma_f32_16x16x32_bf16 v[16:19], a[224:227], v[156:159], v[16:19]\n\t"
    "v_xor_b32 v87, 0x200, v81\n\t"
    "v_mfma_f32_16x16x32_bf16 v[20:23], a[228:231], v[156:159], v[20:23]\n\t"
    "v_writelane_b32 v87, s56, 1\n\t"
    "v_mfma_f32_16x16x32_bf16 v[20:23], a[212:215], v[152:155], v[20:23]\n\t"
    "ds_write_b128 v92, v[12:15] offset:3072\n\t"
    "v_mfma_f32_16x16x32_bf16 v[24:27], a[216:219], v[152:155], v[24:27]\n\t"
    "buffer_load_dwordx4 v[200:203], v83, s[36:39], s55 offen offset:2048 sc1\n\t"
    "v_mfma_f32_16x16x32_bf16 v[24:27], a[232:235], v[156:159], v[24:27]\n\t"
    "s_lshl2_add_u32 s25, s99, s58\n\t"
    "v_mfma_f32_16x16x32_bf16 v[28:31], a[236:239], v[156:159], v[28:31]\n\t"
    "buffer_load_dwordx4 v[204:207], v83, s[36:39], s55 offen offset:3072 sc1\n\t"
    "v_mfma_f32_16x16x32_bf16 v[28:31], a[220:223], v[152:155], v[28:31]\n\t"
    "v_mfma_f32_16x16x32_bf16 v[16:19], a[128:131], v[160:163], v[16:19]\n\t"
    "v_mfma_f32_16x16x32_bf16 v[16:19], a[144:147], v[164:167], v[16:19]\n\t"
    "v_mfma_f32_16x16x32_bf16 v[20:23], a[148:151], v[164:167], v[20:23]\n\t"
    "buffer_load_dwordx4 v[208:211], v84, s[36:39], s52 offen offset:0 sc1\n\t"
    "v_mfma_f32_16x16x32_bf16 v[20:23], a[132:135], v[160:163], v[20:23]\n\t"
    "v_mfma_f32_16x16x32_bf16 v[24:27], a[136:139], v[160:163], v[24:27]\n\t"
    "s_waitcnt lgkmcnt(0)\n\t"
    "s_barrier\n\t"
    "v_mfma_f32_16x16x32_bf16 v[24:27], a[152:155], v[164:167], v[24:27]\n\t"
    "buffer_load_dwordx4 v[212:215], v84, s[36:39], s52 offen offset:1024 sc1\n\t"
    "v_mfma_f32_16x16x32_bf16 v[28:31], a[156:159], v[164:167], v[28:31]\n\t"
    "buffer_load_dwordx4 v[216:219], v84, s[36:39], s52 offen offset:2048 sc1\n\t"
    "v_mfma_f32_16x16x32_bf16 v[28:31], a[140:143], v[160:163], v[28:31]\n\t"
    "v_mfma_f32_16x16x32_bf16 v[16:19], a[0:3], v[168:171], v[16:19]\n\t"
    "v_mfma_f32_16x16x32_bf16 v[16:19], a[16:19], v[172:175], v[16:19]\n\t"
    "v_mfma_f32_16x16x32_bf16 v[20:23], a[20:23], v[172:175], v[20:23]\n\t"
    "ds_read_b128 v[32:35], v94 offset:0\n\t"
    "v_mfma_f32_16x16x32_bf16 v[20:23], a[4:7], v[168:171], v[20:23]\n\t"
    "buffer_load_dwordx4 v[220:223], v84, s[36:39], s52 offen offset:3072 sc1\n\t"
    "v_mfma_f32_16x16x32_bf16 v[24:27], a[8:11], v[168:171], v[24:27]\n\t"
    "s_lshl_b32 s25, s25, 12\n\t"
    "v_mfma_f32_16x16x32_bf16 v[24:27], a[24:27], v[172:175], v[24:27]\n\t"
    "buffer_load_dwordx4 v[96:99], v84, s[36:39], s53 offen offset:0 sc1\n\t"
    "v_mfma_f32_16x16x32_bf16 v[28:31], a[28:31], v[172:175], v[28:31]\n\t"
    "ds_read_b128 v[40:43], v94 offset:2048\n\t"
    "v_mfma_f32_16x16x32_bf16 v[28:31], a[12:15], v[168:171], v[28:31]\n\t"
    "v_mfma_f32_16x16x32_bf16 v[16:19], a[32:35], v[48:51], v[16:19]\n\t"
    "ds_read_b128 v[36:39], v94 offset:1024\n\t"
    "v_mfma_f32_16x16x32_bf16 v[16:19], a[48:51], v[52:55], v[16:19]\n\t"
    "buffer_load_dwordx4 v[100:103], v84, s[36:39], s53 offen offset:1024 sc1\n\t"
    "v_mfma_f32_16x16x32_bf16 v[20:23], a[52:55], v[52:55], v[20:23]\n\t"
    "buffer_load_dwordx4 v[104:107], v84, s[36:39], s53 offen offset:2048 sc1\n\t"
    "v_mfma_f32_16x16x32_bf16 v[20:23], a[36:39], v[48:51], v[20:23]\n\t"
    "ds_read_b128 v[44:47], v94 offset:3072\n\t"
    "v_mfma_f32_16x16x32_bf16 v[24:27], a[40:43], v[48:51], v[24:27]\n\t"
    "v_mfma_f32_16x16x32_bf16 v[24:27], a[56:59], v[52:55], v[24:27]\n\t"
    "buffer_load_dwordx4 v[108:111], v84, s[36:39], s53 offen offset:3072 sc1\n\t"
    "v_mfma_f32_16x16x32_bf16 v[28:31], a[60:63], v[52:55], v[28:31]\n\t"
    "s_add_u32 s25, s25, 0xc00000\n\t"
    "v_mfma_f32_16x16x32_bf16 v[28:31], a[44:47], v[48:51], v[28:31]\n\t"
    "s_waitcnt vmcnt(24)\n\t"
    "v_mfma_f32_16x16x32_bf16 v[16:19], a[64:67], v[56:59], v[16:19]\n\t"
    "buffer_load_dwordx4 v[112:115], v84, s[36:39], s54 offen offset:0 sc1\n\t"
    "v_mfma_f32_16x16x32_bf16 v[16:19], a[80:83], v[60:63], v[16:19]\n\t"
    "v_add_u32 v70, s25, v76\n\t"
    "v_mfma_f32_16x16x32_bf16 v[20:23], a[84:87], v[60:63], v[20:23]\n\t"
    "v_mfma_f32_16x16x32_bf16 v[20:23], a[68:71], v[56:59], v[20:23]\n\t"
    "v_mfma_f32_16x16x32_bf16 v[24:27], a[72:75], v[56:59], v[24:27]\n\t"
    "v_mfma_f32_16x16x32_bf16 v[24:27], a[88:91], v[60:63], v[24:27]\n\t"
    "buffer_load_dwordx4 v[116:119], v84, s[36:39], s54 offen offset:1024 sc1\n\t"
    "v_mfma_f32_16x16x32_bf16 v[28:31], a[92:95], v[60:63], v[28:31]\n\t"
    "buffer_load_dwordx4 v[120:123], v84, s[36:39], s54 offen offset:2048 sc1\n\t"
    "v_mfma_f32_16x16x32_bf16 v[28:31], a[76:79], v[56:59], v[28:31]\n\t"
    "s_waitcnt vmcnt(25)\n\t"
    "v_mfma_f32_16x16x32_bf16 v[0:3], a[192:195], v[224:227], 0\n\t"
    "s_waitcnt lgkmcnt(1)\n\t"
    "v_add_f32 v34, v34, v18\n\t"
    "v_add_f32 v35, v35, v19\n\t"
    "v_mfma_f32_16x16x32_bf16 v[0:3], a[240:243], v[228:231], v[0:3]\n\t"
    "v_add_f32 v36, v36, v20\n\t"
    "v_add_f32 v37, v37, v21\n\t"
    "v_mfma_f32_16x16x32_bf16 v[4:7], a[244:247], v[228:231], 0\n\t"
    "v_add_f32 v38, v38, v22\n\t"
    "v_add_f32 v39, v39, v23\n\t"
    "v_mfma_f32_16x16x32_bf16 v[4:7], a[196:199], v[224:227], v[4:7]\n\t"
    "v_add_f32 v32, v32, v16\n\t"
    "v_add_f32 v33, v33, v17\n\t"
    "v_mfma_f32_16x16x32_bf16 v[8:11], a[200:203], v[224:227], 0\n\t"
    "s_waitcnt lgkmcnt(0)\n\t"
    "v_add_f32 v46, v46, v30\n\t"
    "v_add_f32 v47, v47, v31\n\t"
    "v_mfma_f32_16x16x32_bf16 v[8:11], a[248:251], v[228:231], v[8:11]\n\t"
    "v_add_f32 v40, v40, v24\n\t"
    "v_add_f32 v41, v41, v25\n\t"
    "v_mfma_f32_16x16x32_bf16 v[12:15], a[252:255], v[228:231], 0\n\t"
    "global_store_dwordx4 v70, v[32:35], s[84:85] offset:0 sc1\n\t"
    "v_mfma_f32_16x16x32_bf16 v[12:15], a[204:207], v[224:227], v[12:15]\n\t"
    "global_store_dwordx4 v70, v[36:39], s[84:85] offset:1024 sc1\n\t"
    "s_waitcnt vmcnt(25)\n\t"
    "v_mfma_f32_16x16x32_bf16 v[0:3], a[160:163], v[232:235], v[0:3]\n\t"
    "v_add_f32 v42, v42, v26\n\t"
    "v_add_f32 v43, v43, v27\n\t"
    "v_mfma_f32_16x16x32_bf16 v[0:3], a[176:179], v[236:239], v[0:3]\n\t"
    "global_store_dwordx4 v70, v[40:43], s[84:85] offset:2048 sc1\n\t"
    "v_mfma_f32_16x16x32_bf16 v[4:7], a[180:183], v[236:239], v[4:7]\n\t"
    "v_add_f32 v44, v44, v28\n\t"
    "v_add_f32 v45, v45, v29\n\t"
    "v_mfma_f32_16x16x32_bf16 v[4:7], a[164:167], v[232:235], v[4:7]\n\t"
    "global_store_dwordx4 v70, v[44:47], s[84:85] offset:3072 sc1\n\t"
    "v_mfma_f32_16x16x32_bf16 v[8:11], a[168:171], v[232:235], v[8:11]\n\t"
    "v_mfma_f32_16x16x32_bf16 v[8:11], a[184:187], v[236:239], v[8:11]\n\t"
    "v_mfma_f32_16x16x32_bf16 v[12:15], a[188:191], v[236:239], v[12:15]\n\t"
    "v_mfma_f32_16x16x32_bf16 v[12:15], a[172:175], v[232:235], v[12:15]\n\t"
    "s_waitcnt vmcnt(25)\n\t"
    "v_mfma_f32_16x16x32_bf16 v[0:3], a[208:211], v[240:243], v[0:3]\n\t"
    "s_lshl_b32 s25, s72, 17\n\t"
    "v_mfma_f32_16x16x32_bf16 v[0:3], a[224:227], v[244:247], v[0:3]\n\t"
    "v_mfma_f32_16x16x32_bf16 v[4:7], a[228:231], v[244:247], v[4:7]\n\t"
    "s_lshl_b32 s26, s58, 15\n\t"
    "v_mfma_f32_16x16x32_bf16 v[4:7], a[212:215], v[240:243], v[4:7]\n\t"
    "v_mfma_f32_16x16x32_bf16 v[8:11], a[216:219], v[240:243], v[8:11]\n\t"
    "v_mfma_f32_16x16x32_bf16 v[8:11], a[232:235], v[244:247], v[8:11]\n\t"
    "s_add_u32 s25, s25, s26\n\t"
    "v_mfma_f32_16x16x32_bf16 v[12:15], a[236:239], v[244:247], v[12:15]\n\t"
    "v_mfma_f32_16x16x32_bf16 v[12:15], a[220:223], v[240:243], v[12:15]\n\t"
    "s_waitcnt vmcnt(23)\n\t"
    "v_mfma_f32_16x16x32_bf16 v[0:3], a[128:131], v[248:251], v[0:3]\n\t"
    "v_mfma_f32_16x16x32_bf16 v[0:3], a[144:147], v[252:255], v[0:3]\n\t"
    "s_lshl_b32 s26, s71, 7\n\t"
    "v_mfma_f32_16x16x32_bf16 v[4:7], a[148:151], v[252:255], v[4:7]\n\t"
    "s_add_u32 s25, s25, s26\n\t"
    "v_mfma_f32_16x16x32_bf16 v[4:7], a[132:135], v[248:251], v[4:7]\n\t"
    "v_mfma_f32_16x16x32_bf16 v[8:11], a[136:139], v[248:251], v[8:11]\n\t"
    "v_mfma_f32_16x16x32_bf16 v[8:11], a[152:155], v[252:255], v[8:11]\n\t"
    "v_mfma_f32_16x16x32_bf16 v[12:15], a[156:159], v[252:255], v[12:15]\n\t"
    "v_mfma_f32_16x16x32_bf16 v[12:15], a[140:143], v[248:251], v[12:15]\n\t"
    "s_waitcnt vmcnt(21)\n\t"
    "v_mfma_f32_16x16x32_bf16 v[0:3], a[0:3], v[176:179], v[0:3]\n\t"
    "v_mfma_f32_16x16x32_bf16 v[0:3], a[16:19], v[180:183], v[0:3]\n\t"
    "v_mfma_f32_16x16x32_bf16 v[4:7], a[20:23], v[180:183], v[4:7]\n\t"
    "s_add_u32 s25, s25, 1024\n\t"
    "v_mfma_f32_16x16x32_bf16 v[4:7], a[4:7], v[176:179], v[4:7]\n\t"
    "v_mfma_f32_16x16x32_bf16 v[8:11], a[8:11], v[176:179], v[8:11]\n\t"
    "v_mfma_f32_16x16x32_bf16 v[8:11], a[24:27], v[180:183], v[8:11]\n\t"
    "v_mfma_f32_16x16x32_bf16 v[12:15], a[28:31], v[180:183], v[12:15]\n\t"
    "v_mfma_f32_16x16x32_bf16 v[12:15], a[12:15], v[176:179], v[12:15]\n\t"
    "s_waitcnt vmcnt(19)\n\t"
    "v_mfma_f32_16x16x32_bf16 v[0:3], a[32:35], v[184:187], v[0:3]\n\t"
    "s_add_u32 s12, s50, s25\n\t"
    "s_addc_u32 s13, s51, 0\n\t"
    "v_mfma_f32_16x16x32_bf16 v[0:3], a[48:51], v[188:191], v[0:3]\n\t"
    "v_lshlrev_b32 v69, 4, v74\n\t"
    "v_mfma_f32_16x16x32_bf16 v[4:7], a[52:55], v[188:191], v[4:7]\n\t"
    "v_mfma_f32_16x16x32_bf16 v[4:7], a[36:39], v[184:187], v[4:7]\n\t"
    "v_mfma_f32_16x16x32_bf16 v[8:11], a[40:43], v[184:187], v[8:11]\n\t"
    "v_mfma_f32_16x16x32_bf16 v[8:11], a[56:59], v[188:191], v[8:11]\n\t"
    "v_mfma_f32_16x16x32_bf16 v[12:15], a[60:63], v[188:191], v[12:15]\n\t"
    "v_lshl_add_u32 v69, v73, 11, v69\n\t"
    "v_mfma_f32_16x16x32_bf16 v[12:15], a[44:47], v[184:187], v[12:15]\n\t"
    "s_waitcnt vmcnt(17)\n\t"
    "v_mfma_f32_16x16x32_bf16 v[0:3], a[64:67], v[192:195], v[0:3]\n\t"
    "v_mfma_f32_16x16x32_bf16 v[0:3], a[80:83], v[196:199], v[0:3]\n\t"
    "v_mfma_f32_16x16x32_bf16 v[4:7], a[84:87], v[196:199], v[4:7]\n\t"
    "v_mfma_f32_16x16x32_bf16 v[4:7], a[68:71], v[192:195], v[4:7]\n\t"
    "v_mfma_f32_16x16x32_bf16 v[8:11], a[72:75], v[192:195], v[8:11]\n\t"
    "s_lshl2_add_u32 s26, s72, s58\n\t"
    "v_mfma_f32_16x16x32_bf16 v[8:11], a[88:91], v[196:199], v[8:11]\n\t"
    "v_mfma_f32_16x16x32_bf16 v[12:15], a[92:95], v[196:199], v[12:15]\n\t"
    "s_waitcnt vmcnt(0)\n\t"
    "s_barrier\n\t"
    "s_cmp_lg_u32 s58, 0\n\t"
    "s_cbranch_scc1 L_nz1_%=\n\t"
    "s_mov_b64 exec, 1\n\t"
    "global_atomic_add v81, v95, s[84:85] sc1\n\t"
    "s_mov_b64 exec, -1\n\t"
    "L_nz1_%=:\n\t"
    "v_mfma_f32_16x16x32_bf16 v[12:15], a[76:79], v[192:195], v[12:15]\n\t"
    "v_mfma_f32_16x16x32_bf16 v[0:3], a[96:99], v[200:203], v[0:3]\n\t"
    "buffer_load_dwordx4 v[124:127], v84, s[36:39], s54 offen offset:3072 sc1\n\t"
    "v_mfma_f32_16x16x32_bf16 v[0:3], a[112:115], v[204:207], v[0:3]\n\t"
    "v_mfma_f32_16x16x32_bf16 v[4:7], a[116:119], v[204:207], v[4:7]\n\t"
    "s_lshl_b32 s25, s26, 6\n\t"
    "v_mfma_f32_16x16x32_bf16 v[4:7], a[100:103], v[200:203], v[4:7]\n\t"
    "buffer_load_dwordx4 v[128:131], v84, s[36:39], s55 offen offset:0 sc1\n\t"
    "v_mfma_f32_16x16x32_bf16 v[8:11], a[104:107], v[200:203], v[8:11]\n\t"
    "v_mfma_f32_16x16x32_bf16 v[8:11], a[120:123], v[204:207], v[8:11]\n\t"
    "v_xor_b32 v70, 0x10000, v70\n\t"
    "v_mfma_f32_16x16x32_bf16 v[12:15], a[124:127], v[204:207], v[12:15]\n\t"
    "buffer_load_dwordx4 v[132:135], v84, s[36:39], s55 offen offset:1024 sc1\n\t"
    "v_mfma_f32_16x16x32_bf16 v[12:15], a[108:111], v[200:203], v[12:15]\n\t"
    "v_mfma_f32_16x16x32_bf16 v[16:19], a[192:195], v[208:211], 0\n\t"
    "v_mfma_f32_16x16x32_bf16 v[16:19], a[240:243], v[212:215], v[16:19]\n\t"
    "v_mfma_f32_16x16x32_bf16 v[20:23], a[244:247], v[212:215], 0\n\t"
    "buffer_load_dwordx4 v[136:139], v84, s[36:39], s55 offen offset:2048 sc1\n\t"
    "v_mfma_f32_16x16x32_bf16 v[20:23], a[196:199], v[208:211], v[20:23]\n\t"
    "v_mfma_f32_16x16x32_bf16 v[24:27], a[200:203], v[208:211], 0\n\t"
    "v_mfma_f32_16x16x32_bf16 v[24:27], a[248:251], v[212:215], v[24:27]\n\t"
    "buffer_load_dwordx4 v[140:143], v84, s[36:39], s55 offen offset:3072 sc1\n\t"
    "v_mfma_f32_16x16x32_bf16 v[28:31], a[252:255], v[212:215], 0\n\t"
    "ds_write_b128 v92, v[0:3] offset:4096\n\t"
    "v_mfma_f32_16x16x32_bf16 v[28:31], a[204:207], v[208:211], v[28:31]\n\t"
    "v_mfma_f32_16x16x32_bf16 v[16:19], a[160:163], v[216:219], v[16:19]\n\t"
    "ds_write_b128 v92, v[4:7] offset:5120\n\t"
    "v_mfma_f32_16x16x32_bf16 v[16:19], a[176:179], v[220:223], v[16:19]\n\t"
    "buffer_load_dwordx4 v[144:147], v85, s[36:39], s52 offen offset:0 sc1\n\t"
    "v_mfma_f32_16x16x32_bf16 v[20:23], a[180:183], v[220:223], v[20:23]\n\t"
    "v_mfma_f32_16x16x32_bf16 v[20:23], a[164:167], v[216:219], v[20:23]\n\t"
    "v_mfma_f32_16x16x32_bf16 v[24:27], a[168:171], v[216:219], v[24:27]\n\t"
    "buffer_load_dwordx4 v[148:151], v85, s[36:39], s52 offen offset:1024 sc1\n\t"
    "v_mfma_f32_16x16x32_bf16 v[24:27], a[184:187], v[220:223], v[24:27]\n\t"
    "ds_write_b128 v92, v[8:11] offset:6144\n\t"
    "v_mfma_f32_16x16x32_bf16 v[28:31], a[188:191], v[220:223], v[28:31]\n\t"
    "v_mfma_f32_16x16x32_bf16 v[28:31], a[172:175], v[216:219], v[28:31]\n\t"
    "v_mfma_f32_16x16x32_bf16 v[16:19], a[208:211], v[96:99], v[16:19]\n\t"
    "buffer_load_dwordx4 v[152:155], v85, s[36:39], s52 offen offset:2048 sc1\n\t"
    "v_mfma_f32_16x16x32_bf16 v[16:19], a[224:227], v[100:103], v[16:19]\n\t"
    "s_lshl_b32 s68, s60, 18\n\t"
    "v_mfma_f32_16x16x32_bf16 v[20:23], a[228:231], v[100:103], v[20:23]\n\t"
    "ds_write_b128 v92, v[12:15] offset:7168\n\t"
    "v_mfma_f32_16x16x32_bf16 v[20:23], a[212:215], v[96:99], v[20:23]\n\t"
    "buffer_load_dwordx4 v[156:159], v85, s[36:39], s52 offen offset:3072 sc1\n\t"
    "v_mfma_f32_16x16x32_bf16 v[24:27], a[216:219], v[96:99], v[24:27]\n\t"
    "v_mfma_f32_16x16x32_bf16 v[24:27], a[232:235], v[100:103], v[24:27]\n\t"
    "v_mfma_f32_16x16x32_bf16 v[28:31], a[236:239], v[100:103], v[28:31]\n\t"
    "s_add_u32 s25, s25, s68\n\t"
    "v_mfma_f32_16x16x32_bf16 v[28:31], a[220:223], v[96:99], v[28:31]\n\t"
    "v_mfma_f32_16x16x32_bf16 v[16:19], a[128:131], v[104:107], v[16:19]\n\t"
    "buffer_load_dwordx4 v[160:163], v85, s[36:39], s53 offen offset:0 sc1\n\t"
    "v_mfma_f32_16x16x32_bf16 v[16:19], a[144:147], v[108:111], v[16:19]\n\t"
    "v_mfma_f32_16x16x32_bf16 v[20:23], a[148:151], v[108:111], v[20:23]\n\t"
    "buffer_load_dwordx4 v[164:167], v85, s[36:39], s53 offen offset:1024 sc1\n\t"
    "v_mfma_f32_16x16x32_bf16 v[20:23], a[132:135], v[104:107], v[20:23]\n\t"
    "s_add_u32 s25, s25, 0x400000\n\t"
    "v_mfma_f32_16x16x32_bf16 v[24:27], a[136:139], v[104:107], v[24:27]\n\t"
    "s_waitcnt lgkmcnt(0)\n\t"
    "s_barrier\n\t"
    "v_mfma_f32_16x16x32_bf16 v[24:27], a[152:155], v[108:111], v[24:27]\n\t"
    "v_lshl_add_u32 v86, v73, 14, s25\n\t"
    "v_mfma_f32_16x16x32_bf16 v[28:31], a[156:159], v[108:111], v[28:31]\n\t"
    "buffer_load_dwordx4 v[168:171], v85, s[36:39], s53 offen offset:2048 sc1\n\t"
    "v_mfma_f32_16x16x32_bf16 v[28:31], a[140:143], v[104:107], v[28:31]\n\t"
    "v_mfma_f32_16x16x32_bf16 v[16:19], a[0:3], v[112:115], v[16:19]\n\t"
    "v_mfma_f32_16x16x32_bf16 v[16:19], a[16:19], v[116:119], v[16:19]\n\t"
    "buffer_load_dwordx4 v[172:175], v85, s[36:39], s53 offen offset:3072 sc1\n\t"
    "v_mfma_f32_16x16x32_bf16 v[20:23], a[20:23], v[116:119], v[20:23]\n\t"
    "ds_read_b128 v[8:11], v93 offset:6144\n\t"
    "v_mfma_f32_16x16x32_bf16 v[20:23], a[4:7], v[112:115], v[20:23]\n\t"
    "v_mfma_f32_16x16x32_bf16 v[24:27], a[8:11], v[112:115], v[24:27]\n\t"
    "v_lshl_add_u32 v86, v74, 4, v86\n\t"
    "v_mfma_f32_16x16x32_bf16 v[24:27], a[24:27], v[116:119], v[24:27]\n\t"
    "ds_read_b128 v[4:7], v93 offset:5120\n\t"
    "v_mfma_f32_16x16x32_bf16 v[28:31], a[28:31], v[116:119], v[28:31]\n\t"
    "buffer_load_dwordx4 v[48:51], v85, s[36:39], s54 offen offset:0 sc1\n\t"
    "v_mfma_f32_16x16x32_bf16 v[28:31], a[12:15], v[112:115], v[28:31]\n\t"
    "s_waitcnt vmcnt(13)\n\t"
    "v_mfma_f32_16x16x32_bf16 v[16:19], a[32:35], v[120:123], v[16:19]\n\t"
    "v_mfma_f32_16x16x32_bf16 v[16:19], a[48:51], v[124:127], v[16:19]\n\t"
    "buffer_load_dwordx4 v[52:55], v85, s[36:39], s54 offen offset:1024 sc1\n\t"
    "v_mfma_f32_16x16x32_bf16 v[20:23], a[52:55], v[124:127], v[20:23]\n\t"
    "ds_read_b128 v[12:15], v93 offset:7168\n\t"
    "v_mfma_f32_16x16x32_bf16 v[20:23], a[36:39], v[120:123], v[20:23]\n\t"
    "buffer_load_dwordx4 v[56:59], v85, s[36:39], s54 offen offset:2048 sc1\n\t"
    "v_mfma_f32_16x16x32_bf16 v[24:27], a[40:43], v[120:123], v[24:27]\n\t"
    "v_add_u32 v75, 0x2000, v86\n\t"
    "v_mfma_f32_16x16x32_bf16 v[24:27], a[56:59], v[124:127], v[24:27]\n\t"
    "s_lshl_b32 s25, s26, 15\n\t"
    "v_mfma_f32_16x16x32_bf16 v[28:31], a[60:63], v[124:127], v[28:31]\n\t"
    "v_mfma_f32_16x16x32_bf16 v[28:31], a[44:47], v[120:123], v[28:31]\n\t"
    "s_waitcnt vmcnt(13)\n\t"
    "v_mfma_f32_16x16x32_bf16 v[16:19], a[64:67], v[128:131], v[16:19]\n\t"
    "ds_read_b128 v[0:3], v93 offset:4096\n\t"
    "v_mfma_f32_16x16x32_bf16 v[16:19], a[80:83], v[132:135], v[16:19]\n\t"
    "buffer_load_dwordx4 v[60:63], v85, s[36:39], s54 offen offset:3072 sc1\n\t"
    "v_mfma_f32_16x16x32_bf16 v[20:23], a[84:87], v[132:135], v[20:23]\n\t"
    "buffer_load_dwordx4 v[248:251], v85, s[36:39], s55 offen offset:0 sc1\n\t"
    "v_mfma_f32_16x16x32_bf16 v[20:23], a[68:71], v[128:131], v[20:23]\n\t"
    "v_mfma_f32_16x16x32_bf16 v[24:27], a[72:75], v[128:131], v[24:27]\n\t"
    "v_mfma_f32_16x16x32_bf16 v[24:27], a[88:91], v[132:135], v[24:27]\n\t"
    "s_lshl_b32 s26, s60, 7\n\t"
    "v_mfma_f32_16x16x32_bf16 v[28:31], a[92:95], v[132:135], v[28:31]\n\t"
    "global_load_dword v88, v87, s[84:85] sc1\n\t"
    "v_mfma_f32_16x16x32_bf16 v[28:31], a[76:79], v[128:131], v[28:31]\n\t"
    "s_waitcnt vmcnt(14)\n\t"
    "v_mfma_f32_16x16x32_bf16 v[16:19], a[96:99], v[136:139], v[16:19]\n\t"
    "buffer_load_dwordx4 v[252:255], v85, s[36:39], s55 offen offset:1024 sc1\n\t"
    "v_mfma_f32_16x16x32_bf16 v[16:19], a[112:115], v[140:143], v[16:19]\n\t"
    "v_mfma_f32_16x16x32_bf16 v[20:23], a[116:119], v[140:143], v[20:23]\n\t"
    "v_mfma_f32_16x16x32_bf16 v[20:23], a[100:103], v[136:139], v[20:23]\n\t"
    "buffer_load_dwordx4 v[176:179], v85, s[36:39], s55 offen offset:2048 sc1\n\t"
    "v_mfma_f32_16x16x32_bf16 v[24:27], a[104:107], v[136:139], v[24:27]\n\t"
    "v_mfma_f32_16x16x32_bf16 v[24:27], a[120:123], v[140:143], v[24:27]\n\t"
    "v_mfma_f32_16x16x32_bf16 v[28:31], a[124:127], v[140:143], v[28:31]\n\t"
    "buffer_load_dwordx4 v[180:183], v85, s[36:39], s55 offen offset:3072 sc1\n\t"
    "v_mfma_f32_16x16x32_bf16 v[28:31], a[108:111], v[136:139], v[28:31]\n\t"
    "s_waitcnt vmcnt(15)\n\t"
    "s_waitcnt lgkmcnt(0)\n\t"
    "v_mfma_f32_16x16x32_bf16 v[0:3], a[192:195], v[144:147], v[0:3]\n\t"
    "buffer_load_dwordx4 v[184:187], v82, s[36:39], s52 offen offset:0 sc1\n\t"
    "v_mfma_f32_16x16x32_bf16 v[0:3], a[240:243], v[148:151], v[0:3]\n\t"
    "s_add_u32 s25, s25, s26\n\t"
    "v_mfma_f32_16x16x32_bf16 v[4:7], a[244:247], v[148:151], v[4:7]\n\t"
    "v_mfma_f32_16x16x32_bf16 v[4:7], a[196:199], v[144:147], v[4:7]\n\t"
    "v_mfma_f32_16x16x32_bf16 v[8:11], a[200:203], v[144:147], v[8:11]\n\t"
    "v_mfma_f32_16x16x32_bf16 v[8:11], a[248:251], v[148:151], v[8:11]\n\t"
    "buffer_load_dwordx4 v[188:191], v82, s[36:39], s52 offen offset:1024 sc1\n\t"
    "v_mfma_f32_16x16x32_bf16 v[12:15], a[252:255], v[148:151], v[12:15]\n\t"
    "ds_write_b128 v92, v[16:19] offset:0\n\t"
    "v_mfma_f32_16x16x32_bf16 v[12:15], a[204:207], v[144:147], v[12:15]\n\t"
    "s_waitcnt vmcnt(15)\n\t"
    "v_mfma_f32_16x16x32_bf16 v[0:3], a[160:163], v[152:155], v[0:3]\n\t"
    "buffer_load_dwordx4 v[192:195], v82, s[36:39], s52 offen offset:2048 sc1\n\t"
    "v_mfma_f32_16x16x32_bf16 v[0:3], a[176:179], v[156:159], v[0:3]\n\t"
    "v_mfma_f32_16x16x32_bf16 v[4:7], a[180:183], v[156:159], v[4:7]\n\t"
    "v_mfma_f32_16x16x32_bf16 v[4:7], a[164:167], v[152:155], v[4:7]\n\t"
    "ds_write_b128 v92, v[20:23] offset:1024\n\t"
    "v_mfma_f32_16x16x32_bf16 v[8:11], a[168:171], v[152:155], v[8:11]\n\t"
    "ds_write_b128 v92, v[24:27] offset:2048\n\t"
    "v_mfma_f32_16x16x32_bf16 v[8:11], a[184:187], v[156:159], v[8:11]\n\t"
    "buffer_load_dwordx4 v[196:199], v82, s[36:39], s52 offen offset:3072 sc1\n\t"
    "v_mfma_f32_16x16x32_bf16 v[12:15], a[188:191], v[156:159], v[12:15]\n\t"
    "v_mfma_f32_16x16x32_bf16 v[12:15], a[172:175], v[152:155], v[12:15]\n\t"
    "s_waitcnt vmcnt(15)\n\t"
    "v_mfma_f32_16x16x32_bf16 v[0:3], a[208:211], v[160:163], v[0:3]\n\t"
    "buffer_load_dwordx4 v[200:203], v82, s[36:39], s53 offen offset:0 sc1\n\t"
    "v_mfma_f32_16x16x32_bf16 v[0:3], a[224:227], v[164:167], v[0:3]\n\t"
    "ds_write_b128 v92, v[28:31] offset:3072\n\t"
    "v_mfma_f32_16x16x32_bf16 v[4:7], a[228:231], v[164:167], v[4:7]\n\t"
    "v_mfma_f32_16x16x32_bf16 v[4:7], a[212:215], v[160:163], v[4:7]\n\t"
    "buffer_load_dwordx4 v[204:207], v82, s[36:39], s53 offen offset:1024 sc1\n\t"
    "v_mfma_f32_16x16x32_bf16 v[8:11], a[216:219], v[160:163], v[8:11]\n\t"
    "v_mfma_f32_16x16x32_bf16 v[8:11], a[232:235], v[164:167], v[8:11]\n\t"
    "buffer_load_dwordx4 v[208:211], v82, s[36:39], s53 offen offset:2048 sc1\n\t"
    "v_mfma_f32_16x16x32_bf16 v[12:15], a[236:239], v[164:167], v[12:15]\n\t"
    "v_mfma_f32_16x16x32_bf16 v[12:15], a[220:223], v[160:163], v[12:15]\n\t"
    "s_waitcnt vmcnt(16)\n\t"
    "v_mfma_f32_16x16x32_bf16 v[0:3], a[128:131], v[168:171], v[0:3]\n\t"
    "v_mfma_f32_16x16x32_bf16 v[0:3], a[144:147], v[172:175], v[0:3]\n\t"
    "v_mfma_f32_16x16x32_bf16 v[4:7], a[148:151], v[172:175], v[4:7]\n\t"
    "buffer_load_dwordx4 v[212:215], v82, s[36:39], s53 offen offset:3072 sc1\n\t"
    "v_mfma_f32_16x16x32_bf16 v[4:7], a[132:135], v[168:171], v[4:7]\n\t"
    "v_mfma_f32_16x16x32_bf16 v[8:11], a[136:139], v[168:171], v[8:11]\n\t"
    "s_waitcnt lgkmcnt(0)\n\t"
    "s_barrier\n\t"
    "v_mfma_f32_16x16x32_bf16 v[8:11], a[152:155], v[172:175], v[8:11]\n\t"
    "s_add_u32 s25, s25, 0x1000\n\t"
    "v_mfma_f32_16x16x32_bf16 v[12:15], a[156:159], v[172:175], v[12:15]\n\t"
    "buffer_load_dwordx4 v[216:219], v82, s[36:39], s54 offen offset:0 sc1\n\t"
    "v_mfma_f32_16x16x32_bf16 v[12:15], a[140:143], v[168:171], v[12:15]\n\t"
    "s_waitcnt vmcnt(16)\n\t"
    "v_mfma_f32_16x16x32_bf16 v[0:3], a[0:3], v[48:51], v[0:3]\n\t"
    "v_mfma_f32_16x16x32_bf16 v[0:3], a[16:19], v[52:55], v[0:3]\n\t"
    "buffer_load_dwordx4 v[220:223], v82, s[36:39], s54 offen offset:1024 sc1\n\t"
    "v_mfma_f32_16x16x32_bf16 v[4:7], a[20:23], v[52:55], v[4:7]\n\t"
    "v_mfma_f32_16x16x32_bf16 v[4:7], a[4:7], v[48:51], v[4:7]\n\t"
    "s_add_u32 s68, s50, s25\n\t"
    "s_addc_u32 s69, s51, 0\n\t"
    "v_mfma_f32_16x16x32_bf16 v[8:11], a[8:11], v[48:51], v[8:11]\n\t"
    "v_lshlrev_b32 v71, 13, v74\n\t"
    "v_mfma_f32_16x16x32_bf16 v[8:11], a[24:27], v[52:55], v[8:11]\n\t"
    "buffer_load_dwordx4 v[96:99], v82, s[36:39], s54 offen offset:2048 sc1\n\t"
    "v_mfma_f32_16x16x32_bf16 v[12:15], a[28:31], v[52:55], v[12:15]\n\t"
    "ds_read_b128 v[16:19], v93 offset:0\n\t"
    "v_mfma_f32_16x16x32_bf16 v[12:15], a[12:15], v[48:51], v[12:15]\n\t"
    "s_waitcnt vmcnt(16)\n\t"
    "v_mfma_f32_16x16x32_bf16 v[0:3], a[32:35], v[56:59], v[0:3]\n\t"
    "ds_read_b128 v[24:27], v93 offset:2048\n\t"
    "v_mfma_f32_16x16x32_bf16 v[0:3], a[48:51], v[60:63], v[0:3]\n\t"
    "buffer_load_dwordx4 v[100:103], v82, s[36:39], s54 offen offset:3072 sc1\n\t"
    "v_mfma_f32_16x16x32_bf16 v[4:7], a[52:55], v[60:63], v[4:7]\n\t"
    "v_lshl_add_u32 v71, v73, 3, v71\n\t"
    "v_mfma_f32_16x16x32_bf16 v[4:7], a[36:39], v[56:59], v[4:7]\n\t"
    "buffer_load_dwordx4 v[104:107], v82, s[36:39], s55 offen offset:0 sc1\n\t"
    "v_mfma_f32_16x16x32_bf16 v[8:11], a[40:43], v[56:59], v[8:11]\n\t"
    "ds_read_b128 v[28:31], v93 offset:3072\n\t"
    "v_mfma_f32_16x16x32_bf16 v[8:11], a[56:59], v[60:63], v[8:11]\n\t"
    "ds_read_b128 v[20:23], v93 offset:1024\n\t"
    "v_mfma_f32_16x16x32_bf16 v[12:15], a[60:63], v[60:63], v[12:15]\n\t"
    "v_mfma_f32_16x16x32_bf16 v[12:15], a[44:47], v[56:59], v[12:15]\n\t"
    "s_waitcnt vmcnt(15)\n\t"
    "v_mfma_f32_16x16x32_bf16 v[0:3], a[64:67], v[248:251], v[0:3]\n\t"
    "buffer_load_dwordx4 v[108:111], v82, s[36:39], s55 offen offset:1024 sc1\n\t"
    "v_mfma_f32_16x16x32_bf16 v[0:3], a[80:83], v[252:255], v[0:3]\n\t"
    "buffer_load_dwordx4 v[112:115], v82, s[36:39], s55 offen offset:2048 sc1\n\t"
    "v_mfma_f32_16x16x32_bf16 v[4:7], a[84:87], v[252:255], v[4:7]\n\t"
    "v_mfma_f32_16x16x32_bf16 v[4:7], a[68:71], v[248:251], v[4:7]\n\t"
    "v_cmp_gt_u32 vcc, 32, v88\n\t"
    "v_mfma_f32_16x16x32_bf16 v[8:11], a[72:75], v[248:251], v[8:11]\n\t"
    "s_cbranch_vccnz L_pY1_%=\n\t"
    "L_rY1_%=:\n\t"
    "v_mfma_f32_16x16x32_bf16 v[8:11], a[88:91], v[252:255], v[8:11]\n\t"
    "buffer_load_dwordx4 v[116:119], v82, s[36:39], s55 offen offset:3072 sc1\n\t"
    "v_mfma_f32_16x16x32_bf16 v[12:15], a[92:95], v[252:255], v[12:15]\n\t"
    "global_load_dwordx4 v[240:243], v86, s[84:85] offset:0 sc1\n\t"
    "v_mfma_f32_16x16x32_bf16 v[12:15], a[76:79], v[248:251], v[12:15]\n\t"
    "s_waitcnt vmcnt(17)\n\t"
    "v_mfma_f32_16x16x32_bf16 v[0:3], a[96:99], v[176:179], v[0:3]\n\t"
    "global_load_dwordx4 v[244:247], v75, s[84:85] offset:0 sc1\n\t"
    "v_mfma_f32_16x16x32_bf16 v[0:3], a[112:115], v[180:183], v[0:3]\n\t"
    "global_load_dwordx4 v[224:227], v70, s[84:85] offset:0 sc1\n\t"
    "v_mfma_f32_16x16x32_bf16 v[4:7], a[116:119], v[180:183], v[4:7]\n\t"
    "v_mfma_f32_16x16x32_bf16 v[4:7], a[100:103], v[176:179], v[4:7]\n\t"
    "global_load_dwordx4 v[228:231], v70, s[84:85] offset:1024 sc1\n\t"
    "v_mfma_f32_16x16x32_bf16 v[8:11], a[104:107], v[176:179], v[8:11]\n\t"
    "v_mfma_f32_16x16x32_bf16 v[8:11], a[120:123], v[180:183], v[8:11]\n\t"
    "v_mfma_f32_16x16x32_bf16 v[12:15], a[124:127], v[180:183], v[12:15]\n\t"
    "v_mfma_f32_16x16x32_bf16 v[12:15], a[108:111], v[176:179], v[12:15]\n\t"
    "s_waitcnt vmcnt(18)\n\t"
    "s_waitcnt lgkmcnt(0)\n\t"
    "v_mfma_f32_16x16x32_bf16 v[16:19], a[192:195], v[184:187], v[16:19]\n\t"
    "v_mfma_f32_16x16x32_bf16 v[16:19], a[240:243], v[188:191], v[16:19]\n\t"
    "v_mfma_f32_16x16x32_bf16 v[20:23], a[244:247], v[188:191], v[20:23]\n\t"
    "v_mfma_f32_16x16x32_bf16 v[20:23], a[196:199], v[184:187], v[20:23]\n\t"
    "ds_write_b128 v92, v[0:3] offset:4096\n\t"
    "v_mfma_f32_16x16x32_bf16 v[24:27], a[200:203], v[184:187], v[24:27]\n\t"
    "v_mfma_f32_16x16x32_bf16 v[24:27], a[248:251], v[188:191], v[24:27]\n\t"
    "v_mfma_f32_16x16x32_bf16 v[28:31], a[252:255], v[188:191], v[28:31]\n\t"
    "global_load_dwordx4 v[232:235], v70, s[84:85] offset:2048 sc1\n\t"
    "v_mfma_f32_16x16x32_bf16 v[28:31], a[204:207], v[184:187], v[28:31]\n\t"
    "s_waitcnt vmcnt(17)\n\t"
    "v_mfma_f32_16x16x32_bf16 v[16:19], a[160:163], v[192:195], v[16:19]\n\t"
    "global_load_dwordx4 v[236:239], v70, s[84:85] offset:3072 sc1\n\t"
    "v_mfma_f32_16x16x32_bf16 v[16:19], a[176:179], v[196:199], v[16:19]\n\t"
    "ds_write_b128 v92, v[4:7] offset:5120\n\t"
    "v_mfma_f32_16x16x32_bf16 v[20:23], a[180:183], v[196:199], v[20:23]\n\t"
    "v_mfma_f32_16x16x32_bf16 v[20:23], a[164:167], v[192:195], v[20:23]\n\t"
    "v_mfma_f32_16x16x32_bf16 v[24:27], a[168:171], v[192:195], v[24:27]\n\t"
    "ds_write_b128 v92, v[8:11] offset:6144\n\t"
    "v_mfma_f32_16x16x32_bf16 v[24:27], a[184:187], v[196:199], v[24:27]\n\t"
    "v_mfma_f32_16x16x32_bf16 v[28:31], a[188:191], v[196:199], v[28:31]\n\t"
    "v_mfma_f32_16x16x32_bf16 v[28:31], a[172:175], v[192:195], v[28:31]\n\t"
    "s_waitcnt vmcnt(16)\n\t"
    "v_mfma_f32_16x16x32_bf16 v[16:19], a[208:211], v[200:203], v[16:19]\n\t"
    "v_mfma_f32_16x16x32_bf16 v[16:19], a[224:227], v[204:207], v[16:19]\n\t"
    "ds_write_b128 v92, v[12:15] offset:7168\n\t"
    "v_mfma_f32_16x16x32_bf16 v[20:23], a[228:231], v[204:207], v[20:23]\n\t"
    "v_mfma_f32_16x16x32_bf16 v[20:23], a[212:215], v[200:203], v[20:23]\n\t"
    "v_mfma_f32_16x16x32_bf16 v[24:27], a[216:219], v[200:203], v[24:27]\n\t"
    "v_mfma_f32_16x16x32_bf16 v[24:27], a[232:235], v[204:207], v[24:27]\n\t"
    "Lis14_%=:\n\t"
    "s_cbranch_execz Lis99_%=\n\t"
    "v_mfma_f32_16x16x32_bf16 v[28:31], a[236:239], v[204:207], v[28:31]\n\t"
    "v_mfma_f32_16x16x32_bf16 v[28:31], a[220:223], v[200:203], v[28:31]\n\t"
    "s_waitcnt vmcnt(14)\n\t"
    "v_mfma_f32_16x16x32_bf16 v[16:19], a[128:131], v[208:211], v[16:19]\n\t"
    "v_mfma_f32_16x16x32_bf16 v[16:19], a[144:147], v[212:215], v[16:19]\n\t"
    "v_mfma_f32_16x16x32_bf16 v[20:23], a[148:151], v[212:215], v[20:23]\n\t"
    "v_mfma_f32_16x16x32_bf16 v[20:23], a[132:135], v[208:211], v[20:23]\n\t"
    "v_mfma_f32_16x16x32_bf16 v[24:27], a[136:139], v[208:211], v[24:27]\n\t"
    "s_waitcnt lgkmcnt(0)\n\t"
    "s_barrier\n\t"
    "v_mfma_f32_16x16x32_bf16 v[24:27], a[152:155], v[212:215], v[24:27]\n\t"
    "v_mfma_f32_16x16x32_bf16 v[28:31], a[156:159], v[212:215], v[28:31]\n\t"
    "ds_read_b128 v[44:47], v94 offset:7168\n\t"
    "v_mfma_f32_16x16x32_bf16 v[28:31], a[140:143], v[208:211], v[28:31]\n\t"
    "s_waitcnt vmcnt(12)\n\t"
    "v_mfma_f32_16x16x32_bf16 v[16:19], a[0:3], v[216:219], v[16:19]\n\t"
    "v_mfma_f32_16x16x32_bf16 v[16:19], a[16:19], v[220:223], v[16:19]\n\t"
    "v_mfma_f32_16x16x32_bf16 v[20:23], a[20:23], v[220:223], v[20:23]\n\t"
    "ds_read_b128 v[32:35], v94 offset:4096\n\t"
    "v_mfma_f32_16x16x32_bf16 v[20:23], a[4:7], v[216:219], v[20:23]\n\t"
    "v_mfma_f32_16x16x32_bf16 v[24:27], a[8:11], v[216:219], v[24:27]\n\t"
    "ds_read_b128 v[36:39], v94 offset:5120\n\t"
    "v_mfma_f32_16x16x32_bf16 v[24:27], a[24:27], v[220:223], v[24:27]\n\t"
    "v_mfma_f32_16x16x32_bf16 v[28:31], a[28:31], v[220:223], v[28:31]\n\t"
    "ds_read_b128 v[40:43], v94 offset:6144\n\t"
    "v_mfma_f32_16x16x32_bf16 v[28:31], a[12:15], v[216:219], v[28:31]\n\t"
    "s_waitcnt vmcnt(10)\n\t"
    "v_mfma_f32_16x16x32_bf16 v[16:19], a[32:35], v[96:99], v[16:19]\n\t"
    "v_mfma_f32_16x16x32_bf16 v[16:19], a[48:51], v[100:103], v[16:19]\n\t"
    "v_mfma_f32_16x16x32_bf16 v[20:23], a[52:55], v[100:103], v[20:23]\n\t"
    "v_mfma_f32_16x16x32_bf16 v[20:23], a[36:39], v[96:99], v[20:23]\n\t"
    "v_mfma_f32_16x16x32_bf16 v[24:27], a[40:43], v[96:99], v[24:27]\n\t"
    "v_mfma_f32_16x16x32_bf16 v[24:27], a[56:59], v[100:103], v[24:27]\n\t"
    "v_mfma_f32_16x16x32_bf16 v[28:31], a[60:63], v[100:103], v[28:31]\n\t"
    "s_waitcnt vmcnt(4)\n\t"
    "v_mfma_f32_16x16x32_bf16 v[28:31], a[44:47], v[96:99], v[28:31]\n\t"
    "v_mfma_f32_16x16x32_bf16 v[16:19], a[64:67], v[104:107], v[16:19]\n\t"
    "v_mfma_f32_16x16x32_bf16 v[16:19], a[80:83], v[108:111], v[16:19]\n\t"
    "v_mfma_f32_16x16x32_bf16 v[20:23], a[84:87], v[108:111], v[20:23]\n\t"
    "v_mfma_f32_16x16x32_bf16 v[20:23], a[68:71], v[104:107], v[20:23]\n\t"
    "v_swap_b32 v241, v244\n\t"
    "v_mfma_f32_16x16x32_bf16 v[24:27], a[72:75], v[104:107], v[24:27]\n\t"
    "v_swap_b32 v243, v246\n\t"
    "v_mfma_f32_16x16x32_bf16 v[24:27], a[88:91], v[108:111], v[24:27]\n\t"
    "global_store_dwordx2 v71, v[240:241], s[68:69] offset:-4096\n\t"
    "v_mfma_f32_16x16x32_bf16 v[28:31], a[92:95], v[108:111], v[28:31]\n\t"
    "global_store_dwordx2 v71, v[244:245], s[68:69] offset:-2048\n\t"
    "s_waitcnt vmcnt(2)\n\t"
    "v_mfma_f32_16x16x32_bf16 v[28:31], a[76:79], v[104:107], v[28:31]\n\t"
    "s_waitcnt lgkmcnt(0)\n\t"
    "v_add_f32 v46, v46, v238\n\t"
    "v_add_f32 v47, v47, v239\n\t"
    "v_mfma_f32_16x16x32_bf16 v[16:19], a[96:99], v[112:115], v[16:19]\n\t"
    "v_add_f32 v38, v38, v230\n\t"
    "v_add_f32 v39, v39, v231\n\t"
    "v_mfma_f32_16x16x32_bf16 v[16:19], a[112:115], v[116:119], v[16:19]\n\t"
    "global_store_dwordx2 v71, v[242:243], s[68:69] offset:0\n\t"
    "v_mfma_f32_16x16x32_bf16 v[20:23], a[116:119], v[116:119], v[20:23]\n\t"
    "v_add_f32 v44, v44, v236\n\t"
    "v_add_f32 v45, v45, v237\n\t"
    "v_mfma_f32_16x16x32_bf16 v[20:23], a[100:103], v[112:115], v[20:23]\n\t"
    "v_add_f32 v40, v40, v232\n\t"
    "v_add_f32 v41, v41, v233\n\t"
    "v_mfma_f32_16x16x32_bf16 v[24:27], a[104:107], v[112:115], v[24:27]\n\t"
    "v_add_f32 v32, v32, v224\n\t"
    "v_add_f32 v33, v33, v225\n\t"
    "v_mfma_f32_16x16x32_bf16 v[24:27], a[120:123], v[116:119], v[24:27]\n\t"
    "global_store_dwordx2 v71, v[246:247], s[68:69] offset:2048\n\t"
    "v_mfma_f32_16x16x32_bf16 v[28:31], a[124:127], v[116:119], v[28:31]\n\t"
    "v_add_f32 v42, v42, v234\n\t"
    "v_add_f32 v43, v43, v235\n\t"
    "v_mfma_f32_16x16x32_bf16 v[28:31], a[108:111], v[112:115], v[28:31]\n\t"
    "v_add_f32 v34, v34, v226\n\t"
    "v_add_f32 v35, v35, v227\n\t"
    "v_add_f32 v36, v36, v228\n\t"
    "v_add_f32 v37, v37, v229\n\t"
    "v_pk_add_f32 v[32:33], v[32:33], v[16:17]\n\t"
    "v_pk_add_f32 v[34:35], v[34:35], v[18:19]\n\t"
    "v_pk_add_f32 v[36:37], v[36:37], v[20:21]\n\t"
    "v_pk_add_f32 v[38:39], v[38:39], v[22:23]\n\t"
    "v_pk_add_f32 v[40:41], v[40:41], v[24:25]\n\t"
    "v_pk_add_f32 v[42:43], v[42:43], v[26:27]\n\t"
    "v_pk_add_f32 v[44:45], v[44:45], v[28:29]\n\t"
    "v_pk_add_f32 v[46:47], v[46:47], v[30:31]\n\t"
    "v_cvt_pk_bf16_f32 v0, v32, v33\n\t"
    "v_cvt_pk_bf16_f32 v1, v34, v35\n\t"
    "v_cvt_pk_bf16_f32 v2, v36, v37\n\t"
    "v_cvt_pk_bf16_f32 v3, v38, v39\n\t"
    "v_cvt_pk_bf16_f32 v4, v40, v41\n\t"
    "v_cvt_pk_bf16_f32 v5, v42, v43\n\t"
    "v_cvt_pk_bf16_f32 v6, v44, v45\n\t"
    "v_cvt_pk_bf16_f32 v7, v46, v47\n\t"
    "global_store_dwordx4 v69, v[0:3], s[12:13]\n\t"
    "global_store_dwordx4 v69, v[4:7], s[12:13] offset:64\n\t"
    "s_endpgm\n\t"
    "L_v0_%=:\n\t"
    "ds_read_b64 v[0:1], v69 offset:0\n\t"
    "ds_read_b64 v[2:3], v69 offset:8\n\t"
    "ds_read_b128 v[4:7], v70 offset:3584\n\t"
    "ds_read_b128 v[8:11], v70 offset:3072\n\t"
    "ds_read_b128 v[12:15], v70 offset:2560\n\t"
    "ds_read_b128 v[16:19], v70 offset:2048\n\t"
    "ds_read_b128 v[20:23], v70 offset:1536\n\t"
    "ds_read_b128 v[24:27], v70 offset:1024\n\t"
    "ds_read_b128 v[28:31], v70 offset:512\n\t"
    "ds_read_b128 v[32:35], v70 offset:0\n\t"
    "ds_read_b64 v[36:37], v69 offset:512\n\t"
    "ds_read_b64 v[38:39], v69 offset:520\n\t"
    "s_waitcnt lgkmcnt(9)\n\t"
    "v_mfma_f32_16x16x32_f16 v[224:227], v[0:3], v[4:7], 0\n\t"
    "ds_read_b64 v[40:41], v69 offset:1024\n\t"
    "s_lshr_b32 s25, s72, 1\n\t"
    "s_waitcnt lgkmcnt(9)\n\t"
    "v_mfma_f32_16x16x32_f16 v[228:231], v[0:3], v[8:11], 0\n\t"
    "ds_read_b64 v[42:43], v69 offset:1032\n\t"
    "ds_read_b64_tr_b16 a[228:229], v79 offset:32776\n\t"
    "ds_read_b64_tr_b16 a[236:237], v80 offset:32776\n\t"
    "s_waitcnt lgkmcnt(11)\n\t"
    "v_mfma_f32_16x16x32_f16 v[232:235], v[0:3], v[12:15], 0\n\t"
    "ds_read_b64 v[44:45], v69 offset:1536\n\t"
    "ds_read_b64 v[46:47], v69 offset:1544\n\t"
    "s_waitcnt lgkmcnt(12)\n\t"
    "v_mfma_f32_16x16x32_f16 v[236:239], v[0:3], v[16:19], 0\n\t"
    "ds_read_b64_tr_b16 a[238:239], v80 offset:34824\n\t"
    "s_add_u32 m0, s45, 0x0\n\t"
    "s_lshl_b32 s25, s25, 20\n\t"
    "s_waitcnt lgkmcnt(12)\n\t"
    "v_mfma_f32_16x16x32_f16 v[240:243], v[0:3], v[20:23], 0\n\t"
    "buffer_load_dwordx4 v64, s[40:43], s44 offen offset:0 lds\n\t"
    "s_waitcnt lgkmcnt(11)\n\t"
    "v_mfma_f32_16x16x32_f16 v[244:247], v[0:3], v[24:27], 0\n\t"
    "ds_read_b64 v[56:57], v69 offset:3072\n\t"
    "ds_read_b64 v[62:63], v69 offset:3592\n\t"
    "ds_read_b64_tr_b16 a[226:227], v79 offset:34816\n\t"
    "s_waitcnt lgkmcnt(13)\n\t"
    "v_mfma_f32_16x16x32_f16 v[248:251], v[0:3], v[28:31], 0\n\t"
    "ds_read_b64_tr_b16 a[224:225], v79 offset:32768\n\t"
    "ds_read_b64_tr_b16 a[230:231], v79 offset:34824\n\t"
    "s_waitcnt lgkmcnt(13)\n\t"
    "v_mfma_f32_16x16x32_f16 v[252:255], v[0:3], v[32:35], 0\n\t"
    "buffer_load_dwordx4 v65, s[40:43], s44 offen offset:1024 lds\n\t"
    "s_waitcnt lgkmcnt(12)\n\t"
    "v_mfma_f32_16x16x32_f16 v[228:231], v[36:39], v[4:7], v[228:231]\n\t"
    "ds_read_b64_tr_b16 a[232:233], v80 offset:32768\n\t"
    "ds_read_b64_tr_b16 a[132:133], v79 offset:36872\n\t"
    "s_lshl_b32 s26, s73, 10\n\t"
    "v_mfma_f32_16x16x32_f16 v[232:235], v[36:39], v[8:11], v[232:235]\n\t"
    "ds_read_b64 v[54:55], v69 offset:2568\n\t"
    "ds_read_b64_tr_b16 a[140:141], v80 offset:36872\n\t"
    "s_add_u32 s25, s25, s26\n\t"
    "v_mfma_f32_16x16x32_f16 v[236:239], v[36:39], v[12:15], v[236:239]\n\t"
    "buffer_load_dwordx4 v66, s[40:43], s44 offen offset:2048 lds\n\t"
    "v_mfma_f32_16x16x32_f16 v[240:243], v[36:39], v[16:19], v[240:243]\n\t"
    "ds_read_b64 v[52:53], v69 offset:2560\n\t"
    "ds_read_b64_tr_b16 a[234:235], v80 offset:34816\n\t"
    "ds_read_b64_tr_b16 a[134:135], v79 offset:38920\n\t"
    "v_mfma_f32_16x16x32_f16 v[244:247], v[36:39], v[20:23], v[244:247]\n\t"
    "ds_read_b64 v[48:49], v69 offset:2048\n\t"
    "ds_read_b64 v[58:59], v69 offset:3080\n\t"
    "s_add_u32 s25, s25, 0x3000000\n\t"
    "v_mfma_f32_16x16x32_f16 v[248:251], v[36:39], v[24:27], v[248:251]\n\t"
    "buffer_load_dwordx4 v67, s[40:43], s44 offen offset:3072 lds\n\t"
    "v_mfma_f32_16x16x32_f16 v[252:255], v[36:39], v[28:31], v[252:255]\n\t"
    "ds_read_b64_tr_b16 a[130:131], v79 offset:38912\n\t"
    "s_add_u32 s36, s20, s25\n\t"
    "s_addc_u32 s37, s21, 0\n\t"
    "s_waitcnt lgkmcnt(10)\n\t"
    "v_mfma_f32_16x16x32_f16 v[232:235], v[40:43], v[4:7], v[232:235]\n\t"
    "ds_read_b64_tr_b16 a[138:139], v80 offset:38912\n\t"
    "ds_read_b64_tr_b16 a[142:143], v80 offset:38920\n\t"
    "v_mfma_f32_16x16x32_f16 v[236:239], v[40:43], v[8:11], v[236:239]\n\t"
    "ds_read_b64 v[50:51], v69 offset:2056\n\t"
    "ds_read_b64_tr_b16 a[128:129], v79 offset:36864\n\t"
    "s_add_u32 s44, s44, 0x8000\n\t"
    "v_mfma_f32_16x16x32_f16 v[240:243], v[40:43], v[12:15], v[240:243]\n\t"
    "ds_read_b128 v[32:35], v70 offset:3648\n\t"
    "ds_read_b64_tr_b16 a[148:149], v77 offset:32776\n\t"
    "s_waitcnt lgkmcnt(10)\n\t"
    "s_add_u32 m0, s45, 0x18000\n\t"
    "v_mfma_f32_16x16x32_f16 v[244:247], v[40:43], v[16:19], v[244:247]\n\t"
    "ds_read_b64 v[60:61], v69 offset:3584\n\t"
    "ds_read_b64_tr_b16 a[136:137], v80 offset:36864\n\t"
    "ds_read_b64_tr_b16 a[156:157], v78 offset:32776\n\t"
    "v_mfma_f32_16x16x32_f16 v[248:251], v[40:43], v[20:23], v[248:251]\n\t"
    "ds_read_b64_tr_b16 a[144:145], v77 offset:32768\n\t"
    "ds_read_b64_tr_b16 a[158:159], v78 offset:34824\n\t"
    "v_mfma_f32_16x16x32_f16 v[252:255], v[40:43], v[24:27], v[252:255]\n\t"
    "ds_read_b64_tr_b16 a[152:153], v78 offset:32768\n\t"
    "ds_read_b64_tr_b16 a[154:155], v78 offset:34816\n\t"
    "s_mov_b32 s52, 0x80000\n\t"
    "v_mfma_f32_16x16x32_f16 v[236:239], v[44:47], v[4:7], v[236:239]\n\t"
    "buffer_load_dwordx4 v64, s[40:43], s44 offen offset:0 lds\n\t"
    "v_mfma_f32_16x16x32_f16 v[240:243], v[44:47], v[8:11], v[240:243]\n\t"
    "buffer_load_dwordx4 v65, s[40:43], s44 offen offset:1024 lds\n\t"
    "v_mfma_f32_16x16x32_f16 v[244:247], v[44:47], v[12:15], v[244:247]\n\t"
    "ds_read_b128 v[40:43], v70 offset:1600\n\t"
    "ds_read_b64_tr_b16 a[150:151], v77 offset:34824\n\t"
    "s_mov_b32 s53, 0x81000\n\t"
    "v_mfma_f32_16x16x32_f16 v[248:251], v[44:47], v[16:19], v[248:251]\n\t"
    "buffer_load_dwordx4 v66, s[40:43], s44 offen offset:2048 lds\n\t"
    "v_mfma_f32_16x16x32_f16 v[252:255], v[44:47], v[20:23], v[252:255]\n\t"
    "ds_read_b64 v[0:1], v69 offset:64\n\t"
    "ds_read_b64 v[2:3], v69 offset:72\n\t"
    "ds_read_b64_tr_b16 a[146:147], v77 offset:34816\n\t"
    "s_waitcnt lgkmcnt(12)\n\t"
    "v_mfma_f32_16x16x32_f16 v[240:243], v[48:51], v[4:7], v[240:243]\n\t"
    "buffer_load_dwordx4 v67, s[40:43], s44 offen offset:3072 lds\n\t"
    "v_mfma_f32_16x16x32_f16 v[244:247], v[48:51], v[8:11], v[244:247]\n\t"
    "ds_read_b128 v[24:27], v70 offset:2112\n\t"
    "ds_read_b128 v[20:23], v70 offset:1088\n\t"
    "ds_read_b128 v[44:47], v70 offset:576\n\t"
    "v_mfma_f32_16x16x32_f16 v[248:251], v[48:51], v[12:15], v[248:251]\n\t"
    "ds_read_b128 v[28:31], v70 offset:3136\n\t"
    "s_add_u32 s44, s44, 0x8000\n\t"
    "v_mfma_f32_16x16x32_f16 v[252:255], v[48:51], v[16:19], v[252:255]\n\t"
    "ds_read_b128 v[36:39], v70 offset:2624\n\t"
    "ds_read_b128 v[16:19], v70 offset:64\n\t"
    "s_waitcnt lgkmcnt(11)\n\t"
    "s_add_u32 m0, s45, 0x19000\n\t"
    "v_mfma_f32_16x16x32_f16 v[244:247], v[52:55], v[4:7], v[244:247]\n\t"
    "buffer_load_dwordx4 v64, s[40:43], s44 offen offset:0 lds\n\t"
    "v_mfma_f32_16x16x32_f16 v[248:251], v[52:55], v[8:11], v[248:251]\n\t"
    "ds_read_b64 v[48:49], v69 offset:576\n\t"
    "ds_read_b64 v[50:51], v69 offset:584\n\t"
    "ds_read_b64_tr_b16 a[10:11], v78 offset:38912\n\t"
    "v_mfma_f32_16x16x32_f16 v[252:255], v[52:55], v[12:15], v[252:255]\n\t"
    "buffer_load_dwordx4 v65, s[40:43], s44 offen offset:1024 lds\n\t"
    "v_mfma_f32_16x16x32_f16 v[248:251], v[56:59], v[4:7], v[248:251]\n\t"
    "ds_read_b64_tr_b16 a[0:1], v77 offset:36864\n\t"
    "ds_read_b64_tr_b16 a[8:9], v78 offset:36864\n\t"
    "s_mov_b32 s54, 0x82000\n\t"
    "v_mfma_f32_16x16x32_f16 v[252:255], v[56:59], v[8:11], v[252:255]\n\t"
    "buffer_load_dwordx4 v66, s[40:43], s44 offen offset:2048 lds\n\t"
    "v_mfma_f32_16x16x32_f16 v[252:255], v[60:63], v[4:7], v[252:255]\n\t"
    "ds_read_b64_tr_b16 a[4:5], v77 offset:36872\n\t"
    "ds_read_b64_tr_b16 a[6:7], v77 offset:38920\n\t"
    "s_mov_b32 s55, 0x83000\n\t"
    "s_waitcnt lgkmcnt(10)\n\t"
    "v_mfma_f32_16x16x32_f16 v[224:227], v[0:3], v[32:35], v[224:227]\n\t"
    "ds_read_b64 v[56:57], v69 offset:2624\n\t"
    "ds_read_b64 v[58:59], v69 offset:2632\n\t"
    "ds_read_b64_tr_b16 a[12:13], v78 offset:36872\n\t"
    "s_waitcnt lgkmcnt(11)\n\t"
    "v_mfma_f32_16x16x32_f16 v[228:231], v[0:3], v[28:31], v[228:231]\n\t"
    "buffer_load_dwordx4 v67, s[40:43], s44 offen offset:3072 lds\n\t"
    "v_mfma_f32_16x16x32_f16 v[232:235], v[0:3], v[36:39], v[232:235]\n\t"
    "ds_read_b64 v[52:53], v69 offset:1600\n\t"
    "ds_read_b64_tr_b16 a[14:15], v78 offset:38920\n\t"
    "s_add_u32 s44, s44, 0x8000\n\t"
    "v_mfma_f32_16x16x32_f16 v[236:239], v[0:3], v[24:27], v[236:239]\n\t"
    "ds_read_b64 v[54:55], v69 offset:1608\n\t"
    "ds_read_b64 v[10:11], v69 offset:2120\n\t"
    "ds_read_b64_tr_b16 a[2:3], v77 offset:38912\n\t"
    "v_mfma_f32_16x16x32_f16 v[240:243], v[0:3], v[40:43], v[240:243]\n\t"
    "global_load_dword v88, v87, s[84:85] sc1\n\t"
    "v_mfma_f32_16x16x32_f16 v[244:247], v[0:3], v[20:23], v[244:247]\n\t"
    "s_mov_b32 s9, 0x7000\n\t"
    "s_add_u32 m0, s45, 0x8000\n\t"
    "v_mfma_f32_16x16x32_f16 v[248:251], v[0:3], v[44:47], v[248:251]\n\t"
    "ds_read_b64 v[12:13], v69 offset:1088\n\t"
    "ds_read_b64 v[14:15], v69 offset:1096\n\t"
    "ds_read_b64_tr_b16 a[16:17], v77 offset:4096\n\t"
    "s_waitcnt lgkmcnt(11)\n\t"
    "v_mfma_f32_16x16x32_f16 v[252:255], v[0:3], v[16:19], v[252:255]\n\t"
    "buffer_load_dwordx4 v64, s[40:43], s44 offen offset:0 lds\n\t"
    "v_mfma_f32_16x16x32_f16 v[228:231], v[48:51], v[32:35], v[228:231]\n\t"
    "ds_read_b64_tr_b16 a[20:21], v77 offset:4104\n\t"
    "ds_read_b64_tr_b16 a[22:23], v77 offset:6152\n\t"
    "s_add_u32 s25, s58, 0\n\t"
    "v_mfma_f32_16x16x32_f16 v[232:235], v[48:51], v[28:31], v[232:235]\n\t"
    "buffer_load_dwordx4 v65, s[40:43], s44 offen offset:1024 lds\n\t"
    "v_mfma_f32_16x16x32_f16 v[236:239], v[48:51], v[36:39], v[236:239]\n\t"
    "ds_read_b64 v[8:9], v69 offset:2112\n\t"
    "ds_read_b64 v[4:5], v69 offset:3136\n\t"
    "ds_read_b64 v[62:63], v69 offset:3656\n\t"
    "v_mfma_f32_16x16x32_f16 v[240:243], v[48:51], v[24:27], v[240:243]\n\t"
    "ds_read_b64_tr_b16 a[26:27], v78 offset:6144\n\t"
    "ds_read_b64_tr_b16 a[30:31], v78 offset:6152\n\t"
    "s_waitcnt vmcnt(11)\n\t"
    "v_mfma_f32_16x16x32_f16 v[244:247], v[48:51], v[40:43], v[244:247]\n\t"
    "buffer_load_dwordx4 v66, s[40:43], s44 offen offset:2048 lds\n\t"
    "v_mfma_f32_16x16x32_f16 v[248:251], v[48:51], v[20:23], v[248:251]\n\t"
    "ds_read_b64_tr_b16 a[24:25], v78 offset:4096\n\t"
    "ds_read_b64_tr_b16 a[28:29], v78 offset:4104\n\t"
    "ds_read_b64_tr_b16 a[36:37], v77 offset:8\n\t"
    "v_mfma_f32_16x16x32_f16 v[252:255], v[48:51], v[44:47], v[252:255]\n\t"
    "ds_read_b128 v[48:51], v70 offset:2688\n\t"
    "ds_read_b64_tr_b16 a[38:39], v77 offset:2056\n\t"
    "ds_read_b64_tr_b16 a[42:43], v78 offset:2048\n\t"
    "s_waitcnt lgkmcnt(13)\n\t"
    "v_mfma_f32_16x16x32_f16 v[232:235], v[12:15], v[32:35], v[232:235]\n\t"
    "buffer_load_dwordx4 v67, s[40:43], s44 offen offset:3072 lds\n\t"
    "v_mfma_f32_16x16x32_f16 v[236:239], v[12:15], v[28:31], v[236:239]\n\t"
    "ds_read_b64_tr_b16 a[18:19], v77 offset:6144\n\t"
    "ds_read_b64_tr_b16 a[44:45], v78 offset:8\n\t"
    "s_and_b32 s25, s25, 3\n\t"
    "v_mfma_f32_16x16x32_f16 v[240:243], v[12:15], v[36:39], v[240:243]\n\t"
    "ds_read_b64 v[2:3], v69 offset:136\n\t"
    "s_add_u32 s44, s44, 0x8000\n\t"
    "s_add_u32 m0, s45, 0x9000\n\t"
    "v_mfma_f32_16x16x32_f16 v[244:247], v[12:15], v[24:27], v[244:247]\n\t"
    "ds_read_b64_tr_b16 a[32:33], v77 offset:0\n\t"
    "s_lshl_b32 s25, s25, 17\n\t"
    "v_mfma_f32_16x16x32_f16 v[248:251], v[12:15], v[40:43], v[248:251]\n\t"
    "buffer_load_dwordx4 v64, s[40:43], s44 offen offset:0 lds\n\t"
    "v_mfma_f32_16x16x32_f16 v[252:255], v[12:15], v[20:23], v[252:255]\n\t"
    "ds_read_b64 v[60:61], v69 offset:3648\n\t"
    "ds_read_b64 v[0:1], v69 offset:128\n\t"
    "ds_read_b64_tr_b16 a[46:47], v78 offset:2056\n\t"
    "v_mfma_f32_16x16x32_f16 v[236:239], v[52:55], v[32:35], v[236:239]\n\t"
    "ds_read_b64 v[6:7], v69 offset:3144\n\t"
    "ds_read_b128 v[44:47], v70 offset:3200\n\t"
    "ds_read_b64_tr_b16 a[34:35], v77 offset:2048\n\t"
    "v_mfma_f32_16x16x32_f16 v[240:243], v[52:55], v[28:31], v[240:243]\n\t"
    "ds_read_b64_tr_b16 a[40:41], v78 offset:0\n\t"
    "v_add_u32 v82, s25, v76\n\t"
    "v_mfma_f32_16x16x32_f16 v[244:247], v[52:55], v[36:39], v[244:247]\n\t"
    "buffer_load_dwordx4 v65, s[40:43], s44 offen offset:1024 lds\n\t"
    "v_mfma_f32_16x16x32_f16 v[248:251], v[52:55], v[24:27], v[248:251]\n\t"
    "ds_read_b128 v[16:19], v70 offset:3712\n\t"
    "ds_read_b128 v[20:23], v70 offset:1664\n\t"
    "s_add_u32 s25, s58, 1\n\t"
    "v_mfma_f32_16x16x32_f16 v[252:255], v[52:55], v[40:43], v[252:255]\n\t"
    "buffer_load_dwordx4 v66, s[40:43], s44 offen offset:2048 lds\n\t"
    "s_waitcnt lgkmcnt(10)\n\t"
    "v_mfma_f32_16x16x32_f16 v[240:243], v[8:11], v[32:35], v[240:243]\n\t"
    "ds_read_b128 v[12:15], v70 offset:2176\n\t"
    "ds_read_b128 v[40:43], v70 offset:1152\n\t"
    "s_waitcnt vmcnt(12)\n\t"
    "v_mfma_f32_16x16x32_f16 v[244:247], v[8:11], v[28:31], v[244:247]\n\t"
    "buffer_load_dwordx4 v67, s[40:43], s44 offen offset:3072 lds\n\t"
    "v_mfma_f32_16x16x32_f16 v[248:251], v[8:11], v[36:39], v[248:251]\n\t"
    "ds_read_b64_tr_b16 a[62:63], v80 offset:34824\n\t"
    "s_and_b32 s25, s25, 3\n\t"
    "s_add_u32 s44, s44, 0x8000\n\t"
    "v_mfma_f32_16x16x32_f16 v[252:255], v[8:11], v[24:27], v[252:255]\n\t"
    "ds_read_b128 v[52:55], v70 offset:640\n\t"
    "s_lshl_b32 s25, s25, 17\n\t"
    "s_add_u32 m0, s45, 0x1000\n\t"
    "v_mfma_f32_16x16x32_f16 v[244:247], v[56:59], v[32:35], v[244:247]\n\t"
    "ds_read_b64 v[26:27], v69 offset:648\n\t"
    "ds_read_b64_tr_b16 a[60:61], v80 offset:32776\n\t"
    "v_mfma_f32_16x16x32_f16 v[248:251], v[56:59], v[28:31], v[248:251]\n\t"
    "buffer_load_dwordx4 v64, s[40:43], s44 offen offset:0 lds\n\t"
    "v_mfma_f32_16x16x32_f16 v[252:255], v[56:59], v[36:39], v[252:255]\n\t"
    "ds_read_b64_tr_b16 a[56:57], v80 offset:32768\n\t"
    "ds_read_b64_tr_b16 a[58:59], v80 offset:34816\n\t"
    "s_waitcnt lgkmcnt(8)\n\t"
    "v_mfma_f32_16x16x32_f16 v[248:251], v[4:7], v[32:35], v[248:251]\n\t"
    "ds_read_b128 v[8:11], v70 offset:128\n\t"
    "ds_read_b64 v[24:25], v69 offset:640\n\t"
    "v_mfma_f32_16x16x32_f16 v[252:255], v[4:7], v[28:31], v[252:255]\n\t"
    "ds_read_b64_tr_b16 a[48:49], v79 offset:32768\n\t"
    "ds_read_b64_tr_b16 a[52:53], v79 offset:32776\n\t"
    "v_mfma_f32_16x16x32_f16 v[252:255], v[60:63], v[32:35], v[252:255]\n\t"
    "buffer_load_dwordx4 v65, s[40:43], s44 offen offset:1024 lds\n\t"
    "v_mfma_f32_16x16x32_f16 v[224:227], v[0:3], v[16:19], v[224:227]\n\t"
    "ds_read_b64_tr_b16 a[50:51], v79 offset:34816\n\t"
    "v_add_u32 v83, s25, v76\n\t"
    "v_mfma_f32_16x16x32_f16 v[228:231], v[0:3], v[44:47], v[228:231]\n\t"
    "ds_read_b64 v[28:29], v69 offset:2688\n\t"
    "s_waitcnt vmcnt(11)\n\t"
    "v_mfma_f32_16x16x32_f16 v[232:235], v[0:3], v[48:51], v[232:235]\n\t"
    "ds_read_b64_tr_b16 a[54:55], v79 offset:34824\n\t"
    "s_add_u32 s25, s58, 2\n\t"
    "s_waitcnt lgkmcnt(11)\n\t"
    "v_mfma_f32_16x16x32_f16 v[236:239], v[0:3], v[12:15], v[236:239]\n\t"
    "buffer_load_dwordx4 v66, s[40:43], s44 offen offset:2048 lds\n\t"
    "v_mfma_f32_16x16x32_f16 v[240:243], v[0:3], v[20:23], v[240:243]\n\t"
    "buffer_load_dwordx4 v67, s[40:43], s44 offen offset:3072 lds\n\t"
    "v_mfma_f32_16x16x32_f16 v[244:247], v[0:3], v[40:43], v[244:247]\n\t"
    "ds_read_b64 v[38:39], v69 offset:1160\n\t"
    "ds_read_b64 v[58:59], v69 offset:1672\n\t"
    "ds_read_b64 v[32:33], v69 offset:3200\n\t"
    "v_mfma_f32_16x16x32_f16 v[248:251], v[0:3], v[52:55], v[248:251]\n\t"
    "ds_read_b64 v[36:37], v69 offset:1152\n\t"
    "s_and_b32 s25, s25, 3\n\t"
    "s_waitcnt lgkmcnt(9)\n\t"
    "v_mfma_f32_16x16x32_f16 v[252:255], v[0:3], v[8:11], v[252:255]\n\t"
    "ds_read_b64 v[56:57], v69 offset:1664\n\t"
    "ds_read_b64_tr_b16 a[68:69], v79 offset:36872\n\t"
    "v_mfma_f32_16x16x32_f16 v[228:231], v[24:27], v[16:19], v[228:231]\n\t"
    "ds_read_b64_tr_b16 a[64:65], v79 offset:36864\n\t"
    "ds_read_b64_tr_b16 a[72:73], v80 offset:36864\n\t"
    "v_mfma_f32_16x16x32_f16 v[232:235], v[24:27], v[44:47], v[232:235]\n\t"
    "ds_read_b64_tr_b16 a[78:79], v80 offset:38920\n\t"
    "s_lshl_b32 s25, s25, 17\n\t"
    "v_mfma_f32_16x16x32_f16 v[236:239], v[24:27], v[48:51], v[236:239]\n\t"
    "ds_read_b64 v[4:5], v69 offset:2176\n\t"
    "ds_read_b64_tr_b16 a[70:71], v79 offset:38920\n\t"
    "v_add_u32 v84, s25, v76\n\t"
    "v_mfma_f32_16x16x32_f16 v[240:243], v[24:27], v[12:15], v[240:243]\n\t"
    "ds_read_b64 v[60:61], v69 offset:3712\n\t"
    "ds_read_b64_tr_b16 a[74:75], v80 offset:38912\n\t"
    "ds_read_b64_tr_b16 a[76:77], v80 offset:36872\n\t"
    "v_mfma_f32_16x16x32_f16 v[244:247], v[24:27], v[20:23], v[244:247]\n\t"
    "ds_read_b128 v[8:11], v70 offset:3776\n\t"
    "s_add_u32 s25, s58, 3\n\t"
    "v_mfma_f32_16x16x32_f16 v[248:251], v[24:27], v[40:43], v[248:251]\n\t"
    "ds_read_b64 v[6:7], v69 offset:2184\n\t"
    "ds_read_b64_tr_b16 a[66:67], v79 offset:38912\n\t"
    "v_mfma_f32_16x16x32_f16 v[252:255], v[24:27], v[52:55], v[252:255]\n\t"
    "s_waitcnt vmcnt(8)\n\t"
    "s_and_b32 s25, s25, 3\n\t"
    "s_waitcnt lgkmcnt(12)\n\t"
    "v_mfma_f32_16x16x32_f16 v[232:235], v[36:39], v[16:19], v[232:235]\n\t"
    "ds_read_b128 v[52:55], v70 offset:2752\n\t"
    "ds_read_b64_tr_b16 a[86:87], v77 offset:34824\n\t"
    "v_mfma_f32_16x16x32_f16 v[236:239], v[36:39], v[44:47], v[236:239]\n\t"
    "s_lshl_b32 s25, s25, 17\n\t"
    "v_add_u32 v85, s25, v76\n\t"
    "v_mfma_f32_16x16x32_f16 v[240:243], v[36:39], v[48:51], v[240:243]\n\t"
    "ds_read_b64_tr_b16 a[84:85], v77 offset:32776\n\t"
    "s_add_u32 s44, s44, 0x8000\n\t"
    "v_mfma_f32_16x16x32_f16 v[244:247], v[36:39], v[12:15], v[244:247]\n\t"
    "ds_read_b64 v[62:63], v69 offset:3720\n\t"
    "ds_read_b64_tr_b16 a[80:81], v77 offset:32768\n\t"
    "v_mfma_f32_16x16x32_f16 v[248:251], v[36:39], v[20:23], v[248:251]\n\t"
    "ds_read_b64_tr_b16 a[92:93], v78 offset:32776\n\t"
    "v_readfirstlane_b32 s90, v88\n\t"
    "v_mfma_f32_16x16x32_f16 v[252:255], v[36:39], v[40:43], v[252:255]\n\t"
    "ds_read_b64_tr_b16 a[88:89], v78 offset:32768\n\t"
    "s_cmp_ge_u32 s90, 16\n\t"
    "s_cselect_b32 s38, 0xe8000, 0\n\t"
    "v_mfma_f32_16x16x32_f16 v[236:239], v[56:59], v[16:19], v[236:239]\n\t"
    "ds_read_b64 v[30:31], v69 offset:2696\n\t"
    "ds_read_b64_tr_b16 a[82:83], v77 offset:34816\n\t"
    "v_mfma_f32_16x16x32_f16 v[240:243], v[56:59], v[44:47], v[240:243]\n\t"
    "buffer_load_dwordx4 v[96:99], v83, s[36:39], s52 offen offset:0 sc1\n\t"
    "v_mfma_f32_16x16x32_f16 v[244:247], v[56:59], v[48:51], v[244:247]\n\t"
    "buffer_load_dwordx4 v[100:103], v83, s[36:39], s52 offen offset:1024 sc1\n\t"
    "v_mfma_f32_16x16x32_f16 v[248:251], v[56:59], v[12:15], v[248:251]\n\t"
    "ds_read_b64 v[34:35], v69 offset:3208\n\t"
    "ds_read_b64 v[0:1], v69 offset:192\n\t"
    "ds_read_b64_tr_b16 a[90:91], v78 offset:34816\n\t"
    "v_mfma_f32_16x16x32_f16 v[252:255], v[56:59], v[20:23], v[252:255]\n\t"
    "ds_read_b64 v[2:3], v69 offset:200\n\t"
    "ds_read_b64_tr_b16 a[94:95], v78 offset:34824\n\t"
    "s_waitcnt lgkmcnt(11)\n\t"
    "v_mfma_f32_16x16x32_f16 v[240:243], v[4:7], v[16:19], v[240:243]\n\t"
    "buffer_load_dwordx4 v[104:107], v83, s[36:39], s52 offen offset:2048 sc1\n\t"
    "v_mfma_f32_16x16x32_f16 v[244:247], v[4:7], v[44:47], v[244:247]\n\t"
    "ds_read_b128 v[24:27], v70 offset:3264\n\t"
    "ds_read_b128 v[36:39], v70 offset:2240\n\t"
    "s_waitcnt vmcnt(7)\n\t"
    "v_mfma_f32_16x16x32_f16 v[248:251], v[4:7], v[48:51], v[248:251]\n\t"
    "buffer_load_dwordx4 v[108:111], v83, s[36:39], s52 offen offset:3072 sc1\n\t"
    "v_mfma_f32_16x16x32_f16 v[252:255], v[4:7], v[12:15], v[252:255]\n\t"
    "ds_read_b128 v[56:59], v70 offset:704\n\t"
    "ds_read_b128 v[4:7], v70 offset:192\n\t"
    "ds_read_b64_tr_b16 a[102:103], v77 offset:38920\n\t"
    "s_waitcnt lgkmcnt(11)\n\t"
    "v_mfma_f32_16x16x32_f16 v[244:247], v[28:31], v[16:19], v[244:247]\n\t"
    "ds_read_b128 v[40:43], v70 offset:1728\n\t"
    "ds_read_b64_tr_b16 a[100:101], v77 offset:36872\n\t"
    "v_mfma_f32_16x16x32_f16 v[248:251], v[28:31], v[44:47], v[248:251]\n\t"
    "ds_read_b128 v[20:23], v70 offset:1216\n\t"
    "ds_read_b64_tr_b16 a[110:111], v78 offset:38920\n\t"
    "v_mfma_f32_16x16x32_f16 v[252:255], v[28:31], v[48:51], v[252:255]\n\t"
    "buffer_load_dwordx4 v[112:115], v83, s[36:39], s53 offen offset:0 sc1\n\t"
    "s_waitcnt lgkmcnt(12)\n\t"
    "v_mfma_f32_16x16x32_f16 v[248:251], v[32:35], v[16:19], v[248:251]\n\t"
    "ds_read_b64 v[12:13], v69 offset:704\n\t"
    "ds_read_b64 v[30:31], v69 offset:1224\n\t"
    "v_mfma_f32_16x16x32_f16 v[252:255], v[32:35], v[44:47], v[252:255]\n\t"
    "buffer_load_dwordx4 v[116:119], v83, s[36:39], s53 offen offset:1024 sc1\n\t"
    "v_mfma_f32_16x16x32_f16 v[252:255], v[60:63], v[16:19], v[252:255]\n\t"
    "ds_read_b64 v[14:15], v69 offset:712\n\t"
    "ds_read_b64 v[28:29], v69 offset:1216\n\t"
    "ds_read_b64_tr_b16 a[96:97], v77 offset:36864\n\t"
    "s_waitcnt lgkmcnt(12)\n\t"
    "v_mfma_f32_16x16x32_f16 v[224:227], v[0:3], v[8:11], v[224:227]\n\t"
    "buffer_load_dwordx4 v[120:123], v83, s[36:39], s53 offen offset:2048 sc1\n\t"
    "v_mfma_f32_16x16x32_f16 v[228:231], v[0:3], v[24:27], v[228:231]\n\t"
    "ds_read_b64_tr_b16 a[104:105], v78 offset:36864\n\t"
    "ds_read_b64_tr_b16 a[108:109], v78 offset:36872\n\t"
    "v_mfma_f32_16x16x32_f16 v[232:235], v[0:3], v[52:55], v[232:235]\n\t"
    "ds_read_b64 v[48:49], v69 offset:1728\n\t"
    "ds_read_b64_tr_b16 a[98:99], v77 offset:38912\n\t"
    "ds_read_b64_tr_b16 a[106:107], v78 offset:38912\n\t"
    "v_mfma_f32_16x16x32_f16 v[236:239], v[0:3], v[36:39], v[236:239]\n\t"
    "buffer_load_dwordx4 v[124:127], v83, s[36:39], s53 offen offset:3072 sc1\n\t"
    "s_waitcnt lgkmcnt(12)\n\t"
    "v_mfma_f32_16x16x32_f16 v[240:243], v[0:3], v[40:43], v[240:243]\n\t"
    "ds_read_b64 v[32:33], v69 offset:2240\n\t"
    "s_waitcnt vmcnt(8)\n\t"
    "s_waitcnt lgkmcnt(11)\n\t"
    "v_mfma_f32_16x16x32_f16 v[244:247], v[0:3], v[20:23], v[244:247]\n\t"
    "ds_read_b64 v[50:51], v69 offset:1736\n\t"
    "ds_read_b64_tr_b16 a[120:121], v78 offset:4096\n\t"
    "v_mfma_f32_16x16x32_f16 v[248:251], v[0:3], v[56:59], v[248:251]\n\t"
    "buffer_load_dwordx4 v[128:131], v83, s[36:39], s54 offen offset:0 sc1\n\t"
    "v_mfma_f32_16x16x32_f16 v[252:255], v[0:3], v[4:7], v[252:255]\n\t"
    "buffer_load_dwordx4 v[132:135], v83, s[36:39], s54 offen offset:1024 sc1\n\t"
    "s_waitcnt lgkmcnt(10)\n\t"
    "v_mfma_f32_16x16x32_f16 v[228:231], v[12:15], v[8:11], v[228:231]\n\t"
    "ds_read_b64_tr_b16 a[116:117], v77 offset:4104\n\t"
    "ds_read_b64_tr_b16 a[122:123], v78 offset:6144\n\t"
    "ds_read_b64_tr_b16 a[126:127], v78 offset:6152\n\t"
    "v_mfma_f32_16x16x32_f16 v[232:235], v[12:15], v[24:27], v[232:235]\n\t"
    "s_mov_b32 s44, 0xf0000\n\t"
    "s_waitcnt lgkmcnt(6)\n\t"
    "s_add_u32 m0, s45, 0x9000\n\t"
    "v_mfma_f32_16x16x32_f16 v[236:239], v[12:15], v[52:55], v[236:239]\n\t"
    "ds_read_b64_tr_b16 a[112:113], v77 offset:4096\n\t"
    "ds_read_b64_tr_b16 a[118:119], v77 offset:6152\n\t"
    "ds_read_b64_tr_b16 a[124:125], v78 offset:4104\n\t"
    "v_mfma_f32_16x16x32_f16 v[240:243], v[12:15], v[36:39], v[240:243]\n\t"
    "buffer_load_dwordx4 v[136:139], v83, s[36:39], s54 offen offset:2048 sc1\n\t"
    "v_mfma_f32_16x16x32_f16 v[244:247], v[12:15], v[40:43], v[244:247]\n\t"
    "buffer_load_dwordx4 v[140:143], v83, s[36:39], s54 offen offset:3072 sc1\n\t"
    "v_mfma_f32_16x16x32_f16 v[248:251], v[12:15], v[20:23], v[248:251]\n\t"
    "buffer_load_dwordx4 v64, s[40:43], s44 offen offset:0 lds\n\t"
    "v_mfma_f32_16x16x32_f16 v[252:255], v[12:15], v[56:59], v[252:255]\n\t"
    "ds_read_b64 v[34:35], v69 offset:2248\n\t"
    "ds_read_b64_tr_b16 a[114:115], v77 offset:6144\n\t"
    "v_mfma_f32_16x16x32_f16 v[232:235], v[28:31], v[8:11], v[232:235]\n\t"
    "buffer_load_dwordx4 v65, s[40:43], s44 offen offset:1024 lds\n\t"
    "v_mfma_f32_16x16x32_f16 v[236:239], v[28:31], v[24:27], v[236:239]\n\t"
    "buffer_load_dwordx4 v[144:147], v83, s[36:39], s55 offen offset:0 sc1\n\t"
    "v_mfma_f32_16x16x32_f16 v[240:243], v[28:31], v[52:55], v[240:243]\n\t"
    "buffer_load_dwordx4 v[148:151], v83, s[36:39], s55 offen offset:1024 sc1\n\t"
    "v_mfma_f32_16x16x32_f16 v[244:247], v[28:31], v[36:39], v[244:247]\n\t"
    "buffer_load_dwordx4 v66, s[40:43], s44 offen offset:2048 lds\n\t"
    "v_mfma_f32_16x16x32_f16 v[248:251], v[28:31], v[40:43], v[248:251]\n\t"
    "ds_read_b64 v[44:45], v69 offset:2752\n\t"
    "ds_read_b64 v[46:47], v69 offset:2760\n\t"
    "v_mfma_f32_16x16x32_f16 v[252:255], v[28:31], v[20:23], v[252:255]\n\t"
    "buffer_load_dwordx4 v[152:155], v83, s[36:39], s55 offen offset:2048 sc1\n\t"
    "s_waitcnt lgkmcnt(6)\n\t"
    "v_mfma_f32_16x16x32_f16 v[236:239], v[48:51], v[8:11], v[236:239]\n\t"
    "buffer_load_dwordx4 v67, s[40:43], s44 offen offset:3072 lds\n\t"
    "v_mfma_f32_16x16x32_f16 v[240:243], v[48:51], v[24:27], v[240:243]\n\t"
    "buffer_load_dwordx4 v[156:159], v83, s[36:39], s55 offen offset:3072 sc1\n\t"
    "v_mfma_f32_16x16x32_f16 v[244:247], v[48:51], v[52:55], v[244:247]\n\t"
    "ds_read_b64 v[16:17], v69 offset:3264\n\t"
    "v_mfma_f32_16x16x32_f16 v[248:251], v[48:51], v[36:39], v[248:251]\n\t"
    "ds_read_b64 v[18:19], v69 offset:3272\n\t"
    "s_add_u32 s44, s44, 0x8000\n\t"
    "v_mfma_f32_16x16x32_f16 v[252:255], v[48:51], v[40:43], v[252:255]\n\t"
    "buffer_load_dwordx4 v[160:163], v84, s[36:39], s52 offen offset:0 sc1\n\t"
    "s_waitcnt lgkmcnt(4)\n\t"
    "v_mfma_f32_16x16x32_f16 v[240:243], v[32:35], v[8:11], v[240:243]\n\t"
    "ds_read_b64 v[60:61], v69 offset:3776\n\t"
    "ds_read_b64 v[62:63], v69 offset:3784\n\t"
    "v_mfma_f32_16x16x32_f16 v[244:247], v[32:35], v[24:27], v[244:247]\n\t"
    "ds_write_b128 v71, v[224:227] offset:0\n\t"
    "v_mfma_f32_16x16x32_f16 v[248:251], v[32:35], v[52:55], v[248:251]\n\t"
    "buffer_load_dwordx4 v[164:167], v84, s[36:39], s52 offen offset:1024 sc1\n\t"
    "v_mfma_f32_16x16x32_f16 v[252:255], v[32:35], v[36:39], v[252:255]\n\t"
    "ds_write_b128 v71, v[232:235] offset:2048\n\t"
    "s_waitcnt lgkmcnt(6)\n\t"
    "v_mfma_f32_16x16x32_f16 v[244:247], v[44:47], v[8:11], v[244:247]\n\t"
    "ds_write_b128 v71, v[240:243] offset:4096\n\t"
    "v_mfma_f32_16x16x32_f16 v[248:251], v[44:47], v[24:27], v[248:251]\n\t"
    "buffer_load_dwordx4 v[168:171], v84, s[36:39], s52 offen offset:2048 sc1\n\t"
    "v_mfma_f32_16x16x32_f16 v[252:255], v[44:47], v[52:55], v[252:255]\n\t"
    "ds_write_b128 v71, v[228:231] offset:1024\n\t"
    "Lis15_%=:\n\t"
    "s_cbranch_execz Lis99_%=\n\t"
    "s_waitcnt lgkmcnt(6)\n\t"
    "v_mfma_f32_16x16x32_f16 v[248:251], v[16:19], v[8:11], v[248:251]\n\t"
    "buffer_load_dwordx4 v[172:175], v84, s[36:39], s52 offen offset:3072 sc1\n\t"
    "v_mfma_f32_16x16x32_f16 v[252:255], v[16:19], v[24:27], v[252:255]\n\t"
    "ds_write_b128 v71, v[236:239] offset:3072\n\t"
    "s_waitcnt lgkmcnt(5)\n\t"
    "v_mfma_f32_16x16x32_f16 v[252:255], v[60:63], v[8:11], v[252:255]\n\t"
    "ds_write_b128 v71, v[244:247] offset:5120\n\t"
    "s_nop 1\n\t"
    "ds_write_b128 v71, v[248:251] offset:6144\n\t"
    "s_nop 3\n\t"
    "ds_write_b128 v71, v[252:255] offset:7168\n\t"
    "s_waitcnt lgkmcnt(0)\n\t"
    "s_barrier\n\t"
    "ds_read_b128 v[224:227], v72 offset:0\n\t"
    "ds_read_b128 v[240:243], v72 offset:8192\n\t"
    "ds_read_b128 v[228:231], v72 offset:16384\n\t"
    "ds_read_b128 v[244:247], v72 offset:24576\n\t"
    "ds_read_b128 v[232:235], v72 offset:1024\n\t"
    "ds_read_b128 v[248:251], v72 offset:9216\n\t"
    "ds_read_b128 v[236:239], v72 offset:17408\n\t"
    "ds_read_b128 v[252:255], v72 offset:25600\n\t"
    "s_cmp_eq_u32 s38, 0\n\t"
    "s_cbranch_scc1 L_pC0_%=\n\t"
    "L_rC0_%=:\n\t"
    "s_waitcnt vmcnt(18)\n\t"
    "v_mfma_f32_16x16x32_bf16 v[0:3], a[192:195], v[96:99], 0\n\t"
    "v_mfma_f32_16x16x32_bf16 v[0:3], a[240:243], v[100:103], v[0:3]\n\t"
    "buffer_load_dwordx4 v[176:179], v84, s[36:39], s53 offen offset:0 sc1\n\t"
    "v_mfma_f32_16x16x32_bf16 v[4:7], a[244:247], v[100:103], 0\n\t"
    "v_add_u32 v75, 0x10000, v76\n\t"
    "v_add_u32 v68, 0x20000, v76\n\t"
    "v_mfma_f32_16x16x32_bf16 v[4:7], a[196:199], v[96:99], v[4:7]\n\t"
    "ds_read_b128 v[48:51], v75 offset:21248\n\t"
    "ds_read_b128 v[52:55], v68 offset:21248\n\t"
    "s_lshl_b32 s25, s58, 10\n\t"
    "v_mfma_f32_16x16x32_bf16 v[8:11], a[200:203], v[96:99], 0\n\t"
    "buffer_load_dwordx4 v[180:183], v84, s[36:39], s53 offen offset:1024 sc1\n\t"
    "v_mfma_f32_16x16x32_bf16 v[8:11], a[248:251], v[100:103], v[8:11]\n\t"
    "s_add_u32 s25, s25, 0x10000\n\t"
    "v_lshl_add_u32 v75, v73, 5, s25\n\t"
    "v_lshl_add_u32 v75, v74, 3, v75\n\t"
    "v_mfma_f32_16x16x32_bf16 v[12:15], a[252:255], v[100:103], 0\n\t"
    "v_add_u32 v68, 0x10000, v75\n\t"
    "s_waitcnt lgkmcnt(5)\n\t"
    "v_add_f32 v224, v224, v240\n\t"
    "v_add_f32 v225, v225, v241\n\t"
    "v_mfma_f32_16x16x32_bf16 v[12:15], a[204:207], v[96:99], v[12:15]\n\t"
    "v_add_f32 v226, v226, v242\n\t"
    "v_add_f32 v227, v227, v243\n\t"
    "s_waitcnt vmcnt(18)\n\t"
    "v_mfma_f32_16x16x32_bf16 v[0:3], a[160:163], v[104:107], v[0:3]\n\t"
    "buffer_load_dwordx4 v[184:187], v84, s[36:39], s53 offen offset:2048 sc1\n\t"
    "v_mfma_f32_16x16x32_bf16 v[0:3], a[176:179], v[108:111], v[0:3]\n\t"
    "ds_read_b64 v[242:243], v75 offset:32\n\t"
    "v_add_f32 v228, v228, v244\n\t"
    "v_add_f32 v229, v229, v245\n\t"
    "v_mfma_f32_16x16x32_bf16 v[4:7], a[180:183], v[108:111], v[4:7]\n\t"
    "buffer_load_dwordx4 v[188:191], v84, s[36:39], s53 offen offset:3072 sc1\n\t"
    "v_mfma_f32_16x16x32_bf16 v[4:7], a[164:167], v[104:107], v[4:7]\n\t"
    "v_add_f32 v230, v230, v246\n\t"
    "v_add_f32 v231, v231, v247\n\t"
    "v_mfma_f32_16x16x32_bf16 v[8:11], a[168:171], v[104:107], v[8:11]\n\t"
    "ds_read_b64 v[246:247], v68 offset:32\n\t"
    "s_waitcnt lgkmcnt(2)\n\t"
    "v_add_f32 v232, v232, v248\n\t"
    "v_add_f32 v233, v233, v249\n\t"
    "v_mfma_f32_16x16x32_bf16 v[8:11], a[184:187], v[108:111], v[8:11]\n\t"
    "v_xor_b32 v87, 0x80, v87\n\t"
    "v_add_f32 v234, v234, v250\n\t"
    "v_add_f32 v235, v235, v251\n\t"
    "v_mfma_f32_16x16x32_bf16 v[12:15], a[188:191], v[108:111], v[12:15]\n\t"
    "buffer_load_dwordx4 v[192:195], v84, s[36:39], s54 offen offset:0 sc1\n\t"
    "v_mfma_f32_16x16x32_bf16 v[12:15], a[172:175], v[104:107], v[12:15]\n\t"
    "ds_read_b64 v[250:251], v75 offset:544\n\t"
    "v_add_f32 v236, v236, v252\n\t"
    "s_waitcnt vmcnt(19)\n\t"
    "v_mfma_f32_16x16x32_bf16 v[0:3], a[208:211], v[112:115], v[0:3]\n\t"
    "buffer_load_dwordx4 v[196:199], v84, s[36:39], s54 offen offset:1024 sc1\n\t"
    "v_mfma_f32_16x16x32_bf16 v[0:3], a[224:227], v[116:119], v[0:3]\n\t"
    "v_add_f32 v237, v237, v253\n\t"
    "v_add_f32 v238, v238, v254\n\t"
    "v_add_f32 v239, v239, v255\n\t"
    "v_mfma_f32_16x16x32_bf16 v[4:7], a[228:231], v[116:119], v[4:7]\n\t"
    "ds_read_b64 v[254:255], v68 offset:544\n\t"
    "s_lshl2_add_u32 s25, s99, s58\n\t"
    "s_lshl_b32 s25, s25, 11\n\t"
    "v_mfma_f32_16x16x32_bf16 v[4:7], a[212:215], v[112:115], v[4:7]\n\t"
    "s_add_u32 s25, s25, 0x400000\n\t"
    "v_lshl_add_u32 v72, v73, 6, s25\n\t"
    "v_lshl_add_u32 v72, v74, 4, v72\n\t"
    "v_mfma_f32_16x16x32_bf16 v[8:11], a[216:219], v[112:115], v[8:11]\n\t"
    "buffer_load_dwordx4 v[200:203], v84, s[36:39], s54 offen offset:2048 sc1\n\t"
    "v_mfma_f32_16x16x32_bf16 v[8:11], a[232:235], v[116:119], v[8:11]\n\t"
    "v_mul_f32 v224, v224, v50\n\t"
    "v_mul_f32 v225, v225, v50\n\t"
    "v_mfma_f32_16x16x32_bf16 v[12:15], a[236:239], v[116:119], v[12:15]\n\t"
    "buffer_load_dwordx4 v[204:207], v84, s[36:39], s54 offen offset:3072 sc1\n\t"
    "v_mfma_f32_16x16x32_bf16 v[12:15], a[220:223], v[112:115], v[12:15]\n\t"
    "v_mul_f32 v224, v48, v224\n\t"
    "v_mul_f32 v225, v48, v225\n\t"
    "s_waitcnt vmcnt(20)\n\t"
    "v_mfma_f32_16x16x32_bf16 v[0:3], a[128:131], v[120:123], v[0:3]\n\t"
    "v_cvt_pk_bf16_f32 v225, v224, v225\n\t"
    "v_lshlrev_b32 v224, 16, v225\n\t"
    "v_and_b32 v225, 0xffff0000, v225\n\t"
    "v_mfma_f32_16x16x32_bf16 v[0:3], a[144:147], v[124:127], v[0:3]\n\t"
    "v_mul_f32 v226, v226, v50\n\t"
    "v_mul_f32 v227, v227, v50\n\t"
    "v_mul_f32 v226, v48, v226\n\t"
    "v_mfma_f32_16x16x32_bf16 v[4:7], a[148:151], v[124:127], v[4:7]\n\t"
    "v_mul_f32 v227, v48, v227\n\t"
    "v_cvt_pk_bf16_f32 v227, v226, v227\n\t"
    "v_lshlrev_b32 v226, 16, v227\n\t"
    "v_mfma_f32_16x16x32_bf16 v[4:7], a[132:135], v[120:123], v[4:7]\n\t"
    "buffer_load_dwordx4 v[208:211], v84, s[36:39], s55 offen offset:0 sc1\n\t"
    "v_mfma_f32_16x16x32_bf16 v[8:11], a[136:139], v[120:123], v[8:11]\n\t"
    "v_and_b32 v227, 0xffff0000, v227\n\t"
    "v_mul_f32 v228, v228, v54\n\t"
    "v_mul_f32 v229, v229, v54\n\t"
    "v_mfma_f32_16x16x32_bf16 v[8:11], a[152:155], v[124:127], v[8:11]\n\t"
    "v_mul_f32 v228, v52, v228\n\t"
    "v_mul_f32 v229, v52, v229\n\t"
    "v_mfma_f32_16x16x32_bf16 v[12:15], a[156:159], v[124:127], v[12:15]\n\t"
    "buffer_load_dwordx4 v[212:215], v84, s[36:39], s55 offen offset:1024 sc1\n\t"
    "v_mfma_f32_16x16x32_bf16 v[12:15], a[140:143], v[120:123], v[12:15]\n\t"
    "v_cvt_pk_bf16_f32 v229, v228, v229\n\t"
    "v_lshlrev_b32 v228, 16, v229\n\t"
    "s_waitcnt vmcnt(20)\n\t"
    "v_mfma_f32_16x16x32_bf16 v[0:3], a[0:3], v[128:131], v[0:3]\n\t"
    "buffer_load_dwordx4 v[216:219], v84, s[36:39], s55 offen offset:2048 sc1\n\t"
    "v_mfma_f32_16x16x32_bf16 v[0:3], a[16:19], v[132:135], v[0:3]\n\t"
    "v_and_b32 v229, 0xffff0000, v229\n\t"
    "v_mul_f32 v230, v230, v54\n\t"
    "v_mul_f32 v231, v231, v54\n\t"
    "v_mfma_f32_16x16x32_bf16 v[4:7], a[20:23], v[132:135], v[4:7]\n\t"
    "buffer_load_dwordx4 v[220:223], v84, s[36:39], s55 offen offset:3072 sc1\n\t"
    "v_mfma_f32_16x16x32_bf16 v[4:7], a[4:7], v[128:131], v[4:7]\n\t"
    "v_mul_f32 v230, v52, v230\n\t"
    "v_mul_f32 v231, v52, v231\n\t"
    "v_cvt_pk_bf16_f32 v231, v230, v231\n\t"
    "v_mfma_f32_16x16x32_bf16 v[8:11], a[8:11], v[128:131], v[8:11]\n\t"
    "v_lshlrev_b32 v230, 16, v231\n\t"
    "v_and_b32 v231, 0xffff0000, v231\n\t"
    "v_mul_f32 v232, v232, v50\n\t"
    "v_mfma_f32_16x16x32_bf16 v[8:11], a[24:27], v[132:135], v[8:11]\n\t"
    "buffer_load_dwordx4 v[96:99], v85, s[36:39], s52 offen offset:0 sc1\n\t"
    "v_mfma_f32_16x16x32_bf16 v[12:15], a[28:31], v[132:135], v[12:15]\n\t"
    "v_mul_f32 v233, v233, v50\n\t"
    "v_mul_f32 v232, v48, v232\n\t"
    "v_mul_f32 v233, v48, v233\n\t"
    "v_mfma_f32_16x16x32_bf16 v[12:15], a[12:15], v[128:131], v[12:15]\n\t"
    "v_cvt_pk_bf16_f32 v233, v232, v233\n\t"
    "v_lshlrev_b32 v232, 16, v233\n\t"
    "s_waitcnt vmcnt(21)\n\t"
    "v_mfma_f32_16x16x32_bf16 v[0:3], a[32:35], v[136:139], v[0:3]\n\t"
    "v_add_u32 v92, s45, v76\n\t"
    "v_and_b32 v233, 0xffff0000, v233\n\t"
    "v_mul_f32 v234, v234, v50\n\t"
    "v_mfma_f32_16x16x32_bf16 v[0:3], a[48:51], v[140:143], v[0:3]\n\t"
    "v_mul_f32 v235, v235, v50\n\t"
    "v_mul_f32 v234, v48, v234\n\t"
    "v_mul_f32 v235, v48, v235\n\t"
    "v_mfma_f32_16x16x32_bf16 v[4:7], a[52:55], v[140:143], v[4:7]\n\t"
    "buffer_load_dwordx4 v[100:103], v85, s[36:39], s52 offen offset:1024 sc1\n\t"
    "v_mfma_f32_16x16x32_bf16 v[4:7], a[36:39], v[136:139], v[4:7]\n\t"
    "buffer_load_dwordx4 v[104:107], v85, s[36:39], s52 offen offset:2048 sc1\n\t"
    "v_mfma_f32_16x16x32_bf16 v[8:11], a[40:43], v[136:139], v[8:11]\n\t"
    "v_cvt_pk_bf16_f32 v235, v234, v235\n\t"
    "v_lshlrev_b32 v234, 16, v235\n\t"
    "v_and_b32 v235, 0xffff0000, v235\n\t"
    "v_mfma_f32_16x16x32_bf16 v[8:11], a[56:59], v[140:143], v[8:11]\n\t"
    "v_mul_f32 v236, v236, v54\n\t"
    "v_mul_f32 v237, v237, v54\n\t"
    "v_mul_f32 v236, v52, v236\n\t"
    "v_mfma_f32_16x16x32_bf16 v[12:15], a[60:63], v[140:143], v[12:15]\n\t"
    "v_xor_b32 v93, 0x4000, v92\n\t"
    "v_mul_f32 v237, v52, v237\n\t"
    "v_cvt_pk_bf16_f32 v237, v236, v237\n\t"
    "v_mfma_f32_16x16x32_bf16 v[12:15], a[44:47], v[136:139], v[12:15]\n\t"
    "buffer_load_dwordx4 v[108:111], v85, s[36:39], s52 offen offset:3072 sc1\n\t"
    "s_waitcnt vmcnt(22)\n\t"
    "v_mfma_f32_16x16x32_bf16 v[0:3], a[64:67], v[144:147], v[0:3]\n\t"
    "v_lshlrev_b32 v236, 16, v237\n\t"
    "v_and_b32 v237, 0xffff0000, v237\n\t"
    "v_mul_f32 v238, v238, v54\n\t"
    "v_mfma_f32_16x16x32_bf16 v[0:3], a[80:83], v[148:151], v[0:3]\n\t"
    "v_mul_f32 v239, v239, v54\n\t"
    "v_mul_f32 v238, v52, v238\n\t"
    "v_mul_f32 v239, v52, v239\n\t"
    "v_mfma_f32_16x16x32_bf16 v[4:7], a[84:87], v[148:151], v[4:7]\n\t"
    "v_cvt_pk_bf16_f32 v239, v238, v239\n\t"
    "v_lshlrev_b32 v238, 16, v239\n\t"
    "v_and_b32 v239, 0xffff0000, v239\n\t"
    "s_waitcnt lgkmcnt(0)\n\t"
    "v_fma_mix_f32 v240, v242, v51, 0 op_sel_hi:[1,0,0]\n\t"
    "v_mfma_f32_16x16x32_bf16 v[4:7], a[68:71], v[144:147], v[4:7]\n\t"
    "buffer_load_dwordx4 v[112:115], v85, s[36:39], s53 offen offset:0 sc1\n\t"
    "v_mfma_f32_16x16x32_bf16 v[8:11], a[72:75], v[144:147], v[8:11]\n\t"
    "buffer_load_dwordx4 v[116:119], v85, s[36:39], s53 offen offset:1024 sc1\n\t"
    "v_mfma_f32_16x16x32_bf16 v[8:11], a[88:91], v[148:151], v[8:11]\n\t"
    "v_fma_mix_f32 v241, v242, v51, 0 op_sel:[1,0,0] op_sel_hi:[1,0,0]\n\t"
    "v_mul_f32 v240, v240, v49\n\t"
    "v_mul_f32 v241, v241, v49\n\t"
    "v_mfma_f32_16x16x32_bf16 v[12:15], a[92:95], v[148:151], v[12:15]\n\t"
    "v_add_u32 v94, 0x2000, v92\n\t"
    "v_cvt_pk_bf16_f32 v241, v240, v241\n\t"
    "v_lshlrev_b32 v240, 16, v241\n\t"
    "v_mfma_f32_16x16x32_bf16 v[12:15], a[76:79], v[144:147], v[12:15]\n\t"
    "v_and_b32 v241, 0xffff0000, v241\n\t"
    "v_add_f32 v224, v224, v240\n\t"
    "s_waitcnt vmcnt(22)\n\t"
    "v_mfma_f32_16x16x32_bf16 v[0:3], a[96:99], v[152:155], v[0:3]\n\t"
    "buffer_load_dwordx4 v[120:123], v85, s[36:39], s53 offen offset:2048 sc1\n\t"
    "v_mfma_f32_16x16x32_bf16 v[0:3], a[112:115], v[156:159], v[0:3]\n\t"
    "v_add_f32 v225, v225, v241\n\t"
    "v_fma_mix_f32 v244, v246, v55, 0 op_sel_hi:[1,0,0]\n\t"
    "v_fma_mix_f32 v245, v246, v55, 0 op_sel:[1,0,0] op_sel_hi:[1,0,0]\n\t"
    "v_mfma_f32_16x16x32_bf16 v[4:7], a[116:119], v[156:159], v[4:7]\n\t"
    "buffer_load_dwordx4 v[124:127], v85, s[36:39], s53 offen offset:3072 sc1\n\t"
    "v_mfma_f32_16x16x32_bf16 v[4:7], a[100:103], v[152:155], v[4:7]\n\t"
    "v_mul_f32 v244, v244, v53\n\t"
    "v_mul_f32 v245, v245, v53\n\t"
    "v_cvt_pk_bf16_f32 v245, v244, v245\n\t"
    "v_mfma_f32_16x16x32_bf16 v[8:11], a[104:107], v[152:155], v[8:11]\n\t"
    "v_lshlrev_b32 v244, 16, v245\n\t"
    "v_and_b32 v245, 0xffff0000, v245\n\t"
    "v_add_f32 v228, v228, v244\n\t"
    "v_mfma_f32_16x16x32_bf16 v[8:11], a[120:123], v[156:159], v[8:11]\n\t"
    "v_add_f32 v229, v229, v245\n\t"
    "v_cvt_pk_bf16_f32 v224, v224, v228\n\t"
    "v_cvt_pk_bf16_f32 v225, v225, v229\n\t"
    "v_mfma_f32_16x16x32_bf16 v[12:15], a[124:127], v[156:159], v[12:15]\n\t"
    "buffer_load_dwordx4 v[128:131], v85, s[36:39], s54 offen offset:0 sc1\n\t"
    "v_mfma_f32_16x16x32_bf16 v[12:15], a[108:111], v[152:155], v[12:15]\n\t"
    "v_fma_mix_f32 v242, v243, v51, 0 op_sel_hi:[1,0,0]\n\t"
    "v_fma_mix_f32 v243, v243, v51, 0 op_sel:[1,0,0] op_sel_hi:[1,0,0]\n\t"
    "v_mul_f32 v242, v242, v49\n\t"
    "s_waitcnt vmcnt(23)\n\t"
    "v_mfma_f32_16x16x32_bf16 v[16:19], a[192:195], v[160:163], 0\n\t"
    "v_and_b32 v94, 0x7fff, v94\n\t"
    "v_mul_f32 v243, v243, v49\n\t"
    "v_cvt_pk_bf16_f32 v243, v242, v243\n\t"
    "v_mfma_f32_16x16x32_bf16 v[16:19], a[240:243], v[164:167], v[16:19]\n\t"
    "buffer_load_dwordx4 v[132:135], v85, s[36:39], s54 offen offset:1024 sc1\n\t"
    "v_mfma_f32_16x16x32_bf16 v[20:23], a[244:247], v[164:167], 0\n\t"
    "v_lshlrev_b32 v242, 16, v243\n\t"
    "v_and_b32 v243, 0xffff0000, v243\n\t"
    "v_add_f32 v226, v226, v242\n\t"
    "v_add_f32 v227, v227, v243\n\t"
    "v_mfma_f32_16x16x32_bf16 v[20:23], a[196:199], v[160:163], v[20:23]\n\t"
    "ds_write_b128 v92, v[0:3] offset:0\n\t"
    "v_mfma_f32_16x16x32_bf16 v[24:27], a[200:203], v[160:163], 0\n\t"
    "buffer_load_dwordx4 v[136:139], v85, s[36:39], s54 offen offset:2048 sc1\n\t"
    "v_mfma_f32_16x16x32_bf16 v[24:27], a[248:251], v[164:167], v[24:27]\n\t"
    "v_fma_mix_f32 v246, v247, v55, 0 op_sel_hi:[1,0,0]\n\t"
    "v_fma_mix_f32 v247, v247, v55, 0 op_sel:[1,0,0] op_sel_hi:[1,0,0]\n\t"
    "v_mul_f32 v246, v246, v53\n\t"
    "v_mul_f32 v247, v247, v53\n\t"
    "v_mfma_f32_16x16x32_bf16 v[28:31], a[252:255], v[164:167], 0\n\t"
    "buffer_load_dwordx4 v[140:143], v85, s[36:39], s54 offen offset:3072 sc1\n\t"
    "v_mfma_f32_16x16x32_bf16 v[28:31], a[204:207], v[160:163], v[28:31]\n\t"
    "v_cvt_pk_bf16_f32 v247, v246, v247\n\t"
    "v_lshlrev_b32 v246, 16, v247\n\t"
    "v_and_b32 v247, 0xffff0000, v247\n\t"
    "s_waitcnt vmcnt(24)\n\t"
    "v_mfma_f32_16x16x32_bf16 v[16:19], a[160:163], v[168:171], v[16:19]\n\t"
    "ds_write_b128 v92, v[4:7] offset:1024\n\t"
    "v_mfma_f32_16x16x32_bf16 v[16:19], a[176:179], v[172:175], v[16:19]\n\t"
    "buffer_load_dwordx4 v[144:147], v85, s[36:39], s55 offen offset:0 sc1\n\t"
    "v_mfma_f32_16x16x32_bf16 v[20:23], a[180:183], v[172:175], v[20:23]\n\t"
    "v_add_f32 v230, v230, v246\n\t"
    "v_add_f32 v231, v231, v247\n\t"
    "v_cvt_pk_bf16_f32 v226, v226, v230\n\t"
    "v_cvt_pk_bf16_f32 v227, v227, v231\n\t"
    "v_mfma_f32_16x16x32_bf16 v[20:23], a[164:167], v[168:171], v[20:23]\n\t"
    "global_store_dwordx4 v72, v[224:227], s[84:85] offset:0 sc1\n\t"
    "v_mfma_f32_16x16x32_bf16 v[24:27], a[168:171], v[168:171], v[24:27]\n\t"
    "buffer_load_dwordx4 v[148:151], v85, s[36:39], s55 offen offset:1024 sc1\n\t"
    "v_mfma_f32_16x16x32_bf16 v[24:27], a[184:187], v[172:175], v[24:27]\n\t"
    "ds_write_b128 v92, v[8:11] offset:2048\n\t"
    "v_mfma_f32_16x16x32_bf16 v[28:31], a[188:191], v[172:175], v[28:31]\n\t"
    "v_fma_mix_f32 v248, v250, v51, 0 op_sel_hi:[1,0,0]\n\t"
    "v_fma_mix_f32 v249, v250, v51, 0 op_sel:[1,0,0] op_sel_hi:[1,0,0]\n\t"
    "v_mul_f32 v248, v248, v49\n\t"
    "v_mfma_f32_16x16x32_bf16 v[28:31], a[172:175], v[168:171], v[28:31]\n\t"
    "v_mul_f32 v249, v249, v49\n\t"
    "v_cvt_pk_bf16_f32 v249, v248, v249\n\t"
    "s_waitcnt vmcnt(25)\n\t"
    "v_mfma_f32_16x16x32_bf16 v[16:19], a[208:211], v[176:179], v[16:19]\n\t"
    "v_lshlrev_b32 v248, 16, v249\n\t"
    "v_and_b32 v249, 0xffff0000, v249\n\t"
    "v_add_f32 v232, v232, v248\n\t"
    "v_mfma_f32_16x16x32_bf16 v[16:19], a[224:227], v[180:183], v[16:19]\n\t"
    "buffer_load_dwordx4 v[152:155], v85, s[36:39], s55 offen offset:2048 sc1\n\t"
    "v_mfma_f32_16x16x32_bf16 v[20:23], a[228:231], v[180:183], v[20:23]\n\t"
    "v_add_f32 v233, v233, v249\n\t"
    "v_fma_mix_f32 v252, v254, v55, 0 op_sel_hi:[1,0,0]\n\t"
    "v_fma_mix_f32 v253, v254, v55, 0 op_sel:[1,0,0] op_sel_hi:[1,0,0]\n\t"
    "v_mfma_f32_16x16x32_bf16 v[20:23], a[212:215], v[176:179], v[20:23]\n\t"
    "v_mul_f32 v252, v252, v53\n\t"
    "v_mul_f32 v253, v253, v53\n\t"
    "v_cvt_pk_bf16_f32 v253, v252, v253\n\t"
    "v_mfma_f32_16x16x32_bf16 v[24:27], a[216:219], v[176:179], v[24:27]\n\t"
    "buffer_load_dwordx4 v[156:159], v85, s[36:39], s55 offen offset:3072 sc1\n\t"
    "v_mfma_f32_16x16x32_bf16 v[24:27], a[232:235], v[180:183], v[24:27]\n\t"
    "ds_write_b128 v92, v[12:15] offset:3072\n\t"
    "v_mfma_f32_16x16x32_bf16 v[28:31], a[236:239], v[180:183], v[28:31]\n\t"
    "v_lshlrev_b32 v252, 16, v253\n\t"
    "v_and_b32 v253, 0xffff0000, v253\n\t"
    "v_add_f32 v236, v236, v252\n\t"
    "v_mfma_f32_16x16x32_bf16 v[28:31], a[220:223], v[176:179], v[28:31]\n\t"
    "buffer_load_dwordx4 v[160:163], v82, s[36:39], s52 offen offset:0 sc1\n\t"
    "s_waitcnt vmcnt(26)\n\t"
    "v_mfma_f32_16x16x32_bf16 v[16:19], a[128:131], v[184:187], v[16:19]\n\t"
    "v_add_f32 v237, v237, v253\n\t"
    "v_cvt_pk_bf16_f32 v232, v232, v236\n\t"
    "v_cvt_pk_bf16_f32 v233, v233, v237\n\t"
    "v_fma_mix_f32 v250, v251, v51, 0 op_sel_hi:[1,0,0]\n\t"
    "v_mfma_f32_16x16x32_bf16 v[16:19], a[144:147], v[188:191], v[16:19]\n\t"
    "v_fma_mix_f32 v251, v251, v51, 0 op_sel:[1,0,0] op_sel_hi:[1,0,0]\n\t"
    "v_mul_f32 v250, v250, v49\n\t"
    "v_mul_f32 v251, v251, v49\n\t"
    "v_mfma_f32_16x16x32_bf16 v[20:23], a[148:151], v[188:191], v[20:23]\n\t"
    "buffer_load_dwordx4 v[164:167], v82, s[36:39], s52 offen offset:1024 sc1\n\t"
    "v_mfma_f32_16x16x32_bf16 v[20:23], a[132:135], v[184:187], v[20:23]\n\t"
    "v_cvt_pk_bf16_f32 v251, v250, v251\n\t"
    "v_lshlrev_b32 v250, 16, v251\n\t"
    "v_and_b32 v251, 0xffff0000, v251\n\t"
    "v_mfma_f32_16x16x32_bf16 v[24:27], a[136:139], v[184:187], v[24:27]\n\t"
    "v_add_f32 v234, v234, v250\n\t"
    "v_add_f32 v235, v235, v251\n\t"
    "s_waitcnt lgkmcnt(0)\n\t"
    "s_barrier\n\t"
    "v_mfma_f32_16x16x32_bf16 v[24:27], a[152:155], v[188:191], v[24:27]\n\t"
    "buffer_load_dwordx4 v[168:171], v82, s[36:39], s52 offen offset:2048 sc1\n\t"
    "v_mfma_f32_16x16x32_bf16 v[28:31], a[156:159], v[188:191], v[28:31]\n\t"
    "buffer_load_dwordx4 v[172:175], v82, s[36:39], s52 offen offset:3072 sc1\n\t"
    "v_mfma_f32_16x16x32_bf16 v[28:31], a[140:143], v[184:187], v[28:31]\n\t"
    "v_fma_mix_f32 v254, v255, v55, 0 op_sel_hi:[1,0,0]\n\t"
    "v_fma_mix_f32 v255, v255, v55, 0 op_sel:[1,0,0] op_sel_hi:[1,0,0]\n\t"
    "s_waitcnt vmcnt(27)\n\t"
    "v_mfma_f32_16x16x32_bf16 v[16:19], a[0:3], v[192:195], v[16:19]\n\t"
    "v_mul_f32 v254, v254, v53\n\t"
    "v_mul_f32 v255, v255, v53\n\t"
    "v_cvt_pk_bf16_f32 v255, v254, v255\n\t"
    "v_mfma_f32_16x16x32_bf16 v[16:19], a[16:19], v[196:199], v[16:19]\n\t"
    "buffer_load_dwordx4 v[176:179], v82, s[36:39], s53 offen offset:0 sc1\n\t"
    "v_mfma_f32_16x16x32_bf16 v[20:23], a[20:23], v[196:199], v[20:23]\n\t"
    "v_lshlrev_b32 v254, 16, v255\n\t"
    "v_and_b32 v255, 0xffff0000, v255\n\t"
    "v_add_f32 v238, v238, v254\n\t"
    "v_mfma_f32_16x16x32_bf16 v[20:23], a[4:7], v[192:195], v[20:23]\n\t"
    "v_add_f32 v239, v239, v255\n\t"
    "v_cvt_pk_bf16_f32 v234, v234, v238\n\t"
    "v_cvt_pk_bf16_f32 v235, v235, v239\n\t"
    "v_mfma_f32_16x16x32_bf16 v[24:27], a[8:11], v[192:195], v[24:27]\n\t"
    "global_store_dwordx4 v72, v[232:235], s[84:85] offset:1024 sc1\n\t"
    "v_mfma_f32_16x16x32_bf16 v[24:27], a[24:27], v[196:199], v[24:27]\n\t"
    "buffer_load_dwordx4 v[180:183], v82, s[36:39], s53 offen offset:1024 sc1\n\t"
    "v_mfma_f32_16x16x32_bf16 v[28:31], a[28:31], v[196:199], v[28:31]\n\t"
    "v_mfma_f32_16x16x32_bf16 v[28:31], a[12:15], v[192:195], v[28:31]\n\t"
    "s_waitcnt vmcnt(28)\n\t"
    "v_mfma_f32_16x16x32_bf16 v[16:19], a[32:35], v[200:203], v[16:19]\n\t"
    "s_lshl_b32 s56, s60, 7\n\t"
    "v_mfma_f32_16x16x32_bf16 v[16:19], a[48:51], v[204:207], v[16:19]\n\t"
    "buffer_load_dwordx4 v[184:187], v82, s[36:39], s53 offen offset:2048 sc1\n\t"
    "v_mfma_f32_16x16x32_bf16 v[20:23], a[52:55], v[204:207], v[20:23]\n\t"
    "buffer_load_dwordx4 v[188:191], v82, s[36:39], s53 offen offset:3072 sc1\n\t"
    "v_mfma_f32_16x16x32_bf16 v[20:23], a[36:39], v[200:203], v[20:23]\n\t"
    "ds_read_b128 v[12:15], v93 offset:3072\n\t"
    "v_mfma_f32_16x16x32_bf16 v[24:27], a[40:43], v[200:203], v[24:27]\n\t"
    "ds_read_b128 v[0:3], v93 offset:0\n\t"
    "v_mfma_f32_16x16x32_bf16 v[24:27], a[56:59], v[204:207], v[24:27]\n\t"
    "buffer_load_dwordx4 v[48:51], v82, s[36:39], s54 offen offset:0 sc1\n\t"
    "v_mfma_f32_16x16x32_bf16 v[28:31], a[60:63], v[204:207], v[28:31]\n\t"
    "v_mfma_f32_16x16x32_bf16 v[28:31], a[44:47], v[200:203], v[28:31]\n\t"
    "s_waitcnt vmcnt(29)\n\t"
    "v_mfma_f32_16x16x32_bf16 v[16:19], a[64:67], v[208:211], v[16:19]\n\t"
    "buffer_load_dwordx4 v[52:55], v82, s[36:39], s54 offen offset:1024 sc1\n\t"
    "v_mfma_f32_16x16x32_bf16 v[16:19], a[80:83], v[212:215], v[16:19]\n\t"
    "ds_read_b128 v[4:7], v93 offset:1024\n\t"
    "v_mfma_f32_16x16x32_bf16 v[20:23], a[84:87], v[212:215], v[20:23]\n\t"
    "v_mfma_f32_16x16x32_bf16 v[20:23], a[68:71], v[208:211], v[20:23]\n\t"
    "buffer_load_dwordx4 v[56:59], v82, s[36:39], s54 offen offset:2048 sc1\n\t"
    "v_mfma_f32_16x16x32_bf16 v[24:27], a[72:75], v[208:211], v[24:27]\n\t"
    "ds_read_b128 v[8:11], v93 offset:2048\n\t"
    "v_mfma_f32_16x16x32_bf16 v[24:27], a[88:91], v[212:215], v[24:27]\n\t"
    "v_mfma_f32_16x16x32_bf16 v[28:31], a[92:95], v[212:215], v[28:31]\n\t"
    "v_mfma_f32_16x16x32_bf16 v[28:31], a[76:79], v[208:211], v[28:31]\n\t"
    "s_waitcnt vmcnt(29)\n\t"
    "v_mfma_f32_16x16x32_bf16 v[16:19], a[96:99], v[216:219], v[16:19]\n\t"
    "buffer_load_dwordx4 v[60:63], v82, s[36:39], s54 offen offset:3072 sc1\n\t"
    "v_mfma_f32_16x16x32_bf16 v[16:19], a[112:115], v[220:223], v[16:19]\n\t"
    "s_add_u32 s56, s56, 0xa400\n\t"
    "v_mfma_f32_16x16x32_bf16 v[20:23], a[116:119], v[220:223], v[20:23]\n\t"
    "v_mfma_f32_16x16x32_bf16 v[20:23], a[100:103], v[216:219], v[20:23]\n\t"
    "v_mfma_f32_16x16x32_bf16 v[24:27], a[104:107], v[216:219], v[24:27]\n\t"
    "v_mfma_f32_16x16x32_bf16 v[24:27], a[120:123], v[220:223], v[24:27]\n\t"
    "v_mfma_f32_16x16x32_bf16 v[28:31], a[124:127], v[220:223], v[28:31]\n\t"
    "v_mfma_f32_16x16x32_bf16 v[28:31], a[108:111], v[216:219], v[28:31]\n\t"
    "s_waitcnt vmcnt(28)\n\t"
    "s_waitcnt lgkmcnt(2)\n\t"
    "v_mfma_f32_16x16x32_bf16 v[0:3], a[192:195], v[96:99], v[0:3]\n\t"
    "v_mfma_f32_16x16x32_bf16 v[0:3], a[240:243], v[100:103], v[0:3]\n\t"
    "s_waitcnt lgkmcnt(1)\n\t"
    "v_mfma_f32_16x16x32_bf16 v[4:7], a[244:247], v[100:103], v[4:7]\n\t"
    "v_mfma_f32_16x16x32_bf16 v[4:7], a[196:199], v[96:99], v[4:7]\n\t"
    "s_waitcnt lgkmcnt(0)\n\t"
    "v_mfma_f32_16x16x32_bf16 v[8:11], a[200:203], v[96:99], v[8:11]\n\t"
    "ds_write_b128 v92, v[16:19] offset:4096\n\t"
    "v_mfma_f32_16x16x32_bf16 v[8:11], a[248:251], v[100:103], v[8:11]\n\t"
    "v_mfma_f32_16x16x32_bf16 v[12:15], a[252:255], v[100:103], v[12:15]\n\t"
    "v_mov_b32 v81, s56\n\t"
    "v_mfma_f32_16x16x32_bf16 v[12:15], a[204:207], v[96:99], v[12:15]\n\t"
    "s_waitcnt vmcnt(26)\n\t"
    "v_mfma_f32_16x16x32_bf16 v[0:3], a[160:163], v[104:107], v[0:3]\n\t"
    "v_mfma_f32_16x16x32_bf16 v[0:3], a[176:179], v[108:111], v[0:3]\n\t"
    "ds_write_b128 v92, v[20:23] offset:5120\n\t"
    "v_mfma_f32_16x16x32_bf16 v[4:7], a[180:183], v[108:111], v[4:7]\n\t"
    "v_mfma_f32_16x16x32_bf16 v[4:7], a[164:167], v[104:107], v[4:7]\n\t"
    "v_mfma_f32_16x16x32_bf16 v[8:11], a[168:171], v[104:107], v[8:11]\n\t"
    "v_mfma_f32_16x16x32_bf16 v[8:11], a[184:187], v[108:111], v[8:11]\n\t"
    "v_mfma_f32_16x16x32_bf16 v[12:15], a[188:191], v[108:111], v[12:15]\n\t"
    "ds_write_b128 v92, v[24:27] offset:6144\n\t"
    "v_mfma_f32_16x16x32_bf16 v[12:15], a[172:175], v[104:107], v[12:15]\n\t"
    "s_waitcnt vmcnt(24)\n\t"
    "v_mfma_f32_16x16x32_bf16 v[0:3], a[208:211], v[112:115], v[0:3]\n\t"
    "v_mfma_f32_16x16x32_bf16 v[0:3], a[224:227], v[116:119], v[0:3]\n\t"
    "v_mfma_f32_16x16x32_bf16 v[4:7], a[228:231], v[116:119], v[4:7]\n\t"
    "ds_write_b128 v92, v[28:31] offset:7168\n\t"
    "v_mfma_f32_16x16x32_bf16 v[4:7], a[212:215], v[112:115], v[4:7]\n\t"
    "v_mfma_f32_16x16x32_bf16 v[8:11], a[216:219], v[112:115], v[8:11]\n\t"
    "v_mfma_f32_16x16x32_bf16 v[8:11], a[232:235], v[116:119], v[8:11]\n\t"
    "v_mfma_f32_16x16x32_bf16 v[12:15], a[236:239], v[116:119], v[12:15]\n\t"
    "v_mfma_f32_16x16x32_bf16 v[12:15], a[220:223], v[112:115], v[12:15]\n\t"
    "s_waitcnt vmcnt(22)\n\t"
    "v_mfma_f32_16x16x32_bf16 v[0:3], a[128:131], v[120:123], v[0:3]\n\t"
    "v_mfma_f32_16x16x32_bf16 v[0:3], a[144:147], v[124:127], v[0:3]\n\t"
    "v_mfma_f32_16x16x32_bf16 v[4:7], a[148:151], v[124:127], v[4:7]\n\t"
    "v_mov_b32 v95, 1\n\t"
    "v_mfma_f32_16x16x32_bf16 v[4:7], a[132:135], v[120:123], v[4:7]\n\t"
    "v_mfma_f32_16x16x32_bf16 v[8:11], a[136:139], v[120:123], v[8:11]\n\t"
    "s_waitcnt vmcnt(0)\n\t"
    "s_waitcnt lgkmcnt(0)\n\t"
    "s_barrier\n\t"
    "s_cmp_lg_u32 s58, 1\n\t"
    "s_cbranch_scc1 L_ny0_%=\n\t"
    "s_mov_b64 exec, 1\n\t"
    "global_atomic_add v81, v95, s[84:85] sc1\n\t"
    "s_mov_b64 exec, -1\n\t"
    "L_ny0_%=:\n\t"
    "v_mfma_f32_16x16x32_bf16 v[8:11], a[152:155], v[124:127], v[8:11]\n\t"
    "v_mfma_f32_16x16x32_bf16 v[12:15], a[156:159], v[124:127], v[12:15]\n\t"
    "buffer_load_dwordx4 v[224:227], v82, s[36:39], s55 offen offset:0 sc1\n\t"
    "v_mfma_f32_16x16x32_bf16 v[12:15], a[140:143], v[120:123], v[12:15]\n\t"
    "v_mfma_f32_16x16x32_bf16 v[0:3], a[0:3], v[128:131], v[0:3]\n\t"
    "s_add_u32 m0, s45, 0x18000\n\t"
    "v_mfma_f32_16x16x32_bf16 v[0:3], a[16:19], v[132:135], v[0:3]\n\t"
    "v_mfma_f32_16x16x32_bf16 v[4:7], a[20:23], v[132:135], v[4:7]\n\t"
    "buffer_load_dwordx4 v64, s[40:43], s44 offen offset:0 lds\n\t"
    "v_mfma_f32_16x16x32_bf16 v[4:7], a[4:7], v[128:131], v[4:7]\n\t"
    "v_mfma_f32_16x16x32_bf16 v[8:11], a[8:11], v[128:131], v[8:11]\n\t"
    "ds_read_b128 v[20:23], v93 offset:5120\n\t"
    "v_mfma_f32_16x16x32_bf16 v[8:11], a[24:27], v[132:135], v[8:11]\n\t"
    "buffer_load_dwordx4 v[228:231], v82, s[36:39], s55 offen offset:1024 sc1\n\t"
    "v_mfma_f32_16x16x32_bf16 v[12:15], a[28:31], v[132:135], v[12:15]\n\t"
    "buffer_load_dwordx4 v65, s[40:43], s44 offen offset:1024 lds\n\t"
    "v_mfma_f32_16x16x32_bf16 v[12:15], a[12:15], v[128:131], v[12:15]\n\t"
    "v_mfma_f32_16x16x32_bf16 v[0:3], a[32:35], v[136:139], v[0:3]\n\t"
    "global_load_dword v88, v87, s[84:85] sc1\n\t"
    "v_mfma_f32_16x16x32_bf16 v[0:3], a[48:51], v[140:143], v[0:3]\n\t"
    "v_mfma_f32_16x16x32_bf16 v[4:7], a[52:55], v[140:143], v[4:7]\n\t"
    "ds_read_b128 v[28:31], v93 offset:7168\n\t"
    "v_mfma_f32_16x16x32_bf16 v[4:7], a[36:39], v[136:139], v[4:7]\n\t"
    "buffer_load_dwordx4 v[232:235], v82, s[36:39], s55 offen offset:2048 sc1\n\t"
    "v_mfma_f32_16x16x32_bf16 v[8:11], a[40:43], v[136:139], v[8:11]\n\t"
    "buffer_load_dwordx4 v66, s[40:43], s44 offen offset:2048 lds\n\t"
    "v_mfma_f32_16x16x32_bf16 v[8:11], a[56:59], v[140:143], v[8:11]\n\t"
    "v_mfma_f32_16x16x32_bf16 v[12:15], a[60:63], v[140:143], v[12:15]\n\t"
    "v_mfma_f32_16x16x32_bf16 v[12:15], a[44:47], v[136:139], v[12:15]\n\t"
    "v_mfma_f32_16x16x32_bf16 v[0:3], a[64:67], v[144:147], v[0:3]\n\t"
    "ds_read_b128 v[24:27], v93 offset:6144\n\t"
    "v_mfma_f32_16x16x32_bf16 v[0:3], a[80:83], v[148:151], v[0:3]\n\t"
    "buffer_load_dwordx4 v[236:239], v82, s[36:39], s55 offen offset:3072 sc1\n\t"
    "v_mfma_f32_16x16x32_bf16 v[4:7], a[84:87], v[148:151], v[4:7]\n\t"
    "ds_read_b128 v[16:19], v93 offset:4096\n\t"
    "v_mfma_f32_16x16x32_bf16 v[4:7], a[68:71], v[144:147], v[4:7]\n\t"
    "v_mfma_f32_16x16x32_bf16 v[8:11], a[72:75], v[144:147], v[8:11]\n\t"
    "buffer_load_dwordx4 v67, s[40:43], s44 offen offset:3072 lds\n\t"
    "v_mfma_f32_16x16x32_bf16 v[8:11], a[88:91], v[148:151], v[8:11]\n\t"
    "v_mfma_f32_16x16x32_bf16 v[12:15], a[92:95], v[148:151], v[12:15]\n\t"
    "s_xor_b32 s52, s52, 0x80000\n\t"
    "v_mfma_f32_16x16x32_bf16 v[12:15], a[76:79], v[144:147], v[12:15]\n\t"
    "v_mfma_f32_16x16x32_bf16 v[0:3], a[96:99], v[152:155], v[0:3]\n\t"
    "buffer_load_dwordx4 v[240:243], v83, s[36:39], s52 offen offset:0 sc1\n\t"
    "v_mfma_f32_16x16x32_bf16 v[0:3], a[112:115], v[156:159], v[0:3]\n\t"
    "v_mfma_f32_16x16x32_bf16 v[4:7], a[116:119], v[156:159], v[4:7]\n\t"
    "s_add_u32 s44, s44, 0x8000\n\t"
    "v_mfma_f32_16x16x32_bf16 v[4:7], a[100:103], v[152:155], v[4:7]\n\t"
    "v_mfma_f32_16x16x32_bf16 v[8:11], a[104:107], v[152:155], v[8:11]\n\t"
    "v_mfma_f32_16x16x32_bf16 v[8:11], a[120:123], v[156:159], v[8:11]\n\t"
    "buffer_load_dwordx4 v[244:247], v83, s[36:39], s52 offen offset:1024 sc1\n\t"
    "v_mfma_f32_16x16x32_bf16 v[12:15], a[124:127], v[156:159], v[12:15]\n\t"
    "v_mfma_f32_16x16x32_bf16 v[12:15], a[108:111], v[152:155], v[12:15]\n\t"
    "s_waitcnt lgkmcnt(0)\n\t"
    "v_mfma_f32_16x16x32_bf16 v[16:19], a[192:195], v[160:163], v[16:19]\n\t"
    "v_mfma_f32_16x16x32_bf16 v[16:19], a[240:243], v[164:167], v[16:19]\n\t"
    "buffer_load_dwordx4 v[248:251], v83, s[36:39], s52 offen offset:2048 sc1\n\t"
    "v_mfma_f32_16x16x32_bf16 v[20:23], a[244:247], v[164:167], v[20:23]\n\t"
    "ds_write_b128 v92, v[0:3] offset:0\n\t"
    "v_mfma_f32_16x16x32_bf16 v[20:23], a[196:199], v[160:163], v[20:23]\n\t"
    "buffer_load_dwordx4 v[252:255], v83, s[36:39], s52 offen offset:3072 sc1\n\t"
    "v_mfma_f32_16x16x32_bf16 v[24:27], a[200:203], v[160:163], v[24:27]\n\t"
    "s_xor_b32 s53, s53, 0x80000\n\t"
    "v_mfma_f32_16x16x32_bf16 v[24:27], a[248:251], v[164:167], v[24:27]\n\t"
    "v_mfma_f32_16x16x32_bf16 v[28:31], a[252:255], v[164:167], v[28:31]\n\t"
    "v_mfma_f32_16x16x32_bf16 v[28:31], a[204:207], v[160:163], v[28:31]\n\t"
    "v_mfma_f32_16x16x32_bf16 v[16:19], a[160:163], v[168:171], v[16:19]\n\t"
    "buffer_load_dwordx4 v[192:195], v83, s[36:39], s53 offen offset:0 sc1\n\t"
    "v_mfma_f32_16x16x32_bf16 v[16:19], a[176:179], v[172:175], v[16:19]\n\t"
    "v_mfma_f32_16x16x32_bf16 v[20:23], a[180:183], v[172:175], v[20:23]\n\t"
    "buffer_load_dwordx4 v[196:199], v83, s[36:39], s53 offen offset:1024 sc1\n\t"
    "v_mfma_f32_16x16x32_bf16 v[20:23], a[164:167], v[168:171], v[20:23]\n\t"
    "ds_write_b128 v92, v[4:7] offset:1024\n\t"
    "v_mfma_f32_16x16x32_bf16 v[24:27], a[168:171], v[168:171], v[24:27]\n\t"
    "ds_write_b128 v92, v[8:11] offset:2048\n\t"
    "v_mfma_f32_16x16x32_bf16 v[24:27], a[184:187], v[172:175], v[24:27]\n\t"
    "v_mfma_f32_16x16x32_bf16 v[28:31], a[188:191], v[172:175], v[28:31]\n\t"
    "buffer_load_dwordx4 v[200:203], v83, s[36:39], s53 offen offset:2048 sc1\n\t"
    "v_mfma_f32_16x16x32_bf16 v[28:31], a[172:175], v[168:171], v[28:31]\n\t"
    "v_mfma_f32_16x16x32_bf16 v[16:19], a[208:211], v[176:179], v[16:19]\n\t"
    "buffer_load_dwordx4 v[204:207], v83, s[36:39], s53 offen offset:3072 sc1\n\t"
    "v_mfma_f32_16x16x32_bf16 v[16:19], a[224:227], v[180:183], v[16:19]\n\t"
    "v_mfma_f32_16x16x32_bf16 v[20:23], a[228:231], v[180:183], v[20:23]\n\t"
    "s_xor_b32 s54, s54, 0x80000\n\t"
    "v_mfma_f32_16x16x32_bf16 v[20:23], a[212:215], v[176:179], v[20:23]\n\t"
    "buffer_load_dwordx4 v[208:211], v83, s[36:39], s54 offen offset:0 sc1\n\t"
    "v_mfma_f32_16x16x32_bf16 v[24:27], a[216:219], v[176:179], v[24:27]\n\t"
    "v_mfma_f32_16x16x32_bf16 v[24:27], a[232:235], v[180:183], v[24:27]\n\t"
    "ds_write_b128 v92, v[12:15] offset:3072\n\t"
    "v_mfma_f32_16x16x32_bf16 v[28:31], a[236:239], v[180:183], v[28:31]\n\t"
    "buffer_load_dwordx4 v[212:215], v83, s[36:39], s54 offen offset:1024 sc1\n\t"
    "v_mfma_f32_16x16x32_bf16 v[28:31], a[220:223], v[176:179], v[28:31]\n\t"
    "v_mfma_f32_16x16x32_bf16 v[16:19], a[128:131], v[184:187], v[16:19]\n\t"
    "v_mfma_f32_16x16x32_bf16 v[16:19], a[144:147], v[188:191], v[16:19]\n\t"
    "v_mfma_f32_16x16x32_bf16 v[20:23], a[148:151], v[188:191], v[20:23]\n\t"
    "v_mfma_f32_16x16x32_bf16 v[20:23], a[132:135], v[184:187], v[20:23]\n\t"
    "buffer_load_dwordx4 v[96:99], v83, s[36:39], s54 offen offset:2048 sc1\n\t"
    "v_mfma_f32_16x16x32_bf16 v[24:27], a[136:139], v[184:187], v[24:27]\n\t"
    "s_waitcnt lgkmcnt(0)\n\t"
    "s_barrier\n\t"
    "v_mfma_f32_16x16x32_bf16 v[24:27], a[152:155], v[188:191], v[24:27]\n\t"
    "buffer_load_dwordx4 v[100:103], v83, s[36:39], s54 offen offset:3072 sc1\n\t"
    "v_mfma_f32_16x16x32_bf16 v[28:31], a[156:159], v[188:191], v[28:31]\n\t"
    "s_xor_b32 s55, s55, 0x80000\n\t"
    "v_mfma_f32_16x16x32_bf16 v[28:31], a[140:143], v[184:187], v[28:31]\n\t"
    "v_mfma_f32_16x16x32_bf16 v[16:19], a[0:3], v[48:51], v[16:19]\n\t"
    "v_mfma_f32_16x16x32_bf16 v[16:19], a[16:19], v[52:55], v[16:19]\n\t"
    "ds_read_b128 v[44:47], v94 offset:3072\n\t"
    "v_mfma_f32_16x16x32_bf16 v[20:23], a[20:23], v[52:55], v[20:23]\n\t"
    "buffer_load_dwordx4 v[104:107], v83, s[36:39], s55 offen offset:0 sc1\n\t"
    "v_mfma_f32_16x16x32_bf16 v[20:23], a[4:7], v[48:51], v[20:23]\n\t"
    "buffer_load_dwordx4 v[108:111], v83, s[36:39], s55 offen offset:1024 sc1\n\t"
    "v_mfma_f32_16x16x32_bf16 v[24:27], a[8:11], v[48:51], v[24:27]\n\t"
    "ds_read_b128 v[40:43], v94 offset:2048\n\t"
    "v_mfma_f32_16x16x32_bf16 v[24:27], a[24:27], v[52:55], v[24:27]\n\t"
    "s_waitcnt vmcnt(18)\n\t"
    "v_readfirstlane_b32 s90, v88\n\t"
    "v_mfma_f32_16x16x32_bf16 v[28:31], a[28:31], v[52:55], v[28:31]\n\t"
    "s_cmp_lt_u32 s90, 16\n\t"
    "s_cbranch_scc1 L_pD0_%=\n\t"
    "L_rD0_%=:\n\t"
    "v_mfma_f32_16x16x32_bf16 v[28:31], a[12:15], v[48:51], v[28:31]\n\t"
    "v_mfma_f32_16x16x32_bf16 v[16:19], a[32:35], v[56:59], v[16:19]\n\t"
    "buffer_load_dwordx4 v[112:115], v83, s[36:39], s55 offen offset:2048 sc1\n\t"
    "v_mfma_f32_16x16x32_bf16 v[16:19], a[48:51], v[60:63], v[16:19]\n\t"
    "s_lshl_b32 s26, s99, 7\n\t"
    "v_mfma_f32_16x16x32_bf16 v[20:23], a[52:55], v[60:63], v[20:23]\n\t"
    "buffer_load_dwordx4 v[116:119], v83, s[36:39], s55 offen offset:3072 sc1\n\t"
    "v_mfma_f32_16x16x32_bf16 v[20:23], a[36:39], v[56:59], v[20:23]\n\t"
    "ds_read_b128 v[32:35], v94 offset:0\n\t"
    "v_mfma_f32_16x16x32_bf16 v[24:27], a[40:43], v[56:59], v[24:27]\n\t"
    "buffer_load_dwordx4 v[120:123], v83, s[36:39], s9 offen offset:2048 sc1\n\t"
    "v_mfma_f32_16x16x32_bf16 v[24:27], a[56:59], v[60:63], v[24:27]\n\t"
    "s_add_u32 s26, s26, 0xc000\n\t"
    "v_mfma_f32_16x16x32_bf16 v[28:31], a[60:63], v[60:63], v[28:31]\n\t"
    "v_mov_b32 v81, s26\n\t"
    "v_mfma_f32_16x16x32_bf16 v[28:31], a[44:47], v[56:59], v[28:31]\n\t"
    "v_mfma_f32_16x16x32_bf16 v[16:19], a[64:67], v[224:227], v[16:19]\n\t"
    "v_mov_b32 v95, 32\n\t"
    "v_mfma_f32_16x16x32_bf16 v[16:19], a[80:83], v[228:231], v[16:19]\n\t"
    "buffer_load_dwordx4 v[124:127], v83, s[36:39], s9 offen offset:3072 sc1\n\t"
    "Lis16_%=:\n\t"
    "s_cbranch_execz Lis99_%=\n\t"
    "v_mfma_f32_16x16x32_bf16 v[20:23], a[84:87], v[228:231], v[20:23]\n\t"
    "v_xor_b32 v87, 0x200, v81\n\t"
    "v_mfma_f32_16x16x32_bf16 v[20:23], a[68:71], v[224:227], v[20:23]\n\t"
    "buffer_load_dwordx4 v[128:131], v84, s[36:39], s52 offen offset:0 sc1\n\t"
    "v_mfma_f32_16x16x32_bf16 v[24:27], a[72:75], v[224:227], v[24:27]\n\t"
    "ds_read_b128 v[36:39], v94 offset:1024\n\t"
    "v_mfma_f32_16x16x32_bf16 v[24:27], a[88:91], v[228:231], v[24:27]\n\t"
    "v_writelane_b32 v87, s56, 1\n\t"
    "v_mfma_f32_16x16x32_bf16 v[28:31], a[92:95], v[228:231], v[28:31]\n\t"
    "buffer_load_dwordx4 v[132:135], v84, s[36:39], s52 offen offset:1024 sc1\n\t"
    "v_mfma_f32_16x16x32_bf16 v[28:31], a[76:79], v[224:227], v[28:31]\n\t"
    "s_waitcnt vmcnt(21)\n\t"
    "v_mfma_f32_16x16x32_bf16 v[16:19], a[96:99], v[232:235], v[16:19]\n\t"
    "s_lshl2_add_u32 s25, s99, s58\n\t"
    "v_mfma_f32_16x16x32_bf16 v[16:19], a[112:115], v[236:239], v[16:19]\n\t"
    "s_lshl_b32 s25, s25, 12\n\t"
    "v_mfma_f32_16x16x32_bf16 v[20:23], a[116:119], v[236:239], v[20:23]\n\t"
    "buffer_load_dwordx4 v[136:139], v84, s[36:39], s52 offen offset:2048 sc1\n\t"
    "v_mfma_f32_16x16x32_bf16 v[20:23], a[100:103], v[232:235], v[20:23]\n\t"
    "s_add_u32 s25, s25, 0xc00000\n\t"
    "v_mfma_f32_16x16x32_bf16 v[24:27], a[104:107], v[232:235], v[24:27]\n\t"
    "v_mfma_f32_16x16x32_bf16 v[24:27], a[120:123], v[236:239], v[24:27]\n\t"
    "buffer_load_dwordx4 v[140:143], v84, s[36:39], s52 offen offset:3072 sc1\n\t"
    "v_mfma_f32_16x16x32_bf16 v[28:31], a[124:127], v[236:239], v[28:31]\n\t"
    "v_add_u32 v70, s25, v76\n\t"
    "v_mfma_f32_16x16x32_bf16 v[28:31], a[108:111], v[232:235], v[28:31]\n\t"
    "s_waitcnt vmcnt(20)\n\t"
    "v_mfma_f32_16x16x32_bf16 v[0:3], a[192:195], v[240:243], 0\n\t"
    "s_waitcnt lgkmcnt(1)\n\t"
    "v_add_f32 v32, v32, v16\n\t"
    "v_add_f32 v33, v33, v17\n\t"
    "v_mfma_f32_16x16x32_bf16 v[0:3], a[240:243], v[244:247], v[0:3]\n\t"
    "buffer_load_dwordx4 v[144:147], v84, s[36:39], s53 offen offset:0 sc1\n\t"
    "v_mfma_f32_16x16x32_bf16 v[4:7], a[244:247], v[244:247], 0\n\t"
    "v_add_f32 v34, v34, v18\n\t"
    "v_add_f32 v35, v35, v19\n\t"
    "v_mfma_f32_16x16x32_bf16 v[4:7], a[196:199], v[240:243], v[4:7]\n\t"
    "s_waitcnt lgkmcnt(0)\n\t"
    "v_add_f32 v36, v36, v20\n\t"
    "v_add_f32 v37, v37, v21\n\t"
    "v_add_f32 v38, v38, v22\n\t"
    "v_add_f32 v39, v39, v23\n\t"
    "v_mfma_f32_16x16x32_bf16 v[8:11], a[200:203], v[240:243], 0\n\t"
    "v_add_f32 v42, v42, v26\n\t"
    "v_add_f32 v43, v43, v27\n\t"
    "v_mfma_f32_16x16x32_bf16 v[8:11], a[248:251], v[244:247], v[8:11]\n\t"
    "global_store_dwordx4 v70, v[32:35], s[84:85] offset:0 sc1\n\t"
    "v_mfma_f32_16x16x32_bf16 v[12:15], a[252:255], v[244:247], 0\n\t"
    "v_add_f32 v40, v40, v24\n\t"
    "v_add_f32 v41, v41, v25\n\t"
    "v_mfma_f32_16x16x32_bf16 v[12:15], a[204:207], v[240:243], v[12:15]\n\t"
    "global_store_dwordx4 v70, v[36:39], s[84:85] offset:1024 sc1\n\t"
    "s_waitcnt vmcnt(21)\n\t"
    "v_mfma_f32_16x16x32_bf16 v[0:3], a[160:163], v[248:251], v[0:3]\n\t"
    "v_add_f32 v46, v46, v30\n\t"
    "v_add_f32 v47, v47, v31\n\t"
    "v_mfma_f32_16x16x32_bf16 v[0:3], a[176:179], v[252:255], v[0:3]\n\t"
    "global_store_dwordx4 v70, v[40:43], s[84:85] offset:2048 sc1\n\t"
    "v_mfma_f32_16x16x32_bf16 v[4:7], a[180:183], v[252:255], v[4:7]\n\t"
    "v_add_f32 v44, v44, v28\n\t"
    "v_add_f32 v45, v45, v29\n\t"
    "v_mfma_f32_16x16x32_bf16 v[4:7], a[164:167], v[248:251], v[4:7]\n\t"
    "global_store_dwordx4 v70, v[44:47], s[84:85] offset:3072 sc1\n\t"
    "v_mfma_f32_16x16x32_bf16 v[8:11], a[168:171], v[248:251], v[8:11]\n\t"
    "v_mfma_f32_16x16x32_bf16 v[8:11], a[184:187], v[252:255], v[8:11]\n\t"
    "v_mfma_f32_16x16x32_bf16 v[12:15], a[188:191], v[252:255], v[12:15]\n\t"
    "s_lshl_b32 s25, s72, 17\n\t"
    "v_mfma_f32_16x16x32_bf16 v[12:15], a[172:175], v[248:251], v[12:15]\n\t"
    "s_waitcnt vmcnt(21)\n\t"
    "v_mfma_f32_16x16x32_bf16 v[0:3], a[208:211], v[192:195], v[0:3]\n\t"
    "s_lshl_b32 s26, s58, 15\n\t"
    "v_mfma_f32_16x16x32_bf16 v[0:3], a[224:227], v[196:199], v[0:3]\n\t"
    "s_add_u32 s25, s25, s26\n\t"
    "v_mfma_f32_16x16x32_bf16 v[4:7], a[228:231], v[196:199], v[4:7]\n\t"
    "v_mfma_f32_16x16x32_bf16 v[4:7], a[212:215], v[192:195], v[4:7]\n\t"
    "v_mfma_f32_16x16x32_bf16 v[8:11], a[216:219], v[192:195], v[8:11]\n\t"
    "v_mfma_f32_16x16x32_bf16 v[8:11], a[232:235], v[196:199], v[8:11]\n\t"
    "v_mfma_f32_16x16x32_bf16 v[12:15], a[236:239], v[196:199], v[12:15]\n\t"
    "s_lshl_b32 s26, s71, 7\n\t"
    "v_mfma_f32_16x16x32_bf16 v[12:15], a[220:223], v[192:195], v[12:15]\n\t"
    "s_waitcnt vmcnt(19)\n\t"
    "v_mfma_f32_16x16x32_bf16 v[0:3], a[128:131], v[200:203], v[0:3]\n\t"
    "v_mfma_f32_16x16x32_bf16 v[0:3], a[144:147], v[204:207], v[0:3]\n\t"
    "v_mfma_f32_16x16x32_bf16 v[4:7], a[148:151], v[204:207], v[4:7]\n\t"
    "v_mfma_f32_16x16x32_bf16 v[4:7], a[132:135], v[200:203], v[4:7]\n\t"
    "v_mfma_f32_16x16x32_bf16 v[8:11], a[136:139], v[200:203], v[8:11]\n\t"
    "v_mfma_f32_16x16x32_bf16 v[8:11], a[152:155], v[204:207], v[8:11]\n\t"
    "v_mfma_f32_16x16x32_bf16 v[12:15], a[156:159], v[204:207], v[12:15]\n\t"
    "v_mfma_f32_16x16x32_bf16 v[12:15], a[140:143], v[200:203], v[12:15]\n\t"
    "s_waitcnt vmcnt(17)\n\t"
    "v_mfma_f32_16x16x32_bf16 v[0:3], a[0:3], v[208:211], v[0:3]\n\t"
    "v_mfma_f32_16x16x32_bf16 v[0:3], a[16:19], v[212:215], v[0:3]\n\t"
    "v_mfma_f32_16x16x32_bf16 v[4:7], a[20:23], v[212:215], v[4:7]\n\t"
    "v_mfma_f32_16x16x32_bf16 v[4:7], a[4:7], v[208:211], v[4:7]\n\t"
    "v_mfma_f32_16x16x32_bf16 v[8:11], a[8:11], v[208:211], v[8:11]\n\t"
    "s_add_u32 s25, s25, s26\n\t"
    "v_mfma_f32_16x16x32_bf16 v[8:11], a[24:27], v[212:215], v[8:11]\n\t"
    "v_mfma_f32_16x16x32_bf16 v[12:15], a[28:31], v[212:215], v[12:15]\n\t"
    "v_mfma_f32_16x16x32_bf16 v[12:15], a[12:15], v[208:211], v[12:15]\n\t"
    "s_waitcnt vmcnt(15)\n\t"
    "v_mfma_f32_16x16x32_bf16 v[0:3], a[32:35], v[96:99], v[0:3]\n\t"
    "ds_read_b64_tr_b16 v[204:205], v78 offset:36872\n\t"
    "v_mfma_f32_16x16x32_bf16 v[0:3], a[48:51], v[100:103], v[0:3]\n\t"
    "ds_read_b64_tr_b16 v[194:195], v77 offset:38912\n\t"
    "v_mfma_f32_16x16x32_bf16 v[4:7], a[52:55], v[100:103], v[4:7]\n\t"
    "v_mfma_f32_16x16x32_bf16 v[4:7], a[36:39], v[96:99], v[4:7]\n\t"
    "ds_read_b64_tr_b16 v[192:193], v77 offset:36864\n\t"
    "v_mfma_f32_16x16x32_bf16 v[8:11], a[40:43], v[96:99], v[8:11]\n\t"
    "ds_read_b64_tr_b16 v[208:209], v79 offset:32768\n\t"
    "v_mfma_f32_16x16x32_bf16 v[8:11], a[56:59], v[100:103], v[8:11]\n\t"
    "ds_read_b64_tr_b16 v[196:197], v77 offset:36872\n\t"
    "ds_read_b64_tr_b16 v[222:223], v80 offset:34824\n\t"
    "v_mfma_f32_16x16x32_bf16 v[12:15], a[60:63], v[100:103], v[12:15]\n\t"
    "ds_read_b64_tr_b16 v[200:201], v78 offset:36864\n\t"
    "ds_read_b64_tr_b16 v[218:219], v80 offset:34816\n\t"
    "v_mfma_f32_16x16x32_bf16 v[12:15], a[44:47], v[96:99], v[12:15]\n\t"
    "s_waitcnt vmcnt(13)\n\t"
    "v_mfma_f32_16x16x32_bf16 v[0:3], a[64:67], v[104:107], v[0:3]\n\t"
    "ds_read_b64_tr_b16 v[212:213], v79 offset:32776\n\t"
    "v_mfma_f32_16x16x32_bf16 v[0:3], a[80:83], v[108:111], v[0:3]\n\t"
    "ds_read_b64_tr_b16 v[216:217], v80 offset:32768\n\t"
    "v_mfma_f32_16x16x32_bf16 v[4:7], a[84:87], v[108:111], v[4:7]\n\t"
    "ds_read_b64_tr_b16 v[198:199], v77 offset:38920\n\t"
    "v_mfma_f32_16x16x32_bf16 v[4:7], a[68:71], v[104:107], v[4:7]\n\t"
    "ds_read_b64_tr_b16 v[206:207], v78 offset:38920\n\t"
    "v_mfma_f32_16x16x32_bf16 v[8:11], a[72:75], v[104:107], v[8:11]\n\t"
    "ds_read_b64_tr_b16 v[202:203], v78 offset:38912\n\t"
    "ds_read_b64_tr_b16 v[210:211], v79 offset:34816\n\t"
    "v_mfma_f32_16x16x32_bf16 v[8:11], a[88:91], v[108:111], v[8:11]\n\t"
    "ds_read_b64_tr_b16 v[214:215], v79 offset:34824\n\t"
    "v_mfma_f32_16x16x32_bf16 v[12:15], a[92:95], v[108:111], v[12:15]\n\t"
    "s_waitcnt vmcnt(0)\n\t"
    "s_barrier\n\t"
    "s_cmp_lg_u32 s58, 0\n\t"
    "s_cbranch_scc1 L_nz0_%=\n\t"
    "s_mov_b64 exec, 1\n\t"
    "global_atomic_add v81, v95, s[84:85] sc1\n\t"
    "s_mov_b64 exec, -1\n\t"
    "L_nz0_%=:\n\t"
    "v_mfma_f32_16x16x32_bf16 v[12:15], a[76:79], v[104:107], v[12:15]\n\t"
    "v_mfma_f32_16x16x32_bf16 v[0:3], a[96:99], v[112:115], v[0:3]\n\t"
    "ds_read_b64_tr_b16 v[220:221], v80 offset:32776\n\t"
    "v_mfma_f32_16x16x32_bf16 v[0:3], a[112:115], v[116:119], v[0:3]\n\t"
    "buffer_load_dwordx4 v[148:151], v84, s[36:39], s53 offen offset:1024 sc1\n\t"
    "v_mfma_f32_16x16x32_bf16 v[4:7], a[116:119], v[116:119], v[4:7]\n\t"
    "s_add_u32 s25, s25, 1024\n\t"
    "v_mfma_f32_16x16x32_bf16 v[4:7], a[100:103], v[112:115], v[4:7]\n\t"
    "buffer_load_dwordx4 v[152:155], v84, s[36:39], s53 offen offset:2048 sc1\n\t"
    "v_mfma_f32_16x16x32_bf16 v[8:11], a[104:107], v[112:115], v[8:11]\n\t"
    "v_mfma_f32_16x16x32_bf16 v[8:11], a[120:123], v[116:119], v[8:11]\n\t"
    "buffer_load_dwordx4 v[156:159], v84, s[36:39], s53 offen offset:3072 sc1\n\t"
    "v_mfma_f32_16x16x32_bf16 v[12:15], a[124:127], v[116:119], v[12:15]\n\t"
    "s_add_u32 s12, s50, s25\n\t"
    "s_addc_u32 s13, s51, 0\n\t"
    "v_mfma_f32_16x16x32_bf16 v[12:15], a[108:111], v[112:115], v[12:15]\n\t"
    "s_waitcnt lgkmcnt(1)\n\t"
    "v_mfma_f32_16x16x32_bf16 v[0:3], v[192:195], v[120:123], v[0:3]\n\t"
    "v_lshlrev_b32 v69, 4, v74\n\t"
    "v_mfma_f32_16x16x32_bf16 v[0:3], v[208:211], v[124:127], v[0:3]\n\t"
    "buffer_load_dwordx4 v[160:163], v84, s[36:39], s54 offen offset:0 sc1\n\t"
    "v_mfma_f32_16x16x32_bf16 v[4:7], v[212:215], v[124:127], v[4:7]\n\t"
    "v_mfma_f32_16x16x32_bf16 v[4:7], v[196:199], v[120:123], v[4:7]\n\t"
    "v_mfma_f32_16x16x32_bf16 v[8:11], v[200:203], v[120:123], v[8:11]\n\t"
    "v_mfma_f32_16x16x32_bf16 v[8:11], v[216:219], v[124:127], v[8:11]\n\t"
    "buffer_load_dwordx4 v[164:167], v84, s[36:39], s54 offen offset:1024 sc1\n\t"
    "s_waitcnt lgkmcnt(0)\n\t"
    "v_mfma_f32_16x16x32_bf16 v[12:15], v[220:223], v[124:127], v[12:15]\n\t"
    "v_mfma_f32_16x16x32_bf16 v[12:15], v[204:207], v[120:123], v[12:15]\n\t"
    "v_mfma_f32_16x16x32_bf16 v[16:19], a[192:195], v[128:131], 0\n\t"
    "v_lshl_add_u32 v69, v73, 11, v69\n\t"
    "v_mfma_f32_16x16x32_bf16 v[16:19], a[240:243], v[132:135], v[16:19]\n\t"
    "buffer_load_dwordx4 v[168:171], v84, s[36:39], s54 offen offset:2048 sc1\n\t"
    "v_mfma_f32_16x16x32_bf16 v[20:23], a[244:247], v[132:135], 0\n\t"
    "s_lshl2_add_u32 s26, s72, s58\n\t"
    "v_mfma_f32_16x16x32_bf16 v[20:23], a[196:199], v[128:131], v[20:23]\n\t"
    "buffer_load_dwordx4 v[172:175], v84, s[36:39], s54 offen offset:3072 sc1\n\t"
    "v_mfma_f32_16x16x32_bf16 v[24:27], a[200:203], v[128:131], 0\n\t"
    "ds_write_b128 v92, v[0:3] offset:4096\n\t"
    "v_mfma_f32_16x16x32_bf16 v[24:27], a[248:251], v[132:135], v[24:27]\n\t"
    "v_mfma_f32_16x16x32_bf16 v[28:31], a[252:255], v[132:135], 0\n\t"
    "buffer_load_dwordx4 v[176:179], v84, s[36:39], s55 offen offset:0 sc1\n\t"
    "v_mfma_f32_16x16x32_bf16 v[28:31], a[204:207], v[128:131], v[28:31]\n\t"
    "v_mfma_f32_16x16x32_bf16 v[16:19], a[160:163], v[136:139], v[16:19]\n\t"
    "s_lshl_b32 s25, s26, 6\n\t"
    "v_mfma_f32_16x16x32_bf16 v[16:19], a[176:179], v[140:143], v[16:19]\n\t"
    "ds_write_b128 v92, v[4:7] offset:5120\n\t"
    "v_mfma_f32_16x16x32_bf16 v[20:23], a[180:183], v[140:143], v[20:23]\n\t"
    "buffer_load_dwordx4 v[180:183], v84, s[36:39], s55 offen offset:1024 sc1\n\t"
    "v_mfma_f32_16x16x32_bf16 v[20:23], a[164:167], v[136:139], v[20:23]\n\t"
    "v_mfma_f32_16x16x32_bf16 v[24:27], a[168:171], v[136:139], v[24:27]\n\t"
    "v_mfma_f32_16x16x32_bf16 v[24:27], a[184:187], v[140:143], v[24:27]\n\t"
    "v_xor_b32 v70, 0x10000, v70\n\t"
    "v_mfma_f32_16x16x32_bf16 v[28:31], a[188:191], v[140:143], v[28:31]\n\t"
    "buffer_load_dwordx4 v[184:187], v84, s[36:39], s55 offen offset:2048 sc1\n\t"
    "v_mfma_f32_16x16x32_bf16 v[28:31], a[172:175], v[136:139], v[28:31]\n\t"
    "s_waitcnt vmcnt(9)\n\t"
    "v_mfma_f32_16x16x32_bf16 v[16:19], a[208:211], v[144:147], v[16:19]\n\t"
    "ds_write_b128 v92, v[8:11] offset:6144\n\t"
    "v_mfma_f32_16x16x32_bf16 v[16:19], a[224:227], v[148:151], v[16:19]\n\t"
    "buffer_load_dwordx4 v[188:191], v84, s[36:39], s55 offen offset:3072 sc1\n\t"
    "v_mfma_f32_16x16x32_bf16 v[20:23], a[228:231], v[148:151], v[20:23]\n\t"
    "s_lshl_b32 s68, s60, 18\n\t"
    "v_mfma_f32_16x16x32_bf16 v[20:23], a[212:215], v[144:147], v[20:23]\n\t"
    "ds_write_b128 v92, v[12:15] offset:7168\n\t"
    "v_mfma_f32_16x16x32_bf16 v[24:27], a[216:219], v[144:147], v[24:27]\n\t"
    "v_mfma_f32_16x16x32_bf16 v[24:27], a[232:235], v[148:151], v[24:27]\n\t"
    "buffer_load_dwordx4 v[48:51], v84, s[36:39], s9 offen offset:2048 sc1\n\t"
    "v_mfma_f32_16x16x32_bf16 v[28:31], a[236:239], v[148:151], v[28:31]\n\t"
    "buffer_load_dwordx4 v[52:55], v84, s[36:39], s9 offen offset:3072 sc1\n\t"
    "v_mfma_f32_16x16x32_bf16 v[28:31], a[220:223], v[144:147], v[28:31]\n\t"
    "s_waitcnt vmcnt(10)\n\t"
    "v_mfma_f32_16x16x32_bf16 v[16:19], a[128:131], v[152:155], v[16:19]\n\t"
    "v_mfma_f32_16x16x32_bf16 v[16:19], a[144:147], v[156:159], v[16:19]\n\t"
    "buffer_load_dwordx4 v[56:59], v85, s[36:39], s52 offen offset:0 sc1\n\t"
    "v_mfma_f32_16x16x32_bf16 v[20:23], a[148:151], v[156:159], v[20:23]\n\t"
    "v_mfma_f32_16x16x32_bf16 v[20:23], a[132:135], v[152:155], v[20:23]\n\t"
    "s_add_u32 s25, s25, s68\n\t"
    "v_mfma_f32_16x16x32_bf16 v[24:27], a[136:139], v[152:155], v[24:27]\n\t"
    "s_waitcnt lgkmcnt(0)\n\t"
    "s_barrier\n\t"
    "v_mfma_f32_16x16x32_bf16 v[24:27], a[152:155], v[156:159], v[24:27]\n\t"
    "s_add_u32 s25, s25, 0x400000\n\t"
    "v_mfma_f32_16x16x32_bf16 v[28:31], a[156:159], v[156:159], v[28:31]\n\t"
    "buffer_load_dwordx4 v[60:63], v85, s[36:39], s52 offen offset:1024 sc1\n\t"
    "v_mfma_f32_16x16x32_bf16 v[28:31], a[140:143], v[152:155], v[28:31]\n\t"
    "s_waitcnt vmcnt(10)\n\t"
    "v_mfma_f32_16x16x32_bf16 v[16:19], a[0:3], v[160:163], v[16:19]\n\t"
    "v_mfma_f32_16x16x32_bf16 v[16:19], a[16:19], v[164:167], v[16:19]\n\t"
    "v_mfma_f32_16x16x32_bf16 v[20:23], a[20:23], v[164:167], v[20:23]\n\t"
    "buffer_load_dwordx4 v[224:227], v85, s[36:39], s52 offen offset:2048 sc1\n\t"
    "v_mfma_f32_16x16x32_bf16 v[20:23], a[4:7], v[160:163], v[20:23]\n\t"
    "v_mfma_f32_16x16x32_bf16 v[24:27], a[8:11], v[160:163], v[24:27]\n\t"
    "buffer_load_dwordx4 v[228:231], v85, s[36:39], s52 offen offset:3072 sc1\n\t"
    "v_mfma_f32_16x16x32_bf16 v[24:27], a[24:27], v[164:167], v[24:27]\n\t"
    "v_mfma_f32_16x16x32_bf16 v[28:31], a[28:31], v[164:167], v[28:31]\n\t"
    "ds_read_b128 v[12:15], v93 offset:7168\n\t"
    "v_mfma_f32_16x16x32_bf16 v[28:31], a[12:15], v[160:163], v[28:31]\n\t"
    "s_waitcnt vmcnt(10)\n\t"
    "v_mfma_f32_16x16x32_bf16 v[16:19], a[32:35], v[168:171], v[16:19]\n\t"
    "ds_read_b128 v[4:7], v93 offset:5120\n\t"
    "v_mfma_f32_16x16x32_bf16 v[16:19], a[48:51], v[172:175], v[16:19]\n\t"
    "buffer_load_dwordx4 v[232:235], v85, s[36:39], s53 offen offset:0 sc1\n\t"
    "v_mfma_f32_16x16x32_bf16 v[20:23], a[52:55], v[172:175], v[20:23]\n\t"
    "buffer_load_dwordx4 v[236:239], v85, s[36:39], s53 offen offset:1024 sc1\n\t"
    "v_mfma_f32_16x16x32_bf16 v[20:23], a[36:39], v[168:171], v[20:23]\n\t"
    "v_mfma_f32_16x16x32_bf16 v[24:27], a[40:43], v[168:171], v[24:27]\n\t"
    "v_lshl_add_u32 v86, v73, 14, s25\n\t"
    "v_mfma_f32_16x16x32_bf16 v[24:27], a[56:59], v[172:175], v[24:27]\n\t"
    "buffer_load_dwordx4 v[240:243], v85, s[36:39], s53 offen offset:2048 sc1\n\t"
    "v_mfma_f32_16x16x32_bf16 v[28:31], a[60:63], v[172:175], v[28:31]\n\t"
    "v_lshl_add_u32 v86, v74, 4, v86\n\t"
    "v_mfma_f32_16x16x32_bf16 v[28:31], a[44:47], v[168:171], v[28:31]\n\t"
    "s_waitcnt vmcnt(11)\n\t"
    "v_mfma_f32_16x16x32_bf16 v[16:19], a[64:67], v[176:179], v[16:19]\n\t"
    "ds_read_b128 v[8:11], v93 offset:6144\n\t"
    "v_mfma_f32_16x16x32_bf16 v[16:19], a[80:83], v[180:183], v[16:19]\n\t"
    "buffer_load_dwordx4 v[244:247], v85, s[36:39], s53 offen offset:3072 sc1\n\t"
    "v_mfma_f32_16x16x32_bf16 v[20:23], a[84:87], v[180:183], v[20:23]\n\t"
    "v_mfma_f32_16x16x32_bf16 v[20:23], a[68:71], v[176:179], v[20:23]\n\t"
    "v_add_u32 v75, 0x2000, v86\n\t"
    "v_mfma_f32_16x16x32_bf16 v[24:27], a[72:75], v[176:179], v[24:27]\n\t"
    "buffer_load_dwordx4 v[248:251], v85, s[36:39], s54 offen offset:0 sc1\n\t"
    "v_mfma_f32_16x16x32_bf16 v[24:27], a[88:91], v[180:183], v[24:27]\n\t"
    "ds_read_b128 v[0:3], v93 offset:4096\n\t"
    "v_mfma_f32_16x16x32_bf16 v[28:31], a[92:95], v[180:183], v[28:31]\n\t"
    "v_mfma_f32_16x16x32_bf16 v[28:31], a[76:79], v[176:179], v[28:31]\n\t"
    "s_waitcnt vmcnt(11)\n\t"
    "v_mfma_f32_16x16x32_bf16 v[16:19], a[96:99], v[184:187], v[16:19]\n\t"
    "buffer_load_dwordx4 v[252:255], v85, s[36:39], s54 offen offset:1024 sc1\n\t"
    "v_mfma_f32_16x16x32_bf16 v[16:19], a[112:115], v[188:191], v[16:19]\n\t"
    "v_mfma_f32_16x16x32_bf16 v[20:23], a[116:119], v[188:191], v[20:23]\n\t"
    "buffer_load_dwordx4 v[96:99], v85, s[36:39], s54 offen offset:2048 sc1\n\t"
    "v_mfma_f32_16x16x32_bf16 v[20:23], a[100:103], v[184:187], v[20:23]\n\t"
    "v_mfma_f32_16x16x32_bf16 v[24:27], a[104:107], v[184:187], v[24:27]\n\t"
    "s_lshl_b32 s25, s26, 15\n\t"
    "v_mfma_f32_16x16x32_bf16 v[24:27], a[120:123], v[188:191], v[24:27]\n\t"
    "v_mfma_f32_16x16x32_bf16 v[28:31], a[124:127], v[188:191], v[28:31]\n\t"
    "buffer_load_dwordx4 v[100:103], v85, s[36:39], s54 offen offset:3072 sc1\n\t"
    "v_mfma_f32_16x16x32_bf16 v[28:31], a[108:111], v[184:187], v[28:31]\n\t"
    "s_waitcnt vmcnt(12)\n\t"
    "v_mfma_f32_16x16x32_bf16 v[16:19], v[192:195], v[48:51], v[16:19]\n\t"
    "s_lshl_b32 s26, s60, 7\n\t"
    "v_mfma_f32_16x16x32_bf16 v[16:19], v[208:211], v[52:55], v[16:19]\n\t"
    "buffer_load_dwordx4 v[104:107], v85, s[36:39], s55 offen offset:0 sc1\n\t"
    "v_mfma_f32_16x16x32_bf16 v[20:23], v[212:215], v[52:55], v[20:23]\n\t"
    "v_mfma_f32_16x16x32_bf16 v[20:23], v[196:199], v[48:51], v[20:23]\n\t"
    "v_mfma_f32_16x16x32_bf16 v[24:27], v[200:203], v[48:51], v[24:27]\n\t"
    "v_mfma_f32_16x16x32_bf16 v[24:27], v[216:219], v[52:55], v[24:27]\n\t"
    "v_mfma_f32_16x16x32_bf16 v[28:31], v[220:223], v[52:55], v[28:31]\n\t"
    "buffer_load_dwordx4 v[108:111], v85, s[36:39], s55 offen offset:1024 sc1\n\t"
    "v_mfma_f32_16x16x32_bf16 v[28:31], v[204:207], v[48:51], v[28:31]\n\t"
    "s_waitcnt vmcnt(12)\n\t"
    "s_waitcnt lgkmcnt(0)\n\t"
    "v_mfma_f32_16x16x32_bf16 v[0:3], a[192:195], v[56:59], v[0:3]\n\t"
    "buffer_load_dwordx4 v[112:115], v85, s[36:39], s55 offen offset:2048 sc1\n\t"
    "v_mfma_f32_16x16x32_bf16 v[0:3], a[240:243], v[60:63], v[0:3]\n\t"
    "v_mfma_f32_16x16x32_bf16 v[4:7], a[244:247], v[60:63], v[4:7]\n\t"
    "v_mfma_f32_16x16x32_bf16 v[4:7], a[196:199], v[56:59], v[4:7]\n\t"
    "v_mfma_f32_16x16x32_bf16 v[8:11], a[200:203], v[56:59], v[8:11]\n\t"
    "buffer_load_dwordx4 v[116:119], v85, s[36:39], s55 offen offset:3072 sc1\n\t"
    "v_mfma_f32_16x16x32_bf16 v[8:11], a[248:251], v[60:63], v[8:11]\n\t"
    "ds_write_b128 v92, v[16:19] offset:0\n\t"
    "v_mfma_f32_16x16x32_bf16 v[12:15], a[252:255], v[60:63], v[12:15]\n\t"
    "global_load_dword v88, v87, s[84:85] sc1\n\t"
    "v_mfma_f32_16x16x32_bf16 v[12:15], a[204:207], v[56:59], v[12:15]\n\t"
    "s_waitcnt vmcnt(13)\n\t"
    "v_mfma_f32_16x16x32_bf16 v[0:3], a[160:163], v[224:227], v[0:3]\n\t"
    "buffer_load_dwordx4 v[120:123], v85, s[36:39], s9 offen offset:2048 sc1\n\t"
    "v_mfma_f32_16x16x32_bf16 v[0:3], a[176:179], v[228:231], v[0:3]\n\t"
    "s_add_u32 s25, s25, s26\n\t"
    "v_mfma_f32_16x16x32_bf16 v[4:7], a[180:183], v[228:231], v[4:7]\n\t"
    "buffer_load_dwordx4 v[124:127], v85, s[36:39], s9 offen offset:3072 sc1\n\t"
    "v_mfma_f32_16x16x32_bf16 v[4:7], a[164:167], v[224:227], v[4:7]\n\t"
    "ds_write_b128 v92, v[20:23] offset:1024\n\t"
    "v_mfma_f32_16x16x32_bf16 v[8:11], a[168:171], v[224:227], v[8:11]\n\t"
    "v_mfma_f32_16x16x32_bf16 v[8:11], a[184:187], v[228:231], v[8:11]\n\t"
    "buffer_load_dwordx4 v[128:131], v82, s[36:39], s52 offen offset:0 sc1\n\t"
    "v_mfma_f32_16x16x32_bf16 v[12:15], a[188:191], v[228:231], v[12:15]\n\t"
    "ds_write_b128 v92, v[24:27] offset:2048\n\t"
    "v_mfma_f32_16x16x32_bf16 v[12:15], a[172:175], v[224:227], v[12:15]\n\t"
    "s_waitcnt vmcnt(14)\n\t"
    "v_mfma_f32_16x16x32_bf16 v[0:3], a[208:211], v[232:235], v[0:3]\n\t"
    "v_mfma_f32_16x16x32_bf16 v[0:3], a[224:227], v[236:239], v[0:3]\n\t"
    "v_mfma_f32_16x16x32_bf16 v[4:7], a[228:231], v[236:239], v[4:7]\n\t"
    "buffer_load_dwordx4 v[132:135], v82, s[36:39], s52 offen offset:1024 sc1\n\t"
    "v_mfma_f32_16x16x32_bf16 v[4:7], a[212:215], v[232:235], v[4:7]\n\t"
    "ds_write_b128 v92, v[28:31] offset:3072\n\t"
    "v_mfma_f32_16x16x32_bf16 v[8:11], a[216:219], v[232:235], v[8:11]\n\t"
    "s_add_u32 s25, s25, 0x1000\n\t"
    "v_mfma_f32_16x16x32_bf16 v[8:11], a[232:235], v[236:239], v[8:11]\n\t"
    "buffer_load_dwordx4 v[136:139], v82, s[36:39], s52 offen offset:2048 sc1\n\t"
    "v_mfma_f32_16x16x32_bf16 v[12:15], a[236:239], v[236:239], v[12:15]\n\t"
    "v_mfma_f32_16x16x32_bf16 v[12:15], a[220:223], v[232:235], v[12:15]\n\t"
    "s_waitcnt vmcnt(14)\n\t"
    "v_mfma_f32_16x16x32_bf16 v[0:3], a[128:131], v[240:243], v[0:3]\n\t"
    "buffer_load_dwordx4 v[140:143], v82, s[36:39], s52 offen offset:3072 sc1\n\t"
    "v_mfma_f32_16x16x32_bf16 v[0:3], a[144:147], v[244:247], v[0:3]\n\t"
    "v_mfma_f32_16x16x32_bf16 v[4:7], a[148:151], v[244:247], v[4:7]\n\t"
    "v_mfma_f32_16x16x32_bf16 v[4:7], a[132:135], v[240:243], v[4:7]\n\t"
    "buffer_load_dwordx4 v[144:147], v82, s[36:39], s53 offen offset:0 sc1\n\t"
    "v_mfma_f32_16x16x32_bf16 v[8:11], a[136:139], v[240:243], v[8:11]\n\t"
    "s_waitcnt lgkmcnt(0)\n\t"
    "s_barrier\n\t"
    "v_mfma_f32_16x16x32_bf16 v[8:11], a[152:155], v[244:247], v[8:11]\n\t"
    "buffer_load_dwordx4 v[148:151], v82, s[36:39], s53 offen offset:1024 sc1\n\t"
    "v_mfma_f32_16x16x32_bf16 v[12:15], a[156:159], v[244:247], v[12:15]\n\t"
    "ds_read_b128 v[16:19], v93 offset:0\n\t"
    "v_mfma_f32_16x16x32_bf16 v[12:15], a[140:143], v[240:243], v[12:15]\n\t"
    "s_waitcnt vmcnt(15)\n\t"
    "v_mfma_f32_16x16x32_bf16 v[0:3], a[0:3], v[248:251], v[0:3]\n\t"
    "ds_read_b128 v[24:27], v93 offset:2048\n\t"
    "v_mfma_f32_16x16x32_bf16 v[0:3], a[16:19], v[252:255], v[0:3]\n\t"
    "s_add_u32 s68, s50, s25\n\t"
    "s_addc_u32 s69, s51, 0\n\t"
    "v_mfma_f32_16x16x32_bf16 v[4:7], a[20:23], v[252:255], v[4:7]\n\t"
    "v_mfma_f32_16x16x32_bf16 v[4:7], a[4:7], v[248:251], v[4:7]\n\t"
    "buffer_load_dwordx4 v[152:155], v82, s[36:39], s53 offen offset:2048 sc1\n\t"
    "v_mfma_f32_16x16x32_bf16 v[8:11], a[8:11], v[248:251], v[8:11]\n\t"
    "buffer_load_dwordx4 v[156:159], v82, s[36:39], s53 offen offset:3072 sc1\n\t"
    "v_mfma_f32_16x16x32_bf16 v[8:11], a[24:27], v[252:255], v[8:11]\n\t"
    "v_mfma_f32_16x16x32_bf16 v[12:15], a[28:31], v[252:255], v[12:15]\n\t"
    "v_mfma_f32_16x16x32_bf16 v[12:15], a[12:15], v[248:251], v[12:15]\n\t"
    "s_waitcnt vmcnt(15)\n\t"
    "v_mfma_f32_16x16x32_bf16 v[0:3], a[32:35], v[96:99], v[0:3]\n\t"
    "buffer_load_dwordx4 v[160:163], v82, s[36:39], s54 offen offset:0 sc1\n\t"
    "v_mfma_f32_16x16x32_bf16 v[0:3], a[48:51], v[100:103], v[0:3]\n\t"
    "v_mfma_f32_16x16x32_bf16 v[4:7], a[52:55], v[100:103], v[4:7]\n\t"
    "v_lshlrev_b32 v71, 13, v74\n\t"
    "v_mfma_f32_16x16x32_bf16 v[4:7], a[36:39], v[96:99], v[4:7]\n\t"
    "ds_read_b128 v[28:31], v93 offset:3072\n\t"
    "v_mfma_f32_16x16x32_bf16 v[8:11], a[40:43], v[96:99], v[8:11]\n\t"
    "buffer_load_dwordx4 v[164:167], v82, s[36:39], s54 offen offset:1024 sc1\n\t"
    "v_mfma_f32_16x16x32_bf16 v[8:11], a[56:59], v[100:103], v[8:11]\n\t"
    "v_mfma_f32_16x16x32_bf16 v[12:15], a[60:63], v[100:103], v[12:15]\n\t"
    "buffer_load_dwordx4 v[168:171], v82, s[36:39], s54 offen offset:2048 sc1\n\t"
    "v_mfma_f32_16x16x32_bf16 v[12:15], a[44:47], v[96:99], v[12:15]\n\t"
    "s_waitcnt vmcnt(16)\n\t"
    "v_mfma_f32_16x16x32_bf16 v[0:3], a[64:67], v[104:107], v[0:3]\n\t"
    "v_mfma_f32_16x16x32_bf16 v[0:3], a[80:83], v[108:111], v[0:3]\n\t"
    "v_mfma_f32_16x16x32_bf16 v[4:7], a[84:87], v[108:111], v[4:7]\n\t"
    "v_mfma_f32_16x16x32_bf16 v[4:7], a[68:71], v[104:107], v[4:7]\n\t"
    "buffer_load_dwordx4 v[172:175], v82, s[36:39], s54 offen offset:3072 sc1\n\t"
    "v_mfma_f32_16x16x32_bf16 v[8:11], a[72:75], v[104:107], v[8:11]\n\t"
    "buffer_load_dwordx4 v[176:179], v82, s[36:39], s55 offen offset:0 sc1\n\t"
    "v_mfma_f32_16x16x32_bf16 v[8:11], a[88:91], v[108:111], v[8:11]\n\t"
    "v_mfma_f32_16x16x32_bf16 v[12:15], a[92:95], v[108:111], v[12:15]\n\t"
    "v_mfma_f32_16x16x32_bf16 v[12:15], a[76:79], v[104:107], v[12:15]\n\t"
    "s_waitcnt vmcnt(16)\n\t"
    "v_mfma_f32_16x16x32_bf16 v[0:3], a[96:99], v[112:115], v[0:3]\n\t"
    "v_mfma_f32_16x16x32_bf16 v[0:3], a[112:115], v[116:119], v[0:3]\n\t"
    "buffer_load_dwordx4 v[180:183], v82, s[36:39], s55 offen offset:1024 sc1\n\t"
    "v_mfma_f32_16x16x32_bf16 v[4:7], a[116:119], v[116:119], v[4:7]\n\t"
    "v_lshl_add_u32 v71, v73, 3, v71\n\t"
    "v_mfma_f32_16x16x32_bf16 v[4:7], a[100:103], v[112:115], v[4:7]\n\t"
    "ds_read_b128 v[20:23], v93 offset:1024\n\t"
    "v_mfma_f32_16x16x32_bf16 v[8:11], a[104:107], v[112:115], v[8:11]\n\t"
    "v_mfma_f32_16x16x32_bf16 v[8:11], a[120:123], v[116:119], v[8:11]\n\t"
    "buffer_load_dwordx4 v[184:187], v82, s[36:39], s55 offen offset:2048 sc1\n\t"
    "v_mfma_f32_16x16x32_bf16 v[12:15], a[124:127], v[116:119], v[12:15]\n\t"
    "v_mfma_f32_16x16x32_bf16 v[12:15], a[108:111], v[112:115], v[12:15]\n\t"
    "s_waitcnt vmcnt(15)\n\t"
    "v_mfma_f32_16x16x32_bf16 v[0:3], v[192:195], v[120:123], v[0:3]\n\t"
    "buffer_load_dwordx4 v[188:191], v82, s[36:39], s55 offen offset:3072 sc1\n\t"
    "v_mfma_f32_16x16x32_bf16 v[0:3], v[208:211], v[124:127], v[0:3]\n\t"
    "v_mfma_f32_16x16x32_bf16 v[4:7], v[212:215], v[124:127], v[4:7]\n\t"
    "buffer_load_dwordx4 v[48:51], v82, s[36:39], s9 offen offset:2048 sc1\n\t"
    "v_mfma_f32_16x16x32_bf16 v[4:7], v[196:199], v[120:123], v[4:7]\n\t"
    "v_cmp_gt_u32 vcc, 32, v88\n\t"
    "v_mfma_f32_16x16x32_bf16 v[8:11], v[200:203], v[120:123], v[8:11]\n\t"
    "s_cbranch_vccnz L_pY0_%=\n\t"
    "L_rY0_%=:\n\t"
    "v_mfma_f32_16x16x32_bf16 v[8:11], v[216:219], v[124:127], v[8:11]\n\t"
    "global_load_dwordx4 v[240:243], v86, s[84:85] offset:0 sc1\n\t"
    "v_mfma_f32_16x16x32_bf16 v[12:15], v[220:223], v[124:127], v[12:15]\n\t"
    "buffer_load_dwordx4 v[52:55], v82, s[36:39], s9 offen offset:3072 sc1\n\t"
    "v_mfma_f32_16x16x32_bf16 v[12:15], v[204:207], v[120:123], v[12:15]\n\t"
    "s_waitcnt vmcnt(17)\n\t"
    "s_waitcnt lgkmcnt(1)\n\t"
    "v_mfma_f32_16x16x32_bf16 v[16:19], a[192:195], v[128:131], v[16:19]\n\t"
    "global_load_dwordx4 v[244:247], v75, s[84:85] offset:0 sc1\n\t"
    "v_mfma_f32_16x16x32_bf16 v[16:19], a[240:243], v[132:135], v[16:19]\n\t"
    "s_waitcnt lgkmcnt(0)\n\t"
    "v_mfma_f32_16x16x32_bf16 v[20:23], a[244:247], v[132:135], v[20:23]\n\t"
    "v_mfma_f32_16x16x32_bf16 v[20:23], a[196:199], v[128:131], v[20:23]\n\t"
    "global_load_dwordx4 v[224:227], v70, s[84:85] offset:0 sc1\n\t"
    "v_mfma_f32_16x16x32_bf16 v[24:27], a[200:203], v[128:131], v[24:27]\n\t"
    "global_load_dwordx4 v[228:231], v70, s[84:85] offset:1024 sc1\n\t"
    "v_mfma_f32_16x16x32_bf16 v[24:27], a[248:251], v[132:135], v[24:27]\n\t"
    "ds_write_b128 v92, v[0:3] offset:4096\n\t"
    "v_mfma_f32_16x16x32_bf16 v[28:31], a[252:255], v[132:135], v[28:31]\n\t"
    "v_mfma_f32_16x16x32_bf16 v[28:31], a[204:207], v[128:131], v[28:31]\n\t"
    "s_waitcnt vmcnt(18)\n\t"
    "v_mfma_f32_16x16x32_bf16 v[16:19], a[160:163], v[136:139], v[16:19]\n\t"
    "global_load_dwordx4 v[232:235], v70, s[84:85] offset:2048 sc1\n\t"
    "v_mfma_f32_16x16x32_bf16 v[16:19], a[176:179], v[140:143], v[16:19]\n\t"
    "v_mfma_f32_16x16x32_bf16 v[20:23], a[180:183], v[140:143], v[20:23]\n\t"
    "ds_write_b128 v92, v[4:7] offset:5120\n\t"
    "v_mfma_f32_16x16x32_bf16 v[20:23], a[164:167], v[136:139], v[20:23]\n\t"
    "global_load_dwordx4 v[236:239], v70, s[84:85] offset:3072 sc1\n\t"
    "v_mfma_f32_16x16x32_bf16 v[24:27], a[168:171], v[136:139], v[24:27]\n\t"
    "v_mfma_f32_16x16x32_bf16 v[24:27], a[184:187], v[140:143], v[24:27]\n\t"
    "ds_write_b128 v92, v[8:11] offset:6144\n\t"
    "v_mfma_f32_16x16x32_bf16 v[28:31], a[188:191], v[140:143], v[28:31]\n\t"
    "v_mfma_f32_16x16x32_bf16 v[28:31], a[172:175], v[136:139], v[28:31]\n\t"
    "s_waitcnt vmcnt(18)\n\t"
    "v_mfma_f32_16x16x32_bf16 v[16:19], a[208:211], v[144:147], v[16:19]\n\t"
    "v_mfma_f32_16x16x32_bf16 v[16:19], a[224:227], v[148:151], v[16:19]\n\t"
    "v_mfma_f32_16x16x32_bf16 v[20:23], a[228:231], v[148:151], v[20:23]\n\t"
    "v_mfma_f32_16x16x32_bf16 v[20:23], a[212:215], v[144:147], v[20:23]\n\t"
    "v_mfma_f32_16x16x32_bf16 v[24:27], a[216:219], v[144:147], v[24:27]\n\t"
    "ds_write_b128 v92, v[12:15] offset:7168\n\t"
    "v_mfma_f32_16x16x32_bf16 v[24:27], a[232:235], v[148:151], v[24:27]\n\t"
    "v_mfma_f32_16x16x32_bf16 v[28:31], a[236:239], v[148:151], v[28:31]\n\t"
    "v_mfma_f32_16x16x32_bf16 v[28:31], a[220:223], v[144:147], v[28:31]\n\t"
    "s_waitcnt vmcnt(16)\n\t"
    "v_mfma_f32_16x16x32_bf16 v[16:19], a[128:131], v[152:155], v[16:19]\n\t"
    "v_mfma_f32_16x16x32_bf16 v[16:19], a[144:147], v[156:159], v[16:19]\n\t"
    "v_mfma_f32_16x16x32_bf16 v[20:23], a[148:151], v[156:159], v[20:23]\n\t"
    "v_mfma_f32_16x16x32_bf16 v[20:23], a[132:135], v[152:155], v[20:23]\n\t"
    "v_mfma_f32_16x16x32_bf16 v[24:27], a[136:139], v[152:155], v[24:27]\n\t"
    "s_waitcnt lgkmcnt(0)\n\t"
    "s_barrier\n\t"
    "v_mfma_f32_16x16x32_bf16 v[24:27], a[152:155], v[156:159], v[24:27]\n\t"
    "v_mfma_f32_16x16x32_bf16 v[28:31], a[156:159], v[156:159], v[28:31]\n\t"
    "v_mfma_f32_16x16x32_bf16 v[28:31], a[140:143], v[152:155], v[28:31]\n\t"
    "s_waitcnt vmcnt(14)\n\t"
    "v_mfma_f32_16x16x32_bf16 v[16:19], a[0:3], v[160:163], v[16:19]\n\t"
    "v_mfma_f32_16x16x32_bf16 v[16:19], a[16:19], v[164:167], v[16:19]\n\t"
    "v_mfma_f32_16x16x32_bf16 v[20:23], a[20:23], v[164:167], v[20:23]\n\t"
    "v_mfma_f32_16x16x32_bf16 v[20:23], a[4:7], v[160:163], v[20:23]\n\t"
    "ds_read_b128 v[32:35], v94 offset:4096\n\t"
    "v_mfma_f32_16x16x32_bf16 v[24:27], a[8:11], v[160:163], v[24:27]\n\t"
    "v_mfma_f32_16x16x32_bf16 v[24:27], a[24:27], v[164:167], v[24:27]\n\t"
    "ds_read_b128 v[44:47], v94 offset:7168\n\t"
    "v_mfma_f32_16x16x32_bf16 v[28:31], a[28:31], v[164:167], v[28:31]\n\t"
    "v_mfma_f32_16x16x32_bf16 v[28:31], a[12:15], v[160:163], v[28:31]\n\t"
    "s_waitcnt vmcnt(12)\n\t"
    "v_mfma_f32_16x16x32_bf16 v[16:19], a[32:35], v[168:171], v[16:19]\n\t"
    "v_mfma_f32_16x16x32_bf16 v[16:19], a[48:51], v[172:175], v[16:19]\n\t"
    "ds_read_b128 v[36:39], v94 offset:5120\n\t"
    "v_mfma_f32_16x16x32_bf16 v[20:23], a[52:55], v[172:175], v[20:23]\n\t"
    "ds_read_b128 v[40:43], v94 offset:6144\n\t"
    "v_mfma_f32_16x16x32_bf16 v[20:23], a[36:39], v[168:171], v[20:23]\n\t"
    "v_mfma_f32_16x16x32_bf16 v[24:27], a[40:43], v[168:171], v[24:27]\n\t"
    "v_mfma_f32_16x16x32_bf16 v[24:27], a[56:59], v[172:175], v[24:27]\n\t"
    "v_mfma_f32_16x16x32_bf16 v[28:31], a[60:63], v[172:175], v[28:31]\n\t"
    "v_mfma_f32_16x16x32_bf16 v[28:31], a[44:47], v[168:171], v[28:31]\n\t"
    "s_waitcnt vmcnt(10)\n\t"
    "v_mfma_f32_16x16x32_bf16 v[16:19], a[64:67], v[176:179], v[16:19]\n\t"
    "v_mfma_f32_16x16x32_bf16 v[16:19], a[80:83], v[180:183], v[16:19]\n\t"
    "v_mfma_f32_16x16x32_bf16 v[20:23], a[84:87], v[180:183], v[20:23]\n\t"
    "v_mfma_f32_16x16x32_bf16 v[20:23], a[68:71], v[176:179], v[20:23]\n\t"
    "v_mfma_f32_16x16x32_bf16 v[24:27], a[72:75], v[176:179], v[24:27]\n\t"
    "v_mfma_f32_16x16x32_bf16 v[24:27], a[88:91], v[180:183], v[24:27]\n\t"
    "v_mfma_f32_16x16x32_bf16 v[28:31], a[92:95], v[180:183], v[28:31]\n\t"
    "v_mfma_f32_16x16x32_bf16 v[28:31], a[76:79], v[176:179], v[28:31]\n\t"
    "s_waitcnt vmcnt(8)\n\t"
    "v_mfma_f32_16x16x32_bf16 v[16:19], a[96:99], v[184:187], v[16:19]\n\t"
    "s_waitcnt vmcnt(4)\n\t"
    "v_mfma_f32_16x16x32_bf16 v[16:19], a[112:115], v[188:191], v[16:19]\n\t"
    "v_mfma_f32_16x16x32_bf16 v[20:23], a[116:119], v[188:191], v[20:23]\n\t"
    "v_swap_b32 v241, v244\n\t"
    "v_mfma_f32_16x16x32_bf16 v[20:23], a[100:103], v[184:187], v[20:23]\n\t"
    "v_swap_b32 v243, v246\n\t"
    "v_mfma_f32_16x16x32_bf16 v[24:27], a[104:107], v[184:187], v[24:27]\n\t"
    "global_store_dwordx2 v71, v[240:241], s[68:69] offset:-4096\n\t"
    "v_mfma_f32_16x16x32_bf16 v[24:27], a[120:123], v[188:191], v[24:27]\n\t"
    "v_mfma_f32_16x16x32_bf16 v[28:31], a[124:127], v[188:191], v[28:31]\n\t"
    "global_store_dwordx2 v71, v[244:245], s[68:69] offset:-2048\n\t"
    "s_waitcnt vmcnt(2)\n\t"
    "v_mfma_f32_16x16x32_bf16 v[28:31], a[108:111], v[184:187], v[28:31]\n\t"
    "s_waitcnt lgkmcnt(0)\n\t"
    "v_add_f32 v46, v46, v238\n\t"
    "v_add_f32 v47, v47, v239\n\t"
    "v_mfma_f32_16x16x32_bf16 v[16:19], v[192:195], v[48:51], v[16:19]\n\t"
    "v_add_f32 v36, v36, v228\n\t"
    "v_add_f32 v37, v37, v229\n\t"
    "v_mfma_f32_16x16x32_bf16 v[16:19], v[208:211], v[52:55], v[16:19]\n\t"
    "v_add_f32 v44, v44, v236\n\t"
    "v_add_f32 v45, v45, v237\n\t"
    "v_mfma_f32_16x16x32_bf16 v[20:23], v[212:215], v[52:55], v[20:23]\n\t"
    "v_add_f32 v32, v32, v224\n\t"
    "v_add_f32 v33, v33, v225\n\t"
    "v_add_f32 v34, v34, v226\n\t"
    "v_add_f32 v35, v35, v227\n\t"
    "v_mfma_f32_16x16x32_bf16 v[20:23], v[196:199], v[48:51], v[20:23]\n\t"
    "global_store_dwordx2 v71, v[242:243], s[68:69] offset:0\n\t"
    "v_mfma_f32_16x16x32_bf16 v[24:27], v[200:203], v[48:51], v[24:27]\n\t"
    "v_add_f32 v40, v40, v232\n\t"
    "v_add_f32 v41, v41, v233\n\t"
    "v_mfma_f32_16x16x32_bf16 v[24:27], v[216:219], v[52:55], v[24:27]\n\t"
    "v_add_f32 v42, v42, v234\n\t"
    "v_add_f32 v43, v43, v235\n\t"
    "v_mfma_f32_16x16x32_bf16 v[28:31], v[220:223], v[52:55], v[28:31]\n\t"
    "global_store_dwordx2 v71, v[246:247], s[68:69] offset:2048\n\t"
    "v_mfma_f32_16x16x32_bf16 v[28:31], v[204:207], v[48:51], v[28:31]\n\t"
    "v_add_f32 v38, v38, v230\n\t"
    "v_add_f32 v39, v39, v231\n\t"
    "v_pk_add_f32 v[32:33], v[32:33], v[16:17]\n\t"
    "v_pk_add_f32 v[34:35], v[34:35], v[18:19]\n\t"
    "v_pk_add_f32 v[36:37], v[36:37], v[20:21]\n\t"
    "v_pk_add_f32 v[38:39], v[38:39], v[22:23]\n\t"
    "v_pk_add_f32 v[40:41], v[40:41], v[24:25]\n\t"
    "v_pk_add_f32 v[42:43], v[42:43], v[26:27]\n\t"
    "v_pk_add_f32 v[44:45], v[44:45], v[28:29]\n\t"
    "v_pk_add_f32 v[46:47], v[46:47], v[30:31]\n\t"
    "v_cvt_pk_bf16_f32 v0, v32, v33\n\t"
    "v_cvt_pk_bf16_f32 v1, v34, v35\n\t"
    "v_cvt_pk_bf16_f32 v2, v36, v37\n\t"
    "v_cvt_pk_bf16_f32 v3, v38, v39\n\t"
    "v_cvt_pk_bf16_f32 v4, v40, v41\n\t"
    "v_cvt_pk_bf16_f32 v5, v42, v43\n\t"
    "v_cvt_pk_bf16_f32 v6, v44, v45\n\t"
    "v_cvt_pk_bf16_f32 v7, v46, v47\n\t"
    "global_store_dwordx4 v69, v[0:3], s[12:13]\n\t"
    "global_store_dwordx4 v69, v[4:7], s[12:13] offset:64\n\t"
    "s_endpgm\n\t"
    "L_lead_%=:\n\t"
    "s_lshl_b32 s56, s58, 13\n\t"
    "v_lshlrev_b32 v245, 7, v246\n\t"
    "s_mov_b64 s[94:95], -1\n\t"
    "s_cmp_lg_u32 s58, 0\n\t"
    "s_cbranch_scc1 L_l2_%=\n\t"
    "s_mov_b64 s[94:95], -2\n\t"
    "v_mov_b32 v251, 0\n\t"
    "v_add_u32 v252, 0x8000, v245\n\t"
    "global_store_dword v252, v251, s[84:85] sc1\n\t"
    "v_add_u32 v252, 0xa000, v245\n\t"
    "global_store_dword v252, v251, s[84:85] sc1\n\t"
    "v_add_u32 v252, 0xc000, v245\n\t"
    "global_store_dword v252, v251, s[84:85] sc1\n\t"
    "v_add_u32 v252, 0xe000, v245\n\t"
    "global_store_dword v252, v251, s[84:85] sc1\n\t"
    "v_add_u32 v252, 0x10000, v245\n\t"
    "global_store_dword v252, v251, s[84:85] sc1\n\t"
    "v_add_u32 v252, 0x12000, v245\n\t"
    "global_store_dword v252, v251, s[84:85] sc1\n\t"
    "L_l2_%=:\n\t"
    "v_add_u32 v245, s56, v245\n\t"
    "s_branch L_sy_%=\n\t"
    "L_hsM_%=:\n\t"
    "v_add_u32 v251, 1, v251\n\t"
    "s_mov_b32 s92, 131072\n\t"
    "L_hlM_%=:\n\t"
    "v_cmp_ne_u32 vcc, v252, v251\n\t"
    "s_nop 1\n\t"
    "s_and_b64 s[90:91], vcc, s[86:87]\n\t"
    "s_cbranch_scc0 L_hnM_%=\n\t"
    "s_mov_b64 exec, s[90:91]\n\t"
    "global_atomic_add v245, v250, s[84:85] sc1\n\t"
    "s_mov_b64 exec, -1\n\t"
    "s_andn2_b64 s[86:87], s[86:87], s[90:91]\n\t"
    "s_cbranch_scc0 L_hrM_%=\n\t"
    "L_hnM_%=:\n\t"
    "s_sleep 4\n\t"
    "global_load_dword v252, v245, s[84:85] sc1\n\t"
    "s_waitcnt vmcnt(0)\n\t"
    "s_sub_u32 s92, s92, 1\n\t"
    "s_cbranch_scc0 L_hlM_%=\n\t"
    "s_branch L_hrM_%=\n\t"
    "L_hsL_%=:\n\t"
    "v_add_u32 v251, 1, v251\n\t"
    "s_mov_b32 s92, 131072\n\t"
    "L_hlL_%=:\n\t"
    "v_cmp_ne_u32 vcc, v252, v251\n\t"
    "s_nop 1\n\t"
    "s_and_b64 s[90:91], vcc, s[94:95]\n\t"
    "s_cbranch_scc0 L_hnL_%=\n\t"
    "s_mov_b64 exec, s[90:91]\n\t"
    "global_atomic_add v245, v250, s[84:85] sc1\n\t"
    "s_mov_b64 exec, -1\n\t"
    "s_andn2_b64 s[94:95], s[94:95], s[90:91]\n\t"
    "s_cbranch_scc0 L_hrL_%=\n\t"
    "L_hnL_%=:\n\t"
    "s_sleep 4\n\t"
    "global_load_dword v252, v245, s[84:85] sc1\n\t"
    "s_waitcnt vmcnt(0)\n\t"
    "s_sub_u32 s92, s92, 1\n\t"
    "s_cbranch_scc0 L_hlL_%=\n\t"
    "s_branch L_hrL_%=\n\t"
    "L_pU_%=:\n\t"
    "s_mov_b32 s92, 131072\n\t"
    "L_qU_%=:\n\t"
    "s_sleep 2\n\t"
    "global_load_dword v77, v127, s[84:85] sc1\n\t"
    "s_waitcnt vmcnt(0)\n\t"
    "v_readfirstlane_b32 s90, v77\n\t"
    "s_cmp_ge_u32 s90, 32\n\t"
    "s_cbranch_scc1 L_rU_%=\n\t"
    "s_sub_u32 s92, s92, 1\n\t"
    "s_cbranch_scc0 L_qU_%=\n\t"
    "s_branch L_rU_%=\n\t"
    "L_pK_%=:\n\t"
    "s_cmp_gt_u32 s54, 0x1000\n\t"
    "s_cbranch_scc1 L_bK_%=\n\t"
    "s_mov_b32 s92, 131072\n\t"
    "L_qK_%=:\n\t"
    "global_load_dword v229, v249, s[84:85] sc1\n\t"
    "s_waitcnt vmcnt(0)\n\t"
    "v_readfirstlane_b32 s90, v229\n\t"
    "s_cmp_ge_u32 s90, 8\n\t"
    "s_cbranch_scc1 L_aK_%=\n\t"
    "s_sleep 2\n\t"
    "s_sub_u32 s92, s92, 1\n\t"
    "s_cbranch_scc0 L_qK_%=\n\t"
    "L_aK_%=:\n\t"
    "buffer_inv sc1\n\t"
    "L_bK_%=:\n\t"
    "s_waitcnt vmcnt(0)\n\t"
    "s_branch L_rK_%=\n\t"
    "L_pC1_%=:\n\t"
    "s_mov_b32 s92, 131072\n\t"
    "L_qC1_%=:\n\t"
    "global_load_dword v88, v87, s[84:85] sc1\n\t"
    "s_waitcnt vmcnt(0)\n\t"
    "v_readfirstlane_b32 s90, v88\n\t"
    "s_cmp_ge_u32 s90, 16\n\t"
    "s_cbranch_scc1 L_aC1_%=\n\t"
    "s_sleep 2\n\t"
    "s_sub_u32 s92, s92, 1\n\t"
    "s_cbranch_scc0 L_qC1_%=\n\t"
    "L_aC1_%=:\n\t"
    "s_mov_b32 s38, 0xe8000\n\t"
    "buffer_load_dwordx4 v[96:99], v83, s[36:39], s52 offen offset:0 sc1\n\t"
    "buffer_load_dwordx4 v[100:103], v83, s[36:39], s52 offen offset:1024 sc1\n\t"
    "buffer_load_dwordx4 v[104:107], v83, s[36:39], s52 offen offset:2048 sc1\n\t"
    "buffer_load_dwordx4 v[108:111], v83, s[36:39], s52 offen offset:3072 sc1\n\t"
    "buffer_load_dwordx4 v[112:115], v83, s[36:39], s53 offen offset:0 sc1\n\t"
    "buffer_load_dwordx4 v[116:119], v83, s[36:39], s53 offen offset:1024 sc1\n\t"
    "buffer_load_dwordx4 v[120:123], v83, s[36:39], s53 offen offset:2048 sc1\n\t"
    "buffer_load_dwordx4 v[124:127], v83, s[36:39], s53 offen offset:3072 sc1\n\t"
    "buffer_load_dwordx4 v[128:131], v83, s[36:39], s54 offen offset:0 sc1\n\t"
    "buffer_load_dwordx4 v[132:135], v83, s[36:39], s54 offen offset:1024 sc1\n\t"
    "buffer_load_dwordx4 v[136:139], v83, s[36:39], s54 offen offset:2048 sc1\n\t"
    "buffer_load_dwordx4 v[140:143], v83, s[36:39], s54 offen offset:3072 sc1\n\t"
    "buffer_load_dwordx4 v[144:147], v83, s[36:39], s55 offen offset:0 sc1\n\t"
    "buffer_load_dwordx4 v[148:151], v83, s[36:39], s55 offen offset:1024 sc1\n\t"
    "buffer_load_dwordx4 v[152:155], v84, s[36:39], s52 offen offset:0 sc1\n\t"
    "buffer_load_dwordx4 v[156:159], v84, s[36:39], s52 offen offset:1024 sc1\n\t"
    "buffer_load_dwordx4 v[160:163], v84, s[36:39], s52 offen offset:2048 sc1\n\t"
    "buffer_load_dwordx4 v[164:167], v84, s[36:39], s52 offen offset:3072 sc1\n\t"
    "buffer_load_dwordx4 v[168:171], v84, s[36:39], s53 offen offset:0 sc1\n\t"
    "buffer_load_dwordx4 v[172:175], v84, s[36:39], s53 offen offset:1024 sc1\n\t"
    "s_branch L_rC1_%=\n\t"
    "L_pY1_%=:\n\t"
    "s_mov_b32 s92, 131072\n\t"
    "L_qY1_%=:\n\t"
    "s_sleep 1\n\t"
    "global_load_dword v88, v87, s[84:85] sc1\n\t"
    "s_waitcnt vmcnt(0)\n\t"
    "v_cmp_gt_u32 vcc, 32, v88\n\t"
    "s_nop 1\n\t"
    "s_cbranch_vccz L_rY1_%=\n\t"
    "s_sub_u32 s92, s92, 1\n\t"
    "s_cbranch_scc0 L_qY1_%=\n\t"
    "s_branch L_rY1_%=\n\t"
    "L_pC0_%=:\n\t"
    "s_mov_b32 s92, 131072\n\t"
    "L_qC0_%=:\n\t"
    "global_load_dword v88, v87, s[84:85] sc1\n\t"
    "s_waitcnt vmcnt(0)\n\t"
    "v_readfirstlane_b32 s90, v88\n\t"
    "s_cmp_ge_u32 s90, 16\n\t"
    "s_cbranch_scc1 L_aC0_%=\n\t"
    "s_sleep 2\n\t"
    "s_sub_u32 s92, s92, 1\n\t"
    "s_cbranch_scc0 L_qC0_%=\n\t"
    "L_aC0_%=:\n\t"
    "s_mov_b32 s38, 0xe8000\n\t"
    "buffer_load_dwordx4 v[96:99], v83, s[36:39], s52 offen offset:0 sc1\n\t"
    "buffer_load_dwordx4 v[100:103], v83, s[36:39], s52 offen offset:1024 sc1\n\t"
    "buffer_load_dwordx4 v[104:107], v83, s[36:39], s52 offen offset:2048 sc1\n\t"
    "buffer_load_dwordx4 v[108:111], v83, s[36:39], s52 offen offset:3072 sc1\n\t"
    "buffer_load_dwordx4 v[112:115], v83, s[36:39], s53 offen offset:0 sc1\n\t"
    "buffer_load_dwordx4 v[116:119], v83, s[36:39], s53 offen offset:1024 sc1\n\t"
    "buffer_load_dwordx4 v[120:123], v83, s[36:39], s53 offen offset:2048 sc1\n\t"
    "buffer_load_dwordx4 v[124:127], v83, s[36:39], s53 offen offset:3072 sc1\n\t"
    "buffer_load_dwordx4 v[128:131], v83, s[36:39], s54 offen offset:0 sc1\n\t"
    "buffer_load_dwordx4 v[132:135], v83, s[36:39], s54 offen offset:1024 sc1\n\t"
    "buffer_load_dwordx4 v[136:139], v83, s[36:39], s54 offen offset:2048 sc1\n\t"
    "buffer_load_dwordx4 v[140:143], v83, s[36:39], s54 offen offset:3072 sc1\n\t"
    "buffer_load_dwordx4 v[144:147], v83, s[36:39], s55 offen offset:0 sc1\n\t"
    "buffer_load_dwordx4 v[148:151], v83, s[36:39], s55 offen offset:1024 sc1\n\t"
    "buffer_load_dwordx4 v[152:155], v83, s[36:39], s55 offen offset:2048 sc1\n\t"
    "buffer_load_dwordx4 v[156:159], v83, s[36:39], s55 offen offset:3072 sc1\n\t"
    "buffer_load_dwordx4 v[160:163], v84, s[36:39], s52 offen offset:0 sc1\n\t"
    "buffer_load_dwordx4 v[164:167], v84, s[36:39], s52 offen offset:1024 sc1\n\t"
    "buffer_load_dwordx4 v[168:171], v84, s[36:39], s52 offen offset:2048 sc1\n\t"
    "buffer_load_dwordx4 v[172:175], v84, s[36:39], s52 offen offset:3072 sc1\n\t"
    "s_branch L_rC0_%=\n\t"
    "L_pY0_%=:\n\t"
    "s_mov_b32 s92, 131072\n\t"
    "L_qY0_%=:\n\t"
    "s_sleep 1\n\t"
    "global_load_dword v88, v87, s[84:85] sc1\n\t"
    "s_waitcnt vmcnt(0)\n\t"
    "v_cmp_gt_u32 vcc, 32, v88\n\t"
    "s_nop 1\n\t"
    "s_cbranch_vccz L_rY0_%=\n\t"
    "s_sub_u32 s92, s92, 1\n\t"
    "s_cbranch_scc0 L_qY0_%=\n\t"
    "s_branch L_rY0_%=\n\t"
    "L_pD0_%=:\n\t"
    "s_mov_b32 s92, 131072\n\t"
    "L_qD0_%=:\n\t"
    "s_sleep 2\n\t"
    "global_load_dword v88, v87, s[84:85] sc1\n\t"
    "s_waitcnt vmcnt(0)\n\t"
    "v_readfirstlane_b32 s90, v88\n\t"
    "s_cmp_ge_u32 s90, 16\n\t"
    "s_cbranch_scc1 L_rD0_%=\n\t"
    "s_sub_u32 s92, s92, 1\n\t"
    "s_cbranch_scc0 L_qD0_%=\n\t"
    "s_branch L_rD0_%=\n\t"
    "Lkend_fused:\n\t"
    "L_end_%=:\n\t"
    "s_mov_b32 %0, s24\n\t"
    "s_waitcnt vmcnt(0) lgkmcnt(0)\n\t"
    : "=&s"(fail), "+s"(ka), "+s"(bid), "+s"(wid) :: "v0", "v1", "v2", "v3", "v4", "v5", "v6", "v7", "v8", "v9", "v10", "v11", "v12", "v13", "v14", "v15", "v16", "v17", "v18", "v19", "v20", "v21", "v22", "v23", "v24", "v25", "v26", "v27", "v28", "v29", "v30", "v31", "v32", "v33", "v34", "v35", "v36", "v37", "v38", "v39", "v40", "v41", "v42", "v43", "v44", "v45", "v46", "v47", "v48", "v49", "v50", "v51", "v52", "v53", "v54", "v55", "v56", "v57", "v58", "v59", "v60", "v61", "v62", "v63", "v64", "v65", "v66", "v67", "v68", "v69", "v70", "v71", "v72", "v73", "v74", "v75", "v76", "v77", "v78", "v79", "v80", "v81", "v82", "v83", "v84", "v85", "v86", "v87", "v88", "v89", "v90", "v91", "v92", "v93", "v94", "v95", "v96", "v97", "v98", "v99", "v100", "v101", "v102", "v103", "v104", "v105", "v106", "v107", "v108", "v109", "v110", "v111", "v112", "v113", "v114", "v115", "v116", "v117", "v118", "v119", "v120", "v121", "v122", "v123", "v124", "v125", "v126", "v127", "v128", "v129", "v130", "v131", "v132", "v133", "v134", "v135", "v136", "v137", "v138", "v139", "v140", "v141", "v142", "v143", "v144", "v145", "v146", "v147", "v148", "v149", "v150", "v151", "v152", "v153", "v154", "v155", "v156", "v157", "v158", "v159", "v160", "v161", "v162", "v163", "v164", "v165", "v166", "v167", "v168", "v169", "v170", "v171", "v172", "v173", "v174", "v175", "v176", "v177", "v178", "v179", "v180", "v181", "v182", "v183", "v184", "v185", "v186", "v187", "v188", "v189", "v190", "v191", "v192", "v193", "v194", "v195", "v196", "v197", "v198", "v199", "v200", "v201", "v202", "v203", "v204", "v205", "v206", "v207", "v208", "v209", "v210", "v211", "v212", "v213", "v214", "v215", "v216", "v217", "v218", "v219", "v220", "v221", "v222", "v223", "v224", "v225", "v226", "v227", "v228", "v229", "v230", "v231", "v232", "v233", "v234", "v235", "v236", "v237", "v238", "v239", "v240", "v241", "v242", "v243", "v244", "v245", "v246", "v247", "v248", "v249", "v250", "v251", "v252", "v253", "v254", "v255", "a0", "a1", "a2", "a3", "a4", "a5", "a6", "a7", "a8", "a9", "a10", "a11", "a12", "a13", "a14", "a15", "a16", "a17", "a18", "a19", "a20", "a21", "a22", "a23", "a24", "a25", "a26", "a27", "a28", "a29", "a30", "a31", "a32", "a33", "a34", "a35", "a36", "a37", "a38", "a39", "a40", "a41", "a42", "a43", "a44", "a45", "a46", "a47", "a48", "a49", "a50", "a51", "a52", "a53", "a54", "a55", "a56", "a57", "a58", "a59", "a60", "a61", "a62", "a63", "a64", "a65", "a66", "a67", "a68", "a69", "a70", "a71", "a72", "a73", "a74", "a75", "a76", "a77", "a78", "a79", "a80", "a81", "a82", "a83", "a84", "a85", "a86", "a87", "a88", "a89", "a90", "a91", "a92", "a93", "a94", "a95", "a96", "a97", "a98", "a99", "a100", "a101", "a102", "a103", "a104", "a105", "a106", "a107", "a108", "a109", "a110", "a111", "a112", "a113", "a114", "a115", "a116", "a117", "a118", "a119", "a120", "a121", "a122", "a123", "a124", "a125", "a126", "a127", "a128", "a129", "a130", "a131", "a132", "a133", "a134", "a135", "a136", "a137", "a138", "a139", "a140", "a141", "a142", "a143", "a144", "a145", "a146", "a147", "a148", "a149", "a150", "a151", "a152", "a153", "a154", "a155", "a156", "a157", "a158", "a159", "a160", "a161", "a162", "a163", "a164", "a165", "a166", "a167", "a168", "a169", "a170", "a171", "a172", "a173", "a174", "a175", "a176", "a177", "a178", "a179", "a180", "a181", "a182", "a183", "a184", "a185", "a186", "a187", "a188", "a189", "a190", "a191", "a192", "a193", "a194", "a195", "a196", "a197", "a198", "a199", "a200", "a201", "a202", "a203", "a204", "a205", "a206", "a207", "a208", "a209", "a210", "a211", "a212", "a213", "a214", "a215", "a216", "a217", "a218", "a219", "a220", "a221", "a222", "a223", "a224", "a225", "a226", "a227", "a228", "a229", "a230", "a231", "a232", "a233", "a234", "a235", "a236", "a237", "a238", "a239", "a240", "a241", "a242", "a243", "a244", "a245", "a246", "a247", "a248", "a249", "a250", "a251", "a252", "a253", "a254", "a255", "s8", "s9", "s10", "s11", "s12", "s13", "s14", "s15", "s16", "s17", "s18", "s19", "s20", "s21", "s22", "s23", "s24", "s25", "s26", "s27", "s28", "s29", "s30", "s31", "s34", "s35", "s36", "s37", "s38", "s39", "s40", "s41", "s42", "s43", "s44", "s45", "s46", "s47", "s48", "s49", "s50", "s51", "s52", "s53", "s54", "s55", "s56", "s57", "s58", "s59", "s60", "s61", "s62", "s63", "s64", "s65", "s66", "s67", "s68", "s69", "s70", "s71", "s72", "s73", "s74", "s75", "s76", "s77", "s78", "s79", "s80", "s81", "s82", "s83", "s84", "s85", "s86", "s87", "s88", "s89", "s90", "s91", "s92", "s93", "s94", "s95", "s96", "s97", "s98", "s99", "vcc", "scc", "memory");
    { const int h = (bid & 7) * 4 + (bid >> 6), i = (bid >> 3) & 7;
      for (int k = 0; k < 2; ++k) if ((fail >> k) & 1) attn_slow((char*)ka->ws, (cp16)ka->in[7], (cp16)ka->in[9], h, k ? i : 15 - i, wid);
    }
    asm volatile(
    "s_load_dwordx2 s[20:21], %1, 0x70\n\t"
    "s_load_dwordx2 s[82:83], %1, 0x60\n\t"
    "s_load_dwordx2 s[50:51], %1, 0x68\n\t"
    "v_mbcnt_lo_u32_b32 v76, -1, 0\n\t"
    "s_mov_b32 s58, %3\n\t"
    "v_mbcnt_hi_u32_b32 v76, -1, v76\n\t"
    "v_lshlrev_b32 v76, 4, v76\n\t"
    "s_bfe_u32 s60, %2, 0x30003\n\t"
    "s_and_b32 s59, %2, 7\n\t"
    "s_lshl_b32 s59, s59, 2\n\t"
    "s_lshr_b32 s25, %2, 6\n\t"
    "s_add_u32 s59, s59, s25\n\t"
    "s_lshr_b32 s61, s58, 1\n\t"
    "s_and_b32 s98, s58, 1\n\t"
    "s_lshl_b32 s99, s60, 5\n\t"
    "s_add_u32 s99, s99, s59\n\t"
    "s_mov_b32 s39, 0x00020000\n\t"
    "s_mov_b32 s43, 0x00020000\n\t"
    "s_waitcnt lgkmcnt(0)\n\t"
    "s_add_u32 s84, s20, 0x6000000\n\t"
    "s_addc_u32 s85, s21, 0\n\t"
    "s_bfe_u32 s71, %2, 0x30003\n\t"
    "s_and_b32 s72, %2, 6\n\t"
    "s_lshl_b32 s72, s72, 1\n\t"
    "s_lshr_b32 s25, %2, 6\n\t"
    "s_add_u32 s72, s72, s25\n\t"
    "s_and_b32 s25, %2, 1\n\t"
    "s_lshl1_add_u32 s72, s72, s25\n\t"
    "s_lshl1_add_u32 s73, s58, s25\n\t"
    "s_lshl_b32 s73, s73, 4\n\t"
    "s_lshl_b32 s25, s73, 15\n\t"
    "s_lshl_b32 s26, s71, 7\n\t"
    "s_add_u32 s25, s25, s26\n\t"
    "s_sub_u32 s66, 0x400000, s25\n\t"
    "s_add_u32 s62, s82, s25\n\t"
    "s_addc_u32 s63, s83, 0\n\t"
    "s_mov_b64 s[40:41], s[62:63]\n\t"
    "s_mov_b32 s42, s66\n\t"
    "s_lshl_b32 s45, s58, 13\n\t"
    "v_and_b32 v254, 0xc0, v76\n\t"
    "v_and_b32 v255, 0x30, v76\n\t"
    "v_lshl_add_u32 v254, v254, 4, v255\n\t"
    "v_lshrrev_b32 v255, 8, v76\n\t"
    "v_lshl_add_u32 v254, v255, 7, v254\n\t"
    "v_add_u32 v81, s45, v254\n\t"
    "v_add_u32 v86, 0x10000, v81\n\t"
    "v_lshrrev_b32 v254, 2, v76\n\t"
    "v_and_b32 v254, 64, v254\n\t"
    "v_xor_b32 v254, v254, v76\n\t"
    "v_and_b32 v255, 0x380, v76\n\t"
    "v_mad_u32_u24 v64, v255, 7, v254\n\t"
    "v_add_u32 v65, 0x1c00, v64\n\t"
    "v_add_u32 v66, 0x3800, v64\n\t"
    "v_add_u32 v67, 0x5400, v64\n\t"
    "v_and_b32 v123, 0x380, v76\n\t"
    "v_mad_u32_u24 v121, v123, 31, v76\n\t"
    "v_add_u32 v123, 0x8000, v121\n\t"
    "s_mov_b32 s44, 0\n\t"
    "s_add_u32 m0, s45, 0x18000\n\t"
    "s_nop 0\n\t"
    "buffer_load_dwordx4 v121, s[40:43], s44 offen offset:0 lds\n\t"
    "buffer_load_dwordx4 v121, s[40:43], s44 offen offset:1024 lds\n\t"
    "buffer_load_dwordx4 v121, s[40:43], s44 offen offset:2048 lds\n\t"
    "buffer_load_dwordx4 v121, s[40:43], s44 offen offset:3072 lds\n\t"
    "s_add_u32 s44, s44, 0x8000\n\t"
    "s_add_u32 m0, s45, 0x19000\n\t"
    "s_nop 0\n\t"
    "buffer_load_dwordx4 v121, s[40:43], s44 offen offset:0 lds\n\t"
    "buffer_load_dwordx4 v121, s[40:43], s44 offen offset:1024 lds\n\t"
    "buffer_load_dwordx4 v121, s[40:43], s44 offen offset:2048 lds\n\t"
    "buffer_load_dwordx4 v121, s[40:43], s44 offen offset:3072 lds\n\t"
    "s_add_u32 s44, s44, 0x8000\n\t"
    "s_add_u32 m0, s45, 0x8000\n\t"
    "s_nop 0\n\t"
    "buffer_load_dwordx4 v121, s[40:43], s44 offen offset:0 lds\n\t"
    "buffer_load_dwordx4 v121, s[40:43], s44 offen offset:1024 lds\n\t"
    "buffer_load_dwordx4 v121, s[40:43], s44 offen offset:2048 lds\n\t"
    "buffer_load_dwordx4 v121, s[40:43], s44 offen offset:3072 lds\n\t"
    "s_add_u32 s44, s44, 0x8000\n\t"
    "s_add_u32 m0, s45, 0x9000\n\t"
    "s_nop 0\n\t"
    "buffer_load_dwordx4 v121, s[40:43], s44 offen offset:0 lds\n\t"
    "buffer_load_dwordx4 v121, s[40:43], s44 offen offset:1024 lds\n\t"
    "buffer_load_dwordx4 v121, s[40:43], s44 offen offset:2048 lds\n\t"
    "buffer_load_dwordx4 v121, s[40:43], s44 offen offset:3072 lds\n\t"
    "s_add_u32 s44, s44, 0x8000\n\t"
    "s_lshl1_add_u32 s25, s99, s61\n\t"
    "s_lshl_b32 s25, s25, 12\n\t"
    "s_add_u32 s25, s25, 0x6400000\n\t"
    "s_add_u32 s64, s20, s25\n\t"
    "s_addc_u32 s65, s21, 0\n\t"
    "s_movk_i32 s66, 0x1000\n\t"
    "s_mov_b32 s67, 0x00020000\n\t"
    "s_lshl_b32 s25, s98, 11\n\t"
    "v_lshl_add_u32 v252, v76, 1, s25\n\t"
    "buffer_load_dwordx4 v[228:231], v252, s[64:67], 0 offen offset:0 sc1\n\t"
    "v_subrev_u32 v253, 16, v252\n\t"
    "buffer_load_dwordx4 v[232:235], v252, s[64:67], 0 offen offset:16 sc1\n\t"
    "buffer_load_dwordx4 v[224:227], v253, s[64:67], 0 offen offset:0 sc1\n\t"
    "s_lshl2_add_u32 s26, s99, s58\n\t"
    "s_lshl_b32 s26, s26, 12\n\t"
    "s_add_u32 s26, s26, 0xc00000\n\t"
    "v_add_u32 v252, s26, v76\n\t"
    "global_load_dwordx4 v[244:247], v252, s[84:85] offset:0 sc1\n\t"
    "global_load_dwordx4 v[248:251], v252, s[84:85] offset:1024 sc1\n\t"
    "s_xor_b32 s25, s25, 0x800\n\t"
    "buffer_load_dwordx4 v[236:239], v76, s[64:67], s25 offen offset:0 sc1\n\t"
    "buffer_load_dwordx4 v[240:243], v76, s[64:67], s25 offen offset:1024 sc1\n\t"
    "v_mov_b32 v253, s26\n\t"
    "global_load_dwordx4 v[92:95], v253, s[84:85] offset:2048 sc1\n\t"
    "s_waitcnt vmcnt(0)\n\t"
    "v_and_b32 v32, 0x7fff7fff, v228\n\t"
    "v_and_b32 v33, 0x7fff7fff, v229\n\t"
    "v_and_b32 v34, 0x7fff7fff, v230\n\t"
    "v_and_b32 v35, 0x7fff7fff, v231\n\t"
    "v_and_b32 v36, 0x7fff7fff, v232\n\t"
    "v_and_b32 v37, 0x7fff7fff, v233\n\t"
    "v_and_b32 v38, 0x7fff7fff, v234\n\t"
    "v_and_b32 v39, 0x7fff7fff, v235\n\t"
    "v_and_b32 v40, 0x7fff7fff, v236\n\t"
    "v_and_b32 v41, 0x7fff7fff, v237\n\t"
    "v_and_b32 v42, 0x7fff7fff, v238\n\t"
    "v_and_b32 v43, 0x7fff7fff, v239\n\t"
    "v_and_b32 v44, 0x7fff7fff, v240\n\t"
    "v_and_b32 v45, 0x7fff7fff, v241\n\t"
    "v_and_b32 v46, 0x7fff7fff, v242\n\t"
    "v_and_b32 v47, 0x7fff7fff, v243\n\t"
    "v_pk_max_u16 v32, v32, v40\n\t"
    "v_pk_max_u16 v33, v33, v41\n\t"
    "v_pk_max_u16 v34, v34, v42\n\t"
    "v_pk_max_u16 v35, v35, v43\n\t"
    "v_pk_max_u16 v36, v36, v44\n\t"
    "v_pk_max_u16 v37, v37, v45\n\t"
    "v_pk_max_u16 v38, v38, v46\n\t"
    "v_pk_max_u16 v39, v39, v47\n\t"
    "v_pk_max_u16 v32, v32, v36\n\t"
    "v_pk_max_u16 v33, v33, v37\n\t"
    "v_pk_max_u16 v34, v34, v38\n\t"
    "v_pk_max_u16 v35, v35, v39\n\t"
    "v_pk_max_u16 v32, v32, v34\n\t"
    "v_pk_max_u16 v33, v33, v35\n\t"
    "v_pk_max_u16 v32, v32, v33\n\t"
    "v_max_u32_sdwa v236, v32, v32 dst_sel:DWORD dst_unused:UNUSED_PAD src0_sel:WORD_0 src1_sel:WORD_1\n\t"
    "v_readfirstlane_b32 s27, v93\n\t"
    "v_readfirstlane_b32 s34, v94\n\t"
    "v_readfirstlane_b32 s35, v95\n\t"
    "v_and_b32 v77, 0x3c0, v76\n\t"
    "v_add_u32 v77, v77, v76\n\t"
    "v_lshrrev_b32 v78, 1, v76\n\t"
    "v_and_b32 v78, 64, v78\n\t"
    "v_add3_u32 v77, v77, v78, s45\n\t"
    "v_xor_b32 v78, 64, v77\n\t"
    "v_add_u32 v79, 0x10000, v77\n\t"
    "v_add_u32 v80, 0x10000, v78\n\t"
    "ds_read_b64_tr_b16 a[192:193], v86 offset:32768\n\t"
    "ds_read_b64_tr_b16 a[194:195], v86 offset:33280\n\t"
    "ds_read_b64_tr_b16 a[196:197], v86 offset:32776\n\t"
    "ds_read_b64_tr_b16 a[198:199], v86 offset:33288\n\t"
    "ds_read_b64_tr_b16 a[200:201], v86 offset:32832\n\t"
    "ds_read_b64_tr_b16 a[202:203], v86 offset:33344\n\t"
    "ds_read_b64_tr_b16 a[204:205], v86 offset:32840\n\t"
    "ds_read_b64_tr_b16 a[206:207], v86 offset:33352\n\t"
    "s_add_u32 m0, s45, 0x0\n\t"
    "s_nop 0\n\t"
    "buffer_load_dwordx4 v64, s[40:43], s44 offen offset:0 lds\n\t"
    "buffer_load_dwordx4 v65, s[40:43], s44 offen offset:1024 lds\n\t"
    "buffer_load_dwordx4 v66, s[40:43], s44 offen offset:2048 lds\n\t"
    "buffer_load_dwordx4 v67, s[40:43], s44 offen offset:3072 lds\n\t"
    "s_add_u32 s44, s44, 0x8000\n\t"
    "ds_read_b64_tr_b16 a[240:241], v86 offset:36864\n\t"
    "ds_read_b64_tr_b16 a[242:243], v86 offset:37376\n\t"
    "ds_read_b64_tr_b16 a[244:245], v86 offset:36872\n\t"
    "ds_read_b64_tr_b16 a[246:247], v86 offset:37384\n\t"
    "ds_read_b64_tr_b16 a[248:249], v86 offset:36928\n\t"
    "ds_read_b64_tr_b16 a[250:251], v86 offset:37440\n\t"
    "ds_read_b64_tr_b16 a[252:253], v86 offset:36936\n\t"
    "ds_read_b64_tr_b16 a[254:255], v86 offset:37448\n\t"
    "s_waitcnt lgkmcnt(8)\n\t"
    "s_add_u32 m0, s45, 0x18000\n\t"
    "s_nop 0\n\t"
    "buffer_load_dwordx4 v64, s[40:43], s44 offen offset:0 lds\n\t"
    "buffer_load_dwordx4 v65, s[40:43], s44 offen offset:1024 lds\n\t"
    "buffer_load_dwordx4 v66, s[40:43], s44 offen offset:2048 lds\n\t"
    "buffer_load_dwordx4 v67, s[40:43], s44 offen offset:3072 lds\n\t"
    "s_add_u32 s44, s44, 0x8000\n\t"
    "ds_read_b64_tr_b16 a[160:161], v81 offset:32768\n\t"
    "ds_read_b64_tr_b16 a[162:163], v81 offset:33280\n\t"
    "ds_read_b64_tr_b16 a[164:165], v81 offset:32776\n\t"
    "ds_read_b64_tr_b16 a[166:167], v81 offset:33288\n\t"
    "ds_read_b64_tr_b16 a[168:169], v81 offset:32832\n\t"
    "ds_read_b64_tr_b16 a[170:171], v81 offset:33344\n\t"
    "ds_read_b64_tr_b16 a[172:173], v81 offset:32840\n\t"
    "ds_read_b64_tr_b16 a[174:175], v81 offset:33352\n\t"
    "s_waitcnt lgkmcnt(8)\n\t"
    "s_add_u32 m0, s45, 0x19000\n\t"
    "s_nop 0\n\t"
    "buffer_load_dwordx4 v64, s[40:43], s44 offen offset:0 lds\n\t"
    "buffer_load_dwordx4 v65, s[40:43], s44 offen offset:1024 lds\n\t"
    "buffer_load_dwordx4 v66, s[40:43], s44 offen offset:2048 lds\n\t"
    "buffer_load_dwordx4 v67, s[40:43], s44 offen offset:3072 lds\n\t"
    "s_add_u32 s44, s44, 0x8000\n\t"
    "ds_read_b64_tr_b16 a[176:177], v81 offset:36864\n\t"
    "ds_read_b64_tr_b16 a[178:179], v81 offset:37376\n\t"
    "ds_read_b64_tr_b16 a[180:181], v81 offset:36872\n\t"
    "ds_read_b64_tr_b16 a[182:183], v81 offset:37384\n\t"
    "ds_read_b64_tr_b16 a[184:185], v81 offset:36928\n\t"
    "ds_read_b64_tr_b16 a[186:187], v81 offset:37440\n\t"
    "ds_read_b64_tr_b16 a[188:189], v81 offset:36936\n\t"
    "ds_read_b64_tr_b16 a[190:191], v81 offset:37448\n\t"
    "s_waitcnt lgkmcnt(8)\n\t"
    "s_add_u32 m0, s45, 0x8000\n\t"
    "s_nop 0\n\t"
    "buffer_load_dwordx4 v64, s[40:43], s44 offen offset:0 lds\n\t"
    "buffer_load_dwordx4 v65, s[40:43], s44 offen offset:1024 lds\n\t"
    "buffer_load_dwordx4 v66, s[40:43], s44 offen offset:2048 lds\n\t"
    "buffer_load_dwordx4 v67, s[40:43], s44 offen offset:3072 lds\n\t"
    "s_add_u32 s44, s44, 0x8000\n\t"
    "s_waitcnt vmcnt(12)\n\t"
    "ds_read_b64_tr_b16 a[208:209], v77 offset:0\n\t"
    "ds_read_b64_tr_b16 a[210:211], v77 offset:2048\n\t"
    "ds_read_b64_tr_b16 a[212:213], v77 offset:8\n\t"
    "ds_read_b64_tr_b16 a[214:215], v77 offset:2056\n\t"
    "ds_read_b64_tr_b16 a[216:217], v78 offset:0\n\t"
    "ds_read_b64_tr_b16 a[218:219], v78 offset:2048\n\t"
    "ds_read_b64_tr_b16 a[220:221], v78 offset:8\n\t"
    "ds_read_b64_tr_b16 a[222:223], v78 offset:2056\n\t"
    "s_waitcnt lgkmcnt(8)\n\t"
    "s_add_u32 m0, s45, 0x9000\n\t"
    "s_nop 0\n\t"
    "buffer_load_dwordx4 v64, s[40:43], s44 offen offset:0 lds\n\t"
    "buffer_load_dwordx4 v65, s[40:43], s44 offen offset:1024 lds\n\t"
    "buffer_load_dwordx4 v66, s[40:43], s44 offen offset:2048 lds\n\t"
    "buffer_load_dwordx4 v67, s[40:43], s44 offen offset:3072 lds\n\t"
    "s_add_u32 s44, s44, 0x8000\n\t"
    "s_add_u32 m0, s45, 0x1000\n\t"
    "s_nop 0\n\t"
    "buffer_load_dwordx4 v64, s[40:43], s44 offen offset:0 lds\n\t"
    "buffer_load_dwordx4 v65, s[40:43], s44 offen offset:1024 lds\n\t"
    "buffer_load_dwordx4 v66, s[40:43], s44 offen offset:2048 lds\n\t"
    "buffer_load_dwordx4 v67, s[40:43], s44 offen offset:3072 lds\n\t"
    "s_add_u32 s44, s44, 0x8000\n\t"
    "s_waitcnt vmcnt(0) lgkmcnt(0)\n\t"
    "s_branch Ltail_fused\n\t"
    : "=&s"(fail), "+s"(ka), "+s"(bid), "+s"(wid) :: "v0", "v1", "v2", "v3", "v4", "v5", "v6", "v7", "v8", "v9", "v10", "v11", "v12", "v13", "v14", "v15", "v16", "v17", "v18", "v19", "v20", "v21", "v22", "v23", "v24", "v25", "v26", "v27", "v28", "v29", "v30", "v31", "v32", "v33", "v34", "v35", "v36", "v37", "v38", "v39", "v40", "v41", "v42", "v43", "v44", "v45", "v46", "v47", "v48", "v49", "v50", "v51", "v52", "v53", "v54", "v55", "v56", "v57", "v58", "v59", "v60", "v61", "v62", "v63", "v64", "v65", "v66", "v67", "v68", "v69", "v70", "v71", "v72", "v73", "v74", "v75", "v76", "v77", "v78", "v79", "v80", "v81", "v82", "v83", "v84", "v85", "v86", "v87", "v88", "v89", "v90", "v91", "v92", "v93", "v94", "v95", "v96", "v97", "v98", "v99", "v100", "v101", "v102", "v103", "v104", "v105", "v106", "v107", "v108", "v109", "v110", "v111", "v112", "v113", "v114", "v115", "v116", "v117", "v118", "v119", "v120", "v121", "v122", "v123", "v124", "v125", "v126", "v127", "v128", "v129", "v130", "v131", "v132", "v133", "v134", "v135", "v136", "v137", "v138", "v139", "v140", "v141", "v142", "v143", "v144", "v145", "v146", "v147", "v148", "v149", "v150", "v151", "v152", "v153", "v154", "v155", "v156", "v157", "v158", "v159", "v160", "v161", "v162", "v163", "v164", "v165", "v166", "v167", "v168", "v169", "v170", "v171", "v172", "v173", "v174", "v175", "v176", "v177", "v178", "v179", "v180", "v181", "v182", "v183", "v184", "v185", "v186", "v187", "v188", "v189", "v190", "v191", "v192", "v193", "v194", "v195", "v196", "v197", "v198", "v199", "v200", "v201", "v202", "v203", "v204", "v205", "v206", "v207", "v208", "v209", "v210", "v211", "v212", "v213", "v214", "v215", "v216", "v217", "v218", "v219", "v220", "v221", "v222", "v223", "v224", "v225", "v226", "v227", "v228", "v229", "v230", "v231", "v232", "v233", "v234", "v235", "v236", "v237", "v238", "v239", "v240", "v241", "v242", "v243", "v244", "v245", "v246", "v247", "v248", "v249", "v250", "v251", "v252", "v253", "v254", "v255", "a0", "a1", "a2", "a3", "a4", "a5", "a6", "a7", "a8", "a9", "a10", "a11", "a12", "a13", "a14", "a15", "a16", "a17", "a18", "a19", "a20", "a21", "a22", "a23", "a24", "a25", "a26", "a27", "a28", "a29", "a30", "a31", "a32", "a33", "a34", "a35", "a36", "a37", "a38", "a39", "a40", "a41", "a42", "a43", "a44", "a45", "a46", "a47", "a48", "a49", "a50", "a51", "a52", "a53", "a54", "a55", "a56", "a57", "a58", "a59", "a60", "a61", "a62", "a63", "a64", "a65", "a66", "a67", "a68", "a69", "a70", "a71", "a72", "a73", "a74", "a75", "a76", "a77", "a78", "a79", "a80", "a81", "a82", "a83", "a84", "a85", "a86", "a87", "a88", "a89", "a90", "a91", "a92", "a93", "a94", "a95", "a96", "a97", "a98", "a99", "a100", "a101", "a102", "a103", "a104", "a105", "a106", "a107", "a108", "a109", "a110", "a111", "a112", "a113", "a114", "a115", "a116", "a117", "a118", "a119", "a120", "a121", "a122", "a123", "a124", "a125", "a126", "a127", "a128", "a129", "a130", "a131", "a132", "a133", "a134", "a135", "a136", "a137", "a138", "a139", "a140", "a141", "a142", "a143", "a144", "a145", "a146", "a147", "a148", "a149", "a150", "a151", "a152", "a153", "a154", "a155", "a156", "a157", "a158", "a159", "a160", "a161", "a162", "a163", "a164", "a165", "a166", "a167", "a168", "a169", "a170", "a171", "a172", "a173", "a174", "a175", "a176", "a177", "a178", "a179", "a180", "a181", "a182", "a183", "a184", "a185", "a186", "a187", "a188", "a189", "a190", "a191", "a192", "a193", "a194", "a195", "a196", "a197", "a198", "a199", "a200", "a201", "a202", "a203", "a204", "a205", "a206", "a207", "a208", "a209", "a210", "a211", "a212", "a213", "a214", "a215", "a216", "a217", "a218", "a219", "a220", "a221", "a222", "a223", "a224", "a225", "a226", "a227", "a228", "a229", "a230", "a231", "a232", "a233", "a234", "a235", "a236", "a237", "a238", "a239", "a240", "a241", "a242", "a243", "a244", "a245", "a246", "a247", "a248", "a249", "a250", "a251", "a252", "a253", "a254", "a255", "s8", "s9", "s10", "s11", "s12", "s13", "s14", "s15", "s16", "s17", "s18", "s19", "s20", "s21", "s22", "s23", "s24", "s25", "s26", "s27", "s28", "s29", "s30", "s31", "s34", "s35", "s36", "s37", "s38", "s39", "s40", "s41", "s42", "s43", "s44", "s45", "s46", "s47", "s48", "s49", "s50", "s51", "s52", "s53", "s54", "s55", "s56", "s57", "s58", "s59", "s60", "s61", "s62", "s63", "s64", "s65", "s66", "s67", "s68", "s69", "s70", "s71", "s72", "s73", "s74", "s75", "s76", "s77", "s78", "s79", "s80", "s81", "s82", "s83", "s84", "s85", "s86", "s87", "s88", "s89", "s90", "s91", "s92", "s93", "s94", "s95", "s96", "s97", "s98", "s99", "vcc", "scc", "memory");
}
extern "C" void kernel_launch(void* const* d_in, const int* in_sizes, int n_in, void* d_out, int out_size, void* d_ws, size_t ws_size, hipStream_t stream) {
    const u16* const* in = (const u16* const*)d_in;
    (void)hipFuncSetAttribute((const void*)fused_kernel, hipFuncAttributeMaxDynamicSharedMemorySize, 163840);
    fused_kernel<<<256, 256, 163840, stream>>>(in[0], in[1], in[2], in[3], in[4], in[5], in[6], in[7], in[8], in[9], in[10], in[11], in[12], (u16*)d_out, (char*)d_ws);
}
```
